```python
import torch

def _softmax(x, axis=-1):
    return torch.softmax(x, dim=axis)
def _gelu(x):
    return torch.nn.functional.gelu(x, approximate="tanh")
def _silu(x):
    return torch.nn.functional.silu(x)
def _sigmoid(x):
    return torch.sigmoid(x)
def _layernorm(x, axis=-1, eps=1e-5):
    m = x.mean(dim=axis, keepdim=True)
    v = x.var(dim=axis, keepdim=True, unbiased=False)
    return (x - m) / torch.sqrt(v + eps)
def _scatter_add(tgt, idx, src, axis=0):
    return tgt.clone().index_add_(axis, idx, src)
def _segment_sum(src, seg, n):
    out = torch.zeros((n,) + src.shape[1:], dtype=src.dtype)
    return out.index_add_(0, seg, src)
def _pad(x, pw):
    flat = tuple(v for pair in reversed(pw) for v in pair)
    return torch.nn.functional.pad(x, flat)

kernel_name = 'interleave_layers__sliding_local_dilated__dense_activation_squared_relu__prompt'

def setup_inputs():

    _w = (lambda a: (lambda t: t.to(torch.bfloat16) if t.is_floating_point() else t)(torch.as_tensor(a)))
    def _A_setup():
        B, S, D = 1, 2048, 640
        return {
            "x": _w((lambda *s: torch.randn(*s, dtype=torch.bfloat16) * 0.05)(B, S, D)),
            "wqkv": _w((lambda *s: torch.randn(*s, dtype=torch.bfloat16) * 0.05)(D, 3 * D)),
            "wo": _w((lambda *s: torch.randn(*s, dtype=torch.bfloat16) * 0.05)(D, D)),
        }
    _A_in = _A_setup()

    def _B_setup():
        B, S, D, F = 1, 2048, 640, 1728
        return {
            "x": _w((lambda *s: torch.randn(*s, dtype=torch.bfloat16) * 0.05)(B, S, D)),
            "w1": _w((lambda *s: torch.randn(*s, dtype=torch.bfloat16) * 0.05)(D, F)),
            "b1": _w((lambda *s: torch.randn(*s, dtype=torch.bfloat16) * 0.2)(F)),
            "w2": _w((lambda *s: torch.randn(*s, dtype=torch.bfloat16) * 0.05)(F, D)),
            "b2": _w((lambda *s: torch.randn(*s, dtype=torch.bfloat16) * 0.8)(D)),
        }
    _B_in = _B_setup()

    return {
        "A_x": _A_in["x"],
        "A_wqkv": _A_in["wqkv"],
        "A_wo": _A_in["wo"],
        "B_x": _B_in["x"],
        "B_w1": _B_in["w1"],
        "B_b1": _B_in["b1"],
        "B_w2": _B_in["w2"],
        "B_b2": _B_in["b2"],
    }

def reference(A_x, A_wqkv, A_wo, B_x, B_w1, B_b1, B_w2, B_b2):
    def _A_ref(x, wqkv, wo):
        B, S, D = x.shape
        H = 32; Dh = D // H
        W, dil = 8, 2
        qkv = (lambda x, *d: x.permute(*d))((x @ wqkv).reshape(B, S, 3, H, Dh), 2, 0, 3, 1, 4)
        q, k, v = qkv[0], qkv[1], qkv[2]
        scale = Dh ** -0.5
        pos = torch.arange(S)
        off = torch.arange(W) * dil
        idx = pos[:, None] - off[None, :]
        mask = idx >= 0
        idx_c = torch.where(mask, idx, 0).reshape(-1)
        kg = k[:, :, idx_c, :].reshape(B, H, S, W, Dh)
        vg = v[:, :, idx_c, :].reshape(B, H, S, W, Dh)
        logits = torch.einsum("bhsd,bhswd->bhsw", q, kg) * scale
        logits = torch.where(mask[None, None, :, :], logits, -1e9)
        attn = _softmax(logits, axis=-1)
        o = torch.einsum("bhsw,bhswd->bhsd", attn, vg)
        return (lambda x, *d: x.permute(*d))(o, 0, 2, 1, 3).reshape(B, S, D) @ wo

    def _B_ref(x, w1, b1, w2, b2):
        h = torch.einsum("bsd,df->bsf", x, w1) + b1
        zero = torch.zeros_like(h)
        r = torch.where(h > zero, h, zero)
        h = r * r
        out = torch.einsum("bsf,fd->bsd", h, w2) + b2
        return out

    x = A_x
    x = x + _A_ref(_layernorm(x, axis=-1), A_wqkv, A_wo)
    x = x + _B_ref(_layernorm(x, axis=-1), B_w1, B_b1, B_w2, B_b2)
    x = x + _A_ref(_layernorm(x, axis=-1), A_wqkv, A_wo)
    x = x + _B_ref(_layernorm(x, axis=-1), B_w1, B_b1, B_w2, B_b2)
    return x
```

```cpp
#include <hip/hip_runtime.h>
#include <hip/hip_bf16.h>
#include <stdint.h>

typedef __attribute__((ext_vector_type(8))) short bf16x8;
typedef __attribute__((ext_vector_type(4))) float f32x4;
typedef unsigned short u16;

constexpr int S = 2048, D = 640, F = 1728, H = 32, DH = 20;

__device__ __forceinline__ float bf2f(u16 b) { return __builtin_bit_cast(float, (unsigned)b << 16); }
__device__ __forceinline__ u16 f2bf(float f) { __hip_bfloat16 h = __float2bfloat16(f); return __builtin_bit_cast(u16, h); }

__global__ void k_ln(const u16* __restrict__ x, u16* __restrict__ y) {
  int row = blockIdx.x * 4 + (threadIdx.x >> 6), lane = threadIdx.x & 63;
  float v[10]; float s = 0.f;
  for (int i = 0; i < 10; ++i) { v[i] = bf2f(x[row * D + i * 64 + lane]); s += v[i]; }
  for (int o = 32; o; o >>= 1) s += __shfl_xor(s, o);
  float m = s / D; float q = 0.f;
  for (int i = 0; i < 10; ++i) { float d = v[i] - m; q += d * d; }
  for (int o = 32; o; o >>= 1) q += __shfl_xor(q, o);
  float r = rsqrtf(q / D + 1e-5f);
  for (int i = 0; i < 10; ++i) y[row * D + i * 64 + lane] = f2bf((v[i] - m) * r);
}

template <int MODE>
__global__ __launch_bounds__(256) void k_gemm(const u16* __restrict__ A, const u16* __restrict__ B, u16* __restrict__ C,
                       const u16* __restrict__ bias, const u16* __restrict__ res, int N, int K) {
  __shared__ u16 sA[64][40];
  __shared__ u16 sB[64][40];
  int bm = blockIdx.y * 64, bn = blockIdx.x * 64;
  int t = threadIdx.x, lane = t & 63, w = t >> 6;
  int wm = (w >> 1) * 32, wn = (w & 1) * 32;
  f32x4 acc[2][2] = {};
  for (int k0 = 0; k0 < K; k0 += 32) {
    { int r = t >> 2, c = (t & 3) * 8;
      *(uint4*)&sA[r][c] = *(const uint4*)&A[(size_t)(bm + r) * K + k0 + c]; }
    { int kk = t >> 3, c = (t & 7) * 8;
      uint4 v = *(const uint4*)&B[(size_t)(k0 + kk) * N + bn + c];
      const u16* p = (const u16*)&v;
      for (int j = 0; j < 8; ++j) sB[c + j][kk] = p[j]; }
    __syncthreads();
    bf16x8 a[2], b[2];
    for (int i = 0; i < 2; ++i) {
      a[i] = *(const bf16x8*)&sA[wm + i * 16 + (lane & 15)][(lane >> 4) * 8];
      b[i] = *(const bf16x8*)&sB[wn + i * 16 + (lane & 15)][(lane >> 4) * 8];
    }
    for (int i = 0; i < 2; ++i) for (int j = 0; j < 2; ++j)
      acc[i][j] = __builtin_amdgcn_mfma_f32_16x16x32_bf16(a[i], b[j], acc[i][j], 0, 0, 0);
    __syncthreads();
  }
  for (int i = 0; i < 2; ++i) for (int j = 0; j < 2; ++j) for (int r = 0; r < 4; ++r) {
    int row = bm + wm + i * 16 + (lane >> 4) * 4 + r, col = bn + wn + j * 16 + (lane & 15);
    float v = acc[i][j][r];
    if (MODE == 1) { v = bf2f(f2bf(v)) + bf2f(res[(size_t)row * N + col]); }
    if (MODE == 2) { v = bf2f(f2bf(bf2f(f2bf(v)) + bf2f(bias[col]))); v = v > 0.f ? v : 0.f; v = v * v; }
    if (MODE == 3) { v = bf2f(f2bf(bf2f(f2bf(v)) + bf2f(bias[col]))) + bf2f(res[(size_t)row * N + col]); }
    C[(size_t)row * N + col] = f2bf(v);
  }
}

__global__ void k_attn(const u16* __restrict__ qkv, u16* __restrict__ o) {
  int id = blockIdx.x * 256 + threadIdx.x;
  int s = id / H, h = id % H;
  float q[DH];
  for (int d = 0; d < DH; ++d) q[d] = bf2f(qkv[(size_t)s * 1920 + h * DH + d]);
  float lg[8]; float mx = -1e30f;
  for (int w = 0; w < 8; ++w) {
    int p = s - 2 * w;
    if (p >= 0) { float a = 0.f;
      for (int d = 0; d < DH; ++d) a += q[d] * bf2f(qkv[(size_t)p * 1920 + 640 + h * DH + d]);
      lg[w] = a * 0.22360679774997896f; } else lg[w] = -1e9f;
    mx = fmaxf(mx, lg[w]);
  }
  float sum = 0.f;
  for (int w = 0; w < 8; ++w) { lg[w] = __expf(lg[w] - mx); sum += lg[w]; }
  float inv = 1.f / sum;
  float acc[DH] = {};
  for (int w = 0; w < 8; ++w) {
    int p = s - 2 * w; if (p < 0) p = 0;
    float pw = lg[w] * inv;
    for (int d = 0; d < DH; ++d) acc[d] += pw * bf2f(qkv[(size_t)p * 1920 + 1280 + h * DH + d]);
  }
  for (int d = 0; d < DH; ++d) o[(size_t)s * D + h * DH + d] = f2bf(acc[d]);
}

extern "C" void kernel_launch(void* const* d_in, const int* in_sizes, int n_in,
                              void* d_out, int out_size, void* d_ws, size_t ws_size,
                              hipStream_t stream) {
  const u16* x = (const u16*)d_in[0];
  const u16* wqkv = (const u16*)d_in[1];
  const u16* wo = (const u16*)d_in[2];
  const u16* w1 = (const u16*)d_in[4];
  const u16* b1 = (const u16*)d_in[5];
  const u16* w2 = (const u16*)d_in[6];
  const u16* b2 = (const u16*)d_in[7];
  u16* ws = (u16*)d_ws;
  u16* ln = ws; u16* qkv = ln + S * D; u16* o = qkv + S * 1920; u16* x1 = o + S * D; u16* h = x1 + S * D; u16* x2 = h + S * F;
  const u16* xin = x;
  for (int l = 0; l < 2; ++l) {
    u16* xout = l == 0 ? x2 : (u16*)d_out;
    k_ln<<<S / 4, 256, 0, stream>>>(xin, ln);
    k_gemm<0><<<dim3(1920 / 64, S / 64), 256, 0, stream>>>(ln, wqkv, qkv, nullptr, nullptr, 1920, D);
    k_attn<<<S * H / 256, 256, 0, stream>>>(qkv, o);
    k_gemm<1><<<dim3(D / 64, S / 64), 256, 0, stream>>>(o, wo, x1, nullptr, xin, D, D);
    k_ln<<<S / 4, 256, 0, stream>>>(x1, ln);
    k_gemm<2><<<dim3(F / 64, S / 64), 256, 0, stream>>>(ln, w1, h, b1, nullptr, F, D);
    k_gemm<3><<<dim3(D / 64, S / 64), 256, 0, stream>>>(h, w2, xout, b2, x1, D, F);
    xin = x2;
  }
}
```

```cpp
#include <hip/hip_runtime.h>
#include <stdint.h>

typedef unsigned u32;
typedef unsigned short u16;
#define LAS __attribute__((address_space(3)))

constexpr u32 WS_W = 4096;
constexpr u32 WS_WF = WS_W + (8u << 20);
constexpr u32 WS_A = WS_WF + 262144;
constexpr u32 XS = 45056;
constexpr u32 A_O = 4 * XS, A_L = 2 * XS + 81920;
constexpr u32 A_H = A_O + A_L + 81920;
constexpr u32 A_FL = A_H + 2 * 221184;
constexpr u32 GA = A_FL + 4096;
constexpr u32 WS_END = WS_A + 32 * GA;
constexpr u32 WS_DBG = 64u << 20;
constexpr u32 WS_LIM = WS_END;
constexpr u32 OOB = 0x40000000u;

constexpr u32 L_XT = 24576, L_TAB = 106496, L_MK = L_TAB + 4096, L_RAW = L_MK + 5120, L_ST = L_RAW + 8192, L_BN = L_ST + 4096, L_END = L_BN + 35840;

constexpr u32 sel(bool c, u32 a, u32 b) { return c ? a : b; }
constexpr u32 w2_pos(u32 u, u32 pb) {
  const u32 d = u - pb, i = sel(u < pb, u, u - 6);
  return sel(d < 6, d % 4 * 14 + d / 4, i / 12 * 14 + 2 + i % 12);
}

struct Tab { u32 u[52][2][64][4], w[4][10][64][4], s[256][4][64]; };
constexpr Tab make_tab() {
  Tab T{};
  for (u32 w = 0; w < 4; ++w)
    for (u32 l = 0; l < 64; ++l) {
      const u32 tok = l & 15, qq = l >> 4, tt = w & 1, ch = w >> 1, q4 = l & 3, rn = (l >> 2) & 7, rh = l >> 5;
      const u32 vL = l * 16, vLs = qq * 256 + (tok ^ qq) * 16, t16 = tok * 16, aUb = L_BN + w * 8960, img = aUb + 3072;
      const u32 vPW2 = 1 ? 16384 + (8 * w + rn) * 128 + q4 * 8 : 16384 + (l >> 1) * 128 + (l & 1) * 16, vPW = 1 ? (5 * w + l % 20 / 4) * 128 + q4 * 8 : vPW2 - sel(l < 40, 16384, 16384 + 2560);
      const u32 b2 = (l >> 2) & 1, b3 = (l >> 3) & 1, aX = L_XT + (2 * b3 + ch) * 5120 + q4 * 256, th = 8 * b3 + 4 * tt + qq;
      const u32 aXw0 = aX + ((8 * tt + qq) ^ q4) * 16 + b2 * 1024, aXc0 = aXw0 + b2 * 15360;
      const u32 aXh = (10 * ch + b2) * 1024 + q4 * 256 + (th ^ q4) * 16, aOne = (10 * ch + sel(l < 40, l >> 2, 9)) * 1024 + q4 * 272;
      const u32 aQh = L_RAW + 3072 + (2 * ch + b2) * 256 + th * 16 + (l >> 1 & 1) * 8, aQm = L_RAW + b3 * 4096 + ch * 1024 + (l >> 1 & 3) * 256 + (8 * tt + qq) * 16 + (l & 1) * 8;
      const u32 vP = (l & 31) * 8, aTw = L_TAB + w * 1024 + 512 + l * 4, cb = 16 * (tok >> 2) + 8 * (qq & 1) + 2 * q4;
      const u32 vB1 = 64 * w + cb + sel(qq >= 2, 256, 0), vB2 = sel(qq < 2, 64 * ch + cb, sel(qq == 2 && ch == 0, 128 + 2 * tok, OOB));
      const u32 Ua = 4 * ch + qq, Ub = 8 + rh;
      const u32 aRa = L_XT + tt * 5120 + Ua * 256 + (tok ^ (Ua & 3)) * 16, aRb = L_XT + tt * 5120 + Ub * 256 + (tok ^ (Ub & 3)) * 16 + (qq & 1) * 8;
      u32 vMk = 0;
      for (u32 r = 0; r < 4; ++r) {
        const u32 key = 4 * qq + r;
        vMk |= sel(key >= 9 + tok, 0, 0x101) << r | sel(key <= tok && key + 7 >= tok, 0, 0x10) << r | sel(key > tok && key < 8, 0, 0x1000) << r;
      }
      const u32 vHalo = (l >> 3) * 128 + 128 + vL, vSt = rh * 1536 + vL;
      const u32 aR0 = L_XT + vLs, aR1 = aR0 + 40960, aS = aR0 + w * 20480, aSh = w * 5120 + (l >> 3) * 128 + vL, aRh = L_ST + 3072 + (tok >> 3) * 256 + (tok | 8) * 16, aRz = vLs;
      const u32 aD1 = aR0 + (w ^ 1) * 20480, aD2 = aR0 + (w ^ 2) * 20480, aD3 = aR0 + (w ^ 3) * 20480;
      const u32 vOs = (qq & 1) * 20480 + rh * 256 + t16, vOt = sel(rh, vOs - 256, OOB);
      const u32 vXt = rh * 256 + t16 + (qq & 1) * 8, vSs = sel(qq & 1, OOB, t16 + rh * 4), vFl = sel(l, OOB, 0);
      const u32 aT = L_TAB + w * 1024 + qq * 16, aStR = L_ST + t16, aQw = L_RAW + rh * 768 + w * 256 + vXt, aQr = L_RAW + qq * 1024 + t16;
      const u32 aBw = aUb + tok * 32 + qq * 8, aBr = aUb + l * 8, vFs = tok * 2560 + qq * 16, vFt = tok * 2560 + qq * 8;
      const u32 e[40] = {aXw0, aXc0, aXh, aOne, vPW, vPW2, vP, aRz, aQm, aQh, vMk, aTw, vB1, vB2, aRa, aRb, vL, vHalo, aS, aSh, aR0, aR1, aRh, vSt, aD1, aD2, aD3, vOs, vOt, vXt, vSs, vFl, aT, aQw, aStR, aBw, aBr, vFs, vFt, aQr};
      for (u32 i = 0; i < 40; ++i) T.w[w][i >> 2][l][i & 3] = e[i];
      const u32 la = l / 10, lc = l % 10, c0 = sel(l < 30, 2 * lc, 10 * lc + 4), c1 = c0 + sel(l < 30, 1, 5), wB = ((l >> 2) & 3) * 1024 + qq * 512 + q4 * 64;
      for (u32 m = w; m < 52; m += 4) {
        const u32 u = m - 4, y = m >= 4;
        const u32 zA = la * 256 + sel(lc < 8, (lc >> 2) * 2048 + (lc & 3) * 64, 3072 + lc * 128), d1 = sel(lc < 8, 1024, 64);
        const u32 d[4] = {sel(y, zA, c0 / 5 * 1024 + sel(c0 % 5 < 4, la * 256 + c0 % 5 * 64, 768 + la * 64)), sel(y, zA + d1, c1 / 5 * 1024 + sel(c1 % 5 < 4, la * 256 + c1 % 5 * 64, 768 + la * 64)),
                          sel(y, zA, wB), sel(y, zA + d1, wB + 256)};
        for (u32 i = 0; i < 4; ++i) T.u[m][i >> 1][l][i] = img + sel(l < sel(y, 40, 32), d[i], 5120 + (l & 31) * 16);
        const u32 U = 4 * u + la, jp = U / 10, ur = U % 10, row = sel(u < 20, 80 * jp + sel(ur < 8, 20 * (ur >> 1) + 8 * (ur & 1), 40 * ur - 304), 8 * la);
        T.u[m][1][l][0] = sel(l < sel(y, 40, 28), sel(y, row * 1280 + lc * 16, vL), OOB);
        T.u[m][1][l][1] = T.u[m][1][l][0] + sel(u < 20 && ur >= 8, 20 * 1280, sel(y, 4 * 1280, 4 * 3456));
        T.u[m][0][l][2] = sel(y, vL, qq * 1024 + t16);
        T.u[m][0][l][3] = img + vL;
      }
    }
  for (u32 bid = 0; bid < 256; ++bid)
    for (u32 w = 0; w < 4; ++w) {
      const u32 j = (bid & 1) * 4 + (bid >> 3 & 3), g = (bid >> 1 & 3) * 8 + (bid >> 5);
      const u32 tt = w & 1, ch = w >> 1, wj = WS_W + (j << 20), jp = j, pb = 6 * jp + sel(jp < 6, jp, 6), P0 = pb + w, z0 = 0;
      const u32 no1 = sel(w + 4 < sel(jp < 6, 7, 6), 0, OOB), no3 = sel(w == 3, OOB, 0), noc = sel(ch, OOB, 0), ga = WS_A + g * GA, gp = ga - GA, gt = ga + tt * 20480, G0 = sel(g, 0, OOB);
      const u32 gr = (bid >> 5) * 4 + (bid >> 1 & 3), n = 4 * gr + w, u = n - 80;
      const bool qa = n < 80;
      const u32 pA = w2_pos(u, pb), pB = w2_pos(u + 28, pb), eA = !qa && pA >= 28, eB = !qa && sel(u < 20, u >= 10, pB >= 28);
      const u32 uA = wj + sel(qa, gr * 16384 + w * 256, 757760 + pA * 5120), uB = sel(qa, uA + 327680, sel(u < 20, wj + 655360 + u * 5120, sel(u < 26, wj + 757760 + pB * 5120, OOB)));
      const u32 sU = uA + eA * 2048, sU5 = sel(qa, OOB, uA + 4096), sUs = sel(qa, 4096, 1024), sUe = sUs - eA * 4096, sLB = sel(qa, 3456, 1280);
      const u32 sUB = uB + eB * 2048, sUB5 = sel(qa, OOB, uB + 4096), sUBe = sUs - eB * 4096;
      const u32 sUf = WS_WF + j * 32768 + gr * 128 + w * 8, sFw = WS_WF + j * 32768 + (g >> 3) * 4096;
      const u32 sLd0 = sel(qa, n * 27648 + pb * 64, sel(u < 20, 0, sel(u < 26, (u + 28) * 40960, OOB)) + j * 160), sKb = sel(qa, 128, sel(u < 20, 144, 112));
      const u32 sLd1 = sLd0 + sLB, sLd2 = sLd1 + sLB, sLd3 = sLd2 + sLB;
      const u32 sXb0 = sel(g, g * 81920 - 20480 + tt * 10240 + ch * 640, OOB), sB1 = pb * 64, sB2 = j * 160, sDb = WS_DBG + (bid * 4 + w) * 512;
      const u32 sEp = sel(bid | w, OOB, 0), sGm = sel(g, 0, 0xff00), sGp = sel(g, gp, ga) + A_FL + 256;
      const u32 sWq = wj + w * 4096, sWp = sWq + no1, sWo = wj + 655360 + w * 25600, sW2 = wj + 757760 + w * 71680, sW2x = sW2 + noc;
      const u32 sXm = ga + w * 5120, sXh = gp + 20480 + w * 5120 + G0, sO0 = gt + w * 5120, sO1 = sXm + (tt ^ 1) * 20480;
      const u32 sHa = ga + w * 12288, sHb = sHa + 6144, sHm = sel(pb > 12 * w, sel(pb < 12 * w + 12, ~0u << (pb - 12 * w) % 32, 0), ~0u), sHt = tt * 55296;
      const u32 sOs = ga + j * 2560 + w * 512, sOt = ga + j * 2560 + 2048 + ch * 256 + tt * 8;
      const u32 sXs = gt + j * 2560 + ch * 1024, sXt = gt + j * 2560 + 2048 + noc, sHs0 = ga + P0 * 1024, sHs1 = sHs0 + 4096 + no1;
      const u32 sSs = ga + tt * 2048 + j * 256 + ch * 8, sSl = ga + w * 512, sSlh = gp + w * 512 + G0;
      const u32 sSa = ga + (8 * w + j) * 1024, sSb = sSa + noc, sSc = sSa + no3;
      const u32 sSh = ga + A_H + w * 55296 + pb * 1024, sSh7 = sel(jp < 6, sSh, OOB), sFp = ga + A_FL, sFf = ga + A_FL + (4 * j + w) * 8;
      const u32 sFo = g * 81920 + tt * 40960 + 160 * j + 64 * ch, sFot = sFo + 128 + noc, sMk = sel(ch, 0, 0x3f800000u), sRj = (j >> 1) * 15360 + j * 2560, sRx = (j & 1) * 32;
      const u32 cQ0 = 0xffff, cCnt = 200000, sPe = 0x05040100, sPo = 0x07060302, cOne = 0x3f803f80, cNeg = 0xfe967699, cSg = 0x80008000;
      const u32 e[64] = {sLd0, sLd1, sLd2, sLd3, sSb, sSc, sUe, sXb0, sU, sUs, sU5, sUf, sFw, sUBe, sUB, sUB5, sEp, cSg, sGm, sGp, w, sB1, sB2, sKb, sDb, cCnt, cQ0, z0, sPe, sPo, cOne, cNeg, sWq, sWp, sWo, sW2, sW2x, sO0, sO1, sHa, sHb, sHm, sHt, sXm, sXh, sOs, sOt, sXs, sXt, sHs0, sHs1, sSs, sSl, sSlh, sSa, sFp, sFo, sFot, sMk, sFf, sSh, sSh7, sRj, sRx};
      for (u32 i = 0; i < 64; ++i) T.s[bid][w][i] = e[i];
    }
  return T;
}
static __constant__ const Tab tab = make_tab();

#define RS(n) const void* p##n, u32 s##n, u32 t##n
__global__ __launch_bounds__(256, 1) void k_main(RS(a), RS(b), const void* c0, const void* c1, RS(d), RS(e), RS(f), const void* g0, const void* g1, RS(h), RS(i), RS(j), RS(k), RS(l), RS(m)) {
  __shared__ __attribute__((aligned(16))) char smem[L_END];
  asm volatile("" :: "v"((LAS char*)smem));
  asm volatile(
"s_cmp_lt_u32 s2, 0xa0\n"
"s_cselect_b32 s3, 0, 64\n"
"s_load_dwordx16 s[4:19], s[0:1], s3\n"
"s_cselect_b32 s20, 30, 40\n"
"s_cselect_b32 s21, 15, 20\n"
"v_readfirstlane_b32 s90, v0\n"
"s_getpc_b64 s[84:85]\n"
".Lcode_first:\n"
"s_lshl_b32 s91, s2, 10\n"
"s_lshr_b32 s86, s91, 2\n"
"s_and_b32 s86, s86, 0xf800\n"
"v_lshl_add_u32 v32, v0, 3, s86\n"
"v_min_u32 v32, .Lcode_last-.Lcode_first-4, v32\n"
"s_lshl2_add_u32 s91, s90, s91\n"
"global_load_dword v252, v32, s[84:85]\n"
"s_mov_b32 s94, 0x24000\n"
"s_mov_b32 s95, 0x20000\n"
"v_and_b32 v36, 63, v0\n"
"v_lshlrev_b32 v33, 4, v36\n"
"s_and_b32 s22, s91, 0xffff8000\n"
"s_and_b32 s89, s91, 0x1800\n"
"s_lshl2_add_u32 s22, s89, s22\n"
"s_add_i32 s88, s22, 0xfffda000\n"
"s_max_i32 s88, s88, 0\n"
"s_lshl_b32 s89, s90, 5\n"
"s_add_u32 s88, s88, s89\n"
"buffer_load_dwordx4 v[248:251], v33, s[92:95], s88 offen offset:0\n"
"buffer_load_dwordx4 v[244:247], v33, s[92:95], s88 offen offset:1024\n"
"s_mul_i32 s96, s90, 0xa0\n"
"s_add_u32 s96, s96, 0x1a000\n"
"s_mov_b32 s24, 0x1a000\n"
"buffer_load_dword v240, v32, s[92:95], s24 offen\n"
"s_mul_i32 s23, s3, 0x8c\n"
"s_addk_i32 s23, 0x460\n"
"v_mul_u32_u24 v37, 26, v36\n"
"v_lshrrev_b32 v37, 8, v37\n"
"v_mad_u32_u24 v37, v37, s23, v33\n"
"v_cmp_gt_u32 vcc, s20, v36\n"
"s_mul_i32 s23, s3, 0xffffffd8\n"
"s_addk_i32 s23, 0xf00\n"
"v_cndmask_b32 v34, 2.0, v37, vcc\n"
"v_lshl_add_u32 v253, s23, 2, v34\n"
"s_add_u32 s22, s22, s89\n"
"s_mul_i32 s24, s3, 0xa00\n"
"s_sub_u32 s22, s22, s24\n"
"s_mul_i32 s22, s22, s21\n"
"s_and_b32 s24, s2, 1\n"
"s_bfe_u32 s25, s2, 0x20003\n"
"s_lshl2_add_u32 s27, s24, s25\n"
"s_mul_i32 s24, s27, 0xa0\n"
"s_add_u32 s84, s22, s24\n"
"s_add_u32 s85, s84, s23\n"
"s_add_u32 s86, s85, s23\n"
"s_add_u32 s87, s86, s23\n"
"s_lshl_b32 s26, s27, 8\n"
"s_add_u32 s26, s26, s90\n"
"s_mul_i32 s26, s26, 50\n"
"v_min_u32 v38, 24, v36\n"
"v_lshlrev_b32 v242, 7, v38\n"
"s_and_b32 s24, s2, 6\n"
"s_lshr_b32 s25, s2, 5\n"
"s_lshl2_add_u32 s24, s24, s25\n"
"s_mul_i32 s30, s24, 0xd7000\n"
"s_mul_i32 s24, s24, 0x14000\n"
"s_add_u32 s26, s26, s24\n"
"s_addk_i32 s26, 0xb000\n"
"s_max_i32 s3, s26, 0\n"
"s_mul_i32 s25, s90, 0x140\n"
"s_add_u32 s2, s24, s25\n"
"v_lshlrev_b32 v37, 12, v36\n"
"v_cmp_gt_u32 vcc, 4, v36\n"
"s_lshr_b32 s28, s91, 13\n"
"s_min_u32 s29, s28, 9\n"
"s_min_u32 s28, s28, 26\n"
"v_cndmask_b32 v243, 2.0, v37, vcc\n"
"s_lshl_b32 s28, s28, 7\n"
"s_lshl_b32 s29, s29, 7\n"
"s_mov_b32 vcc_lo, s88\n"
"v_lshrrev_b32 v37, 3, v36\n"
"v_mul_u32_u24 v37, 0x480, v37\n"
"v_add_u32 v35, v33, v37\n"
"s_add_u32 s30, s30, 0x741000\n"
"s_lshl_b32 s31, s27, 20\n"
"s_lshl_b32 s32, s27, 15\n"
"s_add_u32 s32, s32, 0x801000\n"
"v_lshlrev_b32 v37, 19, v36\n"
"v_add_u32 v238, s31, v37\n"
"v_add_u32 v37, s30, v37\n"
"v_cmp_gt_u32 s[34:35], 2, v36\n"
"v_cmp_gt_u32 s[36:37], 4, v36\n"
"v_mov_b32 v38, s32\n"
"v_cndmask_b32 v238, v37, v238, s[34:35]\n"
"v_cndmask_b32 v238, v38, v238, s[36:37]\n"
"s_waitcnt lgkmcnt(0)\n"
"s_load_dword vcc_hi, s[12:13], s28\n"
"s_load_dword vcc_hi, s[14:15], s29\n"
"s_load_dwordx16 s[20:35], s[92:93], s91 offset:147456\n"
"s_load_dwordx16 s[36:51], s[92:93], s91 offset:147520\n"
"s_load_dwordx16 s[52:67], s[92:93], s91 offset:147584\n"
"s_load_dwordx16 s[68:83], s[92:93], s91 offset:147648\n"
"s_load_dwordx2 s[88:89], s[8:9], 0\n"
"buffer_load_dwordx4 v[232:235], v34, s[16:19], s84 offen\n"
"buffer_load_dwordx4 v[228:231], v34, s[16:19], s85 offen\n"
"buffer_load_dwordx4 v[224:227], v34, s[16:19], s86 offen\n"
"buffer_load_dwordx4 v[220:223], v34, s[16:19], s87 offen\n"
"buffer_load_dwordx4 v[216:219], v253, s[16:19], s84 offen\n"
"buffer_load_dwordx4 v[212:215], v253, s[16:19], s85 offen\n"
"buffer_load_dwordx4 v[208:211], v253, s[16:19], s86 offen\n"
"buffer_load_dwordx4 v[204:207], v253, s[16:19], s87 offen\n"
"buffer_load_dword v239, v238, s[8:11], 0 offen offset:4000\n"
"s_waitcnt lgkmcnt(0)\n"
"s_load_dwordx4 s[84:87], s[0:1], s43\n"
"s_add_u32 s88, s88, 0x7f4a7c15\n"
"s_addc_u32 s89, s89, 0x9e3779b9\n"
"v_mov_b32 v254, s88\n"
"v_mov_b32 v255, s89\n"
"s_add_u32 s90, s52, 0x0\n"
"s_waitcnt vmcnt(7)\n"
"v_perm_b32 v28, v228, v232, s48\n"
"s_waitcnt vmcnt(5)\n"
"v_perm_b32 v29, v220, v224, s48\n"
"s_waitcnt vmcnt(3)\n"
"v_perm_b32 v30, v212, v216, s48\n"
"s_waitcnt vmcnt(1)\n"
"v_perm_b32 v31, v204, v208, s48\n"
"buffer_load_dword v241, v242, s[4:7], s3 offen\n"
"v_perm_b32 v36, v228, v232, s49\n"
"v_perm_b32 v37, v220, v224, s49\n"
"v_perm_b32 v38, v212, v216, s49\n"
"v_perm_b32 v39, v204, v208, s49\n"
"v_perm_b32 v40, v229, v233, s48\n"
"v_perm_b32 v41, v221, v225, s48\n"
"v_perm_b32 v42, v213, v217, s48\n"
"v_perm_b32 v43, v205, v209, s48\n"
"v_perm_b32 v44, v229, v233, s49\n"
"v_perm_b32 v45, v221, v225, s49\n"
"v_perm_b32 v46, v213, v217, s49\n"
"v_perm_b32 v47, v205, v209, s49\n"
"ds_write_b128 v248, v[28:31] offset:0\n"
"ds_write_b128 v248, v[36:39] offset:16\n"
"ds_write_b128 v248, v[40:43] offset:32\n"
"ds_write_b128 v248, v[44:47] offset:48\n"
"v_perm_b32 v48, v230, v234, s48\n"
"v_perm_b32 v49, v222, v226, s48\n"
"v_perm_b32 v50, v214, v218, s48\n"
"v_perm_b32 v51, v206, v210, s48\n"
"v_perm_b32 v52, v230, v234, s49\n"
"v_perm_b32 v53, v222, v226, s49\n"
"v_perm_b32 v54, v214, v218, s49\n"
"v_perm_b32 v55, v206, v210, s49\n"
"v_perm_b32 v56, v231, v235, s48\n"
"v_perm_b32 v57, v223, v227, s48\n"
"v_perm_b32 v58, v215, v219, s48\n"
"v_perm_b32 v59, v207, v211, s48\n"
"v_perm_b32 v60, v231, v235, s49\n"
"v_perm_b32 v61, v223, v227, s49\n"
"v_perm_b32 v62, v215, v219, s49\n"
"v_perm_b32 v63, v207, v211, s49\n"
"ds_write_b128 v249, v[48:51] offset:0\n"
"ds_write_b128 v249, v[52:55] offset:16\n"
"ds_write_b128 v249, v[56:59] offset:32\n"
"ds_write_b128 v249, v[60:63] offset:48\n"
"ds_read_b128 v[64:67], v251 offset:0\n"
"ds_read_b128 v[68:71], v251 offset:1024\n"
"ds_read_b128 v[72:75], v251 offset:2048\n"
"ds_read_b128 v[76:79], v251 offset:3072\n"
"ds_read_b128 v[80:83], v251 offset:4096\n"
"s_waitcnt lgkmcnt(4)\n"
"buffer_store_dwordx4 v[64:67], v250, s[8:11], s28 offen sc1\n"
"s_add_u32 s28, s28, s29\n"
"s_waitcnt lgkmcnt(3)\n"
"buffer_store_dwordx4 v[68:71], v250, s[8:11], s28 offen sc1\n"
"s_add_u32 s28, s28, s26\n"
"s_waitcnt lgkmcnt(2)\n"
"buffer_store_dwordx4 v[72:75], v250, s[8:11], s28 offen sc1\n"
"s_add_u32 s28, s28, s29\n"
"s_waitcnt lgkmcnt(1)\n"
"buffer_store_dwordx4 v[76:79], v250, s[8:11], s28 offen sc1\n"
"v_mov_b32 v84, 0\n"
"v_mov_b32 v85, 0\n"
"v_mov_b32 v86, 0\n"
"v_mov_b32 v87, 0\n"
"s_waitcnt lgkmcnt(0)\n"
"buffer_store_dwordx4 v[80:83], v250, s[8:11], s30 offen sc1\n"
"s_barrier\n"
"buffer_load_dwordx4 v[88:91], v33, s[92:95], s96 offen offset:0\n"
"buffer_load_dwordx4 v[16:19], v33, s[92:95], s96 offen offset:1024\n"
"s_add_u32 s3, s2, 0x0\n"
"buffer_load_dwordx4 v[92:95], v35, s[4:7], s3 offen offset:0\n"
"buffer_load_dwordx4 v[96:99], v35, s[4:7], s3 offen offset:128\n"
"buffer_load_dwordx4 v[100:103], v35, s[4:7], s3 offen offset:256\n"
"buffer_load_dwordx4 v[104:107], v35, s[4:7], s3 offen offset:384\n"
"buffer_load_dwordx4 v[108:111], v35, s[4:7], s3 offen offset:512\n"
"buffer_load_dwordx4 v[112:115], v35, s[4:7], s3 offen offset:640\n"
"buffer_load_dwordx4 v[116:119], v35, s[4:7], s3 offen offset:768\n"
"buffer_load_dwordx4 v[120:123], v35, s[4:7], s3 offen offset:896\n"
"buffer_load_dwordx4 v[124:127], v35, s[4:7], s3 offen offset:1024\n"
"buffer_load_dwordx4 v[128:131], v35, s[4:7], s3 offen offset:1152\n"
"s_add_u32 s3, s2, 0x2800\n"
"buffer_load_dwordx4 v[132:135], v35, s[4:7], s3 offen offset:0\n"
"buffer_load_dwordx4 v[136:139], v35, s[4:7], s3 offen offset:128\n"
"s_waitcnt vmcnt(15)\n"
"buffer_store_dwordx2 v[254:255], v243, s[8:11], s31 offen sc1\n"
"s_waitcnt lgkmcnt(0)\n"
"buffer_load_dwordx4 v[140:143], v35, s[4:7], s3 offen offset:256\n"
"buffer_load_dwordx4 v[144:147], v35, s[4:7], s3 offen offset:384\n"
"buffer_load_dwordx4 v[148:151], v35, s[4:7], s3 offen offset:512\n"
"s_waitcnt vmcnt(15)\n"
"ds_write_b128 v88, v[92:95] offset:0\n"
"buffer_load_dwordx4 v[152:155], v35, s[4:7], s3 offen offset:640\n"
"s_waitcnt vmcnt(15)\n"
"ds_write_b128 v88, v[96:99] offset:2048\n"
"buffer_load_dwordx4 v[156:159], v35, s[4:7], s3 offen offset:768\n"
"s_waitcnt vmcnt(15)\n"
"ds_write_b128 v89, v[100:103] offset:4096\n"
"buffer_load_dwordx4 v[160:163], v35, s[4:7], s3 offen offset:896\n"
"s_waitcnt vmcnt(15)\n"
"ds_write_b128 v88, v[104:107] offset:21504\n"
"buffer_load_dwordx4 v[164:167], v35, s[4:7], s3 offen offset:1024\n"
"s_waitcnt vmcnt(15) lgkmcnt(3)\n"
"ds_write_b128 v88, v[108:111] offset:23552\n"
"buffer_load_dwordx4 v[168:171], v35, s[4:7], s3 offen offset:1152\n"
"s_add_u32 s3, s27, 0x0\n"
"s_waitcnt vmcnt(15) lgkmcnt(3)\n"
"ds_write_b128 v88, v[112:115] offset:40960\n"
"buffer_load_dwordx4 v[172:175], v35, s[4:7], s3 offen offset:0\n"
"s_waitcnt vmcnt(15) lgkmcnt(3)\n"
"ds_write_b128 v88, v[116:119] offset:43008\n"
"buffer_load_dwordx4 v[176:179], v35, s[4:7], s3 offen offset:128\n"
"s_waitcnt vmcnt(15) lgkmcnt(3)\n"
"ds_write_b128 v89, v[120:123] offset:45056\n"
"buffer_load_dwordx4 v[180:183], v35, s[4:7], s3 offen offset:256\n"
"s_waitcnt vmcnt(15) lgkmcnt(3)\n"
"ds_write_b128 v88, v[124:127] offset:62464\n"
"buffer_load_dwordx4 v[184:187], v35, s[4:7], s3 offen offset:384\n"
"s_waitcnt vmcnt(15) lgkmcnt(3)\n"
"ds_write_b128 v88, v[128:131] offset:64512\n"
"buffer_load_dwordx4 v[188:191], v35, s[4:7], s3 offen offset:512\n"
"s_waitcnt vmcnt(15) lgkmcnt(3)\n"
"ds_write_b128 v88, v[132:135] offset:64\n"
"buffer_load_dwordx4 v[192:195], v33, s[92:95], s96 offen offset:2048\n"
"s_waitcnt vmcnt(15) lgkmcnt(3)\n"
"ds_write_b128 v88, v[136:139] offset:2112\n"
"buffer_load_dwordx4 v[196:199], v33, s[92:95], s96 offen offset:3072\n"
"s_add_u32 s96, s96, 0x1000\n"
"buffer_load_dwordx4 v[0:3], v33, s[92:95], s96 offen offset:0\n"
"s_waitcnt vmcnt(15) lgkmcnt(2)\n"
"ds_write_b128 v89, v[140:143] offset:4160\n"
"buffer_load_dwordx4 v[4:7], v33, s[92:95], s96 offen offset:1024\n"
"s_waitcnt vmcnt(15) lgkmcnt(2)\n"
"ds_write_b128 v88, v[144:147] offset:21568\n"
"buffer_load_dwordx4 v[8:11], v33, s[92:95], s96 offen offset:2048\n"
"s_waitcnt vmcnt(15) lgkmcnt(2)\n"
"ds_write_b128 v88, v[148:151] offset:23616\n"
"buffer_load_dwordx4 v[12:15], v33, s[92:95], s96 offen offset:3072\n"
"s_waitcnt vmcnt(15)\n"
"ds_write_b128 v88, v[152:155] offset:41024\n"
"s_add_u32 s96, s96, 0x1000\n"
"buffer_load_dwordx4 v[20:23], v33, s[92:95], s96 offen offset:0\n"
"s_waitcnt vmcnt(15) lgkmcnt(3)\n"
"ds_write_b128 v88, v[156:159] offset:43072\n"
"buffer_load_dwordx4 v[24:27], v33, s[92:95], s96 offen offset:1024\n"
"s_waitcnt vmcnt(15) lgkmcnt(3)\n"
"ds_write_b128 v89, v[160:163] offset:45120\n"
"s_load_dwordx4 s[16:19], s[0:1], 0xa0\n"
"s_load_dwordx4 s[4:7], s[0:1], 0xb0\n"
"s_load_dwordx4 s[12:15], s[0:1], 0xc0\n"
"buffer_load_dwordx2 v[200:201], v16, s[8:11], s32 offen sc1\n"
"s_waitcnt vmcnt(15) lgkmcnt(3)\n"
"ds_write_b128 v88, v[164:167] offset:62528\n"
"buffer_load_dwordx4 v[28:31], v244, s[84:87], s20 offen\n"
"s_waitcnt vmcnt(15) lgkmcnt(3)\n"
"ds_write_b128 v88, v[168:171] offset:64576\n"
"buffer_load_dwordx4 v[36:39], v244, s[84:87], s21 offen\n"
"s_waitcnt vmcnt(15) lgkmcnt(3)\n"
"ds_write_b128 v90, v[172:175] offset:0\n"
"buffer_load_dwordx4 v[40:43], v244, s[84:87], s22 offen\n"
"s_waitcnt vmcnt(15) lgkmcnt(3)\n"
"ds_write_b128 v90, v[176:179] offset:2048\n"
"buffer_load_dwordx4 v[44:47], v244, s[84:87], s23 offen\n"
"s_waitcnt vmcnt(15) lgkmcnt(3)\n"
"ds_write_b128 v90, v[180:183] offset:4096\n"
"buffer_load_dwordx4 v[228:231], v245, s[84:87], s20 offen\n"
"s_waitcnt vmcnt(15) lgkmcnt(3)\n"
"ds_write_b128 v90, v[184:187] offset:6144\n"
"buffer_load_dwordx4 v[232:235], v245, s[84:87], s21 offen\n"
"s_waitcnt vmcnt(15) lgkmcnt(3)\n"
"ds_write_b128 v90, v[188:191] offset:8192\n"
"v_mov_b32 v220, s50\n"
"v_mov_b32 v221, s50\n"
"v_mov_b32 v222, s50\n"
"v_mov_b32 v223, s50\n"
"ds_write_b128 v91, v[220:223] offset:0\n"
"buffer_load_dwordx4 v[224:227], v245, s[84:87], s22 offen\n"
"buffer_load_dwordx4 v[212:215], v245, s[84:87], s23 offen\n"
"s_waitcnt vmcnt(8) lgkmcnt(0)\n"
"v_cmp_ne_u64 vcc, s[88:89], v[200:201]\n"
"s_cbranch_vccnz Ls0_%=\n"
"Lb0_%=:\n"
"s_barrier\n"
"buffer_load_dwordx4 a[0:3], v0, s[8:11], s90 offen offset:0 sc1\n"
"buffer_load_dwordx4 a[4:7], v0, s[8:11], s90 offen offset:1024 sc1\n"
"ds_read_b128 v[216:219], v19 offset:0\n"
"ds_read_b128 v[204:207], v4 offset:0\n"
"ds_read_b128 v[208:211], v4 offset:5120\n"
"ds_read_b128 v[48:51], v19 offset:1024\n"
"ds_read_b128 v[52:55], v4 offset:1024\n"
"ds_read_b128 v[56:59], v4 offset:6144\n"
"ds_read_b128 v[60:63], v4 offset:10240\n"
"ds_read_b128 v[64:67], v4 offset:15360\n"
"buffer_load_dwordx4 a[8:11], v0, s[8:11], s90 offen offset:2048 sc1\n"
"ds_read_b128 v[68:71], v4 offset:11264\n"
"ds_read_b128 v[72:75], v4 offset:16384\n"
"ds_read_b128 v[76:79], v4 offset:12288\n"
"ds_read_b128 v[80:83], v4 offset:17408\n"
"v_dot2c_f32_bf16 v84, s50, v92\n"
"v_dot2c_f32_bf16 v85, v92, v92\n"
"v_dot2c_f32_bf16 v86, s50, v93\n"
"v_dot2c_f32_bf16 v87, v93, v93\n"
"v_dot2c_f32_bf16 v84, s50, v94\n"
"v_dot2c_f32_bf16 v85, v94, v94\n"
"v_dot2c_f32_bf16 v86, s50, v95\n"
"v_dot2c_f32_bf16 v87, v95, v95\n"
"v_dot2c_f32_bf16 v84, s50, v96\n"
"v_dot2c_f32_bf16 v85, v96, v96\n"
"v_dot2c_f32_bf16 v86, s50, v97\n"
"v_dot2c_f32_bf16 v87, v97, v97\n"
"v_dot2c_f32_bf16 v84, s50, v98\n"
"v_dot2c_f32_bf16 v85, v98, v98\n"
"v_dot2c_f32_bf16 v86, s50, v99\n"
"v_dot2c_f32_bf16 v87, v99, v99\n"
"v_dot2c_f32_bf16 v84, s50, v100\n"
"v_dot2c_f32_bf16 v85, v100, v100\n"
"v_dot2c_f32_bf16 v86, s50, v101\n"
"v_dot2c_f32_bf16 v87, v101, v101\n"
"v_dot2c_f32_bf16 v84, s50, v102\n"
"v_dot2c_f32_bf16 v85, v102, v102\n"
"v_dot2c_f32_bf16 v86, s50, v103\n"
"v_dot2c_f32_bf16 v87, v103, v103\n"
"v_dot2c_f32_bf16 v84, s50, v104\n"
"v_dot2c_f32_bf16 v85, v104, v104\n"
"v_dot2c_f32_bf16 v86, s50, v105\n"
"v_dot2c_f32_bf16 v87, v105, v105\n"
"buffer_load_dwordx4 a[12:15], v0, s[8:11], s90 offen offset:3072 sc1\n"
"s_add_u32 s90, s52, 0x4000\n"
"buffer_load_dwordx4 a[16:19], v0, s[8:11], s90 offen offset:0 sc1\n"
"v_dot2c_f32_bf16 v84, s50, v106\n"
"v_dot2c_f32_bf16 v85, v106, v106\n"
"v_dot2c_f32_bf16 v86, s50, v107\n"
"v_dot2c_f32_bf16 v87, v107, v107\n"
"v_dot2c_f32_bf16 v84, s50, v108\n"
"v_dot2c_f32_bf16 v85, v108, v108\n"
"v_dot2c_f32_bf16 v86, s50, v109\n"
"v_dot2c_f32_bf16 v87, v109, v109\n"
"v_dot2c_f32_bf16 v84, s50, v110\n"
"v_dot2c_f32_bf16 v85, v110, v110\n"
"v_dot2c_f32_bf16 v86, s50, v111\n"
"v_dot2c_f32_bf16 v87, v111, v111\n"
"v_dot2c_f32_bf16 v84, s50, v112\n"
"v_dot2c_f32_bf16 v85, v112, v112\n"
"v_dot2c_f32_bf16 v86, s50, v113\n"
"v_dot2c_f32_bf16 v87, v113, v113\n"
"v_dot2c_f32_bf16 v84, s50, v114\n"
"v_dot2c_f32_bf16 v85, v114, v114\n"
"v_dot2c_f32_bf16 v86, s50, v115\n"
"v_dot2c_f32_bf16 v87, v115, v115\n"
"v_dot2c_f32_bf16 v84, s50, v116\n"
"v_dot2c_f32_bf16 v85, v116, v116\n"
"v_dot2c_f32_bf16 v86, s50, v117\n"
"v_dot2c_f32_bf16 v87, v117, v117\n"
"v_dot2c_f32_bf16 v84, s50, v118\n"
"v_dot2c_f32_bf16 v85, v118, v118\n"
"v_dot2c_f32_bf16 v86, s50, v119\n"
"v_dot2c_f32_bf16 v87, v119, v119\n"
"v_dot2c_f32_bf16 v84, s50, v120\n"
"buffer_load_dwordx4 a[20:23], v0, s[8:11], s90 offen offset:1024 sc1\n"
"buffer_load_dwordx4 a[24:27], v0, s[8:11], s90 offen offset:2048 sc1\n"
"v_dot2c_f32_bf16 v85, v120, v120\n"
"v_dot2c_f32_bf16 v86, s50, v121\n"
"v_dot2c_f32_bf16 v87, v121, v121\n"
"v_dot2c_f32_bf16 v84, s50, v122\n"
"v_dot2c_f32_bf16 v85, v122, v122\n"
"v_dot2c_f32_bf16 v86, s50, v123\n"
"v_dot2c_f32_bf16 v87, v123, v123\n"
"v_dot2c_f32_bf16 v84, s50, v124\n"
"v_dot2c_f32_bf16 v85, v124, v124\n"
"v_dot2c_f32_bf16 v86, s50, v125\n"
"v_dot2c_f32_bf16 v87, v125, v125\n"
"v_dot2c_f32_bf16 v84, s50, v126\n"
"v_dot2c_f32_bf16 v85, v126, v126\n"
"v_dot2c_f32_bf16 v86, s50, v127\n"
"v_dot2c_f32_bf16 v87, v127, v127\n"
"v_dot2c_f32_bf16 v84, s50, v128\n"
"v_dot2c_f32_bf16 v85, v128, v128\n"
"v_dot2c_f32_bf16 v86, s50, v129\n"
"v_dot2c_f32_bf16 v87, v129, v129\n"
"v_dot2c_f32_bf16 v84, s50, v130\n"
"v_dot2c_f32_bf16 v85, v130, v130\n"
"v_dot2c_f32_bf16 v86, s50, v131\n"
"v_dot2c_f32_bf16 v87, v131, v131\n"
"buffer_load_dwordx4 a[28:31], v0, s[8:11], s90 offen offset:3072 sc1\n"
"s_add_u32 s90, s52, 0x8000\n"
"v_add_f32 v84, v84, v86\n"
"v_add_f32 v85, v85, v87\n"
"ds_write_b64 v192, v[84:85] offset:0\n"
"buffer_load_dwordx4 a[32:35], v0, s[8:11], s90 offen offset:0 sc1\n"
"s_add_u32 s31, s31, 0x4000\n"
"v_mov_b32 v32, 0\n"
"v_mov_b32 v33, 0\n"
"v_mov_b32 v34, 0\n"
"v_mov_b32 v35, 0\n"
"v_dot2c_f32_bf16 v32, s50, v132\n"
"v_dot2c_f32_bf16 v33, v132, v132\n"
"v_dot2c_f32_bf16 v34, s50, v133\n"
"v_dot2c_f32_bf16 v35, v133, v133\n"
"v_dot2c_f32_bf16 v32, s50, v134\n"
"v_dot2c_f32_bf16 v33, v134, v134\n"
"v_dot2c_f32_bf16 v34, s50, v135\n"
"v_dot2c_f32_bf16 v35, v135, v135\n"
"v_dot2c_f32_bf16 v32, s50, v136\n"
"v_dot2c_f32_bf16 v33, v136, v136\n"
"v_dot2c_f32_bf16 v34, s50, v137\n"
"v_dot2c_f32_bf16 v35, v137, v137\n"
"v_dot2c_f32_bf16 v32, s50, v138\n"
"v_dot2c_f32_bf16 v33, v138, v138\n"
"v_dot2c_f32_bf16 v34, s50, v139\n"
"v_dot2c_f32_bf16 v35, v139, v139\n"
"v_dot2c_f32_bf16 v32, s50, v140\n"
"v_dot2c_f32_bf16 v33, v140, v140\n"
"v_dot2c_f32_bf16 v34, s50, v141\n"
"v_dot2c_f32_bf16 v35, v141, v141\n"
"v_dot2c_f32_bf16 v32, s50, v142\n"
"v_dot2c_f32_bf16 v33, v142, v142\n"
"v_dot2c_f32_bf16 v34, s50, v143\n"
"v_dot2c_f32_bf16 v35, v143, v143\n"
"v_dot2c_f32_bf16 v32, s50, v144\n"
"v_dot2c_f32_bf16 v33, v144, v144\n"
"buffer_load_dwordx4 a[36:39], v0, s[8:11], s90 offen offset:1024 sc1\n"
"buffer_load_dwordx4 a[40:43], v0, s[8:11], s90 offen offset:2048 sc1\n"
"v_dot2c_f32_bf16 v34, s50, v145\n"
"v_dot2c_f32_bf16 v35, v145, v145\n"
"v_dot2c_f32_bf16 v32, s50, v146\n"
"v_dot2c_f32_bf16 v33, v146, v146\n"
"v_dot2c_f32_bf16 v34, s50, v147\n"
"v_dot2c_f32_bf16 v35, v147, v147\n"
"v_dot2c_f32_bf16 v32, s50, v148\n"
"v_dot2c_f32_bf16 v33, v148, v148\n"
"v_dot2c_f32_bf16 v34, s50, v149\n"
"v_dot2c_f32_bf16 v35, v149, v149\n"
"v_dot2c_f32_bf16 v32, s50, v150\n"
"v_dot2c_f32_bf16 v33, v150, v150\n"
"v_dot2c_f32_bf16 v34, s50, v151\n"
"v_dot2c_f32_bf16 v35, v151, v151\n"
"v_dot2c_f32_bf16 v32, s50, v152\n"
"v_dot2c_f32_bf16 v33, v152, v152\n"
"v_dot2c_f32_bf16 v34, s50, v153\n"
"v_dot2c_f32_bf16 v35, v153, v153\n"
"v_dot2c_f32_bf16 v32, s50, v154\n"
"v_dot2c_f32_bf16 v33, v154, v154\n"
"v_dot2c_f32_bf16 v34, s50, v155\n"
"v_dot2c_f32_bf16 v35, v155, v155\n"
"v_dot2c_f32_bf16 v32, s50, v156\n"
"v_dot2c_f32_bf16 v33, v156, v156\n"
"v_dot2c_f32_bf16 v34, s50, v157\n"
"v_dot2c_f32_bf16 v35, v157, v157\n"
"v_dot2c_f32_bf16 v32, s50, v158\n"
"v_dot2c_f32_bf16 v33, v158, v158\n"
"v_dot2c_f32_bf16 v34, s50, v159\n"
"v_dot2c_f32_bf16 v35, v159, v159\n"
"v_dot2c_f32_bf16 v32, s50, v160\n"
"buffer_load_dwordx4 a[44:47], v0, s[8:11], s90 offen offset:3072 sc1\n"
"s_add_u32 s90, s52, 0xc000\n"
"buffer_load_dwordx4 a[48:51], v0, s[8:11], s90 offen offset:0 sc1\n"
"v_dot2c_f32_bf16 v33, v160, v160\n"
"v_dot2c_f32_bf16 v34, s50, v161\n"
"v_dot2c_f32_bf16 v35, v161, v161\n"
"v_dot2c_f32_bf16 v32, s50, v162\n"
"v_dot2c_f32_bf16 v33, v162, v162\n"
"v_dot2c_f32_bf16 v34, s50, v163\n"
"v_dot2c_f32_bf16 v35, v163, v163\n"
"v_dot2c_f32_bf16 v32, s50, v164\n"
"v_dot2c_f32_bf16 v33, v164, v164\n"
"v_dot2c_f32_bf16 v34, s50, v165\n"
"v_dot2c_f32_bf16 v35, v165, v165\n"
"v_dot2c_f32_bf16 v32, s50, v166\n"
"v_dot2c_f32_bf16 v33, v166, v166\n"
"v_dot2c_f32_bf16 v34, s50, v167\n"
"v_dot2c_f32_bf16 v35, v167, v167\n"
"v_dot2c_f32_bf16 v32, s50, v168\n"
"v_dot2c_f32_bf16 v33, v168, v168\n"
"v_dot2c_f32_bf16 v34, s50, v169\n"
"v_dot2c_f32_bf16 v35, v169, v169\n"
"v_dot2c_f32_bf16 v32, s50, v170\n"
"v_dot2c_f32_bf16 v33, v170, v170\n"
"v_dot2c_f32_bf16 v34, s50, v171\n"
"v_dot2c_f32_bf16 v35, v171, v171\n"
"buffer_load_dwordx4 a[52:55], v0, s[8:11], s90 offen offset:1024 sc1\n"
"s_waitcnt vmcnt(20) lgkmcnt(0)\n"
"v_perm_b32 v88, v36, v28, s48\n"
"v_add_f32 v32, v32, v34\n"
"v_add_f32 v33, v33, v35\n"
"ds_write_b64 v192, v[32:33] offset:64\n"
"buffer_load_dwordx4 a[56:59], v0, s[8:11], s90 offen offset:2048 sc1\n"
"v_mov_b32 v200, 0\n"
"v_mov_b32 v201, 0\n"
"v_mov_b32 v202, 0\n"
"v_mov_b32 v203, 0\n"
"v_dot2c_f32_bf16 v200, s50, v172\n"
"v_dot2c_f32_bf16 v201, v172, v172\n"
"v_dot2c_f32_bf16 v202, s50, v173\n"
"v_dot2c_f32_bf16 v203, v173, v173\n"
"v_dot2c_f32_bf16 v200, s50, v174\n"
"v_dot2c_f32_bf16 v201, v174, v174\n"
"v_dot2c_f32_bf16 v202, s50, v175\n"
"v_dot2c_f32_bf16 v203, v175, v175\n"
"v_dot2c_f32_bf16 v200, s50, v176\n"
"v_dot2c_f32_bf16 v201, v176, v176\n"
"v_dot2c_f32_bf16 v202, s50, v177\n"
"v_dot2c_f32_bf16 v203, v177, v177\n"
"v_dot2c_f32_bf16 v200, s50, v178\n"
"v_dot2c_f32_bf16 v201, v178, v178\n"
"v_dot2c_f32_bf16 v202, s50, v179\n"
"v_dot2c_f32_bf16 v203, v179, v179\n"
"v_dot2c_f32_bf16 v200, s50, v180\n"
"v_dot2c_f32_bf16 v201, v180, v180\n"
"v_dot2c_f32_bf16 v202, s50, v181\n"
"v_dot2c_f32_bf16 v203, v181, v181\n"
"v_dot2c_f32_bf16 v200, s50, v182\n"
"v_dot2c_f32_bf16 v201, v182, v182\n"
"s_waitcnt vmcnt(19)\n"
"v_perm_b32 v89, v44, v40, s48\n"
"buffer_load_dwordx4 a[60:63], v0, s[8:11], s90 offen offset:3072 sc1\n"
"s_add_u32 s90, s52, 0x10000\n"
"buffer_load_dwordx4 a[64:67], v0, s[8:11], s90 offen offset:0 sc1\n"
"v_dot2c_f32_bf16 v202, s50, v183\n"
"v_dot2c_f32_bf16 v203, v183, v183\n"
"v_dot2c_f32_bf16 v200, s50, v184\n"
"v_dot2c_f32_bf16 v201, v184, v184\n"
"v_dot2c_f32_bf16 v202, s50, v185\n"
"v_dot2c_f32_bf16 v203, v185, v185\n"
"v_dot2c_f32_bf16 v200, s50, v186\n"
"v_dot2c_f32_bf16 v201, v186, v186\n"
"v_dot2c_f32_bf16 v202, s50, v187\n"
"v_dot2c_f32_bf16 v203, v187, v187\n"
"v_dot2c_f32_bf16 v200, s50, v188\n"
"v_dot2c_f32_bf16 v201, v188, v188\n"
"v_dot2c_f32_bf16 v202, s50, v189\n"
"v_dot2c_f32_bf16 v203, v189, v189\n"
"s_waitcnt lgkmcnt(0)\n"
"buffer_load_dwordx4 a[68:71], v0, s[8:11], s90 offen offset:1024 sc1\n"
"v_dot2c_f32_bf16 v200, s50, v190\n"
"v_dot2c_f32_bf16 v201, v190, v190\n"
"v_dot2c_f32_bf16 v202, s50, v191\n"
"v_dot2c_f32_bf16 v203, v191, v191\n"
"buffer_load_ushort v236, v196, s[16:19], s41 offen\n"
"s_waitcnt vmcnt(21) lgkmcnt(0)\n"
"v_perm_b32 v90, v232, v228, s48\n"
"v_add_f32 v200, v200, v202\n"
"v_add_f32 v201, v201, v203\n"
"s_nop 0\n"
"v_add_f32_dpp v202, v200, v200 quad_perm:[1,0,3,2] row_mask:0xf bank_mask:0xf\n"
"v_add_f32_dpp v203, v201, v201 quad_perm:[1,0,3,2] row_mask:0xf bank_mask:0xf\n"
"ds_write_b64 v193, v[202:203] offset:0\n"
"ds_write_b128 v5, v[220:223] offset:48128\n"
"buffer_load_dwordx4 a[72:75], v0, s[8:11], s90 offen offset:2048 sc1\n"
"v_or_b32 v194, s38, v194\n"
"v_bfe_i32 v92, v194, 0, 1\n"
"v_bfe_i32 v93, v194, 1, 1\n"
"v_bfe_i32 v94, v194, 2, 1\n"
"v_bfe_i32 v95, v194, 3, 1\n"
"v_and_b32 v92, s51, v92\n"
"v_and_b32 v93, s51, v93\n"
"v_and_b32 v94, s51, v94\n"
"v_and_b32 v95, s51, v95\n"
"ds_write_b128 v5, v[92:95] offset:45056\n"
"buffer_load_dwordx4 a[76:79], v0, s[8:11], s90 offen offset:3072 sc1\n"
"s_waitcnt vmcnt(19)\n"
"v_mfma_f32_16x16x32_bf16 v[96:99], a[4:7], v[216:219], 0\n"
"s_add_u32 s90, s52, 0x14000\n"
"v_perm_b32 v91, v212, v224, s48\n"
"v_bfe_i32 v100, v194, 4, 1\n"
"v_mfma_f32_16x16x32_bf16 v[104:107], a[4:7], v[204:207], 0\n"
"ds_write_b128 v246, v[88:91] offset:0\n"
"v_mfma_f32_16x16x32_bf16 v[108:111], a[4:7], v[208:211], 0\n"
"s_waitcnt vmcnt(17)\n"
"buffer_load_ushort v237, v197, s[4:7], s42 offen\n"
"v_mfma_f32_16x16x32_bf16 v[112:115], a[12:15], v[216:219], 0\n"
"v_perm_b32 v116, v36, v28, s49\n"
"v_perm_b32 v117, v44, v40, s49\n"
"v_mfma_f32_16x16x32_bf16 v[120:123], a[12:15], v[204:207], 0\n"
"v_perm_b32 v118, v232, v228, s49\n"
"v_perm_b32 v119, v212, v224, s49\n"
"v_mfma_f32_16x16x32_bf16 v[124:127], a[12:15], v[208:211], 0\n"
"ds_write_b128 v246, v[116:119] offset:16\n"
"v_mfma_f32_16x16x32_bf16 v[128:131], a[8:11], v[216:219], 0\n"
"buffer_load_dwordx4 a[80:83], v0, s[8:11], s90 offen offset:0 sc1\n"
"v_mfma_f32_16x16x32_bf16 v[84:87], a[8:11], v[204:207], 0\n"
"v_perm_b32 v132, v37, v29, s48\n"
"v_perm_b32 v133, v45, v41, s48\n"
"v_mfma_f32_16x16x32_bf16 v[136:139], a[8:11], v[208:211], 0\n"
"v_perm_b32 v134, v233, v229, s48\n"
"v_perm_b32 v135, v213, v225, s48\n"
"v_mfma_f32_16x16x32_bf16 v[140:143], a[0:3], v[216:219], 0\n"
"ds_write_b128 v246, v[132:135] offset:32\n"
"ds_read_b128 v[144:147], v19 offset:2048\n"
"v_mfma_f32_16x16x32_bf16 v[148:151], a[0:3], v[204:207], 0\n"
"buffer_load_dwordx4 a[84:87], v0, s[8:11], s90 offen offset:1024 sc1\n"
"ds_read_b128 v[152:155], v4 offset:2048\n"
"v_mfma_f32_16x16x32_bf16 v[156:159], a[0:3], v[208:211], 0\n"
"s_waitcnt vmcnt(18) lgkmcnt(5)\n"
"ds_read_b128 v[160:163], v4 offset:7168\n"
"v_perm_b32 v164, v37, v29, s49\n"
"v_mfma_f32_16x16x32_bf16 v[96:99], a[20:23], v[48:51], v[96:99]\n"
"v_perm_b32 v165, v45, v41, s49\n"
"v_perm_b32 v166, v233, v229, s49\n"
"v_mfma_f32_16x16x32_bf16 v[104:107], a[20:23], v[52:55], v[104:107]\n"
"buffer_load_dwordx4 a[88:91], v0, s[8:11], s90 offen offset:2048 sc1\n"
"v_mfma_f32_16x16x32_bf16 v[108:111], a[20:23], v[56:59], v[108:111]\n"
"s_waitcnt vmcnt(17)\n"
"v_perm_b32 v167, v213, v225, s49\n"
"v_bfe_i32 v101, v194, 5, 1\n"
"v_mfma_f32_16x16x32_bf16 v[112:115], a[28:31], v[48:51], v[112:115]\n"
"ds_write_b128 v246, v[164:167] offset:48\n"
"v_mfma_f32_16x16x32_bf16 v[120:123], a[28:31], v[52:55], v[120:123]\n"
"v_perm_b32 v168, v38, v30, s48\n"
"v_perm_b32 v169, v46, v42, s48\n"
"v_mfma_f32_16x16x32_bf16 v[124:127], a[28:31], v[56:59], v[124:127]\n"
"buffer_load_dwordx4 a[92:95], v0, s[8:11], s90 offen offset:3072 sc1\n"
"v_mfma_f32_16x16x32_bf16 v[128:131], a[24:27], v[48:51], v[128:131]\n"
"s_add_u32 s90, s52, 0x18000\n"
"v_perm_b32 v170, v234, v230, s48\n"
"v_perm_b32 v171, v214, v226, s48\n"
"v_mfma_f32_16x16x32_bf16 v[84:87], a[24:27], v[52:55], v[84:87]\n"
"ds_write_b128 v247, v[168:171] offset:0\n"
"v_mfma_f32_16x16x32_bf16 v[136:139], a[24:27], v[56:59], v[136:139]\n"
"v_perm_b32 v32, v38, v30, s49\n"
"v_perm_b32 v33, v46, v42, s49\n"
"v_mfma_f32_16x16x32_bf16 v[140:143], a[16:19], v[48:51], v[140:143]\n"
"buffer_load_dwordx4 a[96:99], v0, s[8:11], s90 offen offset:0 sc1\n"
"ds_read_b128 v[172:175], v19 offset:3072\n"
"v_mfma_f32_16x16x32_bf16 v[148:151], a[16:19], v[52:55], v[148:151]\n"
"ds_read_b128 v[176:179], v4 offset:3072\n"
"v_perm_b32 v34, v234, v230, s49\n"
"v_mfma_f32_16x16x32_bf16 v[156:159], a[16:19], v[56:59], v[156:159]\n"
"s_waitcnt vmcnt(17) lgkmcnt(6)\n"
"ds_read_b128 v[180:183], v4 offset:8192\n"
"v_perm_b32 v35, v214, v226, s49\n"
"v_mfma_f32_16x16x32_bf16 v[96:99], a[36:39], v[144:147], v[96:99]\n"
"s_waitcnt lgkmcnt(6)\n"
"ds_write_b128 v247, v[32:35] offset:16\n"
"v_mfma_f32_16x16x32_bf16 v[104:107], a[36:39], v[152:155], v[104:107]\n"
"s_waitcnt lgkmcnt(6)\n"
"buffer_load_dwordx4 a[100:103], v0, s[8:11], s90 offen offset:1024 sc1\n"
"v_mfma_f32_16x16x32_bf16 v[108:111], a[36:39], v[160:163], v[108:111]\n"
"s_waitcnt vmcnt(16)\n"
"v_perm_b32 v184, v39, v31, s48\n"
"v_perm_b32 v185, v47, v43, s48\n"
"v_mfma_f32_16x16x32_bf16 v[112:115], a[44:47], v[144:147], v[112:115]\n"
"v_perm_b32 v186, v235, v231, s48\n"
"v_perm_b32 v187, v215, v227, s48\n"
"v_mfma_f32_16x16x32_bf16 v[120:123], a[44:47], v[152:155], v[120:123]\n"
"ds_write_b128 v247, v[184:187] offset:32\n"
"v_mfma_f32_16x16x32_bf16 v[124:127], a[44:47], v[160:163], v[124:127]\n"
"buffer_load_dwordx4 a[104:107], v0, s[8:11], s90 offen offset:2048 sc1\n"
"v_mfma_f32_16x16x32_bf16 v[128:131], a[40:43], v[144:147], v[128:131]\n"
"v_perm_b32 v188, v39, v31, s49\n"
"v_perm_b32 v189, v47, v43, s49\n"
"v_mfma_f32_16x16x32_bf16 v[84:87], a[40:43], v[152:155], v[84:87]\n"
"v_perm_b32 v190, v235, v231, s49\n"
"v_perm_b32 v191, v215, v227, s49\n"
"v_mfma_f32_16x16x32_bf16 v[136:139], a[40:43], v[160:163], v[136:139]\n"
"ds_write_b128 v247, v[188:191] offset:48\n"
"ds_read_b128 v[200:203], v251 offset:0\n"
"ds_read_b128 v[220:223], v251 offset:1024\n"
"ds_read_b128 v[92:95], v251 offset:2048\n"
"ds_read_b128 v[88:91], v251 offset:3072\n"
"ds_read_b128 v[116:119], v251 offset:4096\n"
"v_mfma_f32_16x16x32_bf16 v[140:143], a[32:35], v[144:147], v[140:143]\n"
"buffer_load_dwordx4 a[108:111], v0, s[8:11], s90 offen offset:3072 sc1\n"
"ds_read_b128 v[216:219], v19 offset:4096\n"
"v_mfma_f32_16x16x32_bf16 v[148:151], a[32:35], v[152:155], v[148:151]\n"
"s_add_u32 s90, s52, 0x1c000\n"
"ds_read_b128 v[132:135], v4 offset:4096\n"
"v_bfe_i32 v102, v194, 6, 1\n"
"v_mfma_f32_16x16x32_bf16 v[156:159], a[32:35], v[160:163], v[156:159]\n"
"s_waitcnt vmcnt(16) lgkmcnt(10)\n"
"buffer_load_dwordx4 a[112:115], v0, s[8:11], s90 offen offset:0 sc1\n"
"ds_read_b128 v[204:207], v4 offset:9216\n"
"v_mfma_f32_16x16x32_bf16 v[96:99], a[52:55], v[172:175], v[96:99]\n"
"v_bfe_i32 v103, v194, 7, 1\n"
"v_and_b32 v100, s51, v100\n"
"v_mfma_f32_16x16x32_bf16 v[104:107], a[52:55], v[176:179], v[104:107]\n"
"v_and_b32 v101, s51, v101\n"
"v_and_b32 v102, s51, v102\n"
"v_mfma_f32_16x16x32_bf16 v[108:111], a[52:55], v[180:183], v[108:111]\n"
"s_waitcnt vmcnt(15) lgkmcnt(10)\n"
"v_and_b32 v103, s51, v103\n"
"v_mov_b32 v208, -1\n"
"v_mfma_f32_16x16x32_bf16 v[112:115], a[60:63], v[172:175], v[112:115]\n"
"v_mov_b32 v209, -1\n"
"v_mov_b32 v210, -1\n"
"v_mfma_f32_16x16x32_bf16 v[120:123], a[60:63], v[176:179], v[120:123]\n"
"buffer_load_dwordx4 a[116:119], v0, s[8:11], s90 offen offset:1024 sc1\n"
"v_mfma_f32_16x16x32_bf16 v[124:127], a[60:63], v[180:183], v[124:127]\n"
"ds_write_b128 v5, v[100:103] offset:46080\n"
"v_mfma_f32_16x16x32_bf16 v[128:131], a[56:59], v[172:175], v[128:131]\n"
"s_waitcnt lgkmcnt(8)\n"
"buffer_store_dwordx4 v[200:203], v250, s[8:11], s34 offen sc1\n"
"v_mfma_f32_16x16x32_bf16 v[84:87], a[56:59], v[176:179], v[84:87]\n"
"s_add_u32 s34, s34, s29\n"
"v_bfe_i32 v164, v194, 8, 1\n"
"v_bfe_i32 v165, v194, 9, 1\n"
"v_mfma_f32_16x16x32_bf16 v[136:139], a[56:59], v[180:183], v[136:139]\n"
"v_bfe_i32 v166, v194, 10, 1\n"
"v_bfe_i32 v167, v194, 11, 1\n"
"v_mfma_f32_16x16x32_bf16 v[140:143], a[48:51], v[172:175], v[140:143]\n"
"ds_read_b128 v[168:171], v19 offset:5120\n"
"v_and_b32 v164, s51, v164\n"
"v_mfma_f32_16x16x32_bf16 v[148:151], a[48:51], v[176:179], v[148:151]\n"
"s_waitcnt lgkmcnt(8)\n"
"buffer_store_dwordx4 v[220:223], v250, s[8:11], s34 offen sc1\n"
"ds_read_b128 v[48:51], v4 offset:20480\n"
"v_mfma_f32_16x16x32_bf16 v[156:159], a[48:51], v[180:183], v[156:159]\n"
"s_waitcnt vmcnt(16) lgkmcnt(5)\n"
"ds_read_b128 v[52:55], v4 offset:25600\n"
"s_add_u32 s34, s34, s33\n"
"v_mfma_f32_16x16x32_bf16 v[96:99], a[68:71], v[216:219], v[96:99]\n"
"s_waitcnt lgkmcnt(5)\n"
"v_and_b32 v165, s51, v165\n"
"v_and_b32 v166, s51, v166\n"
"v_mfma_f32_16x16x32_bf16 v[104:107], a[68:71], v[132:135], v[104:107]\n"
"s_waitcnt lgkmcnt(4)\n"
"v_and_b32 v167, s51, v167\n"
"v_mov_b32 v211, -1\n"
"v_mfma_f32_16x16x32_bf16 v[108:111], a[68:71], v[204:207], v[108:111]\n"
"s_waitcnt vmcnt(13)\n"
"buffer_store_dwordx4 v[92:95], v250, s[8:11], s34 offen sc1\n"
"v_mfma_f32_16x16x32_bf16 v[112:115], a[76:79], v[216:219], v[112:115]\n"
"s_add_u32 s34, s34, s29\n"
"ds_write_b128 v5, v[164:167] offset:47104\n"
"v_mfma_f32_16x16x32_bf16 v[120:123], a[76:79], v[132:135], v[120:123]\n"
"s_add_u32 s91, s74, 0x2c000\n"
"v_bfe_i32 v56, v194, 12, 1\n"
"v_bfe_i32 v57, v194, 13, 1\n"
"v_mfma_f32_16x16x32_bf16 v[124:127], a[76:79], v[204:207], v[124:127]\n"
"buffer_store_dwordx4 v[88:91], v250, s[8:11], s34 offen sc1\n"
"v_mfma_f32_16x16x32_bf16 v[128:131], a[72:75], v[216:219], v[128:131]\n"
"v_bfe_i32 v58, v194, 14, 1\n"
"v_bfe_i32 v59, v194, 15, 1\n"
"v_mfma_f32_16x16x32_bf16 v[84:87], a[72:75], v[132:135], v[84:87]\n"
"v_and_b32 v56, s51, v56\n"
"v_and_b32 v57, s51, v57\n"
"v_mfma_f32_16x16x32_bf16 v[136:139], a[72:75], v[204:207], v[136:139]\n"
"v_and_b32 v58, s51, v58\n"
"v_and_b32 v59, s51, v59\n"
"v_mfma_f32_16x16x32_bf16 v[140:143], a[64:67], v[216:219], v[140:143]\n"
"ds_write_b128 v5, v[56:59] offset:49152\n"
"ds_read_b128 v[32:35], v19 offset:6144\n"
"v_mfma_f32_16x16x32_bf16 v[148:151], a[64:67], v[132:135], v[148:151]\n"
"buffer_store_dwordx4 v[116:119], v250, s[8:11], s35 offen sc1\n"
"ds_read_b128 v[184:187], v4 offset:21504\n"
"v_mfma_f32_16x16x32_bf16 v[156:159], a[64:67], v[204:207], v[156:159]\n"
"s_waitcnt vmcnt(13) lgkmcnt(4)\n"
"ds_read_b128 v[36:39], v4 offset:26624\n"
"v_xor_b32 v198, s83, v198\n"
"v_mfma_f32_16x16x32_bf16 v[96:99], a[84:87], v[168:171], v[96:99]\n"
"v_add_u32 v198, s82, v198\n"
"v_xor_b32 v199, s83, v199\n"
"v_mfma_f32_16x16x32_bf16 v[104:107], a[84:87], v[48:51], v[104:107]\n"
"buffer_store_dwordx4 v[208:211], v0, s[8:11], s91 offen offset:0 sc1\n"
"v_mfma_f32_16x16x32_bf16 v[108:111], a[84:87], v[52:55], v[108:111]\n"
"s_waitcnt vmcnt(12)\n"
"v_add_u32 v199, s82, v199\n"
"s_add_u32 s91, s74, 0x34000\n"
"v_mfma_f32_16x16x32_bf16 v[112:115], a[92:95], v[168:171], v[112:115]\n"
"v_lshlrev_b32 v236, 16, v236\n"
"v_mfma_f32_16x16x32_bf16 v[120:123], a[92:95], v[48:51], v[120:123]\n"
"ds_write_b32 v195, v236 offset:0\n"
"v_mfma_f32_16x16x32_bf16 v[124:127], a[92:95], v[52:55], v[124:127]\n"
"buffer_store_dwordx4 v[208:211], v0, s[8:11], s91 offen offset:0 sc1\n"
"v_mfma_f32_16x16x32_bf16 v[128:131], a[88:91], v[168:171], v[128:131]\n"
"s_add_u32 s91, s24, 0x3c000\n"
"v_lshlrev_b32 v237, 16, v237\n"
"v_mfma_f32_16x16x32_bf16 v[84:87], a[88:91], v[48:51], v[84:87]\n"
"ds_write_b32 v195, v237 offset:256\n"
"v_mfma_f32_16x16x32_bf16 v[136:139], a[88:91], v[52:55], v[136:139]\n"
"v_mfma_f32_16x16x32_bf16 v[140:143], a[80:83], v[168:171], v[140:143]\n"
"ds_read_b128 v[28:31], v19 offset:7168\n"
"v_mfma_f32_16x16x32_bf16 v[148:151], a[80:83], v[48:51], v[148:151]\n"
"buffer_store_dwordx4 v[208:211], v0, s[8:11], s91 offen offset:0 sc1\n"
"ds_read_b128 v[44:47], v4 offset:22528\n"
"v_mfma_f32_16x16x32_bf16 v[156:159], a[80:83], v[52:55], v[156:159]\n"
"s_waitcnt vmcnt(12) lgkmcnt(5)\n"
"ds_read_b128 v[40:43], v4 offset:27648\n"
"v_mfma_f32_16x16x32_bf16 v[96:99], a[100:103], v[32:35], v[96:99]\n"
"v_mfma_f32_16x16x32_bf16 v[104:107], a[100:103], v[184:187], v[104:107]\n"
"s_waitcnt lgkmcnt(5)\n"
"v_mfma_f32_16x16x32_bf16 v[108:111], a[100:103], v[36:39], v[108:111]\n"
"s_waitcnt vmcnt(10)\n"
"v_mfma_f32_16x16x32_bf16 v[112:115], a[108:111], v[32:35], v[112:115]\n"
"v_mfma_f32_16x16x32_bf16 v[120:123], a[108:111], v[184:187], v[120:123]\n"
"buffer_load_dwordx4 a[120:123], v0, s[8:11], s90 offen offset:2048 sc1\n"
"v_mfma_f32_16x16x32_bf16 v[124:127], a[108:111], v[36:39], v[124:127]\n"
"v_mfma_f32_16x16x32_bf16 v[128:131], a[104:107], v[32:35], v[128:131]\n"
"v_mfma_f32_16x16x32_bf16 v[84:87], a[104:107], v[184:187], v[84:87]\n"
"v_mfma_f32_16x16x32_bf16 v[136:139], a[104:107], v[36:39], v[136:139]\n"
"v_mfma_f32_16x16x32_bf16 v[140:143], a[96:99], v[32:35], v[140:143]\n"
"buffer_load_dwordx4 a[124:127], v0, s[8:11], s90 offen offset:3072 sc1\n"
"ds_read_b128 v[232:235], v19 offset:8192\n"
"v_mfma_f32_16x16x32_bf16 v[148:151], a[96:99], v[184:187], v[148:151]\n"
"s_add_u32 s90, s52, 0x20000\n"
"ds_read_b128 v[228:231], v4 offset:23552\n"
"v_mfma_f32_16x16x32_bf16 v[156:159], a[96:99], v[36:39], v[156:159]\n"
"s_waitcnt vmcnt(10) lgkmcnt(4)\n"
"ds_read_b128 v[212:215], v4 offset:28672\n"
"v_mfma_f32_16x16x32_bf16 v[96:99], a[116:119], v[28:31], v[96:99]\n"
"s_waitcnt lgkmcnt(4)\n"
"v_mfma_f32_16x16x32_bf16 v[104:107], a[116:119], v[44:47], v[104:107]\n"
"s_waitcnt lgkmcnt(3)\n"
"buffer_load_dwordx4 a[128:131], v0, s[8:11], s90 offen offset:0 sc1\n"
"v_mfma_f32_16x16x32_bf16 v[108:111], a[116:119], v[40:43], v[108:111]\n"
"v_mfma_f32_16x16x32_bf16 v[224:227], a[4:7], v[60:63], 0\n"
"v_mfma_f32_16x16x32_bf16 v[188:191], a[4:7], v[64:67], 0\n"
"v_mfma_f32_16x16x32_bf16 v[244:247], a[12:15], v[60:63], 0\n"
"v_mfma_f32_16x16x32_bf16 v[144:147], a[12:15], v[64:67], 0\n"
"buffer_load_dwordx4 a[132:135], v0, s[8:11], s90 offen offset:1024 sc1\n"
"v_mfma_f32_16x16x32_bf16 v[152:155], a[8:11], v[60:63], 0\n"
"v_mfma_f32_16x16x32_bf16 v[160:163], a[8:11], v[64:67], 0\n"
"v_mfma_f32_16x16x32_bf16 v[100:103], a[0:3], v[60:63], 0\n"
"ds_read_b128 v[200:203], v4 offset:13312\n"
"v_mfma_f32_16x16x32_bf16 v[172:175], a[0:3], v[64:67], 0\n"
"buffer_load_dwordx4 a[136:139], v0, s[8:11], s90 offen offset:2048 sc1\n"
"ds_read_b128 v[176:179], v4 offset:18432\n"
"v_mfma_f32_16x16x32_bf16 v[224:227], a[20:23], v[68:71], v[224:227]\n"
"v_mfma_f32_16x16x32_bf16 v[188:191], a[20:23], v[72:75], v[188:191]\n"
"v_mfma_f32_16x16x32_bf16 v[244:247], a[28:31], v[68:71], v[244:247]\n"
"v_mfma_f32_16x16x32_bf16 v[144:147], a[28:31], v[72:75], v[144:147]\n"
"v_mfma_f32_16x16x32_bf16 v[152:155], a[24:27], v[68:71], v[152:155]\n"
"buffer_load_dwordx4 a[140:143], v0, s[8:11], s90 offen offset:3072 sc1\n"
"v_mfma_f32_16x16x32_bf16 v[160:163], a[24:27], v[72:75], v[160:163]\n"
"s_add_u32 s90, s52, 0x24000\n"
"v_mfma_f32_16x16x32_bf16 v[100:103], a[16:19], v[68:71], v[100:103]\n"
"ds_read_b128 v[220:223], v4 offset:14336\n"
"v_mfma_f32_16x16x32_bf16 v[172:175], a[16:19], v[72:75], v[172:175]\n"
"ds_read_b128 v[180:183], v4 offset:19456\n"
"v_mfma_f32_16x16x32_bf16 v[224:227], a[36:39], v[76:79], v[224:227]\n"
"buffer_load_dwordx4 a[144:147], v0, s[8:11], s90 offen offset:0 sc1\n"
"v_mfma_f32_16x16x32_bf16 v[188:191], a[36:39], v[80:83], v[188:191]\n"
"v_mfma_f32_16x16x32_bf16 v[244:247], a[44:47], v[76:79], v[244:247]\n"
"v_mfma_f32_16x16x32_bf16 v[144:147], a[44:47], v[80:83], v[144:147]\n"
"v_mfma_f32_16x16x32_bf16 v[152:155], a[40:43], v[76:79], v[152:155]\n"
"s_waitcnt vmcnt(10) lgkmcnt(3)\n"
"buffer_store_dwordx2 v[254:255], v243, s[8:11], s31 offen sc1\n"
"v_mfma_f32_16x16x32_bf16 v[160:163], a[40:43], v[80:83], v[160:163]\n"
"v_mfma_f32_16x16x32_bf16 v[100:103], a[32:35], v[76:79], v[100:103]\n"
"ds_read_b128 v[92:95], v4 offset:30720\n"
"v_mfma_f32_16x16x32_bf16 v[172:175], a[32:35], v[80:83], v[172:175]\n"
"buffer_load_dwordx4 a[148:151], v0, s[8:11], s90 offen offset:1024 sc1\n"
"ds_read_b128 v[164:167], v4 offset:35840\n"
"v_mfma_f32_16x16x32_bf16 v[224:227], a[52:55], v[200:203], v[224:227]\n"
"s_waitcnt lgkmcnt(4)\n"
"v_mfma_f32_16x16x32_bf16 v[188:191], a[52:55], v[176:179], v[188:191]\n"
"v_mfma_f32_16x16x32_bf16 v[244:247], a[60:63], v[200:203], v[244:247]\n"
"v_mfma_f32_16x16x32_bf16 v[144:147], a[60:63], v[176:179], v[144:147]\n"
"v_mfma_f32_16x16x32_bf16 v[152:155], a[56:59], v[200:203], v[152:155]\n"
"buffer_load_dwordx4 a[152:155], v0, s[8:11], s90 offen offset:2048 sc1\n"
"v_mfma_f32_16x16x32_bf16 v[160:163], a[56:59], v[176:179], v[160:163]\n"
"v_mfma_f32_16x16x32_bf16 v[100:103], a[48:51], v[200:203], v[100:103]\n"
"ds_read_b128 v[88:91], v4 offset:31744\n"
"v_mfma_f32_16x16x32_bf16 v[172:175], a[48:51], v[176:179], v[172:175]\n"
"s_waitcnt lgkmcnt(3)\n"
"ds_read_b128 v[216:219], v4 offset:36864\n"
"v_mfma_f32_16x16x32_bf16 v[224:227], a[68:71], v[220:223], v[224:227]\n"
"buffer_load_dwordx4 a[156:159], v0, s[8:11], s90 offen offset:3072 sc1\n"
"v_mfma_f32_16x16x32_bf16 v[188:191], a[68:71], v[180:183], v[188:191]\n"
"s_add_u32 s90, s52, 0x28000\n"
"v_mfma_f32_16x16x32_bf16 v[244:247], a[76:79], v[220:223], v[244:247]\n"
"s_waitcnt vmcnt(9)\n"
"v_mfma_f32_16x16x32_bf16 v[112:115], a[124:127], v[28:31], v[112:115]\n"
"buffer_store_dwordx2 v[254:255], v15, s[8:11], s79 offen sc1\n"
"v_mfma_f32_16x16x32_bf16 v[120:123], a[124:127], v[44:47], v[120:123]\n"
"v_mfma_f32_16x16x32_bf16 v[124:127], a[124:127], v[40:43], v[124:127]\n"
"v_mfma_f32_16x16x32_bf16 v[128:131], a[120:123], v[28:31], v[128:131]\n"
"v_mfma_f32_16x16x32_bf16 v[84:87], a[120:123], v[44:47], v[84:87]\n"
"v_mfma_f32_16x16x32_bf16 v[136:139], a[120:123], v[40:43], v[136:139]\n"
"buffer_load_dwordx4 a[160:163], v0, s[8:11], s90 offen offset:0 sc1\n"
"v_mfma_f32_16x16x32_bf16 v[140:143], a[112:115], v[28:31], v[140:143]\n"
"ds_read_b128 v[56:59], v19 offset:9216\n"
"v_mfma_f32_16x16x32_bf16 v[148:151], a[112:115], v[44:47], v[148:151]\n"
"ds_read_b128 v[132:135], v4 offset:24576\n"
"v_mfma_f32_16x16x32_bf16 v[156:159], a[112:115], v[40:43], v[156:159]\n"
"ds_read_b128 v[116:119], v4 offset:29696\n"
"s_waitcnt vmcnt(9) lgkmcnt(4)\n"
"v_mfma_f32_16x16x32_bf16 v[96:99], a[132:135], v[232:235], v[96:99]\n"
"buffer_load_dwordx4 a[164:167], v0, s[8:11], s90 offen offset:1024 sc1\n"
"v_mfma_f32_16x16x32_bf16 v[104:107], a[132:135], v[228:231], v[104:107]\n"
"v_mfma_f32_16x16x32_bf16 v[108:111], a[132:135], v[212:215], v[108:111]\n"
"v_mfma_f32_16x16x32_bf16 v[144:147], a[76:79], v[180:183], v[144:147]\n"
"v_mfma_f32_16x16x32_bf16 v[152:155], a[72:75], v[220:223], v[152:155]\n"
"v_mfma_f32_16x16x32_bf16 v[160:163], a[72:75], v[180:183], v[160:163]\n"
"buffer_load_dwordx4 a[168:171], v0, s[8:11], s90 offen offset:2048 sc1\n"
"v_mfma_f32_16x16x32_bf16 v[100:103], a[64:67], v[220:223], v[100:103]\n"
"ds_read_b128 v[248:251], v4 offset:32768\n"
"v_mfma_f32_16x16x32_bf16 v[172:175], a[64:67], v[180:183], v[172:175]\n"
"ds_read_b128 v[204:207], v4 offset:37888\n"
"v_mfma_f32_16x16x32_bf16 v[224:227], a[84:87], v[92:95], v[224:227]\n"
"s_waitcnt vmcnt(9) lgkmcnt(5)\n"
"v_mfma_f32_16x16x32_bf16 v[112:115], a[140:143], v[232:235], v[112:115]\n"
"buffer_load_dwordx4 a[172:175], v0, s[8:11], s90 offen offset:3072 sc1\n"
"v_mfma_f32_16x16x32_bf16 v[120:123], a[140:143], v[228:231], v[120:123]\n"
"s_add_u32 s90, s52, 0x2c000\n"
"v_mfma_f32_16x16x32_bf16 v[124:127], a[140:143], v[212:215], v[124:127]\n"
"v_mfma_f32_16x16x32_bf16 v[128:131], a[136:139], v[232:235], v[128:131]\n"
"v_mfma_f32_16x16x32_bf16 v[84:87], a[136:139], v[228:231], v[84:87]\n"
"v_mfma_f32_16x16x32_bf16 v[136:139], a[136:139], v[212:215], v[136:139]\n"
"buffer_load_dwordx4 a[176:179], v0, s[8:11], s90 offen offset:0 sc1\n"
"v_mfma_f32_16x16x32_bf16 v[140:143], a[128:131], v[232:235], v[140:143]\n"
"ds_read_b128 v[236:239], v19 offset:10240\n"
"v_mfma_f32_16x16x32_bf16 v[148:151], a[128:131], v[228:231], v[148:151]\n"
"ds_read_b128 v[192:195], v5 offset:0\n"
"v_mfma_f32_16x16x32_bf16 v[156:159], a[128:131], v[212:215], v[156:159]\n"
"ds_read_b128 v[168:171], v5 offset:5120\n"
"v_mfma_f32_16x16x32_bf16 v[188:191], a[84:87], v[164:167], v[188:191]\n"
"v_mfma_f32_16x16x32_bf16 v[244:247], a[92:95], v[92:95], v[244:247]\n"
"buffer_load_dwordx4 a[180:183], v0, s[8:11], s90 offen offset:1024 sc1\n"
"v_mfma_f32_16x16x32_bf16 v[144:147], a[92:95], v[164:167], v[144:147]\n"
"v_mfma_f32_16x16x32_bf16 v[152:155], a[88:91], v[92:95], v[152:155]\n"
"v_mfma_f32_16x16x32_bf16 v[160:163], a[88:91], v[164:167], v[160:163]\n"
"v_mfma_f32_16x16x32_bf16 v[100:103], a[80:83], v[92:95], v[100:103]\n"
"buffer_load_dwordx4 a[184:187], v0, s[8:11], s90 offen offset:2048 sc1\n"
"ds_read_b128 v[48:51], v4 offset:33792\n"
"v_mfma_f32_16x16x32_bf16 v[172:175], a[80:83], v[164:167], v[172:175]\n"
"ds_read_b128 v[208:211], v4 offset:38912\n"
"v_mfma_f32_16x16x32_bf16 v[224:227], a[100:103], v[88:91], v[224:227]\n"
"s_waitcnt vmcnt(10) lgkmcnt(5)\n"
"v_mfma_f32_16x16x32_bf16 v[96:99], a[148:151], v[56:59], v[96:99]\n"
"v_mfma_f32_16x16x32_bf16 v[104:107], a[148:151], v[132:135], v[104:107]\n"
"v_mfma_f32_16x16x32_bf16 v[108:111], a[148:151], v[116:119], v[108:111]\n"
"buffer_load_dwordx4 a[188:191], v0, s[8:11], s90 offen offset:3072 sc1\n"
"v_mfma_f32_16x16x32_bf16 v[188:191], a[100:103], v[216:219], v[188:191]\n"
"s_add_u32 s90, s52, 0x30000\n"
"s_waitcnt vmcnt(9) lgkmcnt(3)\n"
"v_mfma_f32_16x16x32_bf16 v[112:115], a[156:159], v[56:59], v[112:115]\n"
"v_mfma_f32_16x16x32_bf16 v[120:123], a[156:159], v[132:135], v[120:123]\n"
"v_mfma_f32_16x16x32_bf16 v[124:127], a[156:159], v[116:119], v[124:127]\n"
"buffer_load_dwordx4 a[192:195], v0, s[8:11], s90 offen offset:0 sc1\n"
"v_mfma_f32_16x16x32_bf16 v[128:131], a[152:155], v[56:59], v[128:131]\n"
"v_mfma_f32_16x16x32_bf16 v[84:87], a[152:155], v[132:135], v[84:87]\n"
"v_mfma_f32_16x16x32_bf16 v[136:139], a[152:155], v[116:119], v[136:139]\n"
"v_mfma_f32_16x16x32_bf16 v[140:143], a[144:147], v[56:59], v[140:143]\n"
"ds_read_b128 v[52:55], v19 offset:11264\n"
"v_mfma_f32_16x16x32_bf16 v[148:151], a[144:147], v[132:135], v[148:151]\n"
"buffer_load_dwordx4 a[196:199], v0, s[8:11], s90 offen offset:1024 sc1\n"
"ds_read_b128 v[32:35], v5 offset:1024\n"
"v_mfma_f32_16x16x32_bf16 v[156:159], a[144:147], v[116:119], v[156:159]\n"
"ds_read_b128 v[184:187], v5 offset:6144\n"
"v_mfma_f32_16x16x32_bf16 v[244:247], a[108:111], v[88:91], v[244:247]\n"
"v_mfma_f32_16x16x32_bf16 v[144:147], a[108:111], v[216:219], v[144:147]\n"
"v_mfma_f32_16x16x32_bf16 v[152:155], a[104:107], v[88:91], v[152:155]\n"
"buffer_load_dwordx4 a[200:203], v0, s[8:11], s90 offen offset:2048 sc1\n"
"s_waitcnt vmcnt(9) lgkmcnt(3)\n"
"v_mfma_f32_16x16x32_bf16 v[96:99], a[164:167], v[236:239], v[96:99]\n"
"v_mfma_f32_16x16x32_bf16 v[104:107], a[164:167], v[192:195], v[104:107]\n"
"v_mfma_f32_16x16x32_bf16 v[108:111], a[164:167], v[168:171], v[108:111]\n"
"v_mfma_f32_16x16x32_bf16 v[160:163], a[104:107], v[216:219], v[160:163]\n"
"v_mfma_f32_16x16x32_bf16 v[100:103], a[96:99], v[88:91], v[100:103]\n"
"buffer_load_dwordx4 a[204:207], v0, s[8:11], s90 offen offset:3072 sc1\n"
"ds_read_b128 v[36:39], v4 offset:34816\n"
"v_mfma_f32_16x16x32_bf16 v[172:175], a[96:99], v[216:219], v[172:175]\n"
"s_add_u32 s90, s52, 0x34000\n"
"ds_read_b128 v[60:63], v4 offset:39936\n"
"v_mfma_f32_16x16x32_bf16 v[224:227], a[116:119], v[248:251], v[224:227]\n"
"v_mfma_f32_16x16x32_bf16 v[188:191], a[116:119], v[204:207], v[188:191]\n"
"v_mfma_f32_16x16x32_bf16 v[244:247], a[124:127], v[248:251], v[244:247]\n"
"buffer_load_dwordx4 a[208:211], v0, s[8:11], s90 offen offset:0 sc1\n"
"s_waitcnt vmcnt(9) lgkmcnt(3)\n"
"v_mfma_f32_16x16x32_bf16 v[112:115], a[172:175], v[236:239], v[112:115]\n"
"v_mfma_f32_16x16x32_bf16 v[120:123], a[172:175], v[192:195], v[120:123]\n"
"v_mfma_f32_16x16x32_bf16 v[124:127], a[172:175], v[168:171], v[124:127]\n"
"v_mfma_f32_16x16x32_bf16 v[128:131], a[168:171], v[236:239], v[128:131]\n"
"buffer_load_dwordx4 a[212:215], v0, s[8:11], s90 offen offset:1024 sc1\n"
"v_mfma_f32_16x16x32_bf16 v[84:87], a[168:171], v[192:195], v[84:87]\n"
"v_mfma_f32_16x16x32_bf16 v[136:139], a[168:171], v[168:171], v[136:139]\n"
"v_mfma_f32_16x16x32_bf16 v[140:143], a[160:163], v[236:239], v[140:143]\n"
"ds_read_b128 v[64:67], v19 offset:12288\n"
"v_mfma_f32_16x16x32_bf16 v[148:151], a[160:163], v[192:195], v[148:151]\n"
"ds_read_b128 v[68:71], v5 offset:2048\n"
"v_mfma_f32_16x16x32_bf16 v[156:159], a[160:163], v[168:171], v[156:159]\n"
"buffer_load_dwordx4 a[216:219], v0, s[8:11], s90 offen offset:2048 sc1\n"
"ds_read_b128 v[72:75], v5 offset:7168\n"
"s_waitcnt vmcnt(9) lgkmcnt(4)\n"
"v_mfma_f32_16x16x32_bf16 v[96:99], a[180:183], v[52:55], v[96:99]\n"
"v_mfma_f32_16x16x32_bf16 v[104:107], a[180:183], v[32:35], v[104:107]\n"
"v_mfma_f32_16x16x32_bf16 v[108:111], a[180:183], v[184:187], v[108:111]\n"
"v_mfma_f32_16x16x32_bf16 v[144:147], a[124:127], v[204:207], v[144:147]\n"
"buffer_load_dwordx4 a[220:223], v0, s[8:11], s90 offen offset:3072 sc1\n"
"v_mfma_f32_16x16x32_bf16 v[152:155], a[120:123], v[248:251], v[152:155]\n"
"s_add_u32 s90, s52, 0x38000\n"
"v_mfma_f32_16x16x32_bf16 v[160:163], a[120:123], v[204:207], v[160:163]\n"
"v_mfma_f32_16x16x32_bf16 v[100:103], a[112:115], v[248:251], v[100:103]\n"
"ds_read_b128 v[240:243], v5 offset:10240\n"
"v_mfma_f32_16x16x32_bf16 v[172:175], a[112:115], v[204:207], v[172:175]\n"
"ds_read_b128 v[76:79], v5 offset:15360\n"
"v_mfma_f32_16x16x32_bf16 v[224:227], a[132:135], v[48:51], v[224:227]\n"
"s_waitcnt vmcnt(8) lgkmcnt(5)\n"
"buffer_load_dwordx4 a[224:227], v0, s[8:11], s90 offen offset:0 sc1\n"
"v_mfma_f32_16x16x32_bf16 v[112:115], a[188:191], v[52:55], v[112:115]\n"
"v_mfma_f32_16x16x32_bf16 v[120:123], a[188:191], v[32:35], v[120:123]\n"
"v_mfma_f32_16x16x32_bf16 v[124:127], a[188:191], v[184:187], v[124:127]\n"
"v_mfma_f32_16x16x32_bf16 v[128:131], a[184:187], v[52:55], v[128:131]\n"
"buffer_load_dwordx4 a[228:231], v0, s[8:11], s90 offen offset:1024 sc1\n"
"v_mfma_f32_16x16x32_bf16 v[84:87], a[184:187], v[32:35], v[84:87]\n"
"v_mfma_f32_16x16x32_bf16 v[136:139], a[184:187], v[184:187], v[136:139]\n"
"v_mfma_f32_16x16x32_bf16 v[140:143], a[176:179], v[52:55], v[140:143]\n"
"ds_read_b128 v[80:83], v19 offset:13312\n"
"v_mfma_f32_16x16x32_bf16 v[148:151], a[176:179], v[32:35], v[148:151]\n"
"ds_read_b128 v[200:203], v5 offset:3072\n"
"v_mfma_f32_16x16x32_bf16 v[156:159], a[176:179], v[184:187], v[156:159]\n"
"buffer_load_dwordx4 a[232:235], v0, s[8:11], s90 offen offset:2048 sc1\n"
"ds_read_b128 v[176:179], v5 offset:8192\n"
"s_waitcnt vmcnt(9) lgkmcnt(5)\n"
"v_mfma_f32_16x16x32_bf16 v[96:99], a[196:199], v[64:67], v[96:99]\n"
"v_mfma_f32_16x16x32_bf16 v[104:107], a[196:199], v[68:71], v[104:107]\n"
"v_mfma_f32_16x16x32_bf16 v[108:111], a[196:199], v[72:75], v[108:111]\n"
"buffer_load_dwordx2 v[28:29], v17, s[8:11], s32 offen sc1\n"
"v_mfma_f32_16x16x32_bf16 v[188:191], a[132:135], v[208:211], v[188:191]\n"
"v_mfma_f32_16x16x32_bf16 v[244:247], a[140:143], v[48:51], v[244:247]\n"
"v_mfma_f32_16x16x32_bf16 v[144:147], a[140:143], v[208:211], v[144:147]\n"
"v_mfma_f32_16x16x32_bf16 v[152:155], a[136:139], v[48:51], v[152:155]\n"
"buffer_load_dwordx4 a[236:239], v0, s[8:11], s90 offen offset:3072 sc1\n"
"v_mfma_f32_16x16x32_bf16 v[160:163], a[136:139], v[208:211], v[160:163]\n"
"s_add_u32 s90, s52, 0x3c000\n"
"v_mfma_f32_16x16x32_bf16 v[100:103], a[128:131], v[48:51], v[100:103]\n"
"s_waitcnt vmcnt(9) lgkmcnt(3)\n"
"ds_read_b128 v[44:47], v5 offset:11264\n"
"v_mfma_f32_16x16x32_bf16 v[112:115], a[204:207], v[64:67], v[112:115]\n"
"v_mfma_f32_16x16x32_bf16 v[120:123], a[204:207], v[68:71], v[120:123]\n"
"v_mfma_f32_16x16x32_bf16 v[124:127], a[204:207], v[72:75], v[124:127]\n"
"buffer_load_dwordx4 a[240:243], v0, s[8:11], s90 offen offset:0 sc1\n"
"v_mfma_f32_16x16x32_bf16 v[128:131], a[200:203], v[64:67], v[128:131]\n"
"v_mfma_f32_16x16x32_bf16 v[84:87], a[200:203], v[68:71], v[84:87]\n"
"v_mfma_f32_16x16x32_bf16 v[136:139], a[200:203], v[72:75], v[136:139]\n"
"v_mfma_f32_16x16x32_bf16 v[140:143], a[192:195], v[64:67], v[140:143]\n"
"buffer_load_dwordx4 a[244:247], v0, s[8:11], s90 offen offset:1024 sc1\n"
"ds_read_b128 v[40:43], v19 offset:14336\n"
"v_mfma_f32_16x16x32_bf16 v[148:151], a[192:195], v[68:71], v[148:151]\n"
"ds_read_b128 v[220:223], v5 offset:4096\n"
"v_mfma_f32_16x16x32_bf16 v[156:159], a[192:195], v[72:75], v[156:159]\n"
"s_waitcnt vmcnt(9) lgkmcnt(3)\n"
"ds_read_b128 v[180:183], v5 offset:9216\n"
"v_mfma_f32_16x16x32_bf16 v[96:99], a[212:215], v[80:83], v[96:99]\n"
"v_mfma_f32_16x16x32_bf16 v[104:107], a[212:215], v[200:203], v[104:107]\n"
"v_mfma_f32_16x16x32_bf16 v[108:111], a[212:215], v[176:179], v[108:111]\n"
"buffer_load_dwordx4 a[248:251], v0, s[8:11], s90 offen offset:2048 sc1\n"
"v_mfma_f32_16x16x32_bf16 v[172:175], a[128:131], v[208:211], v[172:175]\n"
"ds_read_b128 v[232:235], v5 offset:16384\n"
"v_mfma_f32_16x16x32_bf16 v[224:227], a[148:151], v[36:39], v[224:227]\n"
"v_mfma_f32_16x16x32_bf16 v[188:191], a[148:151], v[60:63], v[188:191]\n"
"v_mfma_f32_16x16x32_bf16 v[244:247], a[156:159], v[36:39], v[244:247]\n"
"buffer_load_dwordx4 a[252:255], v0, s[8:11], s90 offen offset:3072 sc1\n"
"v_mfma_f32_16x16x32_bf16 v[144:147], a[156:159], v[60:63], v[144:147]\n"
"s_add_u32 s90, s52, 0x40000\n"
"v_mfma_f32_16x16x32_bf16 v[152:155], a[152:155], v[36:39], v[152:155]\n"
"s_waitcnt vmcnt(9) lgkmcnt(4)\n"
"v_mfma_f32_16x16x32_bf16 v[112:115], a[220:223], v[80:83], v[112:115]\n"
"v_mfma_f32_16x16x32_bf16 v[120:123], a[220:223], v[200:203], v[120:123]\n"
"v_mfma_f32_16x16x32_bf16 v[124:127], a[220:223], v[176:179], v[124:127]\n"
"buffer_load_dwordx4 a[4:7], v0, s[8:11], s90 offen offset:0 sc1\n"
"v_mfma_f32_16x16x32_bf16 v[128:131], a[216:219], v[80:83], v[128:131]\n"
"v_mfma_f32_16x16x32_bf16 v[84:87], a[216:219], v[200:203], v[84:87]\n"
"v_mfma_f32_16x16x32_bf16 v[136:139], a[216:219], v[176:179], v[136:139]\n"
"v_mfma_f32_16x16x32_bf16 v[140:143], a[208:211], v[80:83], v[140:143]\n"
"buffer_load_dwordx4 a[12:15], v0, s[8:11], s90 offen offset:1024 sc1\n"
"ds_read_b128 v[228:231], v19 offset:15360\n"
"v_mfma_f32_16x16x32_bf16 v[148:151], a[208:211], v[200:203], v[148:151]\n"
"ds_read_b128 v[212:215], v5 offset:20480\n"
"v_mfma_f32_16x16x32_bf16 v[156:159], a[208:211], v[176:179], v[156:159]\n"
"ds_read_b128 v[92:95], v5 offset:25600\n"
"s_waitcnt vmcnt(9) lgkmcnt(3)\n"
"v_mfma_f32_16x16x32_bf16 v[96:99], a[228:231], v[40:43], v[96:99]\n"
"v_mfma_f32_16x16x32_bf16 v[104:107], a[228:231], v[220:223], v[104:107]\n"
"v_mfma_f32_16x16x32_bf16 v[108:111], a[228:231], v[180:183], v[108:111]\n"
"buffer_load_dwordx4 a[8:11], v0, s[8:11], s90 offen offset:2048 sc1\n"
"v_mfma_f32_16x16x32_bf16 v[160:163], a[152:155], v[60:63], v[160:163]\n"
"v_mfma_f32_16x16x32_bf16 v[100:103], a[144:147], v[36:39], v[100:103]\n"
"ds_read_b128 v[164:167], v5 offset:12288\n"
"v_mfma_f32_16x16x32_bf16 v[172:175], a[144:147], v[60:63], v[172:175]\n"
"ds_read_b128 v[56:59], v5 offset:17408\n"
"v_mfma_f32_16x16x32_bf16 v[224:227], a[164:167], v[240:243], v[224:227]\n"
"buffer_load_dwordx4 a[0:3], v0, s[8:11], s90 offen offset:3072 sc1\n"
"v_mfma_f32_16x16x32_bf16 v[188:191], a[164:167], v[76:79], v[188:191]\n"
"s_add_u32 s90, s52, 0x44000\n"
"v_mfma_f32_16x16x32_bf16 v[244:247], a[172:175], v[240:243], v[244:247]\n"
"v_mfma_f32_16x16x32_bf16 v[144:147], a[172:175], v[76:79], v[144:147]\n"
"v_mfma_f32_16x16x32_bf16 v[152:155], a[168:171], v[240:243], v[152:155]\n"
"v_mfma_f32_16x16x32_bf16 v[160:163], a[168:171], v[76:79], v[160:163]\n"
"buffer_load_dwordx4 a[20:23], v0, s[8:11], s90 offen offset:0 sc1\n"
"v_mfma_f32_16x16x32_bf16 v[100:103], a[160:163], v[240:243], v[100:103]\n"
"ds_read_b128 v[132:135], v5 offset:13312\n"
"v_mfma_f32_16x16x32_bf16 v[172:175], a[160:163], v[76:79], v[172:175]\n"
"ds_read_b128 v[116:119], v5 offset:18432\n"
"v_mfma_f32_16x16x32_bf16 v[224:227], a[180:183], v[44:47], v[224:227]\n"
"v_mfma_f32_16x16x32_bf16 v[188:191], a[180:183], v[232:235], v[188:191]\n"
"v_mfma_f32_16x16x32_bf16 v[244:247], a[188:191], v[44:47], v[244:247]\n"
"buffer_load_dwordx4 a[28:31], v0, s[8:11], s90 offen offset:1024 sc1\n"
"v_mfma_f32_16x16x32_bf16 v[144:147], a[188:191], v[232:235], v[144:147]\n"
"v_mfma_f32_16x16x32_bf16 v[152:155], a[184:187], v[44:47], v[152:155]\n"
"v_mfma_f32_16x16x32_bf16 v[160:163], a[184:187], v[232:235], v[160:163]\n"
"s_waitcnt vmcnt(10) lgkmcnt(2)\n"
"v_cmp_ne_u64 vcc, s[88:89], v[28:29]\n"
"v_mfma_f32_16x16x32_bf16 v[112:115], a[236:239], v[40:43], v[112:115]\n"
"buffer_load_dwordx4 a[24:27], v0, s[8:11], s90 offen offset:2048 sc1\n"
"v_mfma_f32_16x16x32_bf16 v[120:123], a[236:239], v[220:223], v[120:123]\n"
"s_cbranch_vccnz Ls1_%=\n"
"Lb1_%=:\n"
"v_mfma_f32_16x16x32_bf16 v[124:127], a[236:239], v[180:183], v[124:127]\n"
"s_barrier\n"
"ds_read_b128 v[88:91], v27 offset:0\n"
"ds_read_b128 v[216:219], v27 offset:256\n"
"ds_read_b128 v[236:239], v27 offset:512\n"
"ds_read_b128 v[192:195], v27 offset:768\n"
"ds_read_b128 v[168:171], v27 offset:4096\n"
"ds_read_b128 v[248:251], v27 offset:4352\n"
"ds_read_b128 v[204:207], v27 offset:4608\n"
"ds_read_b128 v[52:55], v27 offset:4864\n"
"buffer_load_dwordx2 v[32:33], v18, s[8:11], s75 offen sc1\n"
"v_mfma_f32_16x16x32_bf16 v[128:131], a[232:235], v[40:43], v[128:131]\n"
"s_mul_i32 s20, s40, 0x1800\n"
"s_mul_i32 s21, s40, 0x2300\n"
"s_add_u32 s21, s21, 0x1f400\n"
"v_mfma_f32_16x16x32_bf16 v[84:87], a[232:235], v[220:223], v[84:87]\n"
"s_add_u32 s22, s55, 0xb800\n"
"v_mfma_f32_16x16x32_bf16 v[136:139], a[232:235], v[180:183], v[136:139]\n"
"buffer_load_dwordx4 a[16:19], v0, s[8:11], s90 offen offset:3072 sc1\n"
"v_mfma_f32_16x16x32_bf16 v[140:143], a[224:227], v[40:43], v[140:143]\n"
"s_add_u32 s90, s52, 0x48000\n"
"ds_read_b128 v[184:187], v19 offset:16384\n"
"v_mfma_f32_16x16x32_bf16 v[148:151], a[224:227], v[220:223], v[148:151]\n"
"ds_read_b128 v[48:51], v5 offset:21504\n"
"v_mfma_f32_16x16x32_bf16 v[156:159], a[224:227], v[180:183], v[156:159]\n"
"s_waitcnt vmcnt(11) lgkmcnt(10)\n"
"buffer_load_dwordx4 a[36:39], v0, s[8:11], s90 offen offset:0 sc1\n"
"ds_read_b128 v[64:67], v5 offset:26624\n"
"v_mfma_f32_16x16x32_bf16 v[96:99], a[244:247], v[228:231], v[96:99]\n"
"s_waitcnt lgkmcnt(9)\n"
"v_add_f32 v88, v88, v216\n"
"v_mfma_f32_16x16x32_bf16 v[104:107], a[244:247], v[212:215], v[104:107]\n"
"v_mfma_f32_16x16x32_bf16 v[108:111], a[244:247], v[92:95], v[108:111]\n"
"s_waitcnt vmcnt(10)\n"
"s_waitcnt lgkmcnt(7)\n"
"v_add_f32 v236, v236, v192\n"
"v_mfma_f32_16x16x32_bf16 v[112:115], a[252:255], v[228:231], v[112:115]\n"
"v_add_f32 v89, v89, v217\n"
"v_add_f32 v237, v237, v193\n"
"v_mfma_f32_16x16x32_bf16 v[120:123], a[252:255], v[212:215], v[120:123]\n"
"buffer_load_dwordx4 a[44:47], v0, s[8:11], s90 offen offset:1024 sc1\n"
"v_mfma_f32_16x16x32_bf16 v[124:127], a[252:255], v[92:95], v[124:127]\n"
"v_add_f32 v90, v90, v218\n"
"v_add_f32 v238, v238, v194\n"
"v_mfma_f32_16x16x32_bf16 v[128:131], a[248:251], v[228:231], v[128:131]\n"
"v_add_f32 v91, v91, v219\n"
"v_add_f32 v239, v239, v195\n"
"v_mfma_f32_16x16x32_bf16 v[84:87], a[248:251], v[212:215], v[84:87]\n"
"v_add_f32 v88, v88, v236\n"
"v_add_f32 v89, v89, v237\n"
"v_mfma_f32_16x16x32_bf16 v[136:139], a[248:251], v[92:95], v[136:139]\n"
"buffer_load_dwordx4 a[40:43], v0, s[8:11], s90 offen offset:2048 sc1\n"
"v_mfma_f32_16x16x32_bf16 v[140:143], a[240:243], v[228:231], v[140:143]\n"
"ds_read_b128 v[68:71], v19 offset:17408\n"
"v_add_f32 v90, v90, v238\n"
"v_mfma_f32_16x16x32_bf16 v[148:151], a[240:243], v[212:215], v[148:151]\n"
"ds_read_b128 v[72:75], v5 offset:22528\n"
"v_add_f32 v91, v91, v239\n"
"v_mfma_f32_16x16x32_bf16 v[156:159], a[240:243], v[92:95], v[156:159]\n"
"s_waitcnt vmcnt(10) lgkmcnt(4)\n"
"ds_read_b128 v[208:211], v5 offset:27648\n"
"v_add_f32 v88, v88, v90\n"
"v_mfma_f32_16x16x32_bf16 v[96:99], a[12:15], v[184:187], v[96:99]\n"
"s_waitcnt lgkmcnt(4)\n"
"v_add_f32 v89, v89, v91\n"
"v_mul_f32 v216, 0xbacccccd, v88\n"
"v_mfma_f32_16x16x32_bf16 v[104:107], a[12:15], v[48:51], v[104:107]\n"
"s_waitcnt lgkmcnt(3)\n"
"buffer_load_dwordx4 a[32:35], v0, s[8:11], s90 offen offset:3072 sc1\n"
"v_mfma_f32_16x16x32_bf16 v[108:111], a[12:15], v[64:67], v[108:111]\n"
"s_add_u32 s90, s52, 0x4c000\n"
"v_mul_f32 v89, 0x3acccccd, v89\n"
"v_fma_f32 v89, -v216, v216, v89\n"
"v_mfma_f32_16x16x32_bf16 v[100:103], a[176:179], v[44:47], v[100:103]\n"
"s_waitcnt vmcnt(9)\n"
"ds_read_b128 v[80:83], v5 offset:14336\n"
"v_max_f32 v89, 0, v89\n"
"v_mfma_f32_16x16x32_bf16 v[112:115], a[0:3], v[184:187], v[112:115]\n"
"v_add_f32 v89, 0x3727c5ac, v89\n"
"v_add_f32 v168, v168, v248\n"
"v_mfma_f32_16x16x32_bf16 v[120:123], a[0:3], v[48:51], v[120:123]\n"
"v_rsq_f32 v217, v89\n"
"v_mfma_f32_16x16x32_bf16 v[124:127], a[0:3], v[64:67], v[124:127]\n"
"buffer_load_dwordx4 a[52:55], v0, s[8:11], s90 offen offset:0 sc1\n"
"v_mfma_f32_16x16x32_bf16 v[128:131], a[8:11], v[184:187], v[128:131]\n"
"v_mul_f32 v218, v89, v217\n"
"v_mul_f32 v219, 0x3ea52b5c, v217\n"
"v_mfma_f32_16x16x32_bf16 v[84:87], a[8:11], v[48:51], v[84:87]\n"
"ds_write_b128 v27, v[216:219] offset:8192\n"
"ds_read_b128 v[200:203], v22 offset:3072\n"
"ds_read_b128 v[176:179], v22 offset:0\n"
"ds_read_b128 v[36:39], v22 offset:1024\n"
"v_mfma_f32_16x16x32_bf16 v[136:139], a[8:11], v[64:67], v[136:139]\n"
"buffer_load_dwordx4 a[60:63], v0, s[8:11], s90 offen offset:1024 sc1\n"
"v_mfma_f32_16x16x32_bf16 v[140:143], a[4:7], v[184:187], v[140:143]\n"
"ds_read_b128 v[60:63], v19 offset:18432\n"
"v_add_f32 v204, v204, v52\n"
"v_mfma_f32_16x16x32_bf16 v[148:151], a[4:7], v[48:51], v[148:151]\n"
"ds_read_b128 v[240:243], v5 offset:23552\n"
"v_add_f32 v169, v169, v249\n"
"v_mfma_f32_16x16x32_bf16 v[156:159], a[4:7], v[64:67], v[156:159]\n"
"s_waitcnt vmcnt(9) lgkmcnt(7)\n"
"ds_read_b128 v[76:79], v5 offset:28672\n"
"v_add_f32 v205, v205, v53\n"
"v_mfma_f32_16x16x32_bf16 v[96:99], a[28:31], v[68:71], v[96:99]\n"
"buffer_load_dwordx4 a[56:59], v0, s[8:11], s90 offen offset:2048 sc1\n"
"v_mfma_f32_16x16x32_bf16 v[104:107], a[28:31], v[72:75], v[104:107]\n"
"v_add_f32 v170, v170, v250\n"
"v_add_f32 v206, v206, v54\n"
"v_mfma_f32_16x16x32_bf16 v[108:111], a[28:31], v[208:211], v[108:111]\n"
"v_add_f32 v171, v171, v251\n"
"v_add_f32 v207, v207, v55\n"
"v_mfma_f32_16x16x32_bf16 v[172:175], a[176:179], v[232:235], v[172:175]\n"
"ds_read_b128 v[28:31], v5 offset:19456\n"
"v_add_f32 v168, v168, v204\n"
"v_mfma_f32_16x16x32_bf16 v[224:227], a[196:199], v[164:167], v[224:227]\n"
"v_add_f32 v169, v169, v205\n"
"v_add_f32 v170, v170, v206\n"
"v_mfma_f32_16x16x32_bf16 v[188:191], a[196:199], v[56:59], v[188:191]\n"
"buffer_load_dwordx4 a[48:51], v0, s[8:11], s90 offen offset:3072 sc1\n"
"v_mfma_f32_16x16x32_bf16 v[244:247], a[204:207], v[164:167], v[244:247]\n"
"s_add_u32 s90, s54, 0x0\n"
"v_mov_b32 v40, -1\n"
"v_mov_b32 v41, -1\n"
"v_mfma_f32_16x16x32_bf16 v[144:147], a[204:207], v[56:59], v[144:147]\n"
"v_mov_b32 v42, -1\n"
"v_mov_b32 v43, -1\n"
"v_mfma_f32_16x16x32_bf16 v[152:155], a[200:203], v[164:167], v[152:155]\n"
"s_add_u32 s91, s74, 0x0\n"
"v_add_f32 v171, v171, v207\n"
"v_add_f32 v168, v168, v170\n"
"v_mfma_f32_16x16x32_bf16 v[160:163], a[200:203], v[56:59], v[160:163]\n"
"buffer_store_dwordx4 v[40:43], v0, s[8:11], s91 offen offset:0 sc1\n"
"v_mfma_f32_16x16x32_bf16 v[100:103], a[192:195], v[164:167], v[100:103]\n"
"s_add_u32 s91, s74, 0x8000\n"
"ds_read_b128 v[220:223], v5 offset:30720\n"
"v_add_f32 v169, v169, v171\n"
"v_mfma_f32_16x16x32_bf16 v[172:175], a[192:195], v[56:59], v[172:175]\n"
"buffer_store_dwordx4 v[40:43], v0, s[8:11], s91 offen offset:0 sc1\n"
"ds_read_b128 v[180:183], v5 offset:35840\n"
"v_mfma_f32_16x16x32_bf16 v[224:227], a[212:215], v[132:135], v[224:227]\n"
"s_add_u32 s91, s25, 0x10000\n"
"v_mul_f32 v248, 0xbacccccd, v168\n"
"v_mul_f32 v169, 0x3acccccd, v169\n"
"v_mfma_f32_16x16x32_bf16 v[188:191], a[212:215], v[116:119], v[188:191]\n"
"v_fma_f32 v169, -v248, v248, v169\n"
"v_max_f32 v169, 0, v169\n"
"v_mfma_f32_16x16x32_bf16 v[244:247], a[220:223], v[132:135], v[244:247]\n"
"v_add_f32 v169, 0x3727c5ac, v169\n"
"v_mfma_f32_16x16x32_bf16 v[144:147], a[220:223], v[116:119], v[144:147]\n"
"buffer_store_dwordx4 v[40:43], v0, s[8:11], s91 offen offset:0 sc1\n"
"s_waitcnt vmcnt(11) lgkmcnt(2)\n"
"v_mfma_f32_16x16x32_bf16 v[112:115], a[16:19], v[68:71], v[112:115]\n"
"v_rsq_f32 v249, v169\n"
"v_mfma_f32_16x16x32_bf16 v[120:123], a[16:19], v[72:75], v[120:123]\n"
"v_mul_f32 v250, v169, v249\n"
"v_mul_f32 v251, 0x3ea52b5c, v249\n"
"v_mfma_f32_16x16x32_bf16 v[124:127], a[16:19], v[208:211], v[124:127]\n"
"ds_write_b128 v27, v[248:251] offset:8448\n"
"ds_read_b128 v[192:195], v22 offset:256\n"
"ds_read_b128 v[228:231], v22 offset:1280\n"
"v_mfma_f32_16x16x32_bf16 v[128:131], a[24:27], v[68:71], v[128:131]\n"
"v_cmp_ne_u64 vcc, s[88:89], v[32:33]\n"
"v_mfma_f32_16x16x32_bf16 v[84:87], a[24:27], v[72:75], v[84:87]\n"
"v_mfma_f32_16x16x32_bf16 v[136:139], a[24:27], v[208:211], v[136:139]\n"
"s_cbranch_vccnz Ls2_%=\n"
"Lb2_%=:\n"
"v_mfma_f32_16x16x32_bf16 v[140:143], a[20:23], v[68:71], v[140:143]\n"
"buffer_load_dwordx4 a[68:71], v0, s[8:11], s90 offen offset:0 sc1\n"
"ds_read_b128 v[212:215], v19 offset:19456\n"
"v_mfma_f32_16x16x32_bf16 v[148:151], a[20:23], v[72:75], v[148:151]\n"
"ds_read_b128 v[236:239], v5 offset:24576\n"
"v_mfma_f32_16x16x32_bf16 v[156:159], a[20:23], v[208:211], v[156:159]\n"
"s_waitcnt vmcnt(10) lgkmcnt(5)\n"
"ds_read_b128 v[92:95], v5 offset:29696\n"
"s_add_u32 s2, s57, 0x2c000\n"
"v_mfma_f32_16x16x32_bf16 v[96:99], a[44:47], v[60:63], v[96:99]\n"
"buffer_load_dwordx4 a[76:79], v0, s[8:11], s90 offen offset:1024 sc1\n"
"v_mfma_f32_16x16x32_bf16 v[104:107], a[44:47], v[240:243], v[104:107]\n"
"v_mfma_f32_16x16x32_bf16 v[108:111], a[44:47], v[76:79], v[108:111]\n"
"s_waitcnt vmcnt(9)\n"
"v_mfma_f32_16x16x32_bf16 v[112:115], a[32:35], v[60:63], v[112:115]\n"
"v_mfma_f32_16x16x32_bf16 v[120:123], a[32:35], v[240:243], v[120:123]\n"
"v_mfma_f32_16x16x32_bf16 v[124:127], a[32:35], v[76:79], v[124:127]\n"
"buffer_load_dwordx4 a[72:75], v0, s[8:11], s90 offen offset:2048 sc1\n"
"v_mfma_f32_16x16x32_bf16 v[128:131], a[40:43], v[60:63], v[128:131]\n"
"v_mfma_f32_16x16x32_bf16 v[84:87], a[40:43], v[240:243], v[84:87]\n"
"v_mfma_f32_16x16x32_bf16 v[136:139], a[40:43], v[76:79], v[136:139]\n"
"v_mfma_f32_16x16x32_bf16 v[140:143], a[36:39], v[60:63], v[140:143]\n"
"buffer_load_dwordx4 a[64:67], v0, s[8:11], s90 offen offset:3072 sc1\n"
"v_mfma_f32_16x16x32_bf16 v[148:151], a[36:39], v[240:243], v[148:151]\n"
"s_add_u32 s90, s54, 0x1000\n"
"v_mfma_f32_16x16x32_bf16 v[156:159], a[36:39], v[76:79], v[156:159]\n"
"s_waitcnt vmcnt(9) lgkmcnt(2)\n"
"v_mfma_f32_16x16x32_bf16 v[96:99], a[60:63], v[212:215], v[96:99]\n"
"s_waitcnt lgkmcnt(1)\n"
"v_mfma_f32_16x16x32_bf16 v[104:107], a[60:63], v[236:239], v[104:107]\n"
"s_waitcnt lgkmcnt(0)\n"
"v_mfma_f32_16x16x32_bf16 v[108:111], a[60:63], v[92:95], v[108:111]\n"
"buffer_load_dwordx4 a[84:87], v0, s[8:11], s90 offen offset:0 sc1\n"
"v_mfma_f32_16x16x32_bf16 v[152:155], a[216:219], v[132:135], v[152:155]\n"
"v_mfma_f32_16x16x32_bf16 v[160:163], a[216:219], v[116:119], v[160:163]\n"
"v_mfma_f32_16x16x32_bf16 v[100:103], a[208:211], v[132:135], v[100:103]\n"
"ds_read_b128 v[44:47], v5 offset:31744\n"
"s_waitcnt vmcnt(8)\n"
"v_mfma_f32_16x16x32_bf16 v[112:115], a[48:51], v[212:215], v[112:115]\n"
"v_mov_b32_dpp v88, v96 row_newbcast:0 row_mask:0xf bank_mask:0xf\n"
"v_mov_b32_dpp v89, v97 row_newbcast:0 row_mask:0xf bank_mask:0xf\n"
"v_mfma_f32_16x16x32_bf16 v[120:123], a[48:51], v[236:239], v[120:123]\n"
"buffer_load_dwordx4 a[92:95], v0, s[8:11], s90 offen offset:1024 sc1\n"
"v_mfma_f32_16x16x32_bf16 v[124:127], a[48:51], v[92:95], v[124:127]\n"
"v_mov_b32_dpp v90, v98 row_newbcast:0 row_mask:0xf bank_mask:0xf\n"
"v_mov_b32_dpp v91, v99 row_newbcast:0 row_mask:0xf bank_mask:0xf\n"
"v_mfma_f32_16x16x32_bf16 v[128:131], a[56:59], v[212:215], v[128:131]\n"
"ds_write_b128 v20, v[88:91] offset:64\n"
"v_mfma_f32_16x16x32_bf16 v[84:87], a[56:59], v[236:239], v[84:87]\n"
"v_fmac_f32 v96, v200, v88\n"
"v_fmac_f32 v97, v200, v89\n"
"v_mfma_f32_16x16x32_bf16 v[136:139], a[56:59], v[92:95], v[136:139]\n"
"buffer_load_dwordx4 a[88:91], v0, s[8:11], s90 offen offset:2048 sc1\n"
"v_mfma_f32_16x16x32_bf16 v[140:143], a[52:55], v[212:215], v[140:143]\n"
"v_fmac_f32 v98, v200, v90\n"
"v_fmac_f32 v99, v200, v91\n"
"v_mfma_f32_16x16x32_bf16 v[148:151], a[52:55], v[236:239], v[148:151]\n"
"v_fmac_f32 v104, v176, v88\n"
"v_fmac_f32 v105, v176, v89\n"
"v_mfma_f32_16x16x32_bf16 v[156:159], a[52:55], v[92:95], v[156:159]\n"
"v_fmac_f32 v106, v176, v90\n"
"v_fmac_f32 v107, v176, v91\n"
"v_mul_f32 v96, v96, v201\n"
"v_mul_f32 v97, v97, v201\n"
"v_mul_f32 v98, v98, v201\n"
"v_mul_f32 v99, v99, v201\n"
"v_mfma_f32_16x16x32_bf16 v[172:175], a[208:211], v[116:119], v[172:175]\n"
"buffer_load_dwordx4 a[80:83], v0, s[8:11], s90 offen offset:3072 sc1\n"
"ds_read_b128 v[216:219], v5 offset:36864\n"
"v_mfma_f32_16x16x32_bf16 v[224:227], a[228:231], v[80:83], v[224:227]\n"
"s_add_u32 s90, s54, 0x2000\n"
"v_fmac_f32 v108, v36, v88\n"
"v_fmac_f32 v109, v36, v89\n"
"v_mfma_f32_16x16x32_bf16 v[188:191], a[228:231], v[28:31], v[188:191]\n"
"v_fmac_f32 v110, v36, v90\n"
"v_fmac_f32 v111, v36, v91\n"
"v_mfma_f32_16x16x32_bf16 v[244:247], a[236:239], v[80:83], v[244:247]\n"
"v_mov_b32_dpp v184, v112 row_newbcast:0 row_mask:0xf bank_mask:0xf\n"
"v_mov_b32_dpp v185, v113 row_newbcast:0 row_mask:0xf bank_mask:0xf\n"
"v_mfma_f32_16x16x32_bf16 v[144:147], a[236:239], v[28:31], v[144:147]\n"
"buffer_load_dwordx4 a[100:103], v0, s[8:11], s90 offen offset:0 sc1\n"
"v_mfma_f32_16x16x32_bf16 v[152:155], a[232:235], v[80:83], v[152:155]\n"
"v_mov_b32_dpp v186, v114 row_newbcast:0 row_mask:0xf bank_mask:0xf\n"
"v_mov_b32_dpp v187, v115 row_newbcast:0 row_mask:0xf bank_mask:0xf\n"
"v_mfma_f32_16x16x32_bf16 v[160:163], a[232:235], v[28:31], v[160:163]\n"
"ds_write_b128 v20, v[184:187] offset:192\n"
"v_mfma_f32_16x16x32_bf16 v[100:103], a[224:227], v[80:83], v[100:103]\n"
"buffer_load_dwordx4 a[108:111], v0, s[8:11], s90 offen offset:1024 sc1\n"
"ds_read_b128 v[48:51], v5 offset:32768\n"
"v_mfma_f32_16x16x32_bf16 v[172:175], a[224:227], v[28:31], v[172:175]\n"
"ds_read_b128 v[64:67], v5 offset:37888\n"
"v_fmac_f32 v112, v200, v184\n"
"v_mfma_f32_16x16x32_bf16 v[224:227], a[244:247], v[220:223], v[224:227]\n"
"v_fmac_f32 v113, v200, v185\n"
"v_fmac_f32 v114, v200, v186\n"
"v_mfma_f32_16x16x32_bf16 v[188:191], a[244:247], v[180:183], v[188:191]\n"
"v_fmac_f32 v115, v200, v187\n"
"v_fmac_f32 v120, v176, v184\n"
"v_mfma_f32_16x16x32_bf16 v[244:247], a[252:255], v[220:223], v[244:247]\n"
"v_fmac_f32 v121, v176, v185\n"
"v_fmac_f32 v122, v176, v186\n"
"v_mfma_f32_16x16x32_bf16 v[144:147], a[252:255], v[180:183], v[144:147]\n"
"buffer_load_dwordx4 a[104:107], v0, s[8:11], s90 offen offset:2048 sc1\n"
"ds_read_b128 v[52:55], v198 offset:0\n"
"ds_read_b64 v[234:235], v199 offset:0\n"
"v_mfma_f32_16x16x32_bf16 v[152:155], a[248:251], v[220:223], v[152:155]\n"
"v_mov_b32 v204, -1\n"
"v_mov_b32 v205, -1\n"
"v_mfma_f32_16x16x32_bf16 v[160:163], a[248:251], v[180:183], v[160:163]\n"
"v_mov_b32 v206, -1\n"
"v_mov_b32 v207, -1\n"
"v_mfma_f32_16x16x32_bf16 v[100:103], a[240:243], v[220:223], v[100:103]\n"
"s_add_u32 s91, s80, 0x0\n"
"ds_read_b128 v[164:167], v5 offset:33792\n"
"v_fmac_f32 v123, v176, v187\n"
"v_mfma_f32_16x16x32_bf16 v[172:175], a[240:243], v[180:183], v[172:175]\n"
"s_waitcnt lgkmcnt(6)\n"
"buffer_load_dwordx4 a[96:99], v0, s[8:11], s90 offen offset:3072 sc1\n"
"ds_read_b128 v[56:59], v5 offset:38912\n"
"v_mfma_f32_16x16x32_bf16 v[224:227], a[12:15], v[44:47], v[224:227]\n"
"s_add_u32 s90, s54, 0x3000\n"
"v_fmac_f32 v124, v36, v184\n"
"v_fmac_f32 v125, v36, v185\n"
"v_mfma_f32_16x16x32_bf16 v[188:191], a[12:15], v[216:219], v[188:191]\n"
"buffer_store_dwordx4 v[204:207], v0, s[8:11], s91 offen offset:0 sc1\n"
"v_mfma_f32_16x16x32_bf16 v[244:247], a[0:3], v[44:47], v[244:247]\n"
"v_fmac_f32 v126, v36, v186\n"
"v_fmac_f32 v127, v36, v187\n"
"v_mfma_f32_16x16x32_bf16 v[144:147], a[0:3], v[216:219], v[144:147]\n"
"v_mov_b32_dpp v40, v128 row_newbcast:0 row_mask:0xf bank_mask:0xf\n"
"v_mov_b32_dpp v41, v129 row_newbcast:0 row_mask:0xf bank_mask:0xf\n"
"v_mfma_f32_16x16x32_bf16 v[152:155], a[8:11], v[44:47], v[152:155]\n"
"v_mov_b32_dpp v42, v130 row_newbcast:0 row_mask:0xf bank_mask:0xf\n"
"v_mov_b32_dpp v43, v131 row_newbcast:0 row_mask:0xf bank_mask:0xf\n"
"v_mfma_f32_16x16x32_bf16 v[160:163], a[8:11], v[216:219], v[160:163]\n"
"buffer_store_dwordx4 v[204:207], v0, s[8:11], s91 offen offset:1024 sc1\n"
"v_mfma_f32_16x16x32_bf16 v[100:103], a[4:7], v[44:47], v[100:103]\n"
"ds_read_b128 v[168:171], v5 offset:34816\n"
"v_fmac_f32 v128, v200, v40\n"
"v_mfma_f32_16x16x32_bf16 v[172:175], a[4:7], v[216:219], v[172:175]\n"
"s_waitcnt lgkmcnt(4)\n"
"ds_read_b128 v[248:251], v5 offset:39936\n"
"v_fmac_f32 v129, v200, v41\n"
"v_mfma_f32_16x16x32_bf16 v[224:227], a[28:31], v[48:51], v[224:227]\n"
"s_add_u32 s4, s57, 0x36000\n"
"ds_write_b128 v20, v[40:43] offset:128\n"
"v_mfma_f32_16x16x32_bf16 v[188:191], a[28:31], v[64:67], v[188:191]\n"
"buffer_store_dwordx4 v[204:207], v0, s[8:11], s91 offen offset:2048 sc1\n"
"v_mfma_f32_16x16x32_bf16 v[244:247], a[16:19], v[48:51], v[244:247]\n"
"v_fmac_f32 v130, v200, v42\n"
"v_fmac_f32 v131, v200, v43\n"
"v_mfma_f32_16x16x32_bf16 v[144:147], a[16:19], v[64:67], v[144:147]\n"
"v_fmac_f32 v84, v176, v40\n"
"v_fmac_f32 v85, v176, v41\n"
"v_mfma_f32_16x16x32_bf16 v[152:155], a[24:27], v[48:51], v[152:155]\n"
"v_fmac_f32 v86, v176, v42\n"
"v_fmac_f32 v87, v176, v43\n"
"v_mfma_f32_16x16x32_bf16 v[160:163], a[24:27], v[64:67], v[160:163]\n"
"buffer_store_dwordx4 v[204:207], v0, s[8:11], s91 offen offset:3072 sc1\n"
"v_mfma_f32_16x16x32_bf16 v[100:103], a[20:23], v[48:51], v[100:103]\n"
"s_add_u32 s91, s80, 0x1000\n"
"v_fmac_f32 v136, v36, v40\n"
"v_fmac_f32 v137, v36, v41\n"
"v_mfma_f32_16x16x32_bf16 v[172:175], a[20:23], v[64:67], v[172:175]\n"
"s_waitcnt lgkmcnt(3)\n"
"v_fmac_f32 v138, v36, v42\n"
"v_fmac_f32 v139, v36, v43\n"
"v_mfma_f32_16x16x32_bf16 v[224:227], a[44:47], v[164:167], v[224:227]\n"
"v_mov_b32_dpp v140, v140 row_newbcast:0 row_mask:0xf bank_mask:0xf\n"
"v_mov_b32_dpp v141, v141 row_newbcast:0 row_mask:0xf bank_mask:0xf\n"
"v_mfma_f32_16x16x32_bf16 v[188:191], a[44:47], v[56:59], v[188:191]\n"
"buffer_store_dwordx4 v[204:207], v0, s[8:11], s91 offen offset:0 sc1\n"
"v_mfma_f32_16x16x32_bf16 v[244:247], a[32:35], v[164:167], v[244:247]\n"
"v_mov_b32_dpp v142, v142 row_newbcast:0 row_mask:0xf bank_mask:0xf\n"
"v_mov_b32_dpp v143, v143 row_newbcast:0 row_mask:0xf bank_mask:0xf\n"
"v_mfma_f32_16x16x32_bf16 v[144:147], a[32:35], v[56:59], v[144:147]\n"
"ds_write_b128 v20, v[140:143] offset:0\n"
"v_mfma_f32_16x16x32_bf16 v[152:155], a[40:43], v[164:167], v[152:155]\n"
"v_fmac_f32 v148, v176, v140\n"
"v_fmac_f32 v149, v176, v141\n"
"v_mfma_f32_16x16x32_bf16 v[160:163], a[40:43], v[56:59], v[160:163]\n"
"buffer_store_dwordx4 v[204:207], v0, s[8:11], s91 offen offset:1024 sc1\n"
"v_mfma_f32_16x16x32_bf16 v[100:103], a[36:39], v[164:167], v[100:103]\n"
"s_add_u32 s91, s81, 0x1800\n"
"v_fmac_f32 v150, v176, v142\n"
"v_fmac_f32 v151, v176, v143\n"
"v_mfma_f32_16x16x32_bf16 v[172:175], a[36:39], v[56:59], v[172:175]\n"
"s_waitcnt lgkmcnt(1)\n"
"v_fmac_f32 v156, v36, v140\n"
"v_fmac_f32 v157, v36, v141\n"
"v_mfma_f32_16x16x32_bf16 v[224:227], a[60:63], v[168:171], v[224:227]\n"
"v_fmac_f32 v158, v36, v142\n"
"v_fmac_f32 v159, v36, v143\n"
"v_cndmask_b32 v202, v201, 1.0, s[46:47]\n"
"v_cndmask_b32 v178, v177, 1.0, s[46:47]\n"
"v_cndmask_b32 v38, v37, 1.0, s[46:47]\n"
"v_mul_f32 v112, v112, v202\n"
"v_mfma_f32_16x16x32_bf16 v[188:191], a[60:63], v[248:251], v[188:191]\n"
"buffer_store_dwordx4 v[204:207], v0, s[8:11], s91 offen offset:0 sc1\n"
"v_mul_f32 v113, v113, v202\n"
"v_mfma_f32_16x16x32_bf16 v[244:247], a[48:51], v[168:171], v[244:247]\n"
"v_fmac_f32 v224, v192, v88\n"
"v_fmac_f32 v225, v192, v89\n"
"v_mul_f32 v114, v114, v202\n"
"v_mul_f32 v115, v115, v202\n"
"v_cvt_pk_bf16_f32 v96, v96, v97\n"
"v_cvt_pk_bf16_f32 v97, v98, v99\n"
"v_mfma_f32_16x16x32_bf16 v[144:147], a[48:51], v[248:251], v[144:147]\n"
"v_fmac_f32 v226, v192, v90\n"
"v_fmac_f32 v227, v192, v91\n"
"v_cvt_pk_bf16_f32 v98, v112, v113\n"
"v_cvt_pk_bf16_f32 v99, v114, v115\n"
"v_mfma_f32_16x16x32_bf16 v[152:155], a[56:59], v[168:171], v[152:155]\n"
"ds_write_b64 v23, v[98:99] offset:512\n"
"ds_read_b128 v[32:35], v5 offset:49152\n"
"ds_read_b128 v[68:71], v5 offset:46080\n"
"ds_read_b128 v[72:75], v5 offset:45056\n"
"ds_read_b128 v[208:211], v5 offset:46080\n"
"v_mfma_f32_16x16x32_bf16 v[160:163], a[56:59], v[248:251], v[160:163]\n"
"buffer_load_dwordx4 a[116:119], v0, s[8:11], s90 offen offset:0 sc1\n"
"v_mul_f32 v104, v104, v177\n"
"v_mul_f32 v105, v105, v177\n"
"v_mul_f32 v106, v106, v177\n"
"v_mul_f32 v107, v107, v177\n"
"v_mfma_f32_16x16x32_bf16 v[100:103], a[52:55], v[168:171], v[100:103]\n"
"v_fmac_f32 v188, v228, v88\n"
"v_fmac_f32 v189, v228, v89\n"
"v_mul_f32 v120, v120, v178\n"
"v_mul_f32 v121, v121, v178\n"
"v_mul_f32 v122, v122, v178\n"
"v_mul_f32 v123, v123, v178\n"
"v_mfma_f32_16x16x32_bf16 v[172:175], a[52:55], v[248:251], v[172:175]\n"
"buffer_load_dwordx4 a[124:127], v0, s[8:11], s90 offen offset:1024 sc1\n"
"v_fmac_f32 v190, v228, v90\n"
"v_fmac_f32 v191, v228, v91\n"
"v_fmac_f32 v244, v192, v184\n"
"v_fmac_f32 v245, v192, v185\n"
"v_fmac_f32 v246, v192, v186\n"
"v_fmac_f32 v247, v192, v187\n"
"v_fmac_f32 v144, v228, v184\n"
"v_fmac_f32 v145, v228, v185\n"
"v_fmac_f32 v146, v228, v186\n"
"v_fmac_f32 v147, v228, v187\n"
"v_fmac_f32 v152, v192, v40\n"
"v_fmac_f32 v153, v192, v41\n"
"v_fmac_f32 v154, v192, v42\n"
"v_fmac_f32 v155, v192, v43\n"
"v_fmac_f32 v160, v228, v40\n"
"v_fmac_f32 v161, v228, v41\n"
"v_fmac_f32 v162, v228, v42\n"
"v_fmac_f32 v163, v228, v43\n"
"v_fmac_f32 v100, v192, v140\n"
"v_fmac_f32 v101, v192, v141\n"
"v_fmac_f32 v102, v192, v142\n"
"v_fmac_f32 v103, v192, v143\n"
"v_fmac_f32 v172, v228, v140\n"
"v_fmac_f32 v173, v228, v141\n"
"v_fmac_f32 v174, v228, v142\n"
"v_fmac_f32 v175, v228, v143\n"
"v_cndmask_b32 v194, v193, 1.0, s[46:47]\n"
"v_cndmask_b32 v230, v229, 1.0, s[46:47]\n"
"buffer_load_dwordx4 a[120:123], v0, s[8:11], s90 offen offset:2048 sc1\n"
"buffer_load_dwordx4 a[112:115], v0, s[8:11], s90 offen offset:3072 sc1\n"
"s_add_u32 s90, s54, 0x4000\n"
"v_cvt_pk_bf16_f32 v104, v104, v105\n"
"v_cvt_pk_bf16_f32 v105, v106, v107\n"
"v_cvt_pk_bf16_f32 v106, v120, v121\n"
"v_cvt_pk_bf16_f32 v107, v122, v123\n"
"ds_write_b64 v23, v[106:107] offset:1536\n"
"v_mul_f32 v108, v108, v37\n"
"v_mul_f32 v109, v109, v37\n"
"v_mul_f32 v110, v110, v37\n"
"v_mul_f32 v111, v111, v37\n"
"v_mul_f32 v124, v124, v38\n"
"v_mul_f32 v125, v125, v38\n"
"v_mul_f32 v126, v126, v38\n"
"v_mul_f32 v127, v127, v38\n"
"v_cvt_pk_bf16_f32 v108, v108, v109\n"
"v_cvt_pk_bf16_f32 v109, v110, v111\n"
"v_cvt_pk_bf16_f32 v110, v124, v125\n"
"v_cvt_pk_bf16_f32 v111, v126, v127\n"
"v_mul_f32 v224, v224, v193\n"
"v_mul_f32 v225, v225, v193\n"
"v_mul_f32 v226, v226, v193\n"
"v_mul_f32 v227, v227, v193\n"
"v_mul_f32 v244, v244, v194\n"
"v_mul_f32 v245, v245, v194\n"
"v_mul_f32 v246, v246, v194\n"
"v_mul_f32 v247, v247, v194\n"
"v_cvt_pk_bf16_f32 v224, v224, v225\n"
"buffer_load_dwordx4 a[132:135], v0, s[8:11], s90 offen offset:0 sc1\n"
"buffer_load_dwordx4 a[140:143], v0, s[8:11], s90 offen offset:1024 sc1\n"
"ds_write_b64 v23, v[110:111] offset:2560\n"
"v_cvt_pk_bf16_f32 v148, v148, v149\n"
"v_cvt_pk_bf16_f32 v149, v150, v151\n"
"v_mov_b32 v150, 0\n"
"v_mov_b32 v151, 0\n"
"v_cvt_pk_bf16_f32 v225, v226, v227\n"
"v_permlane16_swap_b32 v150, v106\n"
"v_permlane16_swap_b32 v151, v107\n"
"v_cvt_pk_bf16_f32 v156, v156, v157\n"
"v_cvt_pk_bf16_f32 v157, v158, v159\n"
"v_mov_b32 v158, 0\n"
"v_mov_b32 v159, 0\n"
"v_cvt_pk_bf16_f32 v226, v244, v245\n"
"v_permlane16_swap_b32 v158, v110\n"
"v_permlane16_swap_b32 v159, v111\n"
"s_waitcnt lgkmcnt(2)\n"
"v_mfma_f32_16x16x32_bf16 v[32:35], v[96:99], v[148:151], v[32:35]\n"
"buffer_load_dwordx4 a[136:139], v0, s[8:11], s90 offen offset:2048 sc1\n"
"v_mfma_f32_16x16x32_bf16 v[68:71], v[104:107], v[148:151], v[68:71]\n"
"v_cvt_pk_bf16_f32 v227, v246, v247\n"
"v_lshlrev_b32 v60, 16, v52\n"
"v_mfma_f32_16x16x32_bf16 v[72:75], v[104:107], v[156:159], v[72:75]\n"
"ds_write_b64 v23, v[226:227] offset:4608\n"
"ds_read_b128 v[240:243], v5 offset:47104\n"
"ds_read_b128 v[76:79], v5 offset:46080\n"
"ds_read_b128 v[132:135], v5 offset:45056\n"
"ds_read_b128 v[212:215], v5 offset:46080\n"
"v_mfma_f32_16x16x32_bf16 v[208:211], v[108:111], v[156:159], v[208:211]\n"
"buffer_load_dwordx4 a[128:131], v0, s[8:11], s90 offen offset:3072 sc1\n"
"s_add_u32 s90, s54, 0x5000\n"
"v_mul_f32 v128, v128, v201\n"
"v_mul_f32 v129, v129, v201\n"
"v_mul_f32 v130, v130, v201\n"
"v_mul_f32 v131, v131, v201\n"
"v_cvt_pk_bf16_f32 v128, v128, v129\n"
"v_cvt_pk_bf16_f32 v129, v130, v131\n"
"ds_write_b64 v23, v[128:129] offset:0\n"
"buffer_load_dwordx4 a[148:151], v0, s[8:11], s90 offen offset:0 sc1\n"
"v_mul_f32 v84, v84, v177\n"
"v_mul_f32 v85, v85, v177\n"
"v_mul_f32 v86, v86, v177\n"
"v_mul_f32 v87, v87, v177\n"
"v_cvt_pk_bf16_f32 v84, v84, v85\n"
"v_cvt_pk_bf16_f32 v85, v86, v87\n"
"ds_write_b64 v23, v[84:85] offset:1024\n"
"v_mul_f32 v136, v136, v37\n"
"v_mul_f32 v137, v137, v37\n"
"v_mul_f32 v138, v138, v37\n"
"v_mul_f32 v139, v139, v37\n"
"v_cvt_pk_bf16_f32 v136, v136, v137\n"
"v_cvt_pk_bf16_f32 v137, v138, v139\n"
"v_mul_f32 v188, v188, v229\n"
"v_mul_f32 v189, v189, v229\n"
"v_mul_f32 v190, v190, v229\n"
"v_mul_f32 v191, v191, v229\n"
"v_mul_f32 v144, v144, v230\n"
"v_mul_f32 v145, v145, v230\n"
"v_mul_f32 v146, v146, v230\n"
"v_mul_f32 v147, v147, v230\n"
"v_cvt_pk_bf16_f32 v188, v188, v189\n"
"v_cvt_pk_bf16_f32 v189, v190, v191\n"
"v_cvt_pk_bf16_f32 v190, v144, v145\n"
"v_cvt_pk_bf16_f32 v191, v146, v147\n"
"v_and_b32 v61, 0xffff0000, v52\n"
"buffer_load_dwordx4 a[156:159], v0, s[8:11], s90 offen offset:1024 sc1\n"
"buffer_load_dwordx4 a[152:155], v0, s[8:11], s90 offen offset:2048 sc1\n"
"ds_write_b64 v23, v[136:137] offset:2048\n"
"ds_read_b128 v[236:239], v5 offset:48128\n"
"ds_read_b64_tr_b16 v[92:93], v24 offset:0\n"
"ds_read_b64_tr_b16 v[94:95], v24 offset:1024\n"
"ds_read_b64_tr_b16 v[116:117], v24 offset:512\n"
"ds_read_b64_tr_b16 v[118:119], v24 offset:1536\n"
"s_waitcnt vmcnt(31) lgkmcnt(9)\n"
"ds_read_b64_tr_b16 v[80:81], v24 offset:1024\n"
"ds_read_b64_tr_b16 v[82:83], v24 offset:2048\n"
"ds_read_b64_tr_b16 v[28:29], v24 offset:1536\n"
"ds_read_b64_tr_b16 v[30:31], v24 offset:2560\n"
"v_max3_f32 v220, v32, v33, v34\n"
"v_max3_f32 v222, v68, v69, v70\n"
"v_max3_f32 v220, v220, v35, v71\n"
"v_max_f32 v220, v220, v222\n"
"v_max3_f32 v221, v72, v73, v74\n"
"v_max3_f32 v223, v208, v209, v210\n"
"v_max3_f32 v221, v221, v75, v211\n"
"v_max_f32 v221, v221, v223\n"
"v_lshlrev_b32 v62, 16, v53\n"
"v_and_b32 v63, 0xffff0000, v53\n"
"v_permlane32_swap_b32 v220, v221\n"
"v_max_f32 v220, v220, v221\n"
"v_mov_b32 v221, v220\n"
"v_lshlrev_b32 v52, 16, v54\n"
"v_and_b32 v53, 0xffff0000, v54\n"
"v_permlane16_swap_b32 v220, v221\n"
"buffer_load_dwordx4 a[144:147], v0, s[8:11], s90 offen offset:3072 sc1\n"
"ds_write_b64 v23, v[190:191] offset:5632\n"
"s_add_u32 s90, s54, 0x6000\n"
"buffer_load_dwordx4 a[164:167], v0, s[8:11], s90 offen offset:0 sc1\n"
"s_add_u32 s90, s52, 0x50000\n"
"v_max_f32 v220, v220, v221\n"
"v_mov_b32 v221, v220\n"
"v_cvt_pk_bf16_f32 v100, v100, v101\n"
"v_cvt_pk_bf16_f32 v101, v102, v103\n"
"v_permlane32_swap_b32 v220, v221\n"
"v_mul_f32 v220, v220, v179\n"
"v_fma_f32 v32, v32, v179, -v220\n"
"v_fma_f32 v33, v33, v179, -v220\n"
"v_fma_f32 v34, v34, v179, -v220\n"
"v_fma_f32 v35, v35, v179, -v220\n"
"v_fma_f32 v68, v68, v179, -v220\n"
"v_fma_f32 v69, v69, v179, -v220\n"
"v_fma_f32 v70, v70, v179, -v220\n"
"v_fma_f32 v71, v71, v179, -v220\n"
"v_exp_f32 v32, v32\n"
"v_exp_f32 v33, v33\n"
"v_exp_f32 v34, v34\n"
"v_exp_f32 v35, v35\n"
"v_exp_f32 v68, v68\n"
"buffer_load_dwordx4 a[172:175], v0, s[8:11], s90 offen offset:0 sc1\n"
"buffer_load_dwordx4 a[168:171], v0, s[8:11], s90 offen offset:1024 sc1\n"
"ds_read_b128 v[180:183], v5 offset:48128\n"
"s_add_u32 s6, s53, 0x50800\n"
"v_exp_f32 v69, v69\n"
"v_exp_f32 v70, v70\n"
"v_exp_f32 v71, v71\n"
"v_cvt_pk_bf16_f32 v32, v32, v33\n"
"v_cvt_pk_bf16_f32 v33, v34, v35\n"
"v_cvt_pk_bf16_f32 v34, v68, v69\n"
"v_cvt_pk_bf16_f32 v35, v70, v71\n"
"v_mov_b32 v102, 0\n"
"v_mov_b32 v103, 0\n"
"s_waitcnt vmcnt(28) lgkmcnt(2)\n"
"v_mfma_f32_16x16x32_bf16 v[44:47], v[236:239], v[32:35], 0\n"
"buffer_load_dwordx4 a[160:163], v0, s[8:11], s6 offen offset:0 sc1\n"
"v_mfma_f32_16x16x32_bf16 v[216:219], v[92:95], v[32:35], 0\n"
"v_permlane16_swap_b32 v102, v226\n"
"v_mfma_f32_16x16x32_bf16 v[48:51], v[116:119], v[32:35], 0\n"
"v_mul_f32 v221, v221, v39\n"
"v_fma_f32 v72, v72, v39, -v221\n"
"v_fma_f32 v73, v73, v39, -v221\n"
"v_fma_f32 v74, v74, v39, -v221\n"
"v_fma_f32 v75, v75, v39, -v221\n"
"v_fma_f32 v208, v208, v39, -v221\n"
"v_fma_f32 v209, v209, v39, -v221\n"
"v_fma_f32 v210, v210, v39, -v221\n"
"v_fma_f32 v211, v211, v39, -v221\n"
"v_exp_f32 v72, v72\n"
"v_exp_f32 v73, v73\n"
"v_exp_f32 v74, v74\n"
"v_exp_f32 v75, v75\n"
"v_exp_f32 v208, v208\n"
"v_exp_f32 v209, v209\n"
"v_exp_f32 v210, v210\n"
"buffer_load_dwordx4 a[180:183], v0, s[8:11], s6 offen offset:1024 sc1\n"
"s_add_u32 s90, s52, 0x54000\n"
"buffer_load_dwordx4 a[188:191], v0, s[8:11], s90 offen offset:0 sc1\n"
"v_exp_f32 v211, v211\n"
"v_cvt_pk_bf16_f32 v72, v72, v73\n"
"v_cvt_pk_bf16_f32 v73, v74, v75\n"
"v_cvt_pk_bf16_f32 v74, v208, v209\n"
"v_cvt_pk_bf16_f32 v75, v210, v211\n"
"v_permlane16_swap_b32 v103, v227\n"
"v_cvt_pk_bf16_f32 v172, v172, v173\n"
"v_mfma_f32_16x16x32_bf16 v[64:67], v[236:239], v[72:75], 0\n"
"buffer_load_dwordx4 a[184:187], v0, s[8:11], s90 offen offset:1024 sc1\n"
"v_mfma_f32_16x16x32_bf16 v[164:167], v[80:83], v[72:75], 0\n"
"s_add_u32 s6, s53, 0x54800\n"
"v_cvt_pk_bf16_f32 v173, v174, v175\n"
"v_mov_b32 v174, 0\n"
"v_mfma_f32_16x16x32_bf16 v[56:59], v[28:31], v[72:75], 0\n"
"v_rcp_f32 v44, v44\n"
"v_mov_b32 v175, 0\n"
"v_mul_f32 v216, v216, v44\n"
"v_mul_f32 v217, v217, v44\n"
"v_mul_f32 v218, v218, v44\n"
"v_mul_f32 v219, v219, v44\n"
"v_cvt_pk_bf16_f32 v204, v216, v217\n"
"v_cvt_pk_bf16_f32 v205, v218, v219\n"
"v_mul_f32 v48, v48, v44\n"
"v_mul_f32 v49, v49, v44\n"
"v_mul_f32 v50, v50, v44\n"
"v_mul_f32 v51, v51, v44\n"
"v_cvt_pk_bf16_f32 v112, v48, v49\n"
"v_cvt_pk_bf16_f32 v113, v50, v51\n"
"v_rcp_f32 v64, v64\n"
"v_permlane16_swap_b32 v174, v190\n"
"v_mul_f32 v164, v164, v64\n"
"v_mul_f32 v165, v165, v64\n"
"v_mul_f32 v166, v166, v64\n"
"v_mul_f32 v167, v167, v64\n"
"v_cvt_pk_bf16_f32 v206, v164, v165\n"
"buffer_load_dwordx4 a[176:179], v0, s[8:11], s6 offen offset:0 sc1\n"
"buffer_load_dwordx4 a[196:199], v0, s[8:11], s6 offen offset:1024 sc1\n"
"s_add_u32 s90, s52, 0x58000\n"
"v_cvt_pk_bf16_f32 v207, v166, v167\n"
"v_mul_f32 v56, v56, v64\n"
"v_mul_f32 v57, v57, v64\n"
"v_mul_f32 v58, v58, v64\n"
"v_mul_f32 v59, v59, v64\n"
"v_cvt_pk_bf16_f32 v114, v56, v57\n"
"v_cvt_pk_bf16_f32 v115, v58, v59\n"
"v_permlane16_swap_b32 v204, v206\n"
"v_permlane16_swap_b32 v205, v207\n"
"v_permlane16_swap_b32 v112, v114\n"
"v_permlane16_swap_b32 v113, v115\n"
"s_add_u32 s0, s65, 0x2c000\n"
"buffer_load_dwordx4 a[204:207], v0, s[8:11], s90 offen offset:0 sc1\n"
"buffer_store_dwordx4 v[204:207], v11, s[8:11], s0 offen offset:0 sc1\n"
"s_add_u32 s0, s66, 0x2c000\n"
"v_permlane16_swap_b32 v175, v191\n"
"v_mfma_f32_16x16x32_bf16 v[240:243], v[96:99], v[100:103], v[240:243]\n"
"v_lshlrev_b32 v54, 16, v55\n"
"v_and_b32 v55, 0xffff0000, v55\n"
"v_mfma_f32_16x16x32_bf16 v[76:79], v[224:227], v[100:103], v[76:79]\n"
"v_lshlrev_b32 v232, 16, v234\n"
"v_and_b32 v233, 0xffff0000, v234\n"
"v_mfma_f32_16x16x32_bf16 v[132:135], v[224:227], v[172:175], v[132:135]\n"
"v_lshlrev_b32 v234, 16, v235\n"
"v_and_b32 v235, 0xffff0000, v235\n"
"v_mfma_f32_16x16x32_bf16 v[212:215], v[188:191], v[172:175], v[212:215]\n"
"buffer_store_dwordx2 v[112:113], v12, s[8:11], s0 offen offset:0 sc1\n"
"v_mul_f32 v152, v152, v193\n"
"v_mul_f32 v153, v153, v193\n"
"v_mul_f32 v154, v154, v193\n"
"s_waitcnt vmcnt(10) lgkmcnt(0)\n"
"v_mfma_f32_16x16x32_bf16 v[168:171], a[172:175], v[180:183], 0\n"
"v_mul_f32 v155, v155, v193\n"
"v_cvt_pk_bf16_f32 v152, v152, v153\n"
"v_cvt_pk_bf16_f32 v153, v154, v155\n"
"ds_write_b64 v23, v[152:153] offset:4096\n"
"v_mul_f32 v160, v160, v229\n"
"v_mul_f32 v161, v161, v229\n"
"s_waitcnt vmcnt(9)\n"
"v_mfma_f32_16x16x32_bf16 v[248:251], a[168:171], v[180:183], 0\n"
"buffer_load_dwordx4 a[200:203], v0, s[8:11], s90 offen offset:1024 sc1\n"
"s_add_u32 s6, s53, 0x58800\n"
"v_mul_f32 v162, v162, v229\n"
"v_mul_f32 v163, v163, v229\n"
"v_cvt_pk_bf16_f32 v160, v160, v161\n"
"v_cvt_pk_bf16_f32 v161, v162, v163\n"
"ds_write_b64 v23, v[160:161] offset:5120\n"
"ds_read_b128 v[88:91], v5 offset:48128\n"
"ds_read_b64_tr_b16 v[184:185], v24 offset:0\n"
"ds_read_b64_tr_b16 v[186:187], v24 offset:4096\n"
"ds_read_b64_tr_b16 v[40:41], v24 offset:512\n"
"ds_read_b64_tr_b16 v[42:43], v24 offset:4608\n"
"ds_read_b64_tr_b16 v[140:141], v24 offset:4096\n"
"ds_read_b64_tr_b16 v[142:143], v24 offset:5120\n"
"ds_read_b64_tr_b16 v[120:121], v24 offset:4608\n"
"ds_read_b64_tr_b16 v[122:123], v24 offset:5632\n"
"s_waitcnt vmcnt(9)\n"
"v_mfma_f32_16x16x32_bf16 v[124:127], a[160:163], v[180:183], 0\n"
"buffer_load_dwordx4 a[192:195], v0, s[8:11], s6 offen offset:0 sc1\n"
"v_max3_f32 v148, v240, v241, v242\n"
"v_max3_f32 v150, v76, v77, v78\n"
"v_max3_f32 v148, v148, v243, v79\n"
"v_max_f32 v148, v148, v150\n"
"s_waitcnt vmcnt(9)\n"
"v_mfma_f32_16x16x32_bf16 v[244:247], a[180:183], v[180:183], 0\n"
"buffer_load_dwordx4 a[212:215], v0, s[8:11], s6 offen offset:1024 sc1\n"
"s_add_u32 s90, s52, 0x5c000\n"
"v_max3_f32 v149, v132, v133, v134\n"
"v_max3_f32 v151, v212, v213, v214\n"
"v_max3_f32 v149, v149, v135, v215\n"
"v_max_f32 v149, v149, v151\n"
"v_mul_f32 v232, s78, v232\n"
"v_mul_f32 v233, s78, v233\n"
"v_permlane32_swap_b32 v148, v149\n"
"v_max_f32 v148, v148, v149\n"
"v_mov_b32 v149, v148\n"
"v_mul_f32 v234, s78, v234\n"
"s_waitcnt vmcnt(9)\n"
"v_mfma_f32_16x16x32_bf16 v[168:171], a[188:191], v[180:183], v[168:171]\n"
"buffer_load_dwordx4 a[220:223], v0, s[8:11], s90 offen offset:0 sc1\n"
"v_permlane16_swap_b32 v148, v149\n"
"v_max_f32 v148, v148, v149\n"
"v_mov_b32 v149, v148\n"
"v_mul_f32 v235, s78, v235\n"
"ds_read_b128 v[104:107], v198 offset:10240\n"
"ds_read_b64 v[110:111], v199 offset:10240\n"
"v_permlane32_swap_b32 v148, v149\n"
"v_mul_f32 v148, v148, v195\n"
"v_fma_f32 v240, v240, v195, -v148\n"
"s_waitcnt vmcnt(9) lgkmcnt(12)\n"
"v_mfma_f32_16x16x32_bf16 v[248:251], a[184:187], v[180:183], v[248:251]\n"
"buffer_load_dwordx4 a[216:219], v0, s[8:11], s90 offen offset:1024 sc1\n"
"s_add_u32 s6, s53, 0x5c800\n"
"v_fma_f32 v241, v241, v195, -v148\n"
"v_fma_f32 v242, v242, v195, -v148\n"
"v_fma_f32 v243, v243, v195, -v148\n"
"v_fma_f32 v76, v76, v195, -v148\n"
"v_fma_f32 v77, v77, v195, -v148\n"
"v_fma_f32 v78, v78, v195, -v148\n"
"v_fma_f32 v79, v79, v195, -v148\n"
"v_exp_f32 v240, v240\n"
"v_exp_f32 v241, v241\n"
"s_waitcnt vmcnt(9) lgkmcnt(4)\n"
"v_mfma_f32_16x16x32_bf16 v[124:127], a[176:179], v[180:183], v[124:127]\n"
"v_exp_f32 v242, v242\n"
"v_exp_f32 v243, v243\n"
"v_exp_f32 v76, v76\n"
"s_waitcnt lgkmcnt(0)\n"
"s_barrier\n"
"buffer_load_dwordx4 a[208:211], v0, s[8:11], s6 offen offset:0 sc1\n"
"s_waitcnt vmcnt(9)\n"
"v_mfma_f32_16x16x32_bf16 v[244:247], a[196:199], v[180:183], v[244:247]\n"
"buffer_load_dwordx4 a[228:231], v0, s[8:11], s6 offen offset:1024 sc1\n"
"s_add_u32 s90, s52, 0x60000\n"
"v_exp_f32 v77, v77\n"
"v_exp_f32 v78, v78\n"
"v_exp_f32 v79, v79\n"
"s_waitcnt vmcnt(9)\n"
"v_mfma_f32_16x16x32_bf16 v[168:171], a[204:207], v[180:183], v[168:171]\n"
"buffer_load_dwordx4 a[236:239], v0, s[8:11], s90 offen offset:0 sc1\n"
"v_cvt_pk_bf16_f32 v240, v240, v241\n"
"v_cvt_pk_bf16_f32 v241, v242, v243\n"
"v_cvt_pk_bf16_f32 v242, v76, v77\n"
"v_cvt_pk_bf16_f32 v243, v78, v79\n"
"v_lshlrev_b32 v156, 16, v104\n"
"v_and_b32 v157, 0xffff0000, v104\n"
"v_mfma_f32_16x16x32_bf16 v[200:203], v[88:91], v[240:243], 0\n"
"v_lshlrev_b32 v158, 16, v105\n"
"v_and_b32 v159, 0xffff0000, v105\n"
"v_mfma_f32_16x16x32_bf16 v[128:131], v[184:187], v[240:243], 0\n"
"v_lshlrev_b32 v104, 16, v106\n"
"v_and_b32 v105, 0xffff0000, v106\n"
"v_mfma_f32_16x16x32_bf16 v[84:87], v[40:43], v[240:243], 0\n"
"buffer_load_dwordx4 a[232:235], v0, s[8:11], s90 offen offset:1024 sc1\n"
"s_add_u32 s6, s53, 0x60800\n"
"v_mul_f32 v149, v149, v231\n"
"v_fma_f32 v132, v132, v231, -v149\n"
"v_fma_f32 v133, v133, v231, -v149\n"
"v_fma_f32 v134, v134, v231, -v149\n"
"v_fma_f32 v135, v135, v231, -v149\n"
"v_fma_f32 v212, v212, v231, -v149\n"
"v_fma_f32 v213, v213, v231, -v149\n"
"v_fma_f32 v214, v214, v231, -v149\n"
"v_fma_f32 v215, v215, v231, -v149\n"
"v_exp_f32 v132, v132\n"
"v_exp_f32 v133, v133\n"
"v_exp_f32 v134, v134\n"
"v_exp_f32 v135, v135\n"
"v_exp_f32 v212, v212\n"
"v_exp_f32 v213, v213\n"
"v_exp_f32 v214, v214\n"
"v_exp_f32 v215, v215\n"
"v_cvt_pk_bf16_f32 v132, v132, v133\n"
"v_cvt_pk_bf16_f32 v133, v134, v135\n"
"v_cvt_pk_bf16_f32 v134, v212, v213\n"
"buffer_load_dwordx4 a[224:227], v0, s[8:11], s6 offen offset:0 sc1\n"
"buffer_load_dwordx4 a[244:247], v0, s[8:11], s6 offen offset:1024 sc1\n"
"s_add_u32 s90, s52, 0x64000\n"
"v_cvt_pk_bf16_f32 v135, v214, v215\n"
"v_lshlrev_b32 v106, 16, v107\n"
"v_and_b32 v107, 0xffff0000, v107\n"
"v_mfma_f32_16x16x32_bf16 v[144:147], v[88:91], v[132:135], 0\n"
"v_lshlrev_b32 v108, 16, v110\n"
"v_and_b32 v109, 0xffff0000, v110\n"
"v_mfma_f32_16x16x32_bf16 v[136:139], v[140:143], v[132:135], 0\n"
"v_lshlrev_b32 v110, 16, v111\n"
"v_and_b32 v111, 0xffff0000, v111\n"
"v_mfma_f32_16x16x32_bf16 v[176:179], v[120:123], v[132:135], 0\n"
"s_waitcnt vmcnt(10)\n"
"buffer_load_dwordx4 a[252:255], v0, s[8:11], s90 offen offset:0 sc1\n"
"v_rcp_f32 v200, v200\n"
"v_mfma_f32_16x16x32_bf16 v[248:251], a[200:203], v[180:183], v[248:251]\n"
"s_waitcnt vmcnt(10)\n"
"v_mul_f32 v128, v128, v200\n"
"v_mul_f32 v129, v129, v200\n"
"v_mul_f32 v130, v130, v200\n"
"v_mul_f32 v131, v131, v200\n"
"v_cvt_pk_bf16_f32 v68, v128, v129\n"
"v_cvt_pk_bf16_f32 v69, v130, v131\n"
"v_mfma_f32_16x16x32_bf16 v[124:127], a[192:195], v[180:183], v[124:127]\n"
"s_waitcnt vmcnt(9)\n"
"v_mul_f32 v84, v84, v200\n"
"v_mul_f32 v85, v85, v200\n"
"v_mul_f32 v86, v86, v200\n"
"v_mul_f32 v87, v87, v200\n"
"v_cvt_pk_bf16_f32 v92, v84, v85\n"
"v_cvt_pk_bf16_f32 v93, v86, v87\n"
"v_mfma_f32_16x16x32_bf16 v[244:247], a[212:215], v[180:183], v[244:247]\n"
"buffer_load_dwordx4 a[248:251], v0, s[8:11], s90 offen offset:1024 sc1\n"
"s_add_u32 s6, s53, 0x64800\n"
"v_rcp_f32 v144, v144\n"
"v_mul_f32 v108, s78, v108\n"
"s_waitcnt vmcnt(9)\n"
"v_mfma_f32_16x16x32_bf16 v[168:171], a[220:223], v[180:183], v[168:171]\n"
"v_mul_f32 v136, v136, v144\n"
"v_mul_f32 v137, v137, v144\n"
"v_mul_f32 v138, v138, v144\n"
"v_mul_f32 v139, v139, v144\n"
"v_cvt_pk_bf16_f32 v70, v136, v137\n"
"v_cvt_pk_bf16_f32 v71, v138, v139\n"
"v_mul_f32 v176, v176, v144\n"
"v_mul_f32 v177, v177, v144\n"
"v_mul_f32 v178, v178, v144\n"
"v_mul_f32 v179, v179, v144\n"
"v_cvt_pk_bf16_f32 v94, v176, v177\n"
"v_cvt_pk_bf16_f32 v95, v178, v179\n"
"v_permlane16_swap_b32 v68, v70\n"
"v_permlane16_swap_b32 v69, v71\n"
"s_waitcnt vmcnt(8)\n"
"v_mfma_f32_16x16x32_bf16 v[248:251], a[216:219], v[180:183], v[248:251]\n"
"buffer_load_dwordx4 a[240:243], v0, s[8:11], s6 offen offset:0 sc1\n"
"buffer_load_dwordx4 a[12:15], v0, s[8:11], s6 offen offset:1024 sc1\n"
"s_add_u32 s90, s52, 0x68000\n"
"v_permlane16_swap_b32 v92, v94\n"
"v_permlane16_swap_b32 v93, v95\n"
"s_add_u32 s1, s65, 0x36000\n"
"buffer_store_dwordx4 v[68:71], v11, s[8:11], s1 offen offset:0 sc1\n"
"s_waitcnt vmcnt(10)\n"
"v_mfma_f32_16x16x32_bf16 v[124:127], a[208:211], v[180:183], v[124:127]\n"
"s_add_u32 s1, s66, 0x36000\n"
"v_mul_f32 v109, s78, v109\n"
"v_mul_f32 v110, s78, v110\n"
"v_mul_f32 v111, s78, v111\n"
"v_add_u32 v16, s62, v0\n"
"s_xor_b32 s62, s62, 0xd800\n"
"v_add_u32 v17, s62, v0\n"
"buffer_store_dwordx2 v[92:93], v12, s[8:11], s1 offen offset:0 sc1\n"
"s_waitcnt vmcnt(10)\n"
"v_mfma_f32_16x16x32_bf16 v[244:247], a[228:231], v[180:183], v[244:247]\n"
"buffer_load_dwordx4 v[116:119], v0, s[8:11], s2 offen offset:0 sc1\n"
"s_add_u32 s3, s58, 0x2c000\n"
"s_waitcnt vmcnt(10)\n"
"v_mfma_f32_16x16x32_bf16 v[168:171], a[236:239], v[180:183], v[168:171]\n"
"buffer_load_dwordx4 v[32:35], v0, s[8:11], s3 offen offset:0 sc1\n"
"s_waitcnt vmcnt(10)\n"
"v_mfma_f32_16x16x32_bf16 v[248:251], a[232:235], v[180:183], v[248:251]\n"
"buffer_load_dwordx4 v[36:39], v0, s[8:11], s2 offen offset:1024 sc1\n"
"s_waitcnt vmcnt(10)\n"
"v_mfma_f32_16x16x32_bf16 v[124:127], a[224:227], v[180:183], v[124:127]\n"
"buffer_load_dwordx4 v[220:223], v0, s[8:11], s3 offen offset:1024 sc1\n"
"s_waitcnt vmcnt(10)\n"
"v_mfma_f32_16x16x32_bf16 v[244:247], a[244:247], v[180:183], v[244:247]\n"
"buffer_load_dwordx4 v[208:211], v0, s[8:11], s2 offen offset:2048 sc1\n"
"s_waitcnt vmcnt(10)\n"
"v_mfma_f32_16x16x32_bf16 v[168:171], a[252:255], v[180:183], v[168:171]\n"
"buffer_load_dwordx4 v[236:239], v0, s[8:11], s3 offen offset:2048 sc1\n"
"s_waitcnt vmcnt(10)\n"
"v_mfma_f32_16x16x32_bf16 v[248:251], a[248:251], v[180:183], v[248:251]\n"
"buffer_load_dwordx4 v[80:83], v0, s[8:11], s2 offen offset:3072 sc1\n"
"s_waitcnt vmcnt(10)\n"
"v_mfma_f32_16x16x32_bf16 v[124:127], a[240:243], v[180:183], v[124:127]\n"
"s_waitcnt vmcnt(9)\n"
"v_mfma_f32_16x16x32_bf16 v[244:247], a[12:15], v[180:183], v[244:247]\n"
"buffer_load_dwordx4 v[28:31], v0, s[8:11], s3 offen offset:3072 sc1\n"
"s_add_u32 s2, s57, 0x2d000\n"
"buffer_load_dwordx4 v[72:75], v0, s[8:11], s2 offen offset:0 sc1\n"
"s_add_u32 s3, s58, 0x2d000\n"
"buffer_load_dwordx4 v[216:219], v0, s[8:11], s3 offen offset:0 sc1\n"
"buffer_load_dwordx4 a[0:3], v0, s[8:11], s90 offen offset:0 sc1\n"
"buffer_load_dwordx4 a[8:11], v0, s[8:11], s90 offen offset:1024 sc1\n"
"s_add_u32 s6, s53, 0x68800\n"
"v_mov_b32 v44, -1\n"
"v_mov_b32 v45, -1\n"
"v_mov_b32 v46, -1\n"
"v_mov_b32 v47, -1\n"
"s_add_u32 s91, s74, 0x16000\n"
"s_waitcnt vmcnt(11)\n"
"v_max3_u32 v48, v116, v117, v118\n"
"buffer_store_dwordx4 v[44:47], v0, s[8:11], s91 offen offset:0 sc1\n"
"s_add_u32 s91, s74, 0x1e000\n"
"s_waitcnt vmcnt(11)\n"
"v_max3_u32 v48, v48, v119, v32\n"
"v_max3_u32 v48, v48, v33, v34\n"
"v_max_u32 v48, v48, v35\n"
"v_cmp_eq_u32 vcc, -1, v48\n"
"buffer_store_dwordx4 v[44:47], v0, s[8:11], s91 offen offset:0 sc1\n"
"s_add_u32 s91, s25, 0x26000\n"
"s_cbranch_vccnz Ls4_%=\n"
"Lb4_%=:\n"
"v_mfma_f32_16x16x32_bf16 v[60:63], a[68:71], v[116:119], v[60:63]\n"
"v_mfma_f32_16x16x32_bf16 v[52:55], a[76:79], v[116:119], v[52:55]\n"
"s_waitcnt vmcnt(11)\n"
"v_max3_u32 v164, v36, v37, v38\n"
"v_mfma_f32_16x16x32_bf16 v[64:67], a[72:75], v[116:119], 0\n"
"v_mfma_f32_16x16x32_bf16 v[56:59], a[64:67], v[116:119], 0\n"
"buffer_store_dwordx4 v[44:47], v0, s[8:11], s91 offen offset:0 sc1\n"
"v_mfma_f32_16x16x32_bf16 v[232:235], a[84:87], v[116:119], v[232:235]\n"
"s_add_u32 s79, s79, 0x100\n"
"s_waitcnt vmcnt(11)\n"
"v_max3_u32 v164, v164, v39, v220\n"
"v_mfma_f32_16x16x32_bf16 v[204:207], a[68:71], v[32:35], 0\n"
"v_max3_u32 v164, v164, v221, v222\n"
"v_max_u32 v164, v164, v223\n"
"v_mfma_f32_16x16x32_bf16 v[96:99], a[76:79], v[32:35], 0\n"
"v_cmp_eq_u32 vcc, -1, v164\n"
"v_mfma_f32_16x16x32_bf16 v[100:103], a[72:75], v[32:35], 0\n"
"v_mfma_f32_16x16x32_bf16 v[224:227], a[64:67], v[32:35], 0\n"
"s_cbranch_vccnz Ls5_%=\n"
"Lb5_%=:\n"
"v_mfma_f32_16x16x32_bf16 v[188:191], a[84:87], v[32:35], 0\n"
"buffer_load_dwordx4 v[172:175], v0, s[8:11], s4 offen offset:0 sc1\n"
"v_mfma_f32_16x16x32_bf16 v[60:63], a[92:95], v[36:39], v[60:63]\n"
"s_add_u32 s5, s58, 0x36000\n"
"s_waitcnt vmcnt(11)\n"
"v_max3_u32 v112, v208, v209, v210\n"
"v_mfma_f32_16x16x32_bf16 v[52:55], a[88:91], v[36:39], v[52:55]\n"
"v_mfma_f32_16x16x32_bf16 v[64:67], a[80:83], v[36:39], v[64:67]\n"
"buffer_load_dwordx4 v[152:155], v0, s[8:11], s5 offen offset:0 sc1\n"
"v_mfma_f32_16x16x32_bf16 v[56:59], a[100:103], v[36:39], v[56:59]\n"
"v_mfma_f32_16x16x32_bf16 v[232:235], a[108:111], v[36:39], v[232:235]\n"
"s_waitcnt vmcnt(11)\n"
"v_max3_u32 v112, v112, v211, v236\n"
"v_max3_u32 v112, v112, v237, v238\n"
"v_mfma_f32_16x16x32_bf16 v[204:207], a[92:95], v[220:223], v[204:207]\n"
"v_max_u32 v112, v112, v239\n"
"v_cmp_eq_u32 vcc, -1, v112\n"
"v_mfma_f32_16x16x32_bf16 v[96:99], a[88:91], v[220:223], v[96:99]\n"
"v_mfma_f32_16x16x32_bf16 v[100:103], a[80:83], v[220:223], v[100:103]\n"
"s_cbranch_vccnz Ls6_%=\n"
"Lb6_%=:\n"
"v_mfma_f32_16x16x32_bf16 v[224:227], a[100:103], v[220:223], v[224:227]\n"
"buffer_load_dwordx4 v[160:163], v0, s[8:11], s4 offen offset:1024 sc1\n"
"v_mfma_f32_16x16x32_bf16 v[188:191], a[108:111], v[220:223], v[188:191]\n"
"s_waitcnt vmcnt(11)\n"
"v_max3_u32 v196, v80, v81, v82\n"
"v_mfma_f32_16x16x32_bf16 v[60:63], a[104:107], v[208:211], v[60:63]\n"
"v_mfma_f32_16x16x32_bf16 v[52:55], a[96:99], v[208:211], v[52:55]\n"
"buffer_load_dwordx4 v[192:195], v0, s[8:11], s5 offen offset:1024 sc1\n"
"v_mfma_f32_16x16x32_bf16 v[64:67], a[116:119], v[208:211], v[64:67]\n"
"v_mfma_f32_16x16x32_bf16 v[56:59], a[124:127], v[208:211], v[56:59]\n"
"s_waitcnt vmcnt(11)\n"
"v_max3_u32 v196, v196, v83, v28\n"
"v_max3_u32 v196, v196, v29, v30\n"
"v_mfma_f32_16x16x32_bf16 v[232:235], a[120:123], v[208:211], v[232:235]\n"
"v_max_u32 v196, v196, v31\n"
"v_cmp_eq_u32 vcc, -1, v196\n"
"v_mfma_f32_16x16x32_bf16 v[204:207], a[104:107], v[236:239], v[204:207]\n"
"v_mfma_f32_16x16x32_bf16 v[96:99], a[96:99], v[236:239], v[96:99]\n"
"s_cbranch_vccnz Ls7_%=\n"
"Lb7_%=:\n"
"v_mfma_f32_16x16x32_bf16 v[100:103], a[116:119], v[236:239], v[100:103]\n"
"buffer_load_dwordx4 v[76:79], v0, s[8:11], s4 offen offset:2048 sc1\n"
"v_mfma_f32_16x16x32_bf16 v[224:227], a[124:127], v[236:239], v[224:227]\n"
"s_waitcnt vmcnt(11)\n"
"v_max3_u32 v184, v72, v73, v74\n"
"v_mfma_f32_16x16x32_bf16 v[188:191], a[120:123], v[236:239], v[188:191]\n"
"v_mfma_f32_16x16x32_bf16 v[60:63], a[112:115], v[80:83], v[60:63]\n"
"v_mfma_f32_16x16x32_bf16 v[52:55], a[132:135], v[80:83], v[52:55]\n"
"buffer_load_dwordx4 v[40:43], v0, s[8:11], s5 offen offset:2048 sc1\n"
"v_mfma_f32_16x16x32_bf16 v[64:67], a[140:143], v[80:83], v[64:67]\n"
"s_waitcnt vmcnt(11)\n"
"v_max3_u32 v184, v184, v75, v216\n"
"v_max3_u32 v184, v184, v217, v218\n"
"v_mfma_f32_16x16x32_bf16 v[56:59], a[136:139], v[80:83], v[56:59]\n"
"v_max_u32 v184, v184, v219\n"
"v_cmp_eq_u32 vcc, -1, v184\n"
"v_mfma_f32_16x16x32_bf16 v[232:235], a[128:131], v[80:83], v[232:235]\n"
"v_mfma_f32_16x16x32_bf16 v[204:207], a[112:115], v[28:31], v[204:207]\n"
"s_cbranch_vccnz Ls8_%=\n"
"Lb8_%=:\n"
"v_mfma_f32_16x16x32_bf16 v[96:99], a[132:135], v[28:31], v[96:99]\n"
"buffer_load_dwordx4 v[240:243], v0, s[8:11], s4 offen offset:3072 sc1\n"
"v_mfma_f32_16x16x32_bf16 v[100:103], a[140:143], v[28:31], v[100:103]\n"
"v_mfma_f32_16x16x32_bf16 v[224:227], a[136:139], v[28:31], v[224:227]\n"
"v_mfma_f32_16x16x32_bf16 v[188:191], a[128:131], v[28:31], v[188:191]\n"
"v_mfma_f32_16x16x32_bf16 v[64:67], a[152:155], v[72:75], v[64:67]\n"
"buffer_load_dwordx4 v[228:231], v0, s[8:11], s5 offen offset:3072 sc1\n"
"v_mfma_f32_16x16x32_bf16 v[56:59], a[144:147], v[72:75], v[56:59]\n"
"s_add_u32 s4, s57, 0x37000\n"
"v_mfma_f32_16x16x32_bf16 v[232:235], a[164:167], v[72:75], v[232:235]\n"
"v_mfma_f32_16x16x32_bf16 v[204:207], a[148:151], v[216:219], v[204:207]\n"
"v_mfma_f32_16x16x32_bf16 v[96:99], a[156:159], v[216:219], v[96:99]\n"
"v_mfma_f32_16x16x32_bf16 v[100:103], a[152:155], v[216:219], v[100:103]\n"
"buffer_load_dwordx4 v[148:151], v0, s[8:11], s4 offen offset:0 sc1\n"
"ds_write_b128 v2, v[64:67] offset:2048\n"
"v_mfma_f32_16x16x32_bf16 v[224:227], a[144:147], v[216:219], v[224:227]\n"
"s_add_u32 s5, s58, 0x37000\n"
"ds_write_b128 v2, v[56:59] offset:3072\n"
"v_mfma_f32_16x16x32_bf16 v[188:191], a[164:167], v[216:219], v[188:191]\n"
"buffer_load_dwordx4 v[212:215], v0, s[8:11], s5 offen offset:0 sc1\n"
"v_mfma_f32_16x16x32_bf16 v[60:63], a[148:151], v[72:75], v[60:63]\n"
"v_mfma_f32_16x16x32_bf16 v[52:55], a[156:159], v[72:75], v[52:55]\n"
"s_waitcnt vmcnt(14)\n"
"ds_write_b128 v2, v[232:235] offset:6144\n"
"v_mfma_f32_16x16x32_bf16 v[168:171], a[0:3], v[180:183], v[168:171]\n"
"s_waitcnt vmcnt(13)\n"
"v_mfma_f32_16x16x32_bf16 v[248:251], a[8:11], v[180:183], v[248:251]\n"
"ds_write_b128 v2, v[204:207] offset:0\n"
"buffer_load_dwordx4 a[4:7], v0, s[8:11], s6 offen offset:0 sc1\n"
"ds_write_b128 v2, v[96:99] offset:1024\n"
"s_waitcnt vmcnt(11)\n"
"buffer_store_dwordx2 v[254:255], v15, s[8:11], s79 offen sc1\n"
"ds_write_b128 v2, v[100:103] offset:4096\n"
"s_waitcnt vmcnt(11)\n"
"v_max3_u32 v88, v172, v173, v174\n"
"buffer_load_dwordx4 a[28:31], v0, s[8:11], s6 offen offset:1024 sc1\n"
"s_add_u32 s90, s52, 0x6c000\n"
"ds_write_b128 v2, v[224:227] offset:5120\n"
"s_waitcnt vmcnt(11) lgkmcnt(5)\n"
"v_max3_u32 v88, v88, v175, v152\n"
"v_max3_u32 v88, v88, v153, v154\n"
"v_max_u32 v88, v88, v155\n"
"v_cmp_eq_u32 vcc, -1, v88\n"
"buffer_load_dwordx4 a[16:19], v0, s[8:11], s90 offen offset:0 sc1\n"
"ds_write_b128 v2, v[188:191] offset:7168\n"
"s_cbranch_vccnz Ls32_%=\n"
"Lb32_%=:\n"
"v_mfma_f32_16x16x32_bf16 v[156:159], a[68:71], v[172:175], v[156:159]\n"
"v_mfma_f32_16x16x32_bf16 v[104:107], a[76:79], v[172:175], v[104:107]\n"
"s_waitcnt vmcnt(11) lgkmcnt(5)\n"
"v_max3_u32 v140, v160, v161, v162\n"
"v_mfma_f32_16x16x32_bf16 v[120:123], a[72:75], v[172:175], 0\n"
"buffer_load_dwordx4 a[24:27], v0, s[8:11], s90 offen offset:1024 sc1\n"
"v_mfma_f32_16x16x32_bf16 v[132:135], a[64:67], v[172:175], 0\n"
"s_add_u32 s6, s53, 0x6c800\n"
"v_mfma_f32_16x16x32_bf16 v[108:111], a[84:87], v[172:175], v[108:111]\n"
"v_mfma_f32_16x16x32_bf16 v[128:131], a[68:71], v[152:155], 0\n"
"s_waitcnt vmcnt(11) lgkmcnt(3)\n"
"v_max3_u32 v140, v140, v163, v192\n"
"v_max3_u32 v140, v140, v193, v194\n"
"v_mfma_f32_16x16x32_bf16 v[200:203], a[76:79], v[152:155], 0\n"
"buffer_load_dwordx4 a[20:23], v0, s[8:11], s6 offen offset:0 sc1\n"
"v_mfma_f32_16x16x32_bf16 v[84:87], a[72:75], v[152:155], 0\n"
"v_max_u32 v140, v140, v195\n"
"v_cmp_eq_u32 vcc, -1, v140\n"
"v_mfma_f32_16x16x32_bf16 v[136:139], a[64:67], v[152:155], 0\n"
"s_waitcnt lgkmcnt(0)\n"
"s_barrier\n"
"ds_read_b128 v[144:147], v8 offset:0\n"
"ds_read_b128 v[176:179], v9 offset:2048\n"
"ds_read_b128 v[68:71], v10 offset:4096\n"
"ds_read_b128 v[92:95], v8 offset:1024\n"
"ds_read_b128 v[48:51], v9 offset:3072\n"
"ds_read_b128 v[44:47], v10 offset:5120\n"
"v_mfma_f32_16x16x32_bf16 v[116:119], a[84:87], v[152:155], 0\n"
"buffer_load_dwordx4 a[44:47], v0, s[8:11], s6 offen offset:1024 sc1\n"
"s_add_u32 s90, s52, 0x70000\n"
"s_cbranch_vccnz Ls33_%=\n"
"Lb33_%=:\n"
"v_mfma_f32_16x16x32_bf16 v[156:159], a[92:95], v[160:163], v[156:159]\n"
"buffer_load_dwordx4 a[32:35], v0, s[8:11], s90 offen offset:0 sc1\n"
"v_mfma_f32_16x16x32_bf16 v[104:107], a[88:91], v[160:163], v[104:107]\n"
"s_waitcnt vmcnt(13)\n"
"v_max3_u32 v164, v76, v77, v78\n"
"v_mfma_f32_16x16x32_bf16 v[120:123], a[80:83], v[160:163], v[120:123]\n"
"s_waitcnt vmcnt(12)\n"
"v_max3_u32 v164, v164, v79, v40\n"
"v_max3_u32 v164, v164, v41, v42\n"
"v_mfma_f32_16x16x32_bf16 v[132:135], a[100:103], v[160:163], v[132:135]\n"
"v_max_u32 v164, v164, v43\n"
"v_cmp_eq_u32 vcc, -1, v164\n"
"s_waitcnt lgkmcnt(5)\n"
"v_add_f32 v60, v60, v144\n"
"v_add_f32 v61, v61, v145\n"
"v_add_f32 v62, v62, v146\n"
"v_mfma_f32_16x16x32_bf16 v[108:111], a[108:111], v[160:163], v[108:111]\n"
"s_cbranch_vccnz Ls34_%=\n"
"Lb34_%=:\n"
"v_add_f32 v63, v63, v147\n"
"s_waitcnt lgkmcnt(4)\n"
"v_add_f32 v60, v60, v176\n"
"v_add_f32 v61, v61, v177\n"
"v_add_f32 v62, v62, v178\n"
"ds_read_b128 v[32:35], v8 offset:7168\n"
"buffer_load_dwordx4 a[40:43], v0, s[8:11], s90 offen offset:1024 sc1\n"
"v_mfma_f32_16x16x32_bf16 v[128:131], a[92:95], v[192:195], v[128:131]\n"
"s_add_u32 s6, s53, 0x70800\n"
"v_add_f32 v63, v63, v179\n"
"s_waitcnt lgkmcnt(4)\n"
"v_add_f32 v60, v60, v68\n"
"v_add_f32 v61, v61, v69\n"
"v_add_f32 v62, v62, v70\n"
"v_add_f32 v63, v63, v71\n"
"ds_read_b128 v[36:39], v9 offset:6144\n"
"ds_read_b128 v[112:115], v10 offset:7168\n"
"v_mfma_f32_16x16x32_bf16 v[200:203], a[88:91], v[192:195], v[200:203]\n"
"buffer_load_dwordx4 a[36:39], v0, s[8:11], s6 offen offset:0 sc1\n"
"v_cvt_pk_bf16_f32 v220, v60, v61\n"
"v_cvt_pk_bf16_f32 v221, v62, v63\n"
"s_waitcnt lgkmcnt(5)\n"
"v_add_f32 v52, v52, v92\n"
"v_mfma_f32_16x16x32_bf16 v[84:87], a[80:83], v[192:195], v[84:87]\n"
"buffer_load_dwordx4 a[60:63], v0, s[8:11], s6 offen offset:1024 sc1\n"
"v_add_f32 v53, v53, v93\n"
"v_add_f32 v54, v54, v94\n"
"v_add_f32 v55, v55, v95\n"
"v_mfma_f32_16x16x32_bf16 v[136:139], a[100:103], v[192:195], v[136:139]\n"
"s_add_u32 s90, s52, 0x74000\n"
"s_waitcnt vmcnt(14) lgkmcnt(3)\n"
"v_max3_u32 v208, v240, v241, v242\n"
"v_add_f32 v52, v52, v48\n"
"v_add_f32 v53, v53, v49\n"
"v_add_f32 v54, v54, v50\n"
"v_add_f32 v55, v55, v51\n"
"v_mfma_f32_16x16x32_bf16 v[116:119], a[108:111], v[192:195], v[116:119]\n"
"buffer_load_dwordx4 a[48:51], v0, s[8:11], s90 offen offset:0 sc1\n"
"v_add_f32 v52, v52, v44\n"
"v_add_f32 v53, v53, v45\n"
"v_add_f32 v54, v54, v46\n"
"v_add_f32 v55, v55, v47\n"
"v_mfma_f32_16x16x32_bf16 v[156:159], a[104:107], v[76:79], v[156:159]\n"
"s_waitcnt vmcnt(14)\n"
"v_max3_u32 v208, v208, v243, v228\n"
"v_max3_u32 v208, v208, v229, v230\n"
"v_cvt_pk_bf16_f32 v222, v52, v53\n"
"v_cvt_pk_bf16_f32 v223, v54, v55\n"
"s_add_u32 s0, s67, 0x0\n"
"v_mfma_f32_16x16x32_bf16 v[104:107], a[96:99], v[76:79], v[104:107]\n"
"buffer_load_dwordx4 a[56:59], v0, s[8:11], s90 offen offset:1024 sc1\n"
"v_mfma_f32_16x16x32_bf16 v[120:123], a[116:119], v[76:79], v[120:123]\n"
"s_add_u32 s6, s53, 0x74800\n"
"v_max_u32 v208, v208, v231\n"
"v_cmp_eq_u32 vcc, -1, v208\n"
"v_mfma_f32_16x16x32_bf16 v[132:135], a[124:127], v[76:79], v[132:135]\n"
"v_mfma_f32_16x16x32_bf16 v[108:111], a[120:123], v[76:79], v[108:111]\n"
"s_cbranch_vccnz Ls35_%=\n"
"Lb35_%=:\n"
"buffer_store_dwordx4 v[220:223], v0, s[8:11], s0 offen offset:0 sc1\n"
"v_mfma_f32_16x16x32_bf16 v[128:131], a[104:107], v[40:43], v[128:131]\n"
"s_waitcnt vmcnt(15) lgkmcnt(0)\n"
"v_max3_u32 v196, v148, v149, v150\n"
"v_add_f32 v232, v232, v32\n"
"v_add_f32 v233, v233, v33\n"
"v_add_f32 v234, v234, v34\n"
"v_add_f32 v235, v235, v35\n"
"v_add_f32 v232, v232, v36\n"
"v_mfma_f32_16x16x32_bf16 v[200:203], a[96:99], v[40:43], v[200:203]\n"
"s_waitcnt vmcnt(14)\n"
"v_max3_u32 v196, v196, v151, v212\n"
"v_max3_u32 v196, v196, v213, v214\n"
"v_add_f32 v233, v233, v37\n"
"v_add_f32 v234, v234, v38\n"
"v_add_f32 v235, v235, v39\n"
"v_add_f32 v232, v232, v112\n"
"v_mfma_f32_16x16x32_bf16 v[84:87], a[116:119], v[40:43], v[84:87]\n"
"v_max_u32 v196, v196, v215\n"
"v_cmp_eq_u32 vcc, -1, v196\n"
"v_add_f32 v233, v233, v113\n"
"v_add_f32 v234, v234, v114\n"
"v_add_f32 v235, v235, v115\n"
"v_cvt_pk_bf16_f32 v236, v232, v233\n"
"v_mfma_f32_16x16x32_bf16 v[136:139], a[124:127], v[40:43], v[136:139]\n"
"s_cbranch_vccnz Ls36_%=\n"
"Lb36_%=:\n"
"v_cvt_pk_bf16_f32 v237, v234, v235\n"
"s_add_u32 s0, s68, 0x0\n"
"v_mfma_f32_16x16x32_bf16 v[116:119], a[120:123], v[40:43], v[116:119]\n"
"buffer_load_dwordx4 a[52:55], v0, s[8:11], s6 offen offset:0 sc1\n"
"buffer_store_dwordx2 v[236:237], v13, s[8:11], s0 offen offset:0 sc1\n"
"v_add_f32 v80, v60, v61\n"
"v_mfma_f32_16x16x32_bf16 v[156:159], a[112:115], v[240:243], v[156:159]\n"
"v_mul_f32 v81, v60, v60\n"
"v_add_f32 v82, v232, v233\n"
"v_mul_f32 v83, v232, v232\n"
"v_fmac_f32 v81, v61, v61\n"
"v_fmac_f32 v83, v233, v233\n"
"v_add_f32 v80, v80, v62\n"
"v_mfma_f32_16x16x32_bf16 v[104:107], a[132:135], v[240:243], v[104:107]\n"
"v_fmac_f32 v81, v62, v62\n"
"v_add_f32 v82, v82, v234\n"
"v_fmac_f32 v83, v234, v234\n"
"v_add_f32 v80, v80, v63\n"
"v_fmac_f32 v81, v63, v63\n"
"v_add_f32 v82, v82, v235\n"
"v_mfma_f32_16x16x32_bf16 v[120:123], a[140:143], v[240:243], v[120:123]\n"
"buffer_load_dwordx4 a[68:71], v0, s[8:11], s6 offen offset:1024 sc1\n"
"v_fmac_f32 v83, v235, v235\n"
"v_add_f32 v80, v80, v52\n"
"v_fmac_f32 v81, v52, v52\n"
"v_add_f32 v80, v80, v53\n"
"v_mfma_f32_16x16x32_bf16 v[132:135], a[136:139], v[240:243], v[132:135]\n"
"s_add_u32 s90, s52, 0x78000\n"
"v_fmac_f32 v81, v53, v53\n"
"v_add_f32 v80, v80, v54\n"
"v_fmac_f32 v81, v54, v54\n"
"v_add_f32 v80, v80, v55\n"
"v_fmac_f32 v81, v55, v55\n"
"v_fmac_f32 v80, s78, v82\n"
"v_mfma_f32_16x16x32_bf16 v[108:111], a[128:131], v[240:243], v[108:111]\n"
"buffer_load_dwordx4 a[76:79], v0, s[8:11], s90 offen offset:0 sc1\n"
"v_fmac_f32 v81, s78, v83\n"
"v_mfma_f32_16x16x32_bf16 v[128:131], a[112:115], v[228:231], v[128:131]\n"
"v_mfma_f32_16x16x32_bf16 v[200:203], a[132:135], v[228:231], v[200:203]\n"
"v_permlane32_swap_b32 v80, v81\n"
"v_add_f32 v80, v80, v81\n"
"v_mov_b32 v81, v80\n"
"v_mfma_f32_16x16x32_bf16 v[84:87], a[140:143], v[228:231], v[84:87]\n"
"buffer_load_dwordx4 a[72:75], v0, s[8:11], s90 offen offset:1024 sc1\n"
"v_permlane16_swap_b32 v80, v81\n"
"v_add_f32 v80, v80, v81\n"
"s_add_u32 s0, s71, 0xa000\n"
"v_mfma_f32_16x16x32_bf16 v[136:139], a[136:139], v[228:231], v[136:139]\n"
"s_add_u32 s6, s53, 0x78800\n"
"buffer_store_dword v80, v14, s[8:11], s0 offen offset:0 sc1\n"
"ds_read_b128 v[184:187], v20 offset:768\n"
"s_add_u32 s0, s63, 0x0\n"
"v_mfma_f32_16x16x32_bf16 v[116:119], a[128:131], v[228:231], v[116:119]\n"
"v_mfma_f32_16x16x32_bf16 v[120:123], a[152:155], v[148:151], v[120:123]\n"
"v_mfma_f32_16x16x32_bf16 v[132:135], a[144:147], v[148:151], v[132:135]\n"
"v_mfma_f32_16x16x32_bf16 v[108:111], a[164:167], v[148:151], v[108:111]\n"
"buffer_load_dwordx4 a[64:67], v0, s[8:11], s6 offen offset:0 sc1\n"
"v_mfma_f32_16x16x32_bf16 v[128:131], a[148:151], v[212:215], v[128:131]\n"
"v_mfma_f32_16x16x32_bf16 v[200:203], a[156:159], v[212:215], v[200:203]\n"
"v_mfma_f32_16x16x32_bf16 v[84:87], a[152:155], v[212:215], v[84:87]\n"
"v_mfma_f32_16x16x32_bf16 v[136:139], a[144:147], v[212:215], v[136:139]\n"
"buffer_load_dwordx4 a[84:87], v0, s[8:11], s6 offen offset:1024 sc1\n"
"ds_write_b128 v2, v[120:123] offset:12288\n"
"v_mfma_f32_16x16x32_bf16 v[116:119], a[164:167], v[212:215], v[116:119]\n"
"s_add_u32 s90, s52, 0x7c000\n"
"ds_write_b128 v2, v[132:135] offset:13312\n"
"v_mfma_f32_16x16x32_bf16 v[156:159], a[148:151], v[148:151], v[156:159]\n"
"buffer_load_dwordx4 a[92:95], v0, s[8:11], s90 offen offset:0 sc1\n"
"v_mfma_f32_16x16x32_bf16 v[104:107], a[156:159], v[148:151], v[104:107]\n"
"s_waitcnt vmcnt(22)\n"
"s_waitcnt lgkmcnt(2)\n"
"v_add_f32 v60, v60, v184\n"
"v_mfma_f32_16x16x32_bf16 v[124:127], a[4:7], v[180:183], v[124:127]\n"
"s_waitcnt vmcnt(20)\n"
"ds_write_b128 v2, v[108:111] offset:16384\n"
"v_mfma_f32_16x16x32_bf16 v[244:247], a[28:31], v[180:183], v[244:247]\n"
"s_waitcnt vmcnt(19)\n"
"v_add_f32 v61, v61, v185\n"
"v_add_f32 v62, v62, v186\n"
"v_mfma_f32_16x16x32_bf16 v[168:171], a[16:19], v[180:183], v[168:171]\n"
"s_waitcnt vmcnt(18)\n"
"buffer_load_dwordx4 a[88:91], v0, s[8:11], s90 offen offset:1024 sc1\n"
"v_mfma_f32_16x16x32_bf16 v[248:251], a[24:27], v[180:183], v[248:251]\n"
"s_waitcnt vmcnt(18)\n"
"ds_write_b128 v2, v[128:131] offset:10240\n"
"v_mfma_f32_16x16x32_bf16 v[124:127], a[20:23], v[180:183], v[124:127]\n"
"s_waitcnt vmcnt(17)\n"
"s_add_u32 s6, s53, 0x7c800\n"
"v_add_f32 v63, v63, v187\n"
"ds_read_b128 v[28:31], v20 offset:832\n"
"v_mfma_f32_16x16x32_bf16 v[244:247], a[44:47], v[180:183], v[244:247]\n"
"s_waitcnt vmcnt(16)\n"
"buffer_load_dwordx4 a[80:83], v0, s[8:11], s6 offen offset:0 sc1\n"
"v_mfma_f32_16x16x32_bf16 v[168:171], a[32:35], v[180:183], v[168:171]\n"
"s_waitcnt vmcnt(16)\n"
"ds_write_b128 v2, v[200:203] offset:11264\n"
"v_mfma_f32_16x16x32_bf16 v[248:251], a[40:43], v[180:183], v[248:251]\n"
"s_waitcnt vmcnt(15)\n"
"ds_write_b128 v2, v[84:87] offset:14336\n"
"v_mfma_f32_16x16x32_bf16 v[124:127], a[36:39], v[180:183], v[124:127]\n"
"s_waitcnt vmcnt(14) lgkmcnt(6)\n"
"buffer_load_dwordx4 a[100:103], v0, s[8:11], s6 offen offset:1024 sc1\n"
"v_mfma_f32_16x16x32_bf16 v[244:247], a[60:63], v[180:183], v[244:247]\n"
"s_waitcnt vmcnt(14)\n"
"s_add_u32 s90, s52, 0x80000\n"
"v_mfma_f32_16x16x32_bf16 v[168:171], a[48:51], v[180:183], v[168:171]\n"
"s_waitcnt vmcnt(13) lgkmcnt(5)\n"
"ds_write_b128 v2, v[136:139] offset:15360\n"
"v_mfma_f32_16x16x32_bf16 v[248:251], a[56:59], v[180:183], v[248:251]\n"
"s_waitcnt vmcnt(11)\n"
"buffer_load_dwordx4 a[108:111], v0, s[8:11], s90 offen offset:0 sc1\n"
"v_mfma_f32_16x16x32_bf16 v[124:127], a[52:55], v[180:183], v[124:127]\n"
"s_waitcnt lgkmcnt(3)\n"
"v_add_f32 v52, v52, v28\n"
"v_add_f32 v53, v53, v29\n"
"v_add_f32 v54, v54, v30\n"
"v_add_f32 v55, v55, v31\n"
"ds_write_b128 v2, v[116:119] offset:17408\n"
"ds_read_b128 v[64:67], v20 offset:896\n"
"buffer_load_dwordx4 a[104:107], v0, s[8:11], s90 offen offset:1024 sc1\n"
"s_add_u32 s6, s53, 0x80800\n"
"s_waitcnt vmcnt(11)\n"
"v_mfma_f32_16x16x32_bf16 v[244:247], a[68:71], v[180:183], v[244:247]\n"
"s_waitcnt vmcnt(10)\n"
"v_mfma_f32_16x16x32_bf16 v[168:171], a[76:79], v[180:183], v[168:171]\n"
"buffer_load_dwordx4 a[96:99], v0, s[8:11], s6 offen offset:0 sc1\n"
"s_waitcnt vmcnt(10) lgkmcnt(4)\n"
"v_mfma_f32_16x16x32_bf16 v[248:251], a[72:75], v[180:183], v[248:251]\n"
"s_waitcnt lgkmcnt(1)\n"
"s_barrier\n"
"buffer_load_dwordx4 a[116:119], v0, s[8:11], s6 offen offset:1024 sc1\n"
"ds_read_b128 v[56:59], v8 offset:10240\n"
"ds_read_b128 v[216:219], v9 offset:12288\n"
"ds_read_b128 v[72:75], v10 offset:14336\n"
"ds_read_b128 v[204:207], v8 offset:11264\n"
"ds_read_b128 v[96:99], v9 offset:13312\n"
"ds_read_b128 v[252:255], v10 offset:15360\n"
"s_add_u32 s90, s52, 0x84000\n"
"buffer_load_dwordx4 a[124:127], v0, s[8:11], s90 offen offset:0 sc1\n"
"s_waitcnt lgkmcnt(6)\n"
"v_fma_f32 v232, v232, s78, v64\n"
"v_fma_f32 v233, v233, s78, v65\n"
"v_fma_f32 v234, v234, s78, v66\n"
"v_fma_f32 v235, v235, s78, v67\n"
"buffer_load_dwordx4 a[120:123], v0, s[8:11], s90 offen offset:1024 sc1\n"
"s_waitcnt vmcnt(11)\n"
"v_mfma_f32_16x16x32_bf16 v[124:127], a[64:67], v[180:183], v[124:127]\n"
"s_waitcnt vmcnt(10)\n"
"s_add_u32 s6, s53, 0x84800\n"
"v_mfma_f32_16x16x32_bf16 v[244:247], a[84:87], v[180:183], v[244:247]\n"
"s_waitcnt lgkmcnt(5)\n"
"v_add_f32 v156, v156, v56\n"
"buffer_load_dwordx4 v[100:103], v0, s[8:11], s0 offen offset:0 sc1\n"
"v_add_f32 v157, v157, v57\n"
"s_waitcnt vmcnt(10)\n"
"v_mfma_f32_16x16x32_bf16 v[168:171], a[92:95], v[180:183], v[168:171]\n"
"v_add_f32 v158, v158, v58\n"
"v_add_f32 v159, v159, v59\n"
"s_waitcnt lgkmcnt(4)\n"
"v_add_f32 v156, v156, v216\n"
"v_add_f32 v157, v157, v217\n"
"v_add_f32 v158, v158, v218\n"
"v_add_f32 v159, v159, v219\n"
"s_waitcnt lgkmcnt(3)\n"
"v_add_f32 v156, v156, v72\n"
"buffer_load_dwordx4 v[224:227], v0, s[8:11], s0 offen offset:1024 sc1\n"
"ds_read_b128 v[188:191], v8 offset:17408\n"
"ds_read_b128 v[88:91], v9 offset:16384\n"
"s_waitcnt vmcnt(10)\n"
"v_mfma_f32_16x16x32_bf16 v[248:251], a[88:91], v[180:183], v[248:251]\n"
"v_add_f32 v157, v157, v73\n"
"v_add_f32 v158, v158, v74\n"
"v_add_f32 v159, v159, v75\n"
"v_cvt_pk_bf16_f32 v172, v156, v157\n"
"v_cvt_pk_bf16_f32 v173, v158, v159\n"
"s_waitcnt lgkmcnt(4)\n"
"v_add_f32 v104, v104, v204\n"
"v_add_f32 v105, v105, v205\n"
"v_add_f32 v106, v106, v206\n"
"v_add_f32 v107, v107, v207\n"
"buffer_load_dwordx4 v[152:155], v0, s[8:11], s0 offen offset:2048 sc1\n"
"ds_read_b128 v[140:143], v10 offset:17408\n"
"s_waitcnt vmcnt(10) lgkmcnt(4)\n"
"v_mfma_f32_16x16x32_bf16 v[124:127], a[80:83], v[180:183], v[124:127]\n"
"v_add_f32 v104, v104, v96\n"
"v_add_f32 v105, v105, v97\n"
"v_add_f32 v106, v106, v98\n"
"v_add_f32 v107, v107, v99\n"
"s_waitcnt lgkmcnt(3)\n"
"v_add_f32 v104, v104, v252\n"
"v_add_f32 v105, v105, v253\n"
"v_add_f32 v106, v106, v254\n"
"v_add_f32 v107, v107, v255\n"
"v_cvt_pk_bf16_f32 v174, v104, v105\n"
"s_waitcnt vmcnt(9)\n"
"v_mfma_f32_16x16x32_bf16 v[244:247], a[100:103], v[180:183], v[244:247]\n"
"buffer_load_dwordx4 v[160:163], v0, s[8:11], s0 offen offset:3072 sc1\n"
"s_add_u32 s0, s63, 0x1000\n"
"v_cvt_pk_bf16_f32 v175, v106, v107\n"
"s_add_u32 s1, s67, 0xb000\n"
"s_waitcnt vmcnt(9)\n"
"v_mfma_f32_16x16x32_bf16 v[168:171], a[108:111], v[180:183], v[168:171]\n"
"buffer_load_dwordx4 v[164:167], v0, s[8:11], s0 offen offset:0 sc1\n"
"s_add_u32 s0, s63, 0x5000\n"
"s_waitcnt vmcnt(9) lgkmcnt(1)\n"
"v_mfma_f32_16x16x32_bf16 v[248:251], a[104:107], v[180:183], v[248:251]\n"
"buffer_load_dwordx4 v[144:147], v0, s[8:11], s0 offen offset:0 sc1\n"
"s_waitcnt vmcnt(9) lgkmcnt(0)\n"
"v_mfma_f32_16x16x32_bf16 v[124:127], a[96:99], v[180:183], v[124:127]\n"
"buffer_load_dwordx4 v[176:179], v0, s[8:11], s0 offen offset:1024 sc1\n"
"s_waitcnt vmcnt(9)\n"
"v_mfma_f32_16x16x32_bf16 v[244:247], a[116:119], v[180:183], v[244:247]\n"
"buffer_load_dwordx4 v[68:71], v0, s[8:11], s0 offen offset:2048 sc1\n"
"s_waitcnt vmcnt(9)\n"
"v_mfma_f32_16x16x32_bf16 v[168:171], a[124:127], v[180:183], v[168:171]\n"
"buffer_load_dwordx4 v[92:95], v0, s[8:11], s0 offen offset:3072 sc1\n"
"s_add_u32 s0, s63, 0x6000\n"
"s_waitcnt vmcnt(9)\n"
"v_mfma_f32_16x16x32_bf16 v[248:251], a[120:123], v[180:183], v[248:251]\n"
"buffer_load_dwordx4 v[48:51], v0, s[8:11], s0 offen offset:0 sc1\n"
"s_add_u32 s0, s72, 0xa000\n"
"buffer_load_dwordx4 v[192:195], v7, s[8:11], s0 offen offset:0 sc1\n"
"buffer_store_dwordx4 v[172:175], v0, s[8:11], s1 offen offset:0 sc1\n"
"v_add_f32 v108, v108, v188\n"
"v_add_f32 v109, v109, v189\n"
"v_add_f32 v110, v110, v190\n"
"v_add_f32 v111, v111, v191\n"
"v_add_f32 v108, v108, v88\n"
"v_add_f32 v109, v109, v89\n"
"v_add_f32 v110, v110, v90\n"
"v_add_f32 v111, v111, v91\n"
"s_waitcnt vmcnt(11)\n"
"v_max3_u32 v44, v100, v101, v102\n"
"v_add_f32 v108, v108, v140\n"
"v_add_f32 v109, v109, v141\n"
"v_add_f32 v110, v110, v142\n"
"v_add_f32 v111, v111, v143\n"
"v_cvt_pk_bf16_f32 v76, v108, v109\n"
"v_cvt_pk_bf16_f32 v77, v110, v111\n"
"s_add_u32 s1, s68, 0xb000\n"
"buffer_store_dwordx2 v[76:77], v13, s[8:11], s1 offen offset:0 sc1\n"
"v_add_f32 v208, v156, v157\n"
"v_mul_f32 v209, v156, v156\n"
"v_add_f32 v210, v108, v109\n"
"v_mul_f32 v211, v108, v108\n"
"v_fmac_f32 v209, v157, v157\n"
"s_waitcnt vmcnt(11)\n"
"v_max3_u32 v44, v44, v103, v224\n"
"v_max3_u32 v44, v44, v225, v226\n"
"v_fmac_f32 v211, v109, v109\n"
"v_add_f32 v208, v208, v158\n"
"v_fmac_f32 v209, v158, v158\n"
"v_add_f32 v210, v210, v110\n"
"v_fmac_f32 v211, v110, v110\n"
"v_add_f32 v208, v208, v159\n"
"v_fmac_f32 v209, v159, v159\n"
"v_add_f32 v210, v210, v111\n"
"v_fmac_f32 v211, v111, v111\n"
"v_add_f32 v208, v208, v104\n"
"v_fmac_f32 v209, v104, v104\n"
"v_add_f32 v208, v208, v105\n"
"v_fmac_f32 v209, v105, v105\n"
"v_add_f32 v208, v208, v106\n"
"v_fmac_f32 v209, v106, v106\n"
"v_add_f32 v208, v208, v107\n"
"s_waitcnt vmcnt(10)\n"
"v_max3_u32 v44, v44, v227, v152\n"
"v_max3_u32 v44, v44, v153, v154\n"
"v_max_u32 v44, v44, v155\n"
"v_cmp_eq_u32 vcc, -1, v44\n"
"v_fmac_f32 v209, v107, v107\n"
"v_fmac_f32 v208, s78, v210\n"
"v_fmac_f32 v209, s78, v211\n"
"buffer_load_dwordx4 a[112:115], v0, s[8:11], s6 offen offset:0 sc1\n"
"buffer_load_dwordx4 a[132:135], v0, s[8:11], s6 offen offset:1024 sc1\n"
"s_add_u32 s90, s52, 0x88000\n"
"s_cbranch_vccnz Ls9_%=\n"
"Lb9_%=:\n"
"ds_write_b128 v2, v[100:103] offset:0\n"
"s_waitcnt vmcnt(11)\n"
"v_max3_u32 v220, v160, v161, v162\n"
"v_permlane32_swap_b32 v208, v209\n"
"v_add_f32 v208, v208, v209\n"
"v_mov_b32 v209, v208\n"
"buffer_load_dwordx4 a[140:143], v0, s[8:11], s90 offen offset:0 sc1\n"
"ds_write_b128 v2, v[224:227] offset:1024\n"
"s_waitcnt vmcnt(11)\n"
"v_max3_u32 v220, v220, v163, v164\n"
"ds_write_b128 v2, v[152:155] offset:2048\n"
"buffer_load_dwordx4 a[136:139], v0, s[8:11], s90 offen offset:1024 sc1\n"
"s_add_u32 s6, s53, 0x88800\n"
"v_max3_u32 v220, v220, v165, v166\n"
"v_max_u32 v220, v220, v167\n"
"v_cmp_eq_u32 vcc, -1, v220\n"
"v_permlane16_swap_b32 v208, v209\n"
"v_add_f32 v208, v208, v209\n"
"s_add_u32 s1, s71, 0x15000\n"
"buffer_store_dword v208, v14, s[8:11], s1 offen offset:0 sc1\n"
"ds_read_b128 v[32:35], v20 offset:768\n"
"s_cbranch_vccnz Ls10_%=\n"
"Lb10_%=:\n"
"ds_write_b128 v2, v[160:163] offset:3072\n"
"s_waitcnt vmcnt(12)\n"
"v_max3_u32 v36, v144, v145, v146\n"
"s_add_u32 s1, s63, 0xb000\n"
"ds_write_b128 v2, v[164:167] offset:4096\n"
"buffer_load_dwordx4 a[128:131], v0, s[8:11], s6 offen offset:0 sc1\n"
"s_waitcnt vmcnt(12)\n"
"v_max3_u32 v36, v36, v147, v176\n"
"v_max3_u32 v36, v36, v177, v178\n"
"v_max_u32 v36, v36, v179\n"
"v_cmp_eq_u32 vcc, -1, v36\n"
"s_waitcnt lgkmcnt(2)\n"
"v_add_f32 v156, v156, v32\n"
"s_cbranch_vccnz Ls11_%=\n"
"Lb11_%=:\n"
"buffer_load_dwordx4 a[152:155], v0, s[8:11], s6 offen offset:1024 sc1\n"
"ds_write_b128 v2, v[144:147] offset:5120\n"
"s_add_u32 s90, s52, 0x8c000\n"
"ds_write_b128 v2, v[176:179] offset:6144\n"
"buffer_load_dwordx4 a[144:147], v0, s[8:11], s90 offen offset:0 sc1\n"
"s_waitcnt vmcnt(13)\n"
"v_max3_u32 v112, v68, v69, v70\n"
"v_add_f32 v157, v157, v33\n"
"s_waitcnt vmcnt(12)\n"
"v_max3_u32 v112, v112, v71, v92\n"
"v_max3_u32 v112, v112, v93, v94\n"
"v_max_u32 v112, v112, v95\n"
"v_cmp_eq_u32 vcc, -1, v112\n"
"v_add_f32 v158, v158, v34\n"
"v_add_f32 v159, v159, v35\n"
"ds_read_b128 v[196:199], v20 offset:832\n"
"s_cbranch_vccnz Ls12_%=\n"
"Lb12_%=:\n"
"buffer_load_dwordx4 a[164:167], v0, s[8:11], s90 offen offset:1024 sc1\n"
"ds_write_b128 v2, v[68:71] offset:7168\n"
"s_add_u32 s6, s53, 0x8c800\n"
"ds_write_b128 v2, v[92:95] offset:8192\n"
"s_waitcnt vmcnt(12) lgkmcnt(6)\n"
"v_max3_u32 v40, v48, v49, v50\n"
"buffer_load_dwordx4 a[148:151], v0, s[8:11], s6 offen offset:0 sc1\n"
"s_waitcnt vmcnt(12) lgkmcnt(5)\n"
"v_max3_u32 v40, v40, v51, v192\n"
"v_max3_u32 v40, v40, v193, v194\n"
"v_max_u32 v40, v40, v195\n"
"v_cmp_eq_u32 vcc, -1, v40\n"
"s_waitcnt lgkmcnt(2)\n"
"v_add_f32 v104, v104, v196\n"
"v_add_f32 v105, v105, v197\n"
"v_add_f32 v106, v106, v198\n"
"v_add_f32 v107, v107, v199\n"
"s_cbranch_vccnz Ls13_%=\n"
"Lb13_%=:\n"
"buffer_load_dwordx4 a[156:159], v0, s[8:11], s6 offen offset:1024 sc1\n"
"ds_write_b128 v2, v[48:51] offset:9216\n"
"ds_read_b128 v[236:239], v20 offset:896\n"
"s_add_u32 s90, s52, 0x90000\n"
"v_add_f32 v192, v192, v194\n"
"v_add_f32 v193, v193, v195\n"
"ds_write_b64 v21, v[192:193] offset:0\n"
"buffer_load_dwordx4 v[240:243], v0, s[8:11], s90 offen offset:0 sc1\n"
"s_waitcnt vmcnt(11)\n"
"v_mfma_f32_16x16x32_bf16 v[124:127], a[112:115], v[180:183], v[124:127]\n"
"s_waitcnt vmcnt(10)\n"
"v_mfma_f32_16x16x32_bf16 v[244:247], a[132:135], v[180:183], v[244:247]\n"
"buffer_load_dwordx4 v[80:83], v0, s[8:11], s90 offen offset:1024 sc1\n"
"s_waitcnt vmcnt(10) lgkmcnt(4)\n"
"v_mfma_f32_16x16x32_bf16 v[168:171], a[140:143], v[180:183], v[168:171]\n"
"s_add_u32 s6, s53, 0x90800\n"
"s_waitcnt lgkmcnt(1)\n"
"v_fma_f32 v108, v108, s78, v236\n"
"v_fma_f32 v109, v109, s78, v237\n"
"v_fma_f32 v110, v110, s78, v238\n"
"v_fma_f32 v111, v111, s78, v239\n"
"buffer_load_dwordx4 v[228:231], v0, s[8:11], s6 offen offset:0 sc1\n"
"s_waitcnt vmcnt(10)\n"
"v_mfma_f32_16x16x32_bf16 v[248:251], a[136:139], v[180:183], v[248:251]\n"
"s_waitcnt lgkmcnt(0)\n"
"s_barrier\n"
"buffer_load_dwordx4 v[120:123], v0, s[8:11], s6 offen offset:1024 sc1\n"
"ds_read_b128 v[212:215], v27 offset:0\n"
"ds_read_b128 v[132:135], v27 offset:256\n"
"ds_read_b128 v[148:151], v27 offset:512\n"
"ds_read_b128 v[128:131], v27 offset:768\n"
"ds_read_b128 v[184:187], v4 offset:0\n"
"ds_read_b128 v[200:203], v4 offset:5120\n"
"ds_read_b128 v[84:87], v4 offset:1024\n"
"ds_read_b128 v[136:139], v4 offset:6144\n"
"ds_read_b128 v[28:31], v4 offset:2048\n"
"ds_read_b128 v[116:119], v4 offset:7168\n"
"s_add_u32 s90, s52, 0x94000\n"
"buffer_load_dwordx4 v[64:67], v0, s[8:11], s90 offen offset:0 sc1\n"
"buffer_load_dwordx4 v[56:59], v0, s[8:11], s1 offen offset:0 sc1\n"
"s_waitcnt vmcnt(11)\n"
"v_mfma_f32_16x16x32_bf16 v[124:127], a[128:131], v[180:183], v[124:127]\n"
"s_waitcnt vmcnt(10)\n"
"s_waitcnt lgkmcnt(8)\n"
"v_add_f32 v212, v212, v132\n"
"v_mfma_f32_16x16x32_bf16 v[244:247], a[152:155], v[180:183], v[244:247]\n"
"buffer_load_dwordx4 v[216:219], v0, s[8:11], s1 offen offset:1024 sc1\n"
"s_waitcnt lgkmcnt(6)\n"
"v_add_f32 v148, v148, v128\n"
"v_add_f32 v213, v213, v133\n"
"s_waitcnt lgkmcnt(5)\n"
"v_mfma_f32_16x16x32_bf16 v[72:75], a[172:175], v[184:187], 0\n"
"v_add_f32 v149, v149, v129\n"
"s_waitcnt lgkmcnt(4)\n"
"v_mfma_f32_16x16x32_bf16 v[204:207], a[172:175], v[200:203], 0\n"
"v_add_f32 v214, v214, v134\n"
"v_add_f32 v150, v150, v130\n"
"v_mfma_f32_16x16x32_bf16 v[96:99], a[168:171], v[184:187], 0\n"
"v_add_f32 v215, v215, v135\n"
"v_add_f32 v151, v151, v131\n"
"v_mfma_f32_16x16x32_bf16 v[252:255], a[168:171], v[200:203], 0\n"
"buffer_load_dwordx4 v[172:175], v0, s[8:11], s1 offen offset:2048 sc1\n"
"v_mfma_f32_16x16x32_bf16 v[188:191], a[160:163], v[184:187], 0\n"
"v_add_f32 v212, v212, v148\n"
"v_add_f32 v213, v213, v149\n"
"v_mfma_f32_16x16x32_bf16 v[88:91], a[160:163], v[200:203], 0\n"
"v_add_f32 v214, v214, v150\n"
"v_add_f32 v215, v215, v151\n"
"v_mfma_f32_16x16x32_bf16 v[140:143], a[180:183], v[184:187], 0\n"
"ds_read_b128 v[76:79], v4 offset:3072\n"
"v_add_f32 v212, v212, v214\n"
"v_mfma_f32_16x16x32_bf16 v[44:47], a[180:183], v[200:203], 0\n"
"s_waitcnt lgkmcnt(4)\n"
"ds_read_b128 v[100:103], v4 offset:8192\n"
"buffer_load_dwordx4 v[224:227], v0, s[8:11], s1 offen offset:3072 sc1\n"
"s_add_u32 s1, s63, 0xc000\n"
"v_mfma_f32_16x16x32_bf16 v[72:75], a[188:191], v[84:87], v[72:75]\n"
"s_waitcnt lgkmcnt(4)\n"
"v_add_f32 v213, v213, v215\n"
"v_mul_f32 v132, 0xbacccccd, v212\n"
"v_mfma_f32_16x16x32_bf16 v[204:207], a[188:191], v[136:139], v[204:207]\n"
"v_mul_f32 v213, 0x3acccccd, v213\n"
"v_fma_f32 v213, -v132, v132, v213\n"
"v_mfma_f32_16x16x32_bf16 v[96:99], a[184:187], v[84:87], v[96:99]\n"
"v_max_f32 v213, 0, v213\n"
"v_add_f32 v213, 0x3727c5ac, v213\n"
"v_mfma_f32_16x16x32_bf16 v[252:255], a[184:187], v[136:139], v[252:255]\n"
"v_rsq_f32 v133, v213\n"
"v_mfma_f32_16x16x32_bf16 v[188:191], a[176:179], v[84:87], v[188:191]\n"
"buffer_load_dwordx4 v[152:155], v0, s[8:11], s1 offen offset:0 sc1\n"
"s_add_u32 s1, s63, 0x10000\n"
"v_mfma_f32_16x16x32_bf16 v[88:91], a[176:179], v[136:139], v[88:91]\n"
"v_mul_f32 v134, v213, v133\n"
"v_mul_f32 v135, v133, v133\n"
"v_mfma_f32_16x16x32_bf16 v[140:143], a[196:199], v[84:87], v[140:143]\n"
"ds_write_b128 v27, v[132:135] offset:8192\n"
"ds_read_b128 v[208:211], v4 offset:4096\n"
"v_mfma_f32_16x16x32_bf16 v[44:47], a[196:199], v[136:139], v[44:47]\n"
"s_waitcnt lgkmcnt(4)\n"
"ds_read_b128 v[220:223], v4 offset:9216\n"
"buffer_load_dwordx4 v[160:163], v0, s[8:11], s1 offen offset:0 sc1\n"
"v_mfma_f32_16x16x32_bf16 v[72:75], a[204:207], v[28:31], v[72:75]\n"
"v_mfma_f32_16x16x32_bf16 v[204:207], a[204:207], v[116:119], v[204:207]\n"
"v_mfma_f32_16x16x32_bf16 v[96:99], a[200:203], v[28:31], v[96:99]\n"
"v_mfma_f32_16x16x32_bf16 v[252:255], a[200:203], v[116:119], v[252:255]\n"
"v_mfma_f32_16x16x32_bf16 v[188:191], a[192:195], v[28:31], v[188:191]\n"
"buffer_load_dwordx4 v[164:167], v0, s[8:11], s1 offen offset:1024 sc1\n"
"v_mfma_f32_16x16x32_bf16 v[88:91], a[192:195], v[116:119], v[88:91]\n"
"v_mfma_f32_16x16x32_bf16 v[140:143], a[212:215], v[28:31], v[140:143]\n"
"ds_read_b128 v[36:39], v4 offset:20480\n"
"v_mfma_f32_16x16x32_bf16 v[44:47], a[212:215], v[116:119], v[44:47]\n"
"s_waitcnt lgkmcnt(4)\n"
"ds_read_b128 v[144:147], v4 offset:25600\n"
"v_mfma_f32_16x16x32_bf16 v[72:75], a[220:223], v[76:79], v[72:75]\n"
"buffer_load_dwordx4 v[176:179], v0, s[8:11], s1 offen offset:2048 sc1\n"
"v_mfma_f32_16x16x32_bf16 v[204:207], a[220:223], v[100:103], v[204:207]\n"
"v_mfma_f32_16x16x32_bf16 v[96:99], a[216:219], v[76:79], v[96:99]\n"
"v_mfma_f32_16x16x32_bf16 v[252:255], a[216:219], v[100:103], v[252:255]\n"
"v_mfma_f32_16x16x32_bf16 v[188:191], a[208:211], v[76:79], v[188:191]\n"
"v_mfma_f32_16x16x32_bf16 v[88:91], a[208:211], v[100:103], v[88:91]\n"
"buffer_load_dwordx4 v[32:35], v0, s[8:11], s1 offen offset:3072 sc1\n"
"s_add_u32 s1, s63, 0x11000\n"
"v_mfma_f32_16x16x32_bf16 v[140:143], a[228:231], v[76:79], v[140:143]\n"
"ds_read_b128 v[112:115], v4 offset:21504\n"
"v_mfma_f32_16x16x32_bf16 v[44:47], a[228:231], v[100:103], v[44:47]\n"
"s_waitcnt lgkmcnt(3)\n"
"ds_read_b128 v[68:71], v22 offset:0\n"
"ds_read_b128 v[92:95], v22 offset:1024\n"
"ds_read_b128 v[196:199], v4 offset:26624\n"
"v_mfma_f32_16x16x32_bf16 v[72:75], a[236:239], v[208:211], v[72:75]\n"
"v_mfma_f32_16x16x32_bf16 v[204:207], a[236:239], v[220:223], v[204:207]\n"
"buffer_load_dwordx4 v[40:43], v0, s[8:11], s1 offen offset:0 sc1\n"
"s_add_u32 s1, s72, 0x15000\n"
"v_mfma_f32_16x16x32_bf16 v[96:99], a[232:235], v[208:211], v[96:99]\n"
"v_mfma_f32_16x16x32_bf16 v[252:255], a[232:235], v[220:223], v[252:255]\n"
"v_mfma_f32_16x16x32_bf16 v[188:191], a[224:227], v[208:211], v[188:191]\n"
"v_mfma_f32_16x16x32_bf16 v[88:91], a[224:227], v[220:223], v[88:91]\n"
"v_mfma_f32_16x16x32_bf16 v[140:143], a[244:247], v[208:211], v[140:143]\n"
"ds_read_b128 v[48:51], v4 offset:22528\n"
"buffer_load_dwordx4 v[192:195], v7, s[8:11], s1 offen offset:0 sc1\n"
"v_mfma_f32_16x16x32_bf16 v[44:47], a[244:247], v[220:223], v[44:47]\n"
"s_waitcnt lgkmcnt(5)\n"
"ds_read_b128 v[236:239], v4 offset:27648\n"
"v_mfma_f32_16x16x32_bf16 v[72:75], a[252:255], v[36:39], v[72:75]\n"
"v_mfma_f32_16x16x32_bf16 v[204:207], a[252:255], v[144:147], v[204:207]\n"
"v_mfma_f32_16x16x32_bf16 v[96:99], a[248:251], v[36:39], v[96:99]\n"
"v_mfma_f32_16x16x32_bf16 v[252:255], a[248:251], v[144:147], v[252:255]\n"
"v_mfma_f32_16x16x32_bf16 v[188:191], a[240:243], v[36:39], v[188:191]\n"
"v_mfma_f32_16x16x32_bf16 v[88:91], a[240:243], v[144:147], v[88:91]\n"
"s_waitcnt vmcnt(10) lgkmcnt(3)\n"
"v_max3_u32 v128, v56, v57, v58\n"
"v_mfma_f32_16x16x32_bf16 v[140:143], a[12:15], v[36:39], v[140:143]\n"
"ds_read_b128 v[148:151], v4 offset:23552\n"
"v_mfma_f32_16x16x32_bf16 v[44:47], a[12:15], v[144:147], v[44:47]\n"
"ds_read_b128 v[184:187], v4 offset:28672\n"
"v_mfma_f32_16x16x32_bf16 v[168:171], a[144:147], v[180:183], v[168:171]\n"
"s_add_u32 m0, s20, 0x0\n"
"s_nop 0\n"
"buffer_load_dwordx4 v0, s[8:11], s22 offen offset:0 sc1 lds\n"
"v_mfma_f32_16x16x32_bf16 v[248:251], a[164:167], v[180:183], v[248:251]\n"
"s_waitcnt vmcnt(10) lgkmcnt(4)\n"
"v_max3_u32 v128, v128, v59, v216\n"
"v_max3_u32 v128, v128, v217, v218\n"
"v_mfma_f32_16x16x32_bf16 v[124:127], a[148:151], v[180:183], v[124:127]\n"
"v_mfma_f32_16x16x32_bf16 v[244:247], a[156:159], v[180:183], v[244:247]\n"
"buffer_load_dwordx4 v0, s[8:11], s22 offen offset:1024 sc1 lds\n"
"v_mfma_f32_16x16x32_bf16 v[168:171], v[240:243], v[180:183], v[168:171]\n"
"v_mfma_f32_16x16x32_bf16 v[248:251], v[80:83], v[180:183], v[248:251]\n"
"v_mfma_f32_16x16x32_bf16 v[124:127], v[228:231], v[180:183], v[124:127]\n"
"s_waitcnt vmcnt(10) lgkmcnt(2)\n"
"v_max3_u32 v128, v128, v219, v172\n"
"v_max3_u32 v128, v128, v173, v174\n"
"v_max_u32 v128, v128, v175\n"
"v_cmp_eq_u32 vcc, -1, v128\n"
"v_mfma_f32_16x16x32_bf16 v[244:247], v[120:123], v[180:183], v[244:247]\n"
"buffer_load_dwordx4 v0, s[8:11], s22 offen offset:2048 sc1 lds\n"
"v_mfma_f32_16x16x32_bf16 v[168:171], v[64:67], v[180:183], v[168:171]\n"
"s_cbranch_vccnz Ls37_%=\n"
"Lb37_%=:\n"
"ds_write_b128 v2, v[56:59] offset:10240\n"
"s_waitcnt vmcnt(10)\n"
"v_max3_u32 v200, v224, v225, v226\n"
"ds_write_b128 v2, v[216:219] offset:11264\n"
"buffer_load_dwordx4 v0, s[8:11], s22 offen offset:3072 sc1 lds\n"
"s_add_u32 s22, s55, 0xc800\n"
"ds_write_b128 v2, v[172:175] offset:12288\n"
"s_waitcnt vmcnt(10) lgkmcnt(3)\n"
"v_max3_u32 v200, v200, v227, v152\n"
"v_max3_u32 v200, v200, v153, v154\n"
"v_max_u32 v200, v200, v155\n"
"v_cmp_eq_u32 vcc, -1, v200\n"
"s_add_u32 m0, s20, 0x1000\n"
"s_nop 0\n"
"buffer_load_dwordx4 v0, s[8:11], s22 offen offset:0 sc1 lds\n"
"s_cbranch_vccnz Ls38_%=\n"
"Lb38_%=:\n"
"ds_write_b128 v2, v[224:227] offset:13312\n"
"s_waitcnt vmcnt(10)\n"
"v_max3_u32 v212, v160, v161, v162\n"
"ds_write_b128 v2, v[152:155] offset:14336\n"
"buffer_load_dwordx4 v0, s[8:11], s22 offen offset:1024 sc1 lds\n"
"s_add_u32 s22, s55, 0xd000\n"
"s_waitcnt vmcnt(10)\n"
"v_max3_u32 v212, v212, v163, v164\n"
"v_max3_u32 v212, v212, v165, v166\n"
"v_max_u32 v212, v212, v167\n"
"v_cmp_eq_u32 vcc, -1, v212\n"
"s_add_u32 m0, s21, 0x0\n"
"s_nop 0\n"
"buffer_load_dwordx4 v0, s[8:11], s22 offen offset:0 sc1 lds\n"
"s_cbranch_vccnz Ls39_%=\n"
"Lb39_%=:\n"
"ds_write_b128 v2, v[160:163] offset:15360\n"
"s_waitcnt vmcnt(10) lgkmcnt(4)\n"
"v_max3_u32 v84, v176, v177, v178\n"
"ds_write_b128 v2, v[164:167] offset:16384\n"
"buffer_load_dwordx4 v0, s[8:11], s22 offen offset:1024 sc1 lds\n"
"s_waitcnt vmcnt(10) lgkmcnt(4)\n"
"v_max3_u32 v84, v84, v179, v32\n"
"v_max3_u32 v84, v84, v33, v34\n"
"v_max_u32 v84, v84, v35\n"
"v_cmp_eq_u32 vcc, -1, v84\n"
"buffer_load_dwordx4 v0, s[8:11], s22 offen offset:2048 sc1 lds\n"
"s_cbranch_vccnz Ls40_%=\n"
"Lb40_%=:\n"
"ds_write_b128 v2, v[176:179] offset:17408\n"
"buffer_load_dwordx4 v[132:135], v0, s[8:11], s90 offen offset:1024 sc1\n"
"ds_write_b128 v2, v[32:35] offset:18432\n"
"s_add_u32 s6, s53, 0x94800\n"
"s_waitcnt vmcnt(11) lgkmcnt(4)\n"
"v_max3_u32 v136, v40, v41, v42\n"
"buffer_load_dwordx4 v[28:31], v0, s[8:11], s6 offen offset:0 sc1\n"
"s_waitcnt vmcnt(11)\n"
"v_max3_u32 v136, v136, v43, v192\n"
"v_max3_u32 v136, v136, v193, v194\n"
"v_max_u32 v136, v136, v195\n"
"v_cmp_eq_u32 vcc, -1, v136\n"
"s_cbranch_vccnz Ls41_%=\n"
"Lb41_%=:\n"
"ds_write_b128 v2, v[40:43] offset:19456\n"
"buffer_load_dwordx4 v[116:119], v0, s[8:11], s6 offen offset:1024 sc1\n"
"s_add_u32 s90, s52, 0x98000\n"
"v_add_f32 v192, v192, v194\n"
"v_add_f32 v193, v193, v195\n"
"ds_write_b64 v21, v[192:193] offset:4096\n"
"buffer_load_dwordx4 v[76:79], v0, s[8:11], s90 offen offset:0 sc1\n"
"buffer_load_dwordx4 v[100:103], v0, s[8:11], s90 offen offset:1024 sc1\n"
"s_add_u32 s6, s53, 0x98800\n"
"s_waitcnt lgkmcnt(0)\n"
"s_barrier\n"
"ds_read_b128 v[208:211], v27 offset:4096\n"
"ds_read_b128 v[220:223], v27 offset:4352\n"
"ds_read_b128 v[36:39], v27 offset:4608\n"
"ds_read_b128 v[144:147], v27 offset:4864\n"
"ds_read_b128 v[128:131], v4 offset:10240\n"
"ds_read_b128 v[56:59], v4 offset:15360\n"
"ds_read_b128 v[216:219], v4 offset:11264\n"
"ds_read_b128 v[172:175], v4 offset:16384\n"
"ds_read_b128 v[200:203], v4 offset:12288\n"
"ds_read_b128 v[224:227], v4 offset:17408\n"
"buffer_load_dwordx4 v0, s[8:11], s22 offen offset:3072 sc1 lds\n"
"s_add_u32 s22, s55, 0xe000\n"
"s_add_u32 m0, s21, 0x1000\n"
"s_nop 0\n"
"buffer_load_dwordx4 v0, s[8:11], s22 offen offset:0 sc1 lds\n"
"s_waitcnt lgkmcnt(8)\n"
"v_add_f32 v208, v208, v220\n"
"s_waitcnt lgkmcnt(6)\n"
"v_add_f32 v36, v36, v144\n"
"v_add_f32 v209, v209, v221\n"
"v_add_f32 v37, v37, v145\n"
"s_waitcnt lgkmcnt(5)\n"
"v_mfma_f32_16x16x32_bf16 v[152:155], a[172:175], v[128:131], 0\n"
"buffer_load_dwordx4 v0, s[8:11], s22 offen offset:1024 sc1 lds\n"
"s_waitcnt lgkmcnt(4)\n"
"v_mfma_f32_16x16x32_bf16 v[212:215], a[172:175], v[56:59], 0\n"
"v_add_f32 v210, v210, v222\n"
"v_add_f32 v38, v38, v146\n"
"v_mfma_f32_16x16x32_bf16 v[160:163], a[168:171], v[128:131], 0\n"
"buffer_load_dwordx4 a[172:175], v0, s[8:11], s6 offen offset:0 sc1\n"
"v_mfma_f32_16x16x32_bf16 v[164:167], a[168:171], v[56:59], 0\n"
"v_add_f32 v211, v211, v223\n"
"v_add_f32 v39, v39, v147\n"
"v_mfma_f32_16x16x32_bf16 v[84:87], a[160:163], v[128:131], 0\n"
"v_add_f32 v208, v208, v36\n"
"v_add_f32 v209, v209, v37\n"
"v_mfma_f32_16x16x32_bf16 v[176:179], a[160:163], v[56:59], 0\n"
"v_add_f32 v210, v210, v38\n"
"v_add_f32 v211, v211, v39\n"
"v_mfma_f32_16x16x32_bf16 v[32:35], a[180:183], v[128:131], 0\n"
"buffer_load_dwordx4 a[168:171], v0, s[8:11], s6 offen offset:1024 sc1\n"
"ds_read_b128 v[136:139], v4 offset:13312\n"
"v_mfma_f32_16x16x32_bf16 v[40:43], a[180:183], v[56:59], 0\n"
"s_waitcnt lgkmcnt(4)\n"
"ds_read_b128 v[192:195], v4 offset:18432\n"
"s_add_u32 s90, s52, 0x9c000\n"
"v_mfma_f32_16x16x32_bf16 v[152:155], a[188:191], v[216:219], v[152:155]\n"
"s_waitcnt lgkmcnt(4)\n"
"v_add_f32 v208, v208, v210\n"
"v_add_f32 v209, v209, v211\n"
"v_mfma_f32_16x16x32_bf16 v[212:215], a[188:191], v[172:175], v[212:215]\n"
"buffer_load_dwordx4 a[160:163], v0, s[8:11], s90 offen offset:0 sc1\n"
"v_mfma_f32_16x16x32_bf16 v[160:163], a[184:187], v[216:219], v[160:163]\n"
"v_mul_f32 v220, 0xbacccccd, v208\n"
"v_mul_f32 v209, 0x3acccccd, v209\n"
"v_mfma_f32_16x16x32_bf16 v[164:167], a[184:187], v[172:175], v[164:167]\n"
"v_fma_f32 v209, -v220, v220, v209\n"
"v_max_f32 v209, 0, v209\n"
"v_mfma_f32_16x16x32_bf16 v[84:87], a[176:179], v[216:219], v[84:87]\n"
"v_add_f32 v209, 0x3727c5ac, v209\n"
"v_mfma_f32_16x16x32_bf16 v[176:179], a[176:179], v[172:175], v[176:179]\n"
"v_rsq_f32 v221, v209\n"
"v_mfma_f32_16x16x32_bf16 v[32:35], a[196:199], v[216:219], v[32:35]\n"
"buffer_load_dwordx4 a[180:183], v0, s[8:11], s90 offen offset:1024 sc1\n"
"ds_read_b128 v[144:147], v4 offset:14336\n"
"v_mfma_f32_16x16x32_bf16 v[40:43], a[196:199], v[172:175], v[40:43]\n"
"s_waitcnt lgkmcnt(3)\n"
"ds_read_b128 v[36:39], v4 offset:19456\n"
"s_add_u32 s6, s53, 0x9c800\n"
"v_mfma_f32_16x16x32_bf16 v[152:155], a[204:207], v[200:203], v[152:155]\n"
"v_mul_f32 v222, v209, v221\n"
"v_mul_f32 v223, v221, v221\n"
"v_mfma_f32_16x16x32_bf16 v[212:215], a[204:207], v[224:227], v[212:215]\n"
"ds_write_b128 v27, v[220:223] offset:8448\n"
"v_mfma_f32_16x16x32_bf16 v[160:163], a[200:203], v[200:203], v[160:163]\n"
"buffer_load_dwordx4 a[188:191], v0, s[8:11], s6 offen offset:0 sc1\n"
"v_mfma_f32_16x16x32_bf16 v[164:167], a[200:203], v[224:227], v[164:167]\n"
"v_mfma_f32_16x16x32_bf16 v[84:87], a[192:195], v[200:203], v[84:87]\n"
"v_mfma_f32_16x16x32_bf16 v[176:179], a[192:195], v[224:227], v[176:179]\n"
"v_mfma_f32_16x16x32_bf16 v[32:35], a[212:215], v[200:203], v[32:35]\n"
"buffer_load_dwordx4 a[184:187], v0, s[8:11], s6 offen offset:1024 sc1\n"
"ds_read_b128 v[128:131], v4 offset:30720\n"
"v_mfma_f32_16x16x32_bf16 v[40:43], a[212:215], v[224:227], v[40:43]\n"
"s_waitcnt lgkmcnt(4)\n"
"ds_read_b128 v[56:59], v4 offset:35840\n"
"s_add_u32 s90, s55, 0x0\n"
"v_mfma_f32_16x16x32_bf16 v[152:155], a[220:223], v[136:139], v[152:155]\n"
"v_mfma_f32_16x16x32_bf16 v[212:215], a[220:223], v[192:195], v[212:215]\n"
"buffer_load_dwordx4 a[176:179], v0, s[8:11], s90 offen offset:0 sc1\n"
"v_mfma_f32_16x16x32_bf16 v[160:163], a[216:219], v[136:139], v[160:163]\n"
"v_mfma_f32_16x16x32_bf16 v[164:167], a[216:219], v[192:195], v[164:167]\n"
"v_mfma_f32_16x16x32_bf16 v[84:87], a[208:211], v[136:139], v[84:87]\n"
"v_mfma_f32_16x16x32_bf16 v[176:179], a[208:211], v[192:195], v[176:179]\n"
"v_mfma_f32_16x16x32_bf16 v[32:35], a[228:231], v[136:139], v[32:35]\n"
"buffer_load_dwordx4 a[196:199], v0, s[8:11], s90 offen offset:1024 sc1\n"
"ds_read_b128 v[216:219], v4 offset:31744\n"
"v_mfma_f32_16x16x32_bf16 v[40:43], a[228:231], v[192:195], v[40:43]\n"
"s_waitcnt lgkmcnt(4)\n"
"ds_read_b128 v[172:175], v22 offset:256\n"
"ds_read_b128 v[208:211], v22 offset:1280\n"
"ds_read_b128 v[220:223], v4 offset:36864\n"
"v_mfma_f32_16x16x32_bf16 v[152:155], a[236:239], v[144:147], v[152:155]\n"
"v_mfma_f32_16x16x32_bf16 v[212:215], a[236:239], v[36:39], v[212:215]\n"
"v_mfma_f32_16x16x32_bf16 v[160:163], a[232:235], v[144:147], v[160:163]\n"
"buffer_load_dwordx4 a[204:207], v0, s[8:11], s90 offen offset:2048 sc1\n"
"v_mfma_f32_16x16x32_bf16 v[164:167], a[232:235], v[36:39], v[164:167]\n"
"v_mfma_f32_16x16x32_bf16 v[84:87], a[224:227], v[144:147], v[84:87]\n"
"v_mfma_f32_16x16x32_bf16 v[176:179], a[224:227], v[36:39], v[176:179]\n"
"v_mfma_f32_16x16x32_bf16 v[32:35], a[244:247], v[144:147], v[32:35]\n"
"ds_read_b128 v[200:203], v4 offset:32768\n"
"v_mfma_f32_16x16x32_bf16 v[40:43], a[244:247], v[36:39], v[40:43]\n"
"s_waitcnt lgkmcnt(5)\n"
"buffer_load_dwordx4 a[200:203], v0, s[8:11], s90 offen offset:3072 sc1\n"
"ds_read_b128 v[224:227], v4 offset:37888\n"
"v_mfma_f32_16x16x32_bf16 v[152:155], a[252:255], v[128:131], v[152:155]\n"
"s_add_u32 s90, s55, 0x1000\n"
"v_mfma_f32_16x16x32_bf16 v[212:215], a[252:255], v[56:59], v[212:215]\n"
"v_mfma_f32_16x16x32_bf16 v[160:163], a[248:251], v[128:131], v[160:163]\n"
"v_mfma_f32_16x16x32_bf16 v[164:167], a[248:251], v[56:59], v[164:167]\n"
"buffer_load_dwordx4 a[192:195], v0, s[8:11], s90 offen offset:0 sc1\n"
"v_mfma_f32_16x16x32_bf16 v[84:87], a[240:243], v[128:131], v[84:87]\n"
"s_add_u32 s90, s56, 0x1400\n"
"v_mfma_f32_16x16x32_bf16 v[176:179], a[240:243], v[56:59], v[176:179]\n"
"v_mfma_f32_16x16x32_bf16 v[32:35], a[12:15], v[128:131], v[32:35]\n"
"ds_read_b128 v[136:139], v4 offset:33792\n"
"v_mfma_f32_16x16x32_bf16 v[40:43], a[12:15], v[56:59], v[40:43]\n"
"s_waitcnt vmcnt(18) lgkmcnt(3)\n"
"buffer_load_dwordx4 a[212:215], v0, s[8:11], s90 offen offset:0 sc1\n"
"ds_read_b128 v[192:195], v4 offset:38912\n"
"v_mfma_f32_16x16x32_bf16 v[248:251], v[132:135], v[180:183], v[248:251]\n"
"s_waitcnt vmcnt(18)\n"
"v_mfma_f32_16x16x32_bf16 v[124:127], v[28:31], v[180:183], v[124:127]\n"
"s_waitcnt vmcnt(17)\n"
"buffer_load_dwordx2 v[144:145], v18, s[8:11], s39 offen sc1\n"
"v_mfma_f32_16x16x32_bf16 v[244:247], v[116:119], v[180:183], v[244:247]\n"
"s_waitcnt vmcnt(17)\n"
"v_mfma_f32_16x16x32_bf16 v[168:171], v[76:79], v[180:183], v[168:171]\n"
"s_waitcnt vmcnt(16)\n"
"v_mfma_f32_16x16x32_bf16 v[248:251], v[100:103], v[180:183], v[248:251]\n"
"s_waitcnt vmcnt(12) lgkmcnt(3)\n"
"v_mfma_f32_16x16x32_bf16 v[124:127], a[172:175], v[180:183], v[124:127]\n"
"s_waitcnt vmcnt(11) lgkmcnt(2)\n"
"v_mfma_f32_16x16x32_bf16 v[244:247], a[168:171], v[180:183], v[244:247]\n"
"s_waitcnt vmcnt(10)\n"
"buffer_load_dwordx4 a[220:223], v0, s[8:11], s90 offen offset:1024 sc1\n"
"v_mfma_f32_16x16x32_bf16 v[168:171], a[160:163], v[180:183], v[168:171]\n"
"s_waitcnt vmcnt(10)\n"
"v_mfma_f32_16x16x32_bf16 v[248:251], a[180:183], v[180:183], v[248:251]\n"
"s_waitcnt vmcnt(9)\n"
"v_mfma_f32_16x16x32_bf16 v[124:127], a[188:191], v[180:183], v[124:127]\n"
"buffer_load_dwordx4 a[216:219], v0, s[8:11], s90 offen offset:2048 sc1\n"
"s_waitcnt vmcnt(9) lgkmcnt(0)\n"
"v_mfma_f32_16x16x32_bf16 v[244:247], a[184:187], v[180:183], v[244:247]\n"
"buffer_load_dwordx4 a[208:211], v0, s[8:11], s90 offen offset:3072 sc1\n"
"ds_write_b128 v20, v[168:171] offset:256\n"
"s_add_u32 s90, s56, 0x2400\n"
"ds_write_b128 v20, v[248:251] offset:320\n"
"buffer_load_dwordx4 a[228:231], v0, s[8:11], s90 offen offset:0 sc1\n"
"s_add_u32 s90, s55, 0x2800\n"
"ds_write_b128 v20, v[124:127] offset:384\n"
"buffer_load_dwordx4 a[236:239], v0, s[8:11], s90 offen offset:0 sc1\n"
"ds_write_b128 v20, v[244:247] offset:448\n"
"ds_read_b128 v[36:39], v20 offset:256\n"
"ds_read_b128 v[128:131], v20 offset:512\n"
"ds_read_b128 v[56:59], v20 offset:256\n"
"ds_read_b128 v[180:183], v20 offset:512\n"
"buffer_load_dwordx4 a[232:235], v0, s[8:11], s90 offen offset:1024 sc1\n"
"buffer_load_dwordx4 a[224:227], v0, s[8:11], s90 offen offset:2048 sc1\n"
"s_waitcnt lgkmcnt(3)\n"
"v_fmac_f32 v72, v68, v36\n"
"v_fmac_f32 v73, v68, v37\n"
"v_fmac_f32 v74, v68, v38\n"
"v_fmac_f32 v75, v68, v39\n"
"s_waitcnt lgkmcnt(2)\n"
"v_fmac_f32 v72, v70, v128\n"
"v_fmac_f32 v73, v70, v129\n"
"v_fmac_f32 v74, v70, v130\n"
"v_fmac_f32 v75, v70, v131\n"
"s_waitcnt lgkmcnt(1)\n"
"v_fmac_f32 v152, v172, v56\n"
"v_mfma_f32_16x16x32_bf16 v[72:75], a[0:3], v[112:115], v[72:75]\n"
"buffer_load_dwordx4 a[244:247], v0, s[8:11], s90 offen offset:3072 sc1\n"
"v_mfma_f32_16x16x32_bf16 v[204:207], a[0:3], v[196:199], v[204:207]\n"
"s_add_u32 s90, s55, 0x3800\n"
"v_fmac_f32 v153, v172, v57\n"
"v_fmac_f32 v154, v172, v58\n"
"v_mfma_f32_16x16x32_bf16 v[96:99], a[8:11], v[112:115], v[96:99]\n"
"buffer_load_dwordx4 a[252:255], v0, s[8:11], s90 offen offset:0 sc1\n"
"v_mfma_f32_16x16x32_bf16 v[252:255], a[8:11], v[196:199], v[252:255]\n"
"v_fmac_f32 v155, v172, v59\n"
"v_mfma_f32_16x16x32_bf16 v[188:191], a[4:7], v[112:115], v[188:191]\n"
"v_fmac_f32 v204, v92, v36\n"
"v_fmac_f32 v205, v92, v37\n"
"v_mfma_f32_16x16x32_bf16 v[88:91], a[4:7], v[196:199], v[88:91]\n"
"v_fmac_f32 v206, v92, v38\n"
"v_fmac_f32 v207, v92, v39\n"
"v_mfma_f32_16x16x32_bf16 v[140:143], a[28:31], v[112:115], v[140:143]\n"
"ds_read_b128 v[168:171], v4 offset:24576\n"
"v_fmac_f32 v204, v94, v128\n"
"v_mfma_f32_16x16x32_bf16 v[44:47], a[28:31], v[196:199], v[44:47]\n"
"buffer_load_dwordx4 a[248:251], v0, s[8:11], s90 offen offset:1024 sc1\n"
"ds_read_b128 v[248:251], v4 offset:29696\n"
"v_mfma_f32_16x16x32_bf16 v[72:75], a[16:19], v[48:51], v[72:75]\n"
"v_mov_b32 v124, -1\n"
"v_mov_b32 v125, -1\n"
"v_mov_b32 v126, -1\n"
"v_mov_b32 v127, -1\n"
"s_add_u32 s91, s74, 0x40000\n"
"buffer_store_dwordx4 v[124:127], v0, s[8:11], s91 offen offset:0 sc1\n"
"s_add_u32 s91, s74, 0x48000\n"
"v_fmac_f32 v205, v94, v129\n"
"v_fmac_f32 v206, v94, v130\n"
"v_fmac_f32 v207, v94, v131\n"
"s_waitcnt lgkmcnt(2)\n"
"v_fmac_f32 v152, v174, v180\n"
"v_mfma_f32_16x16x32_bf16 v[204:207], a[16:19], v[236:239], v[204:207]\n"
"v_fmac_f32 v153, v174, v181\n"
"v_fmac_f32 v154, v174, v182\n"
"v_mfma_f32_16x16x32_bf16 v[96:99], a[24:27], v[48:51], v[96:99]\n"
"buffer_store_dwordx4 v[124:127], v0, s[8:11], s91 offen offset:0 sc1\n"
"v_mfma_f32_16x16x32_bf16 v[252:255], a[24:27], v[236:239], v[252:255]\n"
"s_add_u32 s91, s25, 0x50000\n"
"v_fmac_f32 v155, v174, v183\n"
"v_mfma_f32_16x16x32_bf16 v[188:191], a[20:23], v[48:51], v[188:191]\n"
"v_mfma_f32_16x16x32_bf16 v[88:91], a[20:23], v[236:239], v[88:91]\n"
"s_waitcnt vmcnt(12)\n"
"v_cmp_ne_u64 vcc, s[88:89], v[144:145]\n"
"v_mfma_f32_16x16x32_bf16 v[140:143], a[44:47], v[48:51], v[140:143]\n"
"buffer_store_dwordx4 v[124:127], v0, s[8:11], s91 offen offset:0 sc1\n"
"ds_read_b128 v[244:247], v5 offset:0\n"
"v_mfma_f32_16x16x32_bf16 v[44:47], a[44:47], v[236:239], v[44:47]\n"
"s_add_u32 s91, s74, 0x56000\n"
"ds_read_b128 v[36:39], v20 offset:320\n"
"ds_read_b128 v[112:115], v20 offset:576\n"
"ds_read_b128 v[196:199], v5 offset:5120\n"
"v_mfma_f32_16x16x32_bf16 v[72:75], a[32:35], v[148:151], v[72:75]\n"
"s_cbranch_vccnz Ls3_%=\n"
"Lb3_%=:\n"
"v_mfma_f32_16x16x32_bf16 v[204:207], a[32:35], v[184:187], v[204:207]\n"
"v_mfma_f32_16x16x32_bf16 v[96:99], a[40:43], v[148:151], v[96:99]\n"
"buffer_store_dwordx4 v[124:127], v0, s[8:11], s91 offen offset:0 sc1\n"
"v_mfma_f32_16x16x32_bf16 v[252:255], a[40:43], v[184:187], v[252:255]\n"
"s_add_u32 s91, s74, 0x5e000\n"
"v_mfma_f32_16x16x32_bf16 v[188:191], a[36:39], v[148:151], v[188:191]\n"
"v_mfma_f32_16x16x32_bf16 v[88:91], a[36:39], v[184:187], v[88:91]\n"
"v_mfma_f32_16x16x32_bf16 v[140:143], a[60:63], v[148:151], v[140:143]\n"
"ds_read_b128 v[128:131], v5 offset:1024\n"
"v_mfma_f32_16x16x32_bf16 v[44:47], a[60:63], v[184:187], v[44:47]\n"
"s_waitcnt lgkmcnt(5)\n"
"buffer_store_dwordx4 v[124:127], v0, s[8:11], s91 offen offset:0 sc1\n"
"ds_read_b128 v[48:51], v5 offset:6144\n"
"v_mfma_f32_16x16x32_bf16 v[72:75], a[48:51], v[168:171], v[72:75]\n"
"s_add_u32 s91, s24, 0x66000\n"
"s_waitcnt lgkmcnt(4)\n"
"v_fmac_f32 v96, v68, v36\n"
"v_mfma_f32_16x16x32_bf16 v[204:207], a[48:51], v[248:251], v[204:207]\n"
"v_fmac_f32 v97, v68, v37\n"
"v_fmac_f32 v98, v68, v38\n"
"v_mfma_f32_16x16x32_bf16 v[152:155], a[0:3], v[216:219], v[152:155]\n"
"v_fmac_f32 v99, v68, v39\n"
"v_mfma_f32_16x16x32_bf16 v[212:215], a[0:3], v[220:223], v[212:215]\n"
"buffer_store_dwordx4 v[124:127], v0, s[8:11], s91 offen offset:0 sc1\n"
"v_mfma_f32_16x16x32_bf16 v[160:163], a[8:11], v[216:219], v[160:163]\n"
"s_waitcnt lgkmcnt(3)\n"
"v_fmac_f32 v96, v70, v112\n"
"v_fmac_f32 v97, v70, v113\n"
"v_mfma_f32_16x16x32_bf16 v[164:167], a[8:11], v[220:223], v[164:167]\n"
"v_fmac_f32 v98, v70, v114\n"
"v_fmac_f32 v99, v70, v115\n"
"v_mfma_f32_16x16x32_bf16 v[84:87], a[4:7], v[216:219], v[84:87]\n"
"v_fmac_f32 v212, v208, v56\n"
"v_fmac_f32 v213, v208, v57\n"
"v_mfma_f32_16x16x32_bf16 v[96:99], a[56:59], v[168:171], v[96:99]\n"
"v_fmac_f32 v214, v208, v58\n"
"v_fmac_f32 v215, v208, v59\n"
"v_mfma_f32_16x16x32_bf16 v[252:255], a[56:59], v[248:251], v[252:255]\n"
"v_fmac_f32 v212, v210, v180\n"
"v_fmac_f32 v213, v210, v181\n"
"v_mfma_f32_16x16x32_bf16 v[188:191], a[52:55], v[168:171], v[188:191]\n"
"v_fmac_f32 v214, v210, v182\n"
"v_fmac_f32 v215, v210, v183\n"
"v_mfma_f32_16x16x32_bf16 v[88:91], a[52:55], v[248:251], v[88:91]\n"
"buffer_load_dwordx4 a[240:243], v0, s[8:11], s90 offen offset:2048 sc1\n"
"v_mfma_f32_16x16x32_bf16 v[140:143], a[68:71], v[168:171], v[140:143]\n"
"ds_read_b128 v[236:239], v5 offset:2048\n"
"v_fmac_f32 v252, v92, v36\n"
"v_mfma_f32_16x16x32_bf16 v[44:47], a[68:71], v[248:251], v[44:47]\n"
"ds_read_b128 v[144:147], v5 offset:7168\n"
"v_fmac_f32 v253, v92, v37\n"
"v_mfma_f32_16x16x32_bf16 v[72:75], a[76:79], v[244:247], v[72:75]\n"
"s_waitcnt lgkmcnt(2)\n"
"v_fmac_f32 v254, v92, v38\n"
"v_fmac_f32 v255, v92, v39\n"
"v_mfma_f32_16x16x32_bf16 v[204:207], a[76:79], v[196:199], v[204:207]\n"
"buffer_load_dwordx4 a[12:15], v0, s[8:11], s90 offen offset:3072 sc1\n"
"v_mfma_f32_16x16x32_bf16 v[96:99], a[72:75], v[244:247], v[96:99]\n"
"s_add_u32 s90, s55, 0x4800\n"
"v_fmac_f32 v252, v94, v112\n"
"v_fmac_f32 v253, v94, v113\n"
"v_mfma_f32_16x16x32_bf16 v[176:179], a[4:7], v[220:223], v[176:179]\n"
"v_fmac_f32 v254, v94, v114\n"
"v_fmac_f32 v255, v94, v115\n"
"v_mfma_f32_16x16x32_bf16 v[32:35], a[28:31], v[216:219], v[32:35]\n"
"ds_read_b128 v[148:151], v4 offset:34816\n"
"v_mfma_f32_16x16x32_bf16 v[252:255], a[72:75], v[196:199], v[252:255]\n"
"v_mfma_f32_16x16x32_bf16 v[188:191], a[64:67], v[244:247], v[188:191]\n"
"buffer_load_dwordx4 a[0:3], v0, s[8:11], s90 offen offset:0 sc1\n"
"v_mfma_f32_16x16x32_bf16 v[88:91], a[64:67], v[196:199], v[88:91]\n"
"v_mfma_f32_16x16x32_bf16 v[140:143], a[84:87], v[244:247], v[140:143]\n"
"ds_read_b128 v[184:187], v5 offset:3072\n"
"v_mfma_f32_16x16x32_bf16 v[44:47], a[84:87], v[196:199], v[44:47]\n"
"ds_read_b128 v[124:127], v20 offset:384\n"
"ds_read_b128 v[56:59], v20 offset:640\n"
"ds_read_b128 v[180:183], v5 offset:8192\n"
"v_mfma_f32_16x16x32_bf16 v[72:75], a[92:95], v[128:131], v[72:75]\n"
"buffer_load_dwordx4 a[8:11], v0, s[8:11], s90 offen offset:1024 sc1\n"
"v_mfma_f32_16x16x32_bf16 v[204:207], a[92:95], v[48:51], v[204:207]\n"
"v_mfma_f32_16x16x32_bf16 v[96:99], a[88:91], v[128:131], v[96:99]\n"
"v_mfma_f32_16x16x32_bf16 v[252:255], a[88:91], v[48:51], v[252:255]\n"
"v_mfma_f32_16x16x32_bf16 v[188:191], a[80:83], v[128:131], v[188:191]\n"
"v_mfma_f32_16x16x32_bf16 v[88:91], a[80:83], v[48:51], v[88:91]\n"
"buffer_load_dwordx4 a[4:7], v0, s[8:11], s90 offen offset:2048 sc1\n"
"v_mfma_f32_16x16x32_bf16 v[140:143], a[100:103], v[128:131], v[140:143]\n"
"ds_read_b128 v[168:171], v5 offset:4096\n"
"v_mfma_f32_16x16x32_bf16 v[44:47], a[100:103], v[48:51], v[44:47]\n"
"s_waitcnt lgkmcnt(6)\n"
"ds_read_b128 v[248:251], v5 offset:9216\n"
"v_mfma_f32_16x16x32_bf16 v[72:75], a[108:111], v[236:239], v[72:75]\n"
"s_waitcnt lgkmcnt(4)\n"
"v_fmac_f32 v188, v68, v124\n"
"v_fmac_f32 v189, v68, v125\n"
"v_mfma_f32_16x16x32_bf16 v[204:207], a[108:111], v[144:147], v[204:207]\n"
"v_fmac_f32 v190, v68, v126\n"
"v_fmac_f32 v191, v68, v127\n"
"v_mfma_f32_16x16x32_bf16 v[96:99], a[104:107], v[236:239], v[96:99]\n"
"s_waitcnt lgkmcnt(3)\n"
"v_fmac_f32 v188, v70, v56\n"
"v_fmac_f32 v189, v70, v57\n"
"v_mfma_f32_16x16x32_bf16 v[252:255], a[104:107], v[144:147], v[252:255]\n"
"v_fmac_f32 v190, v70, v58\n"
"v_fmac_f32 v191, v70, v59\n"
"v_mfma_f32_16x16x32_bf16 v[40:43], a[28:31], v[220:223], v[40:43]\n"
"buffer_load_dwordx4 a[28:31], v0, s[8:11], s90 offen offset:3072 sc1\n"
"ds_read_b128 v[36:39], v4 offset:39936\n"
"v_mfma_f32_16x16x32_bf16 v[188:191], a[96:99], v[236:239], v[188:191]\n"
"s_add_u32 s90, s55, 0x5800\n"
"v_mfma_f32_16x16x32_bf16 v[88:91], a[96:99], v[144:147], v[88:91]\n"
"v_mfma_f32_16x16x32_bf16 v[140:143], a[116:119], v[236:239], v[140:143]\n"
"ds_read_b128 v[112:115], v5 offset:20480\n"
"v_mfma_f32_16x16x32_bf16 v[44:47], a[116:119], v[144:147], v[44:47]\n"
"ds_read_b128 v[216:219], v5 offset:25600\n"
"v_mfma_f32_16x16x32_bf16 v[72:75], a[124:127], v[184:187], v[72:75]\n"
"s_waitcnt lgkmcnt(5)\n"
"buffer_load_dwordx4 v0, s[8:11], s22 offen offset:2048 sc1 lds\n"
"v_mfma_f32_16x16x32_bf16 v[204:207], a[124:127], v[180:183], v[204:207]\n"
"v_fmac_f32 v88, v92, v124\n"
"v_fmac_f32 v89, v92, v125\n"
"v_mfma_f32_16x16x32_bf16 v[96:99], a[120:123], v[184:187], v[96:99]\n"
"v_fmac_f32 v90, v92, v126\n"
"v_fmac_f32 v91, v92, v127\n"
"v_mfma_f32_16x16x32_bf16 v[252:255], a[120:123], v[180:183], v[252:255]\n"
"v_fmac_f32 v88, v94, v56\n"
"v_fmac_f32 v89, v94, v57\n"
"v_mfma_f32_16x16x32_bf16 v[188:191], a[112:115], v[184:187], v[188:191]\n"
"buffer_load_dwordx4 v0, s[8:11], s22 offen offset:3072 sc1 lds\n"
"v_mfma_f32_16x16x32_bf16 v[152:155], a[16:19], v[200:203], v[152:155]\n"
"s_add_u32 s22, s52, 0x48000\n"
"v_fmac_f32 v90, v94, v58\n"
"v_fmac_f32 v91, v94, v59\n"
"v_mfma_f32_16x16x32_bf16 v[212:215], a[16:19], v[224:227], v[212:215]\n"
"buffer_load_dwordx4 a[16:19], v0, s[8:11], s90 offen offset:0 sc1\n"
"v_mfma_f32_16x16x32_bf16 v[88:91], a[112:115], v[180:183], v[88:91]\n"
"v_mfma_f32_16x16x32_bf16 v[140:143], a[132:135], v[184:187], v[140:143]\n"
"ds_read_b128 v[244:247], v5 offset:21504\n"
"v_mfma_f32_16x16x32_bf16 v[44:47], a[132:135], v[180:183], v[44:47]\n"
"s_waitcnt lgkmcnt(3)\n"
"ds_read_b128 v[196:199], v20 offset:448\n"
"ds_read_b128 v[128:131], v20 offset:704\n"
"ds_read_b128 v[48:51], v5 offset:26624\n"
"v_mfma_f32_16x16x32_bf16 v[72:75], a[140:143], v[168:171], v[72:75]\n"
"v_mfma_f32_16x16x32_bf16 v[204:207], a[140:143], v[248:251], v[204:207]\n"
"v_mfma_f32_16x16x32_bf16 v[96:99], a[136:139], v[168:171], v[96:99]\n"
"v_mfma_f32_16x16x32_bf16 v[252:255], a[136:139], v[248:251], v[252:255]\n"
"v_mfma_f32_16x16x32_bf16 v[188:191], a[128:131], v[168:171], v[188:191]\n"
"v_mfma_f32_16x16x32_bf16 v[88:91], a[128:131], v[248:251], v[88:91]\n"
"v_mfma_f32_16x16x32_bf16 v[140:143], a[152:155], v[168:171], v[140:143]\n"
"ds_read_b128 v[220:223], v5 offset:22528\n"
"v_mfma_f32_16x16x32_bf16 v[44:47], a[152:155], v[248:251], v[44:47]\n"
"s_waitcnt lgkmcnt(5)\n"
"ds_read_b128 v[236:239], v5 offset:27648\n"
"v_mfma_f32_16x16x32_bf16 v[72:75], a[144:147], v[112:115], v[72:75]\n"
"v_mfma_f32_16x16x32_bf16 v[204:207], a[144:147], v[216:219], v[204:207]\n"
"v_mfma_f32_16x16x32_bf16 v[96:99], a[164:167], v[112:115], v[96:99]\n"
"v_mfma_f32_16x16x32_bf16 v[252:255], a[164:167], v[216:219], v[252:255]\n"
"s_waitcnt lgkmcnt(4)\n"
"v_fmac_f32 v140, v68, v196\n"
"v_fmac_f32 v141, v68, v197\n"
"v_mfma_f32_16x16x32_bf16 v[188:191], a[148:151], v[112:115], v[188:191]\n"
"v_fmac_f32 v142, v68, v198\n"
"v_fmac_f32 v143, v68, v199\n"
"v_mfma_f32_16x16x32_bf16 v[88:91], a[148:151], v[216:219], v[88:91]\n"
"s_waitcnt lgkmcnt(3)\n"
"v_fmac_f32 v140, v70, v128\n"
"v_fmac_f32 v141, v70, v129\n"
"v_mfma_f32_16x16x32_bf16 v[160:163], a[24:27], v[200:203], v[160:163]\n"
"v_fmac_f32 v142, v70, v130\n"
"v_fmac_f32 v143, v70, v131\n"
"v_mfma_f32_16x16x32_bf16 v[164:167], a[24:27], v[224:227], v[164:167]\n"
"buffer_load_dwordx4 a[24:27], v0, s[8:11], s90 offen offset:1024 sc1\n"
"v_mfma_f32_16x16x32_bf16 v[140:143], a[156:159], v[112:115], v[140:143]\n"
"ds_read_b128 v[144:147], v5 offset:23552\n"
"v_mfma_f32_16x16x32_bf16 v[44:47], a[156:159], v[216:219], v[44:47]\n"
"ds_read_b128 v[124:127], v5 offset:28672\n"
"v_mfma_f32_16x16x32_bf16 v[72:75], v[240:243], v[244:247], v[72:75]\n"
"s_waitcnt lgkmcnt(4)\n"
"v_mfma_f32_16x16x32_bf16 v[204:207], v[240:243], v[48:51], v[204:207]\n"
"v_mfma_f32_16x16x32_bf16 v[96:99], v[80:83], v[244:247], v[96:99]\n"
"v_mfma_f32_16x16x32_bf16 v[252:255], v[80:83], v[48:51], v[252:255]\n"
"v_mfma_f32_16x16x32_bf16 v[188:191], v[228:231], v[244:247], v[188:191]\n"
"v_mfma_f32_16x16x32_bf16 v[88:91], v[228:231], v[48:51], v[88:91]\n"
"v_fmac_f32 v44, v92, v196\n"
"v_fmac_f32 v45, v92, v197\n"
"v_mfma_f32_16x16x32_bf16 v[140:143], v[120:123], v[244:247], v[140:143]\n"
"ds_read_b128 v[56:59], v5 offset:24576\n"
"v_fmac_f32 v46, v92, v198\n"
"v_mfma_f32_16x16x32_bf16 v[84:87], a[20:23], v[200:203], v[84:87]\n"
"v_fmac_f32 v47, v92, v199\n"
"v_fmac_f32 v44, v94, v128\n"
"v_mfma_f32_16x16x32_bf16 v[176:179], a[20:23], v[224:227], v[176:179]\n"
"buffer_load_dwordx4 a[20:23], v0, s[8:11], s90 offen offset:2048 sc1\n"
"v_mfma_f32_16x16x32_bf16 v[32:35], a[44:47], v[200:203], v[32:35]\n"
"ds_read_b128 v[184:187], v5 offset:10240\n"
"v_fmac_f32 v45, v94, v129\n"
"v_mfma_f32_16x16x32_bf16 v[40:43], a[44:47], v[224:227], v[40:43]\n"
"buffer_load_dwordx4 a[44:47], v0, s[8:11], s90 offen offset:3072 sc1\n"
"ds_read_b128 v[180:183], v20 offset:320\n"
"ds_read_b128 v[168:171], v20 offset:576\n"
"ds_read_b128 v[248:251], v5 offset:15360\n"
"v_mfma_f32_16x16x32_bf16 v[152:155], a[32:35], v[136:139], v[152:155]\n"
"s_add_u32 s90, s55, 0x6800\n"
"v_fmac_f32 v46, v94, v130\n"
"v_fmac_f32 v47, v94, v131\n"
"v_mfma_f32_16x16x32_bf16 v[212:215], a[32:35], v[192:195], v[212:215]\n"
"buffer_load_dwordx4 a[32:35], v0, s[8:11], s90 offen offset:0 sc1\n"
"v_mfma_f32_16x16x32_bf16 v[44:47], v[120:123], v[48:51], v[44:47]\n"
"s_waitcnt vmcnt(25) lgkmcnt(5)\n"
"ds_read_b128 v[112:115], v5 offset:29696\n"
"v_mfma_f32_16x16x32_bf16 v[72:75], v[64:67], v[220:223], v[72:75]\n"
"s_bitcmp1_b32 s61, 0\n"
"s_cselect_b32 s2, s60, s59\n"
"s_add_u32 s2, s2, 0x6a000\n"
"v_mfma_f32_16x16x32_bf16 v[204:207], v[64:67], v[236:239], v[204:207]\n"
"v_mfma_f32_16x16x32_bf16 v[96:99], v[132:135], v[220:223], v[96:99]\n"
"v_mfma_f32_16x16x32_bf16 v[252:255], v[132:135], v[236:239], v[252:255]\n"
"v_mfma_f32_16x16x32_bf16 v[188:191], v[28:31], v[220:223], v[188:191]\n"
"v_mfma_f32_16x16x32_bf16 v[88:91], v[28:31], v[236:239], v[88:91]\n"
"v_mfma_f32_16x16x32_bf16 v[140:143], v[116:119], v[220:223], v[140:143]\n"
"v_mfma_f32_16x16x32_bf16 v[44:47], v[116:119], v[236:239], v[44:47]\n"
"v_mfma_f32_16x16x32_bf16 v[72:75], v[76:79], v[144:147], v[72:75]\n"
"v_mfma_f32_16x16x32_bf16 v[204:207], v[76:79], v[124:127], v[204:207]\n"
"v_mfma_f32_16x16x32_bf16 v[96:99], v[100:103], v[144:147], v[96:99]\n"
"v_mfma_f32_16x16x32_bf16 v[252:255], v[100:103], v[124:127], v[252:255]\n"
"v_mfma_f32_16x16x32_bf16 v[188:191], a[172:175], v[144:147], v[188:191]\n"
"v_mfma_f32_16x16x32_bf16 v[88:91], a[172:175], v[124:127], v[88:91]\n"
"v_mfma_f32_16x16x32_bf16 v[140:143], a[168:171], v[144:147], v[140:143]\n"
"v_mfma_f32_16x16x32_bf16 v[44:47], a[168:171], v[124:127], v[44:47]\n"
"s_waitcnt vmcnt(22) lgkmcnt(0)\n"
"v_mfma_f32_16x16x32_bf16 v[72:75], a[160:163], v[56:59], v[72:75]\n"
"v_mfma_f32_16x16x32_bf16 v[204:207], a[160:163], v[112:115], v[204:207]\n"
"v_mfma_f32_16x16x32_bf16 v[96:99], a[180:183], v[56:59], v[96:99]\n"
"v_mfma_f32_16x16x32_bf16 v[252:255], a[180:183], v[112:115], v[252:255]\n"
"v_mfma_f32_16x16x32_bf16 v[188:191], a[188:191], v[56:59], v[188:191]\n"
"v_mfma_f32_16x16x32_bf16 v[88:91], a[188:191], v[112:115], v[88:91]\n"
"v_mfma_f32_16x16x32_bf16 v[140:143], a[184:187], v[56:59], v[140:143]\n"
"v_mfma_f32_16x16x32_bf16 v[44:47], a[184:187], v[112:115], v[44:47]\n"
"v_mfma_f32_16x16x32_bf16 v[160:163], a[40:43], v[136:139], v[160:163]\n"
"v_mul_f32 v216, v72, v71\n"
"v_mul_f32 v217, v73, v71\n"
"v_mul_f32 v218, v74, v71\n"
"v_mul_f32 v219, v75, v71\n"
"v_mul_f32 v216, v216, |v72|\n"
"v_mul_f32 v217, v217, |v73|\n"
"v_mfma_f32_16x16x32_bf16 v[164:167], a[40:43], v[192:195], v[164:167]\n"
"buffer_load_dwordx4 a[40:43], v0, s[8:11], s90 offen offset:1024 sc1\n"
"v_mul_f32 v218, v218, |v74|\n"
"v_mul_f32 v219, v219, |v75|\n"
"v_cvt_pk_bf16_f32 v72, v216, v217\n"
"v_cvt_pk_bf16_f32 v73, v218, v219\n"
"v_mfma_f32_16x16x32_bf16 v[84:87], a[36:39], v[136:139], v[84:87]\n"
"v_fmac_f32 v160, v172, v180\n"
"v_fmac_f32 v161, v172, v181\n"
"v_mul_f32 v216, v96, v71\n"
"v_mul_f32 v217, v97, v71\n"
"v_mul_f32 v218, v98, v71\n"
"v_mul_f32 v219, v99, v71\n"
"v_mfma_f32_16x16x32_bf16 v[176:179], a[36:39], v[192:195], v[176:179]\n"
"buffer_load_dwordx4 a[36:39], v0, s[8:11], s90 offen offset:2048 sc1\n"
"v_mul_f32 v216, v216, |v96|\n"
"v_mul_f32 v217, v217, |v97|\n"
"v_mul_f32 v218, v218, |v98|\n"
"v_mul_f32 v219, v219, |v99|\n"
"v_mfma_f32_16x16x32_bf16 v[32:35], a[60:63], v[136:139], v[32:35]\n"
"ds_read_b128 v[244:247], v5 offset:11264\n"
"v_fmac_f32 v162, v172, v182\n"
"v_cvt_pk_bf16_f32 v74, v216, v217\n"
"v_cvt_pk_bf16_f32 v75, v218, v219\n"
"v_pk_max_i16 v72, v72, 0\n"
"v_pk_max_i16 v73, v73, 0\n"
"v_mfma_f32_16x16x32_bf16 v[40:43], a[60:63], v[192:195], v[40:43]\n"
"buffer_load_dwordx4 a[60:63], v0, s[8:11], s90 offen offset:3072 sc1\n"
"ds_read_b128 v[196:199], v5 offset:16384\n"
"v_pk_max_i16 v74, v74, 0\n"
"v_pk_max_i16 v75, v75, 0\n"
"s_add_u32 s0, s69, 0x6a000\n"
"v_mfma_f32_16x16x32_bf16 v[152:155], a[48:51], v[148:151], v[152:155]\n"
"s_add_u32 s90, s55, 0x7800\n"
"v_fmac_f32 v163, v172, v183\n"
"v_fmac_f32 v160, v174, v168\n"
"v_mfma_f32_16x16x32_bf16 v[212:215], a[48:51], v[36:39], v[212:215]\n"
"buffer_load_dwordx4 a[48:51], v0, s[8:11], s90 offen offset:0 sc1\n"
"buffer_store_dwordx4 v[72:75], v0, s[8:11], s0 offen offset:0 sc1\n"
"v_fmac_f32 v161, v174, v169\n"
"v_fmac_f32 v162, v174, v170\n"
"v_fmac_f32 v163, v174, v171\n"
"v_mul_f32 v200, v204, v95\n"
"v_mul_f32 v201, v205, v95\n"
"v_mfma_f32_16x16x32_bf16 v[160:163], a[56:59], v[148:151], v[160:163]\n"
"v_mul_f32 v202, v206, v95\n"
"v_mul_f32 v203, v207, v95\n"
"v_mul_f32 v200, v200, |v204|\n"
"v_mul_f32 v201, v201, |v205|\n"
"v_mul_f32 v202, v202, |v206|\n"
"v_mul_f32 v203, v203, |v207|\n"
"v_mfma_f32_16x16x32_bf16 v[164:167], a[56:59], v[36:39], v[164:167]\n"
"v_cvt_pk_bf16_f32 v204, v200, v201\n"
"v_cvt_pk_bf16_f32 v205, v202, v203\n"
"v_mul_f32 v200, v252, v95\n"
"v_mul_f32 v201, v253, v95\n"
"v_mul_f32 v202, v254, v95\n"
"v_mul_f32 v203, v255, v95\n"
"v_mfma_f32_16x16x32_bf16 v[84:87], a[52:55], v[148:151], v[84:87]\n"
"buffer_load_dwordx4 a[56:59], v0, s[8:11], s90 offen offset:1024 sc1\n"
"v_mul_f32 v200, v200, |v252|\n"
"v_mul_f32 v201, v201, |v253|\n"
"v_mul_f32 v202, v202, |v254|\n"
"v_mul_f32 v203, v203, |v255|\n"
"v_mfma_f32_16x16x32_bf16 v[176:179], a[52:55], v[36:39], v[176:179]\n"
"v_fmac_f32 v164, v208, v180\n"
"v_fmac_f32 v165, v208, v181\n"
"v_cvt_pk_bf16_f32 v206, v200, v201\n"
"v_cvt_pk_bf16_f32 v207, v202, v203\n"
"v_pk_max_i16 v204, v204, 0\n"
"v_pk_max_i16 v205, v205, 0\n"
"v_mfma_f32_16x16x32_bf16 v[32:35], a[68:71], v[148:151], v[32:35]\n"
"buffer_load_dwordx4 a[52:55], v0, s[8:11], s90 offen offset:2048 sc1\n"
"ds_read_b128 v[224:227], v5 offset:12288\n"
"v_pk_max_i16 v206, v206, 0\n"
"v_pk_max_i16 v207, v207, 0\n"
"s_add_u32 s0, s69, 0x77800\n"
"v_mfma_f32_16x16x32_bf16 v[40:43], a[68:71], v[36:39], v[40:43]\n"
"ds_read_b128 v[128:131], v5 offset:17408\n"
"buffer_store_dwordx4 v[204:207], v0, s[8:11], s0 offen offset:0 sc1\n"
"v_mfma_f32_16x16x32_bf16 v[152:155], a[76:79], v[184:187], v[152:155]\n"
"v_fmac_f32 v166, v208, v182\n"
"v_fmac_f32 v167, v208, v183\n"
"v_mul_f32 v48, v188, v71\n"
"v_mul_f32 v49, v189, v71\n"
"v_mul_f32 v50, v190, v71\n"
"v_mul_f32 v51, v191, v71\n"
"v_mfma_f32_16x16x32_bf16 v[212:215], a[76:79], v[248:251], v[212:215]\n"
"v_fmac_f32 v164, v210, v168\n"
"v_fmac_f32 v165, v210, v169\n"
"v_mul_f32 v48, v48, |v188|\n"
"v_mul_f32 v49, v49, |v189|\n"
"v_mul_f32 v50, v50, |v190|\n"
"v_mul_f32 v51, v51, |v191|\n"
"v_mfma_f32_16x16x32_bf16 v[160:163], a[72:75], v[184:187], v[160:163]\n"
"v_fmac_f32 v166, v210, v170\n"
"v_fmac_f32 v167, v210, v171\n"
"v_cvt_pk_bf16_f32 v188, v48, v49\n"
"v_cvt_pk_bf16_f32 v189, v50, v51\n"
"v_mul_f32 v48, v140, v71\n"
"v_mul_f32 v49, v141, v71\n"
"v_mfma_f32_16x16x32_bf16 v[164:167], a[72:75], v[248:251], v[164:167]\n"
"buffer_load_dwordx4 a[68:71], v0, s[8:11], s90 offen offset:3072 sc1\n"
"v_mul_f32 v50, v142, v71\n"
"v_mul_f32 v51, v143, v71\n"
"v_mul_f32 v48, v48, |v140|\n"
"v_mul_f32 v49, v49, |v141|\n"
"v_mfma_f32_16x16x32_bf16 v[84:87], a[64:67], v[184:187], v[84:87]\n"
"s_add_u32 s90, s55, 0x8800\n"
"v_mul_f32 v50, v50, |v142|\n"
"v_mul_f32 v51, v51, |v143|\n"
"v_cvt_pk_bf16_f32 v190, v48, v49\n"
"v_cvt_pk_bf16_f32 v191, v50, v51\n"
"v_pk_max_i16 v188, v188, 0\n"
"v_pk_max_i16 v189, v189, 0\n"
"v_mfma_f32_16x16x32_bf16 v[176:179], a[64:67], v[248:251], v[176:179]\n"
"v_pk_max_i16 v190, v190, 0\n"
"v_pk_max_i16 v191, v191, 0\n"
"s_add_u32 s0, s70, 0x6a000\n"
"v_mfma_f32_16x16x32_bf16 v[32:35], a[84:87], v[184:187], v[32:35]\n"
"buffer_load_dwordx4 a[76:79], v0, s[8:11], s90 offen offset:0 sc1\n"
"ds_read_b128 v[220:223], v5 offset:13312\n"
"buffer_store_dwordx4 v[188:191], v0, s[8:11], s0 offen offset:0 sc1\n"
"v_mfma_f32_16x16x32_bf16 v[40:43], a[84:87], v[248:251], v[40:43]\n"
"s_waitcnt vmcnt(22) lgkmcnt(1)\n"
"ds_read_b128 v[236:239], v20 offset:384\n"
"ds_read_b128 v[144:147], v20 offset:640\n"
"ds_read_b128 v[124:127], v5 offset:18432\n"
"v_mul_f32 v56, v88, v95\n"
"v_mul_f32 v57, v89, v95\n"
"v_mul_f32 v58, v90, v95\n"
"v_mfma_f32_16x16x32_bf16 v[152:155], a[92:95], v[244:247], v[152:155]\n"
"v_mul_f32 v59, v91, v95\n"
"v_mul_f32 v56, v56, |v88|\n"
"v_mul_f32 v57, v57, |v89|\n"
"v_mul_f32 v58, v58, |v90|\n"
"v_mul_f32 v59, v59, |v91|\n"
"v_cvt_pk_bf16_f32 v88, v56, v57\n"
"v_mfma_f32_16x16x32_bf16 v[212:215], a[92:95], v[196:199], v[212:215]\n"
"v_cvt_pk_bf16_f32 v89, v58, v59\n"
"v_mul_f32 v56, v44, v95\n"
"v_mul_f32 v57, v45, v95\n"
"v_mul_f32 v58, v46, v95\n"
"v_mul_f32 v59, v47, v95\n"
"v_mul_f32 v56, v56, |v44|\n"
"v_mfma_f32_16x16x32_bf16 v[160:163], a[88:91], v[244:247], v[160:163]\n"
"v_mul_f32 v57, v57, |v45|\n"
"v_mul_f32 v58, v58, |v46|\n"
"v_mul_f32 v59, v59, |v47|\n"
"v_cvt_pk_bf16_f32 v90, v56, v57\n"
"v_cvt_pk_bf16_f32 v91, v58, v59\n"
"v_pk_max_i16 v88, v88, 0\n"
"v_mfma_f32_16x16x32_bf16 v[164:167], a[88:91], v[196:199], v[164:167]\n"
"buffer_load_dwordx4 a[72:75], v0, s[8:11], s90 offen offset:1024 sc1\n"
"v_pk_max_i16 v89, v89, 0\n"
"v_pk_max_i16 v90, v90, 0\n"
"v_pk_max_i16 v91, v91, 0\n"
"s_add_u32 s0, s70, 0x77800\n"
"v_mfma_f32_16x16x32_bf16 v[84:87], a[80:83], v[244:247], v[84:87]\n"
"buffer_store_dwordx4 v[88:91], v0, s[8:11], s0 offen offset:0 sc1\n"
"s_cmp_eq_u32 s62, 0\n"
"s_cselect_b64 exec, -1, 0\n"
"v_swap_b32 v72, v204\n"
"v_swap_b32 v73, v205\n"
"v_swap_b32 v74, v206\n"
"v_swap_b32 v75, v207\n"
"s_mov_b64 exec, -1\n"
"v_mfma_f32_16x16x32_bf16 v[176:179], a[80:83], v[196:199], v[176:179]\n"
"s_waitcnt vmcnt(21) lgkmcnt(2)\n"
"v_fmac_f32 v84, v172, v236\n"
"v_fmac_f32 v85, v172, v237\n"
"s_cmp_eq_u32 s62, 0\n"
"s_cselect_b64 exec, -1, 0\n"
"v_swap_b32 v188, v88\n"
"v_swap_b32 v189, v89\n"
"v_swap_b32 v190, v90\n"
"v_swap_b32 v191, v91\n"
"s_mov_b64 exec, -1\n"
"v_mfma_f32_16x16x32_bf16 v[32:35], a[100:103], v[244:247], v[32:35]\n"
"buffer_load_dwordx4 a[64:67], v0, s[8:11], s90 offen offset:2048 sc1\n"
"ds_read_b128 v[112:115], v5 offset:14336\n"
"v_mfma_f32_16x16x32_bf16 v[40:43], a[100:103], v[196:199], v[40:43]\n"
"ds_read_b128 v[96:99], v5 offset:19456\n"
"v_fmac_f32 v86, v172, v238\n"
"v_mfma_f32_16x16x32_bf16 v[152:155], a[108:111], v[224:227], v[152:155]\n"
"buffer_load_dwordx4 a[84:87], v0, s[8:11], s90 offen offset:3072 sc1\n"
"v_mfma_f32_16x16x32_bf16 v[212:215], a[108:111], v[128:131], v[212:215]\n"
"s_add_u32 s90, s55, 0x9800\n"
"v_fmac_f32 v87, v172, v239\n"
"v_mfma_f32_16x16x32_bf16 v[160:163], a[104:107], v[224:227], v[160:163]\n"
"s_waitcnt vmcnt(22) lgkmcnt(2)\n"
"v_fmac_f32 v84, v174, v144\n"
"v_fmac_f32 v85, v174, v145\n"
"v_mfma_f32_16x16x32_bf16 v[164:167], a[104:107], v[128:131], v[164:167]\n"
"v_fmac_f32 v86, v174, v146\n"
"v_fmac_f32 v87, v174, v147\n"
"v_mfma_f32_16x16x32_bf16 v[60:63], a[176:179], v[72:75], v[60:63]\n"
"s_nop 0\n"
"v_mfma_f32_16x16x32_bf16 v[84:87], a[96:99], v[224:227], v[84:87]\n"
"buffer_load_dwordx4 a[92:95], v0, s[8:11], s90 offen offset:0 sc1\n"
"v_mfma_f32_16x16x32_bf16 v[176:179], a[96:99], v[128:131], v[176:179]\n"
"v_mfma_f32_16x16x32_bf16 v[32:35], a[116:119], v[224:227], v[32:35]\n"
"ds_read_b128 v[136:139], v5 offset:30720\n"
"v_mfma_f32_16x16x32_bf16 v[40:43], a[116:119], v[128:131], v[40:43]\n"
"ds_read_b128 v[216:219], v5 offset:35840\n"
"v_mfma_f32_16x16x32_bf16 v[152:155], a[124:127], v[220:223], v[152:155]\n"
"v_mfma_f32_16x16x32_bf16 v[212:215], a[124:127], v[124:127], v[212:215]\n"
"buffer_load_dwordx4 a[88:91], v0, s[8:11], s90 offen offset:1024 sc1\n"
"v_mfma_f32_16x16x32_bf16 v[160:163], a[120:123], v[220:223], v[160:163]\n"
"v_fmac_f32 v176, v208, v236\n"
"v_fmac_f32 v177, v208, v237\n"
"v_mfma_f32_16x16x32_bf16 v[164:167], a[120:123], v[124:127], v[164:167]\n"
"v_fmac_f32 v178, v208, v238\n"
"v_fmac_f32 v179, v208, v239\n"
"v_mfma_f32_16x16x32_bf16 v[84:87], a[112:115], v[220:223], v[84:87]\n"
"v_fmac_f32 v176, v210, v144\n"
"v_fmac_f32 v177, v210, v145\n"
"v_mfma_f32_16x16x32_bf16 v[52:55], a[196:199], v[72:75], v[52:55]\n"
"buffer_load_dwordx4 a[80:83], v0, s[8:11], s90 offen offset:2048 sc1\n"
"v_mfma_f32_16x16x32_bf16 v[192:195], a[204:207], v[72:75], 0\n"
"v_fmac_f32 v178, v210, v146\n"
"v_fmac_f32 v179, v210, v147\n"
"v_mfma_f32_16x16x32_bf16 v[252:255], a[200:203], v[72:75], 0\n"
"s_nop 0\n"
"v_mfma_f32_16x16x32_bf16 v[176:179], a[112:115], v[124:127], v[176:179]\n"
"v_mfma_f32_16x16x32_bf16 v[32:35], a[132:135], v[220:223], v[32:35]\n"
"ds_read_b128 v[200:203], v5 offset:31744\n"
"v_mfma_f32_16x16x32_bf16 v[40:43], a[132:135], v[124:127], v[40:43]\n"
"s_waitcnt vmcnt(22) lgkmcnt(3)\n"
"buffer_load_dwordx4 a[100:103], v0, s[8:11], s90 offen offset:3072 sc1\n"
"ds_read_b128 v[148:151], v20 offset:448\n"
"ds_read_b128 v[36:39], v20 offset:704\n"
"ds_read_b128 v[180:183], v5 offset:36864\n"
"v_mfma_f32_16x16x32_bf16 v[152:155], a[140:143], v[112:115], v[152:155]\n"
"s_add_u32 s90, s55, 0xa800\n"
"v_mfma_f32_16x16x32_bf16 v[212:215], a[140:143], v[96:99], v[212:215]\n"
"v_mfma_f32_16x16x32_bf16 v[160:163], a[136:139], v[112:115], v[160:163]\n"
"v_mfma_f32_16x16x32_bf16 v[164:167], a[136:139], v[96:99], v[164:167]\n"
"buffer_load_dwordx4 a[108:111], v0, s[8:11], s90 offen offset:0 sc1\n"
"v_mfma_f32_16x16x32_bf16 v[84:87], a[128:131], v[112:115], v[84:87]\n"
"v_mfma_f32_16x16x32_bf16 v[176:179], a[128:131], v[96:99], v[176:179]\n"
"v_mfma_f32_16x16x32_bf16 v[32:35], a[152:155], v[112:115], v[32:35]\n"
"ds_read_b128 v[168:171], v5 offset:32768\n"
"v_mfma_f32_16x16x32_bf16 v[40:43], a[152:155], v[96:99], v[40:43]\n"
"s_waitcnt vmcnt(23) lgkmcnt(5)\n"
"buffer_load_dwordx4 a[104:107], v0, s[8:11], s90 offen offset:1024 sc1\n"
"ds_read_b128 v[68:71], v5 offset:37888\n"
"v_mfma_f32_16x16x32_bf16 v[152:155], a[144:147], v[136:139], v[152:155]\n"
"v_mfma_f32_16x16x32_bf16 v[212:215], a[144:147], v[216:219], v[212:215]\n"
"v_mfma_f32_16x16x32_bf16 v[160:163], a[164:167], v[136:139], v[160:163]\n"
"s_waitcnt lgkmcnt(4)\n"
"v_fmac_f32 v32, v172, v148\n"
"v_fmac_f32 v33, v172, v149\n"
"v_mfma_f32_16x16x32_bf16 v[164:167], a[164:167], v[216:219], v[164:167]\n"
"v_fmac_f32 v34, v172, v150\n"
"v_fmac_f32 v35, v172, v151\n"
"v_mfma_f32_16x16x32_bf16 v[84:87], a[148:151], v[136:139], v[84:87]\n"
"buffer_load_dwordx4 a[96:99], v0, s[8:11], s90 offen offset:2048 sc1\n"
"v_mfma_f32_16x16x32_bf16 v[176:179], a[148:151], v[216:219], v[176:179]\n"
"s_waitcnt lgkmcnt(3)\n"
"v_fmac_f32 v32, v174, v36\n"
"v_fmac_f32 v33, v174, v37\n"
"v_mfma_f32_16x16x32_bf16 v[232:235], a[192:195], v[72:75], v[232:235]\n"
"v_fmac_f32 v34, v174, v38\n"
"v_fmac_f32 v35, v174, v39\n"
"v_mfma_f32_16x16x32_bf16 v[140:143], a[176:179], v[204:207], 0\n"
"s_nop 0\n"
"v_mfma_f32_16x16x32_bf16 v[32:35], a[156:159], v[136:139], v[32:35]\n"
"buffer_load_dwordx4 a[116:119], v0, s[8:11], s90 offen offset:3072 sc1\n"
"ds_read_b128 v[48:51], v5 offset:33792\n"
"v_mfma_f32_16x16x32_bf16 v[40:43], a[156:159], v[216:219], v[40:43]\n"
"ds_read_b128 v[184:187], v5 offset:38912\n"
"v_add_u32 v248, s20, v0\n"
"v_mfma_f32_16x16x32_bf16 v[152:155], v[240:243], v[200:203], v[152:155]\n"
"s_waitcnt vmcnt(25) lgkmcnt(4)\n"
"ds_read_b128 a[124:127], v248 offset:0\n"
"ds_read_b128 a[120:123], v248 offset:1024\n"
"v_mfma_f32_16x16x32_bf16 v[212:215], v[240:243], v[180:183], v[212:215]\n"
"ds_read_b128 a[112:115], v248 offset:2048\n"
"ds_read_b128 a[132:135], v248 offset:3072\n"
"v_mfma_f32_16x16x32_bf16 v[160:163], v[80:83], v[200:203], v[160:163]\n"
"ds_read_b128 a[140:143], v248 offset:4096\n"
"ds_read_b128 a[136:139], v248 offset:5120\n"
"v_mfma_f32_16x16x32_bf16 v[164:167], v[80:83], v[180:183], v[164:167]\n"
"v_add_u32 v249, s21, v0\n"
"v_fmac_f32 v40, v208, v148\n"
"v_mfma_f32_16x16x32_bf16 v[84:87], v[228:231], v[200:203], v[84:87]\n"
"ds_read_b128 a[128:131], v249 offset:0\n"
"ds_read_b128 a[152:155], v249 offset:1024\n"
"v_mfma_f32_16x16x32_bf16 v[176:179], v[228:231], v[180:183], v[176:179]\n"
"ds_read_b128 a[144:147], v249 offset:2048\n"
"ds_read_b128 a[164:167], v249 offset:3072\n"
"v_mfma_f32_16x16x32_bf16 v[32:35], v[120:123], v[200:203], v[32:35]\n"
"ds_read_b128 a[148:151], v249 offset:4096\n"
"v_fmac_f32 v41, v208, v149\n"
"s_waitcnt vmcnt(23) lgkmcnt(13)\n"
"ds_read_b128 v[92:95], v5 offset:34816\n"
"ds_read_b128 a[156:159], v249 offset:5120\n"
"v_mfma_f32_16x16x32_bf16 v[44:47], a[196:199], v[204:207], 0\n"
"v_fmac_f32 v42, v208, v150\n"
"v_fmac_f32 v43, v208, v151\n"
"v_mfma_f32_16x16x32_bf16 v[56:59], a[204:207], v[204:207], 0\n"
"v_fmac_f32 v40, v210, v36\n"
"v_fmac_f32 v41, v210, v37\n"
"v_mfma_f32_16x16x32_bf16 v[244:247], a[200:203], v[204:207], 0\n"
"v_fmac_f32 v42, v210, v38\n"
"v_fmac_f32 v43, v210, v39\n"
"v_mfma_f32_16x16x32_bf16 v[196:199], a[192:195], v[204:207], 0\n"
"s_nop 0\n"
"v_mfma_f32_16x16x32_bf16 v[40:43], v[120:123], v[180:183], v[40:43]\n"
"s_waitcnt lgkmcnt(14)\n"
"ds_read_b128 v[224:227], v5 offset:39936\n"
"v_mfma_f32_16x16x32_bf16 v[152:155], v[64:67], v[168:171], v[152:155]\n"
"s_bitcmp1_b32 s61, 0\n"
"s_cselect_b32 s4, s60, s59\n"
"s_add_u32 s4, s4, 0x85000\n"
"v_mfma_f32_16x16x32_bf16 v[212:215], v[64:67], v[68:71], v[212:215]\n"
"v_mfma_f32_16x16x32_bf16 v[160:163], v[132:135], v[168:171], v[160:163]\n"
"v_mfma_f32_16x16x32_bf16 v[164:167], v[132:135], v[68:71], v[164:167]\n"
"s_waitcnt lgkmcnt(11)\n"
"ds_read_b128 v[128:131], v249 offset:6144\n"
"v_mfma_f32_16x16x32_bf16 v[84:87], v[28:31], v[168:171], v[84:87]\n"
"v_mfma_f32_16x16x32_bf16 v[176:179], v[28:31], v[68:71], v[176:179]\n"
"ds_read_b128 v[236:239], v249 offset:7168\n"
"s_add_u32 s90, s55, 0xf000\n"
"buffer_load_dwordx4 v[144:147], v0, s[8:11], s90 offen offset:0 sc1\n"
"v_mfma_f32_16x16x32_bf16 v[32:35], v[116:119], v[168:171], v[32:35]\n"
"buffer_load_dwordx4 v[220:223], v0, s[8:11], s90 offen offset:1024 sc1\n"
"v_mfma_f32_16x16x32_bf16 v[40:43], v[116:119], v[68:71], v[40:43]\n"
"buffer_load_dwordx4 v[124:127], v16, s[8:11], s2 offen sc1\n"
"v_mfma_f32_16x16x32_bf16 v[152:155], v[76:79], v[48:51], v[152:155]\n"
"v_mfma_f32_16x16x32_bf16 v[212:215], v[76:79], v[184:187], v[212:215]\n"
"v_mfma_f32_16x16x32_bf16 v[160:163], v[100:103], v[48:51], v[160:163]\n"
"v_mfma_f32_16x16x32_bf16 v[164:167], v[100:103], v[184:187], v[164:167]\n"
"buffer_load_dwordx4 v[112:115], v17, s[8:11], s2 offen sc1\n"
"v_mfma_f32_16x16x32_bf16 v[84:87], a[172:175], v[48:51], v[84:87]\n"
"s_bitcmp1_b32 s61, 1\n"
"s_cselect_b32 s2, s60, s59\n"
"s_add_u32 s2, s2, 0x6a400\n"
"v_mfma_f32_16x16x32_bf16 v[176:179], a[172:175], v[184:187], v[176:179]\n"
"v_mfma_f32_16x16x32_bf16 v[32:35], a[168:171], v[48:51], v[32:35]\n"
"v_mfma_f32_16x16x32_bf16 v[40:43], a[168:171], v[184:187], v[40:43]\n"
"s_waitcnt lgkmcnt(2)\n"
"buffer_load_dwordx4 v[96:99], v16, s[8:11], s2 offen sc1\n"
"v_mfma_f32_16x16x32_bf16 v[152:155], a[160:163], v[92:95], v[152:155]\n"
"v_mfma_f32_16x16x32_bf16 v[212:215], a[160:163], v[224:227], v[212:215]\n"
"v_mfma_f32_16x16x32_bf16 v[160:163], a[180:183], v[92:95], v[160:163]\n"
"v_mfma_f32_16x16x32_bf16 v[164:167], a[180:183], v[224:227], v[164:167]\n"
"v_mfma_f32_16x16x32_bf16 v[84:87], a[188:191], v[92:95], v[84:87]\n"
"buffer_load_dwordx4 v[72:75], v17, s[8:11], s2 offen sc1\n"
"v_mfma_f32_16x16x32_bf16 v[176:179], a[188:191], v[224:227], v[176:179]\n"
"s_bitcmp1_b32 s61, 2\n"
"s_cselect_b32 s2, s60, s59\n"
"s_add_u32 s2, s2, 0x6a800\n"
"v_mfma_f32_16x16x32_bf16 v[32:35], a[184:187], v[92:95], v[32:35]\n"
"v_mul_f32 v136, v152, v175\n"
"v_mul_f32 v137, v153, v175\n"
"v_mfma_f32_16x16x32_bf16 v[40:43], a[184:187], v[224:227], v[40:43]\n"
"v_mul_f32 v138, v154, v175\n"
"v_mul_f32 v139, v155, v175\n"
"v_mul_f32 v136, v136, |v152|\n"
"v_mul_f32 v137, v137, |v153|\n"
"v_mul_f32 v138, v138, |v154|\n"
"v_mul_f32 v139, v139, |v155|\n"
"v_mfma_f32_16x16x32_bf16 v[60:63], a[212:215], v[188:191], v[60:63]\n"
"buffer_load_dwordx4 v[216:219], v16, s[8:11], s2 offen sc1\n"
"v_cvt_pk_bf16_f32 v152, v136, v137\n"
"v_cvt_pk_bf16_f32 v153, v138, v139\n"
"v_mul_f32 v136, v160, v175\n"
"v_mul_f32 v137, v161, v175\n"
"v_mfma_f32_16x16x32_bf16 v[52:55], a[220:223], v[188:191], v[52:55]\n"
"v_mul_f32 v138, v162, v175\n"
"v_mul_f32 v139, v163, v175\n"
"v_mul_f32 v136, v136, |v160|\n"
"v_mul_f32 v137, v137, |v161|\n"
"v_mul_f32 v138, v138, |v162|\n"
"v_mul_f32 v139, v139, |v163|\n"
"v_mfma_f32_16x16x32_bf16 v[192:195], a[216:219], v[188:191], v[192:195]\n"
"buffer_load_dwordx4 v[240:243], v17, s[8:11], s2 offen sc1\n"
"v_cvt_pk_bf16_f32 v154, v136, v137\n"
"v_cvt_pk_bf16_f32 v155, v138, v139\n"
"v_pk_max_i16 v152, v152, 0\n"
"v_pk_max_i16 v153, v153, 0\n"
"v_mfma_f32_16x16x32_bf16 v[252:255], a[208:211], v[188:191], v[252:255]\n"
"s_bitcmp1_b32 s61, 3\n"
"s_cselect_b32 s2, s60, s59\n"
"s_add_u32 s2, s2, 0x6ac00\n"
"v_pk_max_i16 v154, v154, 0\n"
"v_pk_max_i16 v155, v155, 0\n"
"s_add_u32 s1, s69, 0x85000\n"
"v_mfma_f32_16x16x32_bf16 v[232:235], a[228:231], v[188:191], v[232:235]\n"
"v_mfma_f32_16x16x32_bf16 v[140:143], a[212:215], v[88:91], v[140:143]\n"
"buffer_load_dwordx4 v[80:83], v16, s[8:11], s2 offen sc1\n"
"v_mfma_f32_16x16x32_bf16 v[44:47], a[220:223], v[88:91], v[44:47]\n"
"v_mfma_f32_16x16x32_bf16 v[56:59], a[216:219], v[88:91], v[56:59]\n"
"v_mfma_f32_16x16x32_bf16 v[244:247], a[208:211], v[88:91], v[244:247]\n"
"v_mfma_f32_16x16x32_bf16 v[196:199], a[228:231], v[88:91], v[196:199]\n"
"s_barrier\n"
"buffer_load_dwordx4 v[228:231], v17, s[8:11], s2 offen sc1\n"
"s_bitcmp1_b32 s61, 4\n"
"s_cselect_b32 s2, s60, s59\n"
"s_add_u32 s2, s2, 0x6b000\n"
"buffer_load_dwordx4 v[200:203], v16, s[8:11], s2 offen sc1\n"
"buffer_load_dwordx4 v[148:151], v17, s[8:11], s2 offen sc1\n"
"s_bitcmp1_b32 s61, 5\n"
"s_cselect_b32 s2, s60, s59\n"
"s_add_u32 s2, s2, 0x6b400\n"
"buffer_load_dwordx4 v[36:39], v16, s[8:11], s2 offen sc1\n"
"buffer_load_dwordx4 v[204:207], v17, s[8:11], s2 offen sc1\n"
"s_bitcmp1_b32 s61, 6\n"
"s_cselect_b32 s2, s60, s59\n"
"s_add_u32 s2, s2, 0x6b800\n"
"s_waitcnt vmcnt(11) lgkmcnt(0)\n"
"v_max3_u32 v120, v124, v125, v126\n"
"buffer_store_dwordx4 v[152:155], v0, s[8:11], s1 offen offset:0 sc1\n"
"v_mul_f32 v180, v212, v211\n"
"v_mul_f32 v181, v213, v211\n"
"v_mul_f32 v182, v214, v211\n"
"v_mul_f32 v183, v215, v211\n"
"v_mul_f32 v180, v180, |v212|\n"
"v_mul_f32 v181, v181, |v213|\n"
"v_mul_f32 v182, v182, |v214|\n"
"v_mul_f32 v183, v183, |v215|\n"
"s_waitcnt vmcnt(11)\n"
"v_max3_u32 v120, v120, v127, v112\n"
"v_max3_u32 v120, v120, v113, v114\n"
"v_max_u32 v120, v120, v115\n"
"v_cmp_eq_u32 vcc, -1, v120\n"
"v_cvt_pk_bf16_f32 v212, v180, v181\n"
"v_cvt_pk_bf16_f32 v213, v182, v183\n"
"v_mul_f32 v180, v164, v211\n"
"v_mul_f32 v181, v165, v211\n"
"v_mul_f32 v182, v166, v211\n"
"s_cbranch_vccnz Ls14_%=\n"
"Lb14_%=:\n"
"v_mfma_f32_16x16x32_bf16 v[60:63], a[236:239], v[124:127], v[60:63]\n"
"v_mul_f32 v183, v167, v211\n"
"v_mul_f32 v180, v180, |v164|\n"
"v_mul_f32 v181, v181, |v165|\n"
"v_mul_f32 v182, v182, |v166|\n"
"v_mul_f32 v183, v183, |v167|\n"
"v_cvt_pk_bf16_f32 v214, v180, v181\n"
"v_mfma_f32_16x16x32_bf16 v[52:55], a[232:235], v[124:127], v[52:55]\n"
"buffer_load_dwordx4 a[172:175], v0, s[8:11], s90 offen offset:2048 sc1\n"
"v_cvt_pk_bf16_f32 v215, v182, v183\n"
"v_pk_max_i16 v212, v212, 0\n"
"v_pk_max_i16 v213, v213, 0\n"
"v_pk_max_i16 v214, v214, 0\n"
"v_mfma_f32_16x16x32_bf16 v[192:195], a[224:227], v[124:127], v[192:195]\n"
"s_waitcnt vmcnt(11)\n"
"v_max3_u32 v64, v96, v97, v98\n"
"v_pk_max_i16 v215, v215, 0\n"
"s_add_u32 s1, s69, 0x92800\n"
"v_mfma_f32_16x16x32_bf16 v[252:255], a[244:247], v[124:127], v[252:255]\n"
"buffer_store_dwordx4 v[212:215], v0, s[8:11], s1 offen offset:0 sc1\n"
"v_mul_f32 v132, v84, v175\n"
"v_mfma_f32_16x16x32_bf16 v[232:235], a[252:255], v[124:127], v[232:235]\n"
"s_waitcnt vmcnt(11)\n"
"v_max3_u32 v64, v64, v99, v72\n"
"v_max3_u32 v64, v64, v73, v74\n"
"v_mul_f32 v133, v85, v175\n"
"v_mul_f32 v134, v86, v175\n"
"v_mul_f32 v135, v87, v175\n"
"v_mul_f32 v132, v132, |v84|\n"
"v_mfma_f32_16x16x32_bf16 v[140:143], a[236:239], v[112:115], v[140:143]\n"
"buffer_load_dwordx4 v[28:31], v16, s[8:11], s2 offen sc1\n"
"v_mul_f32 v133, v133, |v85|\n"
"v_mul_f32 v134, v134, |v86|\n"
"v_mul_f32 v135, v135, |v87|\n"
"v_cvt_pk_bf16_f32 v84, v132, v133\n"
"v_mfma_f32_16x16x32_bf16 v[44:47], a[232:235], v[112:115], v[44:47]\n"
"v_max_u32 v64, v64, v75\n"
"v_cmp_eq_u32 vcc, -1, v64\n"
"v_cvt_pk_bf16_f32 v85, v134, v135\n"
"v_mul_f32 v132, v32, v175\n"
"v_mul_f32 v133, v33, v175\n"
"v_mul_f32 v134, v34, v175\n"
"v_mfma_f32_16x16x32_bf16 v[56:59], a[224:227], v[112:115], v[56:59]\n"
"s_cbranch_vccnz Ls15_%=\n"
"Lb15_%=:\n"
"v_mul_f32 v135, v35, v175\n"
"v_mul_f32 v132, v132, |v32|\n"
"v_mul_f32 v133, v133, |v33|\n"
"v_mul_f32 v134, v134, |v34|\n"
"v_mul_f32 v135, v135, |v35|\n"
"v_mfma_f32_16x16x32_bf16 v[244:247], a[244:247], v[112:115], v[244:247]\n"
"buffer_load_dwordx4 a[168:171], v0, s[8:11], s90 offen offset:3072 sc1\n"
"v_cvt_pk_bf16_f32 v86, v132, v133\n"
"v_cvt_pk_bf16_f32 v87, v134, v135\n"
"v_pk_max_i16 v84, v84, 0\n"
"v_pk_max_i16 v85, v85, 0\n"
"v_mfma_f32_16x16x32_bf16 v[196:199], a[252:255], v[112:115], v[196:199]\n"
"s_add_u32 s90, s55, 0x10000\n"
"v_pk_max_i16 v86, v86, 0\n"
"v_pk_max_i16 v87, v87, 0\n"
"s_add_u32 s1, s70, 0x85000\n"
"v_mfma_f32_16x16x32_bf16 v[60:63], a[248:251], v[96:99], v[60:63]\n"
"buffer_load_dwordx4 v[248:251], v17, s[8:11], s2 offen sc1\n"
"v_mfma_f32_16x16x32_bf16 v[52:55], a[240:243], v[96:99], v[52:55]\n"
"s_bitcmp1_b32 s61, 7\n"
"s_cselect_b32 s2, s60, s59\n"
"s_add_u32 s2, s2, 0x6bc00\n"
"v_mfma_f32_16x16x32_bf16 v[192:195], a[12:15], v[96:99], v[192:195]\n"
"s_waitcnt vmcnt(13)\n"
"v_max3_u32 v168, v216, v217, v218\n"
"v_mfma_f32_16x16x32_bf16 v[252:255], a[0:3], v[96:99], v[252:255]\n"
"s_waitcnt vmcnt(12)\n"
"v_max3_u32 v168, v168, v219, v240\n"
"v_max3_u32 v168, v168, v241, v242\n"
"v_mfma_f32_16x16x32_bf16 v[232:235], a[8:11], v[96:99], v[232:235]\n"
"buffer_load_dwordx4 v[116:119], v16, s[8:11], s2 offen sc1\n"
"v_mfma_f32_16x16x32_bf16 v[140:143], a[248:251], v[72:75], v[140:143]\n"
"v_max_u32 v168, v168, v243\n"
"v_cmp_eq_u32 vcc, -1, v168\n"
"v_mfma_f32_16x16x32_bf16 v[44:47], a[240:243], v[72:75], v[44:47]\n"
"v_mfma_f32_16x16x32_bf16 v[56:59], a[12:15], v[72:75], v[56:59]\n"
"s_cbranch_vccnz Ls16_%=\n"
"Lb16_%=:\n"
"v_mfma_f32_16x16x32_bf16 v[244:247], a[0:3], v[72:75], v[244:247]\n"
"s_waitcnt vmcnt(12)\n"
"v_max3_u32 v68, v80, v81, v82\n"
"v_mfma_f32_16x16x32_bf16 v[196:199], a[8:11], v[72:75], v[196:199]\n"
"buffer_load_dwordx4 v[76:79], v17, s[8:11], s2 offen sc1\n"
"v_mfma_f32_16x16x32_bf16 v[60:63], a[4:7], v[216:219], v[60:63]\n"
"s_bitcmp1_b32 s61, 8\n"
"s_cselect_b32 s2, s60, s59\n"
"s_add_u32 s2, s2, 0x6c000\n"
"v_mfma_f32_16x16x32_bf16 v[52:55], a[28:31], v[216:219], v[52:55]\n"
"s_waitcnt vmcnt(12)\n"
"v_max3_u32 v68, v68, v83, v228\n"
"v_max3_u32 v68, v68, v229, v230\n"
"v_mfma_f32_16x16x32_bf16 v[192:195], a[16:19], v[216:219], v[192:195]\n"
"v_max_u32 v68, v68, v231\n"
"v_cmp_eq_u32 vcc, -1, v68\n"
"v_mfma_f32_16x16x32_bf16 v[252:255], a[24:27], v[216:219], v[252:255]\n"
"buffer_store_dwordx4 v[84:87], v0, s[8:11], s1 offen offset:0 sc1\n"
"v_mul_f32 v100, v176, v211\n"
"v_mfma_f32_16x16x32_bf16 v[232:235], a[20:23], v[216:219], v[232:235]\n"
"s_cbranch_vccnz Ls17_%=\n"
"Lb17_%=:\n"
"v_mul_f32 v101, v177, v211\n"
"v_mul_f32 v102, v178, v211\n"
"v_mul_f32 v103, v179, v211\n"
"v_mul_f32 v100, v100, |v176|\n"
"v_mul_f32 v101, v101, |v177|\n"
"v_mfma_f32_16x16x32_bf16 v[140:143], a[4:7], v[240:243], v[140:143]\n"
"s_waitcnt vmcnt(12)\n"
"v_max3_u32 v48, v200, v201, v202\n"
"v_mul_f32 v102, v102, |v178|\n"
"v_mul_f32 v103, v103, |v179|\n"
"v_cvt_pk_bf16_f32 v176, v100, v101\n"
"v_cvt_pk_bf16_f32 v177, v102, v103\n"
"v_mul_f32 v100, v40, v211\n"
"v_mfma_f32_16x16x32_bf16 v[44:47], a[28:31], v[240:243], v[44:47]\n"
"buffer_load_dwordx4 v[184:187], v16, s[8:11], s2 offen sc1\n"
"v_mul_f32 v101, v41, v211\n"
"v_mul_f32 v102, v42, v211\n"
"v_mul_f32 v103, v43, v211\n"
"v_mul_f32 v100, v100, |v40|\n"
"v_mfma_f32_16x16x32_bf16 v[56:59], a[16:19], v[240:243], v[56:59]\n"
"s_waitcnt vmcnt(12)\n"
"v_max3_u32 v48, v48, v203, v148\n"
"v_max3_u32 v48, v48, v149, v150\n"
"v_mul_f32 v101, v101, |v41|\n"
"v_mul_f32 v102, v102, |v42|\n"
"v_mul_f32 v103, v103, |v43|\n"
"v_cvt_pk_bf16_f32 v178, v100, v101\n"
"v_mfma_f32_16x16x32_bf16 v[244:247], a[24:27], v[240:243], v[244:247]\n"
"v_max_u32 v48, v48, v151\n"
"v_cmp_eq_u32 vcc, -1, v48\n"
"v_cvt_pk_bf16_f32 v179, v102, v103\n"
"v_pk_max_i16 v176, v176, 0\n"
"v_pk_max_i16 v177, v177, 0\n"
"v_pk_max_i16 v178, v178, 0\n"
"v_mfma_f32_16x16x32_bf16 v[196:199], a[20:23], v[240:243], v[196:199]\n"
"s_cbranch_vccnz Ls18_%=\n"
"Lb18_%=:\n"
"v_pk_max_i16 v179, v179, 0\n"
"s_add_u32 s1, s70, 0x92800\n"
"buffer_load_dwordx4 v[92:95], v17, s[8:11], s2 offen sc1\n"
"v_mfma_f32_16x16x32_bf16 v[60:63], a[44:47], v[80:83], v[60:63]\n"
"s_bitcmp1_b32 s61, 9\n"
"s_cselect_b32 s2, s60, s59\n"
"s_add_u32 s2, s2, 0x6c400\n"
"buffer_store_dwordx4 v[176:179], v0, s[8:11], s1 offen offset:0 sc1\n"
"v_mfma_f32_16x16x32_bf16 v[52:55], a[32:35], v[80:83], v[52:55]\n"
"s_waitcnt vmcnt(13)\n"
"v_max3_u32 v224, v36, v37, v38\n"
"s_cmp_eq_u32 s62, 0\n"
"s_cselect_b64 exec, -1, 0\n"
"v_swap_b32 v152, v212\n"
"v_swap_b32 v153, v213\n"
"v_swap_b32 v154, v214\n"
"v_swap_b32 v155, v215\n"
"s_mov_b64 exec, -1\n"
"v_mfma_f32_16x16x32_bf16 v[192:195], a[40:43], v[80:83], v[192:195]\n"
"s_waitcnt vmcnt(12)\n"
"v_max3_u32 v224, v224, v39, v204\n"
"v_max3_u32 v224, v224, v205, v206\n"
"s_cmp_eq_u32 s62, 0\n"
"s_cselect_b64 exec, -1, 0\n"
"v_swap_b32 v84, v176\n"
"v_swap_b32 v85, v177\n"
"v_swap_b32 v86, v178\n"
"v_swap_b32 v87, v179\n"
"s_mov_b64 exec, -1\n"
"v_mfma_f32_16x16x32_bf16 v[252:255], a[36:39], v[80:83], v[252:255]\n"
"buffer_load_dwordx4 a[160:163], v0, s[8:11], s90 offen offset:0 sc1\n"
"v_mfma_f32_16x16x32_bf16 v[232:235], a[60:63], v[80:83], v[232:235]\n"
"buffer_load_dwordx4 v[160:163], v16, s[8:11], s2 offen sc1\n"
"v_mfma_f32_16x16x32_bf16 v[140:143], a[44:47], v[228:231], v[140:143]\n"
"v_max_u32 v224, v224, v207\n"
"v_cmp_eq_u32 vcc, -1, v224\n"
"v_mfma_f32_16x16x32_bf16 v[44:47], a[32:35], v[228:231], v[44:47]\n"
"v_mfma_f32_16x16x32_bf16 v[56:59], a[40:43], v[228:231], v[56:59]\n"
"s_cbranch_vccnz Ls19_%=\n"
"Lb19_%=:\n"
"v_mfma_f32_16x16x32_bf16 v[244:247], a[36:39], v[228:231], v[244:247]\n"
"v_mfma_f32_16x16x32_bf16 v[196:199], a[60:63], v[228:231], v[196:199]\n"
"buffer_load_dwordx4 v[136:139], v17, s[8:11], s2 offen sc1\n"
"v_mfma_f32_16x16x32_bf16 v[60:63], a[48:51], v[200:203], v[60:63]\n"
"s_bitcmp1_b32 s61, 10\n"
"s_cselect_b32 s2, s60, s59\n"
"s_add_u32 s2, s2, 0x6c800\n"
"v_mfma_f32_16x16x32_bf16 v[52:55], a[56:59], v[200:203], v[52:55]\n"
"s_waitcnt vmcnt(11)\n"
"v_max3_u32 v188, v28, v29, v30\n"
"v_mfma_f32_16x16x32_bf16 v[192:195], a[52:55], v[200:203], v[192:195]\n"
"v_mfma_f32_16x16x32_bf16 v[252:255], a[68:71], v[200:203], v[252:255]\n"
"v_mfma_f32_16x16x32_bf16 v[232:235], a[76:79], v[200:203], v[232:235]\n"
"buffer_load_dwordx4 v[88:91], v16, s[8:11], s2 offen sc1\n"
"v_mfma_f32_16x16x32_bf16 v[140:143], a[48:51], v[148:151], v[140:143]\n"
"v_mfma_f32_16x16x32_bf16 v[44:47], a[56:59], v[148:151], v[44:47]\n"
"v_mfma_f32_16x16x32_bf16 v[56:59], a[52:55], v[148:151], v[56:59]\n"
"v_mfma_f32_16x16x32_bf16 v[244:247], a[68:71], v[148:151], v[244:247]\n"
"v_mfma_f32_16x16x32_bf16 v[196:199], a[76:79], v[148:151], v[196:199]\n"
"buffer_load_dwordx4 v[120:123], v17, s[8:11], s2 offen sc1\n"
"v_mfma_f32_16x16x32_bf16 v[60:63], a[72:75], v[36:39], v[60:63]\n"
"s_bitcmp1_b32 s61, 11\n"
"s_cselect_b32 s2, s60, s59\n"
"s_add_u32 s2, s2, 0x6cc00\n"
"v_mfma_f32_16x16x32_bf16 v[52:55], a[64:67], v[36:39], v[52:55]\n"
"s_waitcnt vmcnt(11)\n"
"v_max3_u32 v188, v188, v31, v248\n"
"v_max3_u32 v188, v188, v249, v250\n"
"v_mfma_f32_16x16x32_bf16 v[192:195], a[84:87], v[36:39], v[192:195]\n"
"v_max_u32 v188, v188, v251\n"
"v_cmp_eq_u32 vcc, -1, v188\n"
"v_mfma_f32_16x16x32_bf16 v[252:255], a[92:95], v[36:39], v[252:255]\n"
"v_mfma_f32_16x16x32_bf16 v[232:235], a[88:91], v[36:39], v[232:235]\n"
"s_cbranch_vccnz Ls20_%=\n"
"Lb20_%=:\n"
"v_mfma_f32_16x16x32_bf16 v[140:143], a[72:75], v[204:207], v[140:143]\n"
"buffer_load_dwordx4 a[180:183], v0, s[8:11], s90 offen offset:1024 sc1\n"
"v_mfma_f32_16x16x32_bf16 v[44:47], a[64:67], v[204:207], v[44:47]\n"
"buffer_load_dwordx4 v[164:167], v16, s[8:11], s2 offen sc1\n"
"v_mfma_f32_16x16x32_bf16 v[56:59], a[84:87], v[204:207], v[56:59]\n"
"s_waitcnt vmcnt(12)\n"
"v_max3_u32 v180, v116, v117, v118\n"
"v_mfma_f32_16x16x32_bf16 v[244:247], a[92:95], v[204:207], v[244:247]\n"
"v_mfma_f32_16x16x32_bf16 v[196:199], a[88:91], v[204:207], v[196:199]\n"
"s_waitcnt vmcnt(11)\n"
"v_max3_u32 v180, v180, v119, v76\n"
"v_max3_u32 v180, v180, v77, v78\n"
"v_mfma_f32_16x16x32_bf16 v[60:63], a[80:83], v[28:31], v[60:63]\n"
"v_max_u32 v180, v180, v79\n"
"v_cmp_eq_u32 vcc, -1, v180\n"
"v_mfma_f32_16x16x32_bf16 v[52:55], a[100:103], v[28:31], v[52:55]\n"
"buffer_load_dwordx4 v[124:127], v17, s[8:11], s2 offen sc1\n"
"v_mfma_f32_16x16x32_bf16 v[192:195], a[108:111], v[28:31], v[192:195]\n"
"s_cbranch_vccnz Ls21_%=\n"
"Lb21_%=:\n"
"v_mfma_f32_16x16x32_bf16 v[252:255], a[104:107], v[28:31], v[252:255]\n"
"v_mfma_f32_16x16x32_bf16 v[232:235], a[96:99], v[28:31], v[232:235]\n"
"v_mfma_f32_16x16x32_bf16 v[140:143], a[80:83], v[248:251], v[140:143]\n"
"v_mfma_f32_16x16x32_bf16 v[44:47], a[100:103], v[248:251], v[44:47]\n"
"v_mfma_f32_16x16x32_bf16 v[56:59], a[108:111], v[248:251], v[56:59]\n"
"v_mfma_f32_16x16x32_bf16 v[244:247], a[104:107], v[248:251], v[244:247]\n"
"buffer_load_dwordx4 a[188:191], v0, s[8:11], s90 offen offset:2048 sc1\n"
"v_mfma_f32_16x16x32_bf16 v[196:199], a[96:99], v[248:251], v[196:199]\n"
"s_waitcnt vmcnt(11)\n"
"v_max3_u32 v64, v184, v185, v186\n"
"v_mfma_f32_16x16x32_bf16 v[60:63], a[116:119], v[116:119], v[60:63]\n"
"v_mfma_f32_16x16x32_bf16 v[52:55], a[124:127], v[116:119], v[52:55]\n"
"v_mfma_f32_16x16x32_bf16 v[192:195], a[120:123], v[116:119], v[192:195]\n"
"v_mfma_f32_16x16x32_bf16 v[252:255], a[112:115], v[116:119], v[252:255]\n"
"buffer_load_dwordx4 a[184:187], v0, s[8:11], s90 offen offset:3072 sc1\n"
"v_mfma_f32_16x16x32_bf16 v[232:235], a[132:135], v[116:119], v[232:235]\n"
"s_add_u32 s90, s55, 0x11000\n"
"s_waitcnt vmcnt(11)\n"
"v_max3_u32 v64, v64, v187, v92\n"
"v_mfma_f32_16x16x32_bf16 v[140:143], a[116:119], v[76:79], v[140:143]\n"
"v_max3_u32 v64, v64, v93, v94\n"
"v_max_u32 v64, v64, v95\n"
"v_mfma_f32_16x16x32_bf16 v[44:47], a[124:127], v[76:79], v[44:47]\n"
"v_cmp_eq_u32 vcc, -1, v64\n"
"v_mfma_f32_16x16x32_bf16 v[56:59], a[120:123], v[76:79], v[56:59]\n"
"buffer_load_dwordx4 v[172:175], v0, s[8:11], s90 offen offset:0 sc1\n"
"v_mfma_f32_16x16x32_bf16 v[244:247], a[112:115], v[76:79], v[244:247]\n"
"s_cbranch_vccnz Ls22_%=\n"
"Lb22_%=:\n"
"v_mfma_f32_16x16x32_bf16 v[196:199], a[132:135], v[76:79], v[196:199]\n"
"v_mfma_f32_16x16x32_bf16 v[60:63], a[140:143], v[184:187], v[60:63]\n"
"v_mfma_f32_16x16x32_bf16 v[52:55], a[136:139], v[184:187], v[52:55]\n"
"v_mfma_f32_16x16x32_bf16 v[192:195], a[128:131], v[184:187], v[192:195]\n"
"buffer_load_dwordx4 v[32:35], v0, s[8:11], s90 offen offset:1024 sc1\n"
"v_mfma_f32_16x16x32_bf16 v[252:255], a[152:155], v[184:187], v[252:255]\n"
"s_add_u32 s90, s52, 0x0\n"
"v_mfma_f32_16x16x32_bf16 v[232:235], a[144:147], v[184:187], v[232:235]\n"
"v_mfma_f32_16x16x32_bf16 v[140:143], a[140:143], v[92:95], v[140:143]\n"
"v_mfma_f32_16x16x32_bf16 v[44:47], a[136:139], v[92:95], v[44:47]\n"
"v_mfma_f32_16x16x32_bf16 v[56:59], a[128:131], v[92:95], v[56:59]\n"
"buffer_load_dwordx4 v[132:135], v0, s[8:11], s90 offen offset:0 sc1\n"
"v_mfma_f32_16x16x32_bf16 v[244:247], a[152:155], v[92:95], v[244:247]\n"
"s_waitcnt vmcnt(11)\n"
"v_max3_u32 v112, v160, v161, v162\n"
"v_mfma_f32_16x16x32_bf16 v[196:199], a[144:147], v[92:95], v[196:199]\n"
"v_mfma_f32_16x16x32_bf16 v[156:159], a[176:179], v[152:155], v[156:159]\n"
"v_mfma_f32_16x16x32_bf16 v[104:107], a[196:199], v[152:155], v[104:107]\n"
"buffer_load_dwordx4 v[96:99], v0, s[8:11], s90 offen offset:1024 sc1\n"
"v_mfma_f32_16x16x32_bf16 v[168:171], a[204:207], v[152:155], 0\n"
"s_waitcnt vmcnt(11)\n"
"v_max3_u32 v112, v112, v163, v136\n"
"v_max3_u32 v112, v112, v137, v138\n"
"v_mfma_f32_16x16x32_bf16 v[72:75], a[200:203], v[152:155], 0\n"
"buffer_load_dwordx4 v[216:219], v16, s[8:11], s4 offen sc1\n"
"v_mfma_f32_16x16x32_bf16 v[108:111], a[192:195], v[152:155], v[108:111]\n"
"v_max_u32 v112, v112, v139\n"
"v_cmp_eq_u32 vcc, -1, v112\n"
"v_mfma_f32_16x16x32_bf16 v[68:71], a[176:179], v[212:215], 0\n"
"v_mfma_f32_16x16x32_bf16 v[208:211], a[196:199], v[212:215], 0\n"
"s_cbranch_vccnz Ls23_%=\n"
"Lb23_%=:\n"
"v_mfma_f32_16x16x32_bf16 v[60:63], a[164:167], v[160:163], v[60:63]\n"
"s_waitcnt vmcnt(11)\n"
"v_max3_u32 v40, v88, v89, v90\n"
"v_mfma_f32_16x16x32_bf16 v[52:55], a[148:151], v[160:163], v[52:55]\n"
"buffer_load_dwordx4 v[100:103], v17, s[8:11], s4 offen sc1\n"
"v_mfma_f32_16x16x32_bf16 v[192:195], a[156:159], v[160:163], v[192:195]\n"
"s_bitcmp1_b32 s61, 1\n"
"s_cselect_b32 s4, s60, s59\n"
"s_add_u32 s4, s4, 0x85400\n"
"v_mfma_f32_16x16x32_bf16 v[252:255], v[128:131], v[160:163], v[252:255]\n"
"v_mfma_f32_16x16x32_bf16 v[232:235], v[236:239], v[160:163], v[232:235]\n"
"s_waitcnt vmcnt(11)\n"
"v_max3_u32 v40, v40, v91, v120\n"
"v_max3_u32 v40, v40, v121, v122\n"
"v_mfma_f32_16x16x32_bf16 v[140:143], a[164:167], v[136:139], v[140:143]\n"
"v_max_u32 v40, v40, v123\n"
"v_cmp_eq_u32 vcc, -1, v40\n"
"v_mfma_f32_16x16x32_bf16 v[44:47], a[148:151], v[136:139], v[44:47]\n"
"buffer_load_dwordx4 v[240:243], v16, s[8:11], s4 offen sc1\n"
"v_mfma_f32_16x16x32_bf16 v[56:59], a[156:159], v[136:139], v[56:59]\n"
"s_cbranch_vccnz Ls24_%=\n"
"Lb24_%=:\n"
"v_mfma_f32_16x16x32_bf16 v[244:247], v[128:131], v[136:139], v[244:247]\n"
"v_mfma_f32_16x16x32_bf16 v[196:199], v[236:239], v[136:139], v[196:199]\n"
"v_mfma_f32_16x16x32_bf16 v[60:63], v[144:147], v[88:91], v[60:63]\n"
"buffer_load_dwordx4 v[48:51], v17, s[8:11], s4 offen sc1\n"
"v_mfma_f32_16x16x32_bf16 v[52:55], v[220:223], v[88:91], v[52:55]\n"
"s_bitcmp1_b32 s61, 2\n"
"s_cselect_b32 s4, s60, s59\n"
"s_add_u32 s4, s4, 0x85800\n"
"v_mfma_f32_16x16x32_bf16 v[192:195], a[172:175], v[88:91], v[192:195]\n"
"v_mfma_f32_16x16x32_bf16 v[252:255], a[168:171], v[88:91], v[252:255]\n"
"s_waitcnt vmcnt(11)\n"
"v_max3_u32 v80, v164, v165, v166\n"
"v_mfma_f32_16x16x32_bf16 v[232:235], a[160:163], v[88:91], v[232:235]\n"
"v_mfma_f32_16x16x32_bf16 v[140:143], v[144:147], v[120:123], v[140:143]\n"
"buffer_load_dwordx4 v[224:227], v16, s[8:11], s4 offen sc1\n"
"v_mfma_f32_16x16x32_bf16 v[44:47], v[220:223], v[120:123], v[44:47]\n"
"v_mfma_f32_16x16x32_bf16 v[56:59], a[172:175], v[120:123], v[56:59]\n"
"v_mfma_f32_16x16x32_bf16 v[244:247], a[168:171], v[120:123], v[244:247]\n"
"s_waitcnt vmcnt(11)\n"
"v_max3_u32 v80, v80, v167, v124\n"
"v_max3_u32 v80, v80, v125, v126\n"
"v_mfma_f32_16x16x32_bf16 v[196:199], a[160:163], v[120:123], v[196:199]\n"
"buffer_load_dwordx4 v[228:231], v17, s[8:11], s4 offen sc1\n"
"v_mfma_f32_16x16x32_bf16 v[200:203], a[204:207], v[212:215], 0\n"
"s_bitcmp1_b32 s61, 3\n"
"s_cselect_b32 s4, s60, s59\n"
"s_add_u32 s4, s4, 0x85c00\n"
"v_mfma_f32_16x16x32_bf16 v[148:151], a[200:203], v[212:215], 0\n"
"v_max_u32 v80, v80, v127\n"
"v_cmp_eq_u32 vcc, -1, v80\n"
"v_mfma_f32_16x16x32_bf16 v[36:39], a[192:195], v[212:215], 0\n"
"v_mfma_f32_16x16x32_bf16 v[156:159], a[212:215], v[84:87], v[156:159]\n"
"s_cbranch_vccnz Ls25_%=\n"
"Lb25_%=:\n"
"s_waitcnt vmcnt(10)\n"
"v_mfma_f32_16x16x32_bf16 v[192:195], a[184:187], v[164:167], v[192:195]\n"
"s_waitcnt vmcnt(9)\n"
"buffer_load_dwordx4 v[188:191], v16, s[8:11], s4 offen sc1\n"
"v_mfma_f32_16x16x32_bf16 v[252:255], v[172:175], v[164:167], v[252:255]\n"
"v_mfma_f32_16x16x32_bf16 v[104:107], a[220:223], v[84:87], v[104:107]\n"
"s_waitcnt vmcnt(9)\n"
"v_mfma_f32_16x16x32_bf16 v[232:235], v[32:35], v[164:167], v[232:235]\n"
"v_mfma_f32_16x16x32_bf16 v[140:143], a[180:183], v[124:127], v[140:143]\n"
"v_mfma_f32_16x16x32_bf16 v[44:47], a[188:191], v[124:127], v[44:47]\n"
"buffer_load_dwordx4 v[204:207], v17, s[8:11], s4 offen sc1\n"
"ds_write_b128 v2, v[192:195] offset:2048\n"
"v_mfma_f32_16x16x32_bf16 v[56:59], a[184:187], v[124:127], v[56:59]\n"
"s_bitcmp1_b32 s61, 4\n"
"s_cselect_b32 s4, s60, s59\n"
"s_add_u32 s4, s4, 0x86000\n"
"v_mfma_f32_16x16x32_bf16 v[244:247], v[172:175], v[124:127], v[244:247]\n"
"ds_write_b128 v2, v[252:255] offset:3072\n"
"v_mfma_f32_16x16x32_bf16 v[196:199], v[32:35], v[124:127], v[196:199]\n"
"buffer_load_dwordx4 v[180:183], v16, s[8:11], s4 offen sc1\n"
"v_mfma_f32_16x16x32_bf16 v[60:63], a[180:183], v[164:167], v[60:63]\n"
"ds_write_b128 v2, v[232:235] offset:6144\n"
"v_mfma_f32_16x16x32_bf16 v[52:55], a[188:191], v[164:167], v[52:55]\n"
"v_mfma_f32_16x16x32_bf16 v[168:171], a[216:219], v[84:87], v[168:171]\n"
"buffer_load_dwordx4 v[28:31], v17, s[8:11], s4 offen sc1\n"
"v_mfma_f32_16x16x32_bf16 v[72:75], a[208:211], v[84:87], v[72:75]\n"
"s_bitcmp1_b32 s61, 5\n"
"s_cselect_b32 s4, s60, s59\n"
"s_add_u32 s4, s4, 0x86400\n"
"v_mfma_f32_16x16x32_bf16 v[108:111], a[228:231], v[84:87], v[108:111]\n"
"ds_write_b128 v2, v[140:143] offset:0\n"
"v_mfma_f32_16x16x32_bf16 v[68:71], a[212:215], v[176:179], v[68:71]\n"
"ds_write_b128 v2, v[44:47] offset:1024\n"
"v_mfma_f32_16x16x32_bf16 v[208:211], a[220:223], v[176:179], v[208:211]\n"
"buffer_load_dwordx4 v[248:251], v16, s[8:11], s4 offen sc1\n"
"v_mfma_f32_16x16x32_bf16 v[200:203], a[216:219], v[176:179], v[200:203]\n"
"v_mfma_f32_16x16x32_bf16 v[148:151], a[208:211], v[176:179], v[148:151]\n"
"ds_write_b128 v2, v[56:59] offset:4096\n"
"v_mfma_f32_16x16x32_bf16 v[36:39], a[228:231], v[176:179], v[36:39]\n"
"buffer_load_dwordx4 v[116:119], v17, s[8:11], s4 offen sc1\n"
"s_bitcmp1_b32 s61, 6\n"
"s_cselect_b32 s4, s60, s59\n"
"s_add_u32 s4, s4, 0x86800\n"
"ds_write_b128 v2, v[244:247] offset:5120\n"
"s_waitcnt vmcnt(11) lgkmcnt(5)\n"
"v_max3_u32 v64, v216, v217, v218\n"
"ds_write_b128 v2, v[196:199] offset:7168\n"
"buffer_load_dwordx4 a[176:179], v0, s[8:11], s90 offen offset:2048 sc1\n"
"s_waitcnt vmcnt(11) lgkmcnt(5)\n"
"v_max3_u32 v64, v64, v219, v100\n"
"v_max3_u32 v64, v64, v101, v102\n"
"v_max_u32 v64, v64, v103\n"
"v_cmp_eq_u32 vcc, -1, v64\n"
"s_cbranch_vccnz Ls42_%=\n"
"Lb42_%=:\n"
"v_mfma_f32_16x16x32_bf16 v[156:159], a[236:239], v[216:219], v[156:159]\n"
"v_mfma_f32_16x16x32_bf16 v[104:107], a[232:235], v[216:219], v[104:107]\n"
"buffer_load_dwordx4 a[196:199], v0, s[8:11], s90 offen offset:3072 sc1\n"
"v_mfma_f32_16x16x32_bf16 v[168:171], a[224:227], v[216:219], v[168:171]\n"
"s_add_u32 s90, s52, 0x4000\n"
"s_waitcnt vmcnt(11) lgkmcnt(3)\n"
"v_max3_u32 v76, v240, v241, v242\n"
"v_mfma_f32_16x16x32_bf16 v[72:75], a[244:247], v[216:219], v[72:75]\n"
"v_mfma_f32_16x16x32_bf16 v[108:111], a[252:255], v[216:219], v[108:111]\n"
"buffer_load_dwordx4 v[184:187], v16, s[8:11], s4 offen sc1\n"
"v_mfma_f32_16x16x32_bf16 v[68:71], a[236:239], v[100:103], v[68:71]\n"
"s_waitcnt lgkmcnt(0)\n"
"s_barrier\n"
"ds_read_b128 v[92:95], v8 offset:0\n"
"ds_read_b128 v[152:155], v9 offset:2048\n"
"ds_read_b128 v[112:115], v10 offset:4096\n"
"ds_read_b128 v[160:163], v8 offset:1024\n"
"ds_read_b128 v[40:43], v9 offset:3072\n"
"ds_read_b128 v[136:139], v10 offset:5120\n"
"v_mfma_f32_16x16x32_bf16 v[208:211], a[232:235], v[100:103], v[208:211]\n"
"s_waitcnt vmcnt(11)\n"
"v_max3_u32 v76, v76, v243, v48\n"
"v_max3_u32 v76, v76, v49, v50\n"
"v_mfma_f32_16x16x32_bf16 v[200:203], a[224:227], v[100:103], v[200:203]\n"
"buffer_load_dwordx4 a[204:207], v0, s[8:11], s90 offen offset:0 sc1\n"
"v_mfma_f32_16x16x32_bf16 v[148:151], a[244:247], v[100:103], v[148:151]\n"
"v_max_u32 v76, v76, v51\n"
"v_cmp_eq_u32 vcc, -1, v76\n"
"v_mfma_f32_16x16x32_bf16 v[36:39], a[252:255], v[100:103], v[36:39]\n"
"buffer_load_dwordx4 v[88:91], v17, s[8:11], s4 offen sc1\n"
"s_bitcmp1_b32 s61, 7\n"
"s_cselect_b32 s4, s60, s59\n"
"s_add_u32 s4, s4, 0x86c00\n"
"s_cbranch_vccnz Ls43_%=\n"
"Lb43_%=:\n"
"v_mfma_f32_16x16x32_bf16 v[156:159], a[248:251], v[240:243], v[156:159]\n"
"s_waitcnt vmcnt(12)\n"
"v_max3_u32 v120, v224, v225, v226\n"
"s_waitcnt lgkmcnt(5)\n"
"v_add_f32 v60, v60, v92\n"
"v_add_f32 v61, v61, v93\n"
"v_add_f32 v62, v62, v94\n"
"v_add_f32 v63, v63, v95\n"
"ds_read_b128 v[212:215], v8 offset:7168\n"
"v_mfma_f32_16x16x32_bf16 v[104:107], a[240:243], v[240:243], v[104:107]\n"
"s_waitcnt vmcnt(11)\n"
"v_max3_u32 v120, v120, v227, v228\n"
"v_max3_u32 v120, v120, v229, v230\n"
"s_waitcnt lgkmcnt(5)\n"
"v_add_f32 v60, v60, v152\n"
"v_add_f32 v61, v61, v153\n"
"v_add_f32 v62, v62, v154\n"
"v_mfma_f32_16x16x32_bf16 v[168:171], a[12:15], v[240:243], v[168:171]\n"
"buffer_load_dwordx4 a[200:203], v0, s[8:11], s90 offen offset:1024 sc1\n"
"v_add_f32 v63, v63, v155\n"
"s_waitcnt lgkmcnt(4)\n"
"v_add_f32 v60, v60, v112\n"
"v_add_f32 v61, v61, v113\n"
"ds_read_b128 v[80:83], v9 offset:6144\n"
"v_mfma_f32_16x16x32_bf16 v[72:75], a[0:3], v[240:243], v[72:75]\n"
"v_max_u32 v120, v120, v231\n"
"v_cmp_eq_u32 vcc, -1, v120\n"
"v_add_f32 v62, v62, v114\n"
"v_add_f32 v63, v63, v115\n"
"v_cvt_pk_bf16_f32 v192, v60, v61\n"
"v_cvt_pk_bf16_f32 v193, v62, v63\n"
"ds_read_b128 v[252:255], v10 offset:7168\n"
"v_mfma_f32_16x16x32_bf16 v[108:111], a[8:11], v[240:243], v[108:111]\n"
"s_cbranch_vccnz Ls44_%=\n"
"Lb44_%=:\n"
"buffer_load_dwordx4 v[124:127], v16, s[8:11], s4 offen sc1\n"
"s_waitcnt lgkmcnt(3)\n"
"v_add_f32 v52, v52, v160\n"
"v_add_f32 v53, v53, v161\n"
"v_mfma_f32_16x16x32_bf16 v[68:71], a[248:251], v[48:51], v[68:71]\n"
"s_waitcnt vmcnt(12)\n"
"v_max3_u32 v164, v188, v189, v190\n"
"v_add_f32 v54, v54, v162\n"
"v_add_f32 v55, v55, v163\n"
"v_add_f32 v52, v52, v40\n"
"v_add_f32 v53, v53, v41\n"
"v_add_f32 v54, v54, v42\n"
"v_mfma_f32_16x16x32_bf16 v[208:211], a[240:243], v[48:51], v[208:211]\n"
"buffer_load_dwordx4 a[192:195], v0, s[8:11], s90 offen offset:2048 sc1\n"
"v_add_f32 v55, v55, v43\n"
"v_add_f32 v52, v52, v136\n"
"v_add_f32 v53, v53, v137\n"
"v_add_f32 v54, v54, v138\n"
"v_mfma_f32_16x16x32_bf16 v[200:203], a[12:15], v[48:51], v[200:203]\n"
"s_waitcnt vmcnt(12) lgkmcnt(2)\n"
"v_max3_u32 v164, v164, v191, v204\n"
"v_max3_u32 v164, v164, v205, v206\n"
"v_add_f32 v55, v55, v139\n"
"v_cvt_pk_bf16_f32 v194, v52, v53\n"
"v_cvt_pk_bf16_f32 v195, v54, v55\n"
"s_add_u32 s0, s67, 0x16000\n"
"v_mfma_f32_16x16x32_bf16 v[148:151], a[0:3], v[48:51], v[148:151]\n"
"buffer_load_dwordx4 a[212:215], v0, s[8:11], s90 offen offset:3072 sc1\n"
"v_mfma_f32_16x16x32_bf16 v[36:39], a[8:11], v[48:51], v[36:39]\n"
"s_add_u32 s90, s52, 0x8000\n"
"v_max_u32 v164, v164, v207\n"
"v_cmp_eq_u32 vcc, -1, v164\n"
"v_mfma_f32_16x16x32_bf16 v[156:159], a[4:7], v[224:227], v[156:159]\n"
"buffer_load_dwordx4 v[84:87], v17, s[8:11], s4 offen sc1\n"
"v_mfma_f32_16x16x32_bf16 v[104:107], a[28:31], v[224:227], v[104:107]\n"
"s_cbranch_vccnz Ls45_%=\n"
"Lb45_%=:\n"
"v_mfma_f32_16x16x32_bf16 v[168:171], a[16:19], v[224:227], v[168:171]\n"
"s_bitcmp1_b32 s61, 8\n"
"s_cselect_b32 s4, s60, s59\n"
"s_add_u32 s4, s4, 0x87000\n"
"v_mfma_f32_16x16x32_bf16 v[72:75], a[24:27], v[224:227], v[72:75]\n"
"s_waitcnt vmcnt(13) lgkmcnt(0)\n"
"v_max3_u32 v140, v180, v181, v182\n"
"v_mfma_f32_16x16x32_bf16 v[108:111], a[20:23], v[224:227], v[108:111]\n"
"s_waitcnt vmcnt(12)\n"
"v_max3_u32 v140, v140, v183, v28\n"
"v_max3_u32 v140, v140, v29, v30\n"
"v_mfma_f32_16x16x32_bf16 v[68:71], a[4:7], v[228:231], v[68:71]\n"
"buffer_load_dwordx4 v[44:47], v16, s[8:11], s4 offen sc1\n"
"v_mfma_f32_16x16x32_bf16 v[208:211], a[28:31], v[228:231], v[208:211]\n"
"v_max_u32 v140, v140, v31\n"
"v_cmp_eq_u32 vcc, -1, v140\n"
"v_mfma_f32_16x16x32_bf16 v[200:203], a[16:19], v[228:231], v[200:203]\n"
"v_mfma_f32_16x16x32_bf16 v[148:151], a[24:27], v[228:231], v[148:151]\n"
"s_cbranch_vccnz Ls46_%=\n"
"Lb46_%=:\n"
"v_mfma_f32_16x16x32_bf16 v[36:39], a[20:23], v[228:231], v[36:39]\n"
"s_waitcnt vmcnt(12)\n"
"v_max3_u32 v56, v248, v249, v250\n"
"v_mfma_f32_16x16x32_bf16 v[156:159], a[44:47], v[188:191], v[156:159]\n"
"buffer_load_dwordx4 v[176:179], v17, s[8:11], s4 offen sc1\n"
"v_mfma_f32_16x16x32_bf16 v[104:107], a[32:35], v[188:191], v[104:107]\n"
"s_bitcmp1_b32 s61, 9\n"
"s_cselect_b32 s4, s60, s59\n"
"s_add_u32 s4, s4, 0x87400\n"
"v_mfma_f32_16x16x32_bf16 v[168:171], a[40:43], v[188:191], v[168:171]\n"
"s_waitcnt vmcnt(12)\n"
"v_max3_u32 v56, v56, v251, v116\n"
"v_max3_u32 v56, v56, v117, v118\n"
"v_mfma_f32_16x16x32_bf16 v[72:75], a[36:39], v[188:191], v[72:75]\n"
"v_max_u32 v56, v56, v119\n"
"v_cmp_eq_u32 vcc, -1, v56\n"
"v_mfma_f32_16x16x32_bf16 v[108:111], a[60:63], v[188:191], v[108:111]\n"
"buffer_load_dwordx4 v[244:247], v16, s[8:11], s4 offen sc1\n"
"v_mfma_f32_16x16x32_bf16 v[68:71], a[44:47], v[204:207], v[68:71]\n"
"s_cbranch_vccnz Ls47_%=\n"
"Lb47_%=:\n"
"v_mfma_f32_16x16x32_bf16 v[208:211], a[32:35], v[204:207], v[208:211]\n"
"v_mfma_f32_16x16x32_bf16 v[200:203], a[40:43], v[204:207], v[200:203]\n"
"v_mfma_f32_16x16x32_bf16 v[148:151], a[36:39], v[204:207], v[148:151]\n"
"v_mfma_f32_16x16x32_bf16 v[36:39], a[60:63], v[204:207], v[36:39]\n"
"buffer_load_dwordx4 v[196:199], v17, s[8:11], s4 offen sc1\n"
"v_mfma_f32_16x16x32_bf16 v[156:159], a[48:51], v[180:183], v[156:159]\n"
"s_bitcmp1_b32 s61, 10\n"
"s_cselect_b32 s4, s60, s59\n"
"s_add_u32 s4, s4, 0x87800\n"
"v_mfma_f32_16x16x32_bf16 v[104:107], a[56:59], v[180:183], v[104:107]\n"
"v_mfma_f32_16x16x32_bf16 v[168:171], a[52:55], v[180:183], v[168:171]\n"
"s_waitcnt vmcnt(11)\n"
"v_max3_u32 v64, v184, v185, v186\n"
"v_mfma_f32_16x16x32_bf16 v[72:75], a[68:71], v[180:183], v[72:75]\n"
"buffer_store_dwordx4 v[192:195], v0, s[8:11], s0 offen offset:0 sc1\n"
"v_add_f32 v232, v232, v212\n"
"v_mfma_f32_16x16x32_bf16 v[108:111], a[76:79], v[180:183], v[108:111]\n"
"v_add_f32 v233, v233, v213\n"
"v_add_f32 v234, v234, v214\n"
"v_add_f32 v235, v235, v215\n"
"v_add_f32 v232, v232, v80\n"
"v_add_f32 v233, v233, v81\n"
"v_add_f32 v234, v234, v82\n"
"v_mfma_f32_16x16x32_bf16 v[68:71], a[48:51], v[28:31], v[68:71]\n"
"v_add_f32 v235, v235, v83\n"
"v_add_f32 v232, v232, v252\n"
"v_add_f32 v233, v233, v253\n"
"v_add_f32 v234, v234, v254\n"
"v_add_f32 v235, v235, v255\n"
"v_cvt_pk_bf16_f32 v216, v232, v233\n"
"v_mfma_f32_16x16x32_bf16 v[208:211], a[56:59], v[28:31], v[208:211]\n"
"buffer_load_dwordx4 v[100:103], v16, s[8:11], s4 offen sc1\n"
"v_cvt_pk_bf16_f32 v217, v234, v235\n"
"s_add_u32 s0, s68, 0x16000\n"
"v_mfma_f32_16x16x32_bf16 v[200:203], a[52:55], v[28:31], v[200:203]\n"
"v_mfma_f32_16x16x32_bf16 v[148:151], a[68:71], v[28:31], v[148:151]\n"
"s_waitcnt vmcnt(11)\n"
"v_max3_u32 v64, v64, v187, v88\n"
"v_max3_u32 v64, v64, v89, v90\n"
"v_mfma_f32_16x16x32_bf16 v[36:39], a[76:79], v[28:31], v[36:39]\n"
"v_max_u32 v64, v64, v91\n"
"v_cmp_eq_u32 vcc, -1, v64\n"
"v_mfma_f32_16x16x32_bf16 v[156:159], a[72:75], v[248:251], v[156:159]\n"
"buffer_load_dwordx4 v[76:79], v17, s[8:11], s4 offen sc1\n"
"v_mfma_f32_16x16x32_bf16 v[104:107], a[64:67], v[248:251], v[104:107]\n"
"s_cbranch_vccnz Ls48_%=\n"
"Lb48_%=:\n"
"v_mfma_f32_16x16x32_bf16 v[168:171], a[84:87], v[248:251], v[168:171]\n"
"s_bitcmp1_b32 s61, 11\n"
"s_cselect_b32 s4, s60, s59\n"
"s_add_u32 s4, s4, 0x87c00\n"
"v_mfma_f32_16x16x32_bf16 v[72:75], a[92:95], v[248:251], v[72:75]\n"
"v_mfma_f32_16x16x32_bf16 v[108:111], a[88:91], v[248:251], v[108:111]\n"
"buffer_load_dwordx4 v[92:95], v16, s[8:11], s4 offen sc1\n"
"v_mfma_f32_16x16x32_bf16 v[68:71], a[72:75], v[116:119], v[68:71]\n"
"v_mfma_f32_16x16x32_bf16 v[208:211], a[64:67], v[116:119], v[208:211]\n"
"v_mfma_f32_16x16x32_bf16 v[200:203], a[84:87], v[116:119], v[200:203]\n"
"s_waitcnt vmcnt(11)\n"
"v_max3_u32 v152, v124, v125, v126\n"
"v_mfma_f32_16x16x32_bf16 v[148:151], a[92:95], v[116:119], v[148:151]\n"
"v_mfma_f32_16x16x32_bf16 v[36:39], a[88:91], v[116:119], v[36:39]\n"
"buffer_load_dwordx4 v[112:115], v17, s[8:11], s4 offen sc1\n"
"v_mfma_f32_16x16x32_bf16 v[156:159], a[80:83], v[184:187], v[156:159]\n"
"v_mfma_f32_16x16x32_bf16 v[104:107], a[100:103], v[184:187], v[104:107]\n"
"v_mfma_f32_16x16x32_bf16 v[168:171], a[108:111], v[184:187], v[168:171]\n"
"v_mfma_f32_16x16x32_bf16 v[72:75], a[104:107], v[184:187], v[72:75]\n"
"v_mfma_f32_16x16x32_bf16 v[108:111], a[96:99], v[184:187], v[108:111]\n"
"buffer_store_dwordx2 v[216:217], v13, s[8:11], s0 offen offset:0 sc1\n"
"v_add_f32 v240, v60, v61\n"
"v_mul_f32 v241, v60, v60\n"
"v_add_f32 v242, v232, v233\n"
"v_mfma_f32_16x16x32_bf16 v[68:71], a[80:83], v[88:91], v[68:71]\n"
"v_mul_f32 v243, v232, v232\n"
"v_fmac_f32 v241, v61, v61\n"
"v_fmac_f32 v243, v233, v233\n"
"v_add_f32 v240, v240, v62\n"
"v_fmac_f32 v241, v62, v62\n"
"v_add_f32 v242, v242, v234\n"
"v_mfma_f32_16x16x32_bf16 v[208:211], a[100:103], v[88:91], v[208:211]\n"
"v_fmac_f32 v243, v234, v234\n"
"v_add_f32 v240, v240, v63\n"
"v_fmac_f32 v241, v63, v63\n"
"v_add_f32 v242, v242, v235\n"
"v_fmac_f32 v243, v235, v235\n"
"v_add_f32 v240, v240, v52\n"
"v_mfma_f32_16x16x32_bf16 v[200:203], a[108:111], v[88:91], v[200:203]\n"
"buffer_load_dwordx4 a[220:223], v0, s[8:11], s90 offen offset:0 sc1\n"
"v_fmac_f32 v241, v52, v52\n"
"v_add_f32 v240, v240, v53\n"
"v_fmac_f32 v241, v53, v53\n"
"v_add_f32 v240, v240, v54\n"
"v_mfma_f32_16x16x32_bf16 v[148:151], a[104:107], v[88:91], v[148:151]\n"
"s_waitcnt vmcnt(11)\n"
"v_max3_u32 v152, v152, v127, v84\n"
"v_max3_u32 v152, v152, v85, v86\n"
"v_fmac_f32 v241, v54, v54\n"
"v_add_f32 v240, v240, v55\n"
"v_fmac_f32 v241, v55, v55\n"
"v_fmac_f32 v240, s78, v242\n"
"v_mfma_f32_16x16x32_bf16 v[36:39], a[96:99], v[88:91], v[36:39]\n"
"buffer_load_dwordx4 a[216:219], v0, s[8:11], s90 offen offset:1024 sc1\n"
"v_max_u32 v152, v152, v87\n"
"v_cmp_eq_u32 vcc, -1, v152\n"
"v_fmac_f32 v241, s78, v243\n"
"v_mul_f32 v232, s78, v232\n"
"v_mul_f32 v233, s78, v233\n"
"v_permlane32_swap_b32 v240, v241\n"
"s_cbranch_vccnz Ls49_%=\n"
"Lb49_%=:\n"
"v_mfma_f32_16x16x32_bf16 v[156:159], a[116:119], v[124:127], v[156:159]\n"
"s_waitcnt vmcnt(11)\n"
"v_max3_u32 v120, v44, v45, v46\n"
"v_add_f32 v240, v240, v241\n"
"v_mov_b32 v241, v240\n"
"v_mfma_f32_16x16x32_bf16 v[104:107], a[124:127], v[124:127], v[104:107]\n"
"buffer_load_dwordx4 a[208:211], v0, s[8:11], s90 offen offset:2048 sc1\n"
"v_permlane16_swap_b32 v240, v241\n"
"v_add_f32 v240, v240, v241\n"
"s_add_u32 s0, s71, 0x20000\n"
"v_mfma_f32_16x16x32_bf16 v[168:171], a[120:123], v[124:127], v[168:171]\n"
"buffer_store_dword v240, v14, s[8:11], s0 offen offset:0 sc1\n"
"s_add_u32 s0, s63, 0x16000\n"
"v_mfma_f32_16x16x32_bf16 v[72:75], a[112:115], v[124:127], v[72:75]\n"
"s_waitcnt vmcnt(12)\n"
"v_max3_u32 v120, v120, v47, v176\n"
"v_max3_u32 v120, v120, v177, v178\n"
"v_mfma_f32_16x16x32_bf16 v[108:111], a[132:135], v[124:127], v[108:111]\n"
"v_max_u32 v120, v120, v179\n"
"v_cmp_eq_u32 vcc, -1, v120\n"
"v_mfma_f32_16x16x32_bf16 v[68:71], a[116:119], v[84:87], v[68:71]\n"
"v_mul_f32 v234, s78, v234\n"
"v_mul_f32 v235, s78, v235\n"
"v_mfma_f32_16x16x32_bf16 v[208:211], a[124:127], v[84:87], v[208:211]\n"
"s_cbranch_vccnz Ls50_%=\n"
"Lb50_%=:\n"
"v_mfma_f32_16x16x32_bf16 v[200:203], a[120:123], v[84:87], v[200:203]\n"
"buffer_load_dwordx4 a[228:231], v0, s[8:11], s90 offen offset:3072 sc1\n"
"v_mfma_f32_16x16x32_bf16 v[148:151], a[112:115], v[84:87], v[148:151]\n"
"s_add_u32 s90, s52, 0xc000\n"
"s_waitcnt vmcnt(12)\n"
"v_max3_u32 v160, v244, v245, v246\n"
"v_mfma_f32_16x16x32_bf16 v[36:39], a[132:135], v[84:87], v[36:39]\n"
"v_mfma_f32_16x16x32_bf16 v[156:159], a[140:143], v[44:47], v[156:159]\n"
"s_waitcnt vmcnt(11)\n"
"v_max3_u32 v160, v160, v247, v196\n"
"v_max3_u32 v160, v160, v197, v198\n"
"v_mfma_f32_16x16x32_bf16 v[104:107], a[136:139], v[44:47], v[104:107]\n"
"v_max_u32 v160, v160, v199\n"
"v_cmp_eq_u32 vcc, -1, v160\n"
"v_mfma_f32_16x16x32_bf16 v[168:171], a[128:131], v[44:47], v[168:171]\n"
"buffer_load_dwordx4 a[236:239], v0, s[8:11], s90 offen offset:0 sc1\n"
"v_mfma_f32_16x16x32_bf16 v[72:75], a[152:155], v[44:47], v[72:75]\n"
"s_cbranch_vccnz Ls51_%=\n"
"Lb51_%=:\n"
"v_mfma_f32_16x16x32_bf16 v[108:111], a[144:147], v[44:47], v[108:111]\n"
"v_mfma_f32_16x16x32_bf16 v[68:71], a[140:143], v[176:179], v[68:71]\n"
"v_mfma_f32_16x16x32_bf16 v[208:211], a[136:139], v[176:179], v[208:211]\n"
"buffer_load_dwordx4 a[232:235], v0, s[8:11], s90 offen offset:1024 sc1\n"
"v_mfma_f32_16x16x32_bf16 v[200:203], a[128:131], v[176:179], v[200:203]\n"
"v_mfma_f32_16x16x32_bf16 v[148:151], a[152:155], v[176:179], v[148:151]\n"
"v_mfma_f32_16x16x32_bf16 v[36:39], a[144:147], v[176:179], v[36:39]\n"
"v_mfma_f32_16x16x32_bf16 v[156:159], a[164:167], v[244:247], v[156:159]\n"
"s_waitcnt vmcnt(11)\n"
"v_max3_u32 v40, v100, v101, v102\n"
"v_mfma_f32_16x16x32_bf16 v[104:107], a[148:151], v[244:247], v[104:107]\n"
"buffer_load_dwordx4 a[224:227], v0, s[8:11], s90 offen offset:2048 sc1\n"
"v_mfma_f32_16x16x32_bf16 v[168:171], a[156:159], v[244:247], v[168:171]\n"
"v_mfma_f32_16x16x32_bf16 v[72:75], v[128:131], v[244:247], v[72:75]\n"
"v_mfma_f32_16x16x32_bf16 v[108:111], v[236:239], v[244:247], v[108:111]\n"
"v_mfma_f32_16x16x32_bf16 v[68:71], a[164:167], v[196:199], v[68:71]\n"
"s_waitcnt vmcnt(11)\n"
"v_max3_u32 v40, v40, v103, v76\n"
"v_max3_u32 v40, v40, v77, v78\n"
"v_mfma_f32_16x16x32_bf16 v[208:211], a[148:151], v[196:199], v[208:211]\n"
"buffer_load_dwordx4 a[244:247], v0, s[8:11], s90 offen offset:3072 sc1\n"
"v_mfma_f32_16x16x32_bf16 v[200:203], a[156:159], v[196:199], v[200:203]\n"
"s_add_u32 s90, s52, 0x10000\n"
"v_max_u32 v40, v40, v79\n"
"v_cmp_eq_u32 vcc, -1, v40\n"
"v_mfma_f32_16x16x32_bf16 v[148:151], v[128:131], v[196:199], v[148:151]\n"
"v_mfma_f32_16x16x32_bf16 v[36:39], v[236:239], v[196:199], v[36:39]\n"
"s_cbranch_vccnz Ls52_%=\n"
"Lb52_%=:\n"
"v_mfma_f32_16x16x32_bf16 v[156:159], v[144:147], v[100:103], v[156:159]\n"
"buffer_load_dwordx4 a[252:255], v0, s[8:11], s90 offen offset:0 sc1\n"
"v_mfma_f32_16x16x32_bf16 v[104:107], v[220:223], v[100:103], v[104:107]\n"
"s_waitcnt vmcnt(12)\n"
"v_max3_u32 v136, v92, v93, v94\n"
"v_mfma_f32_16x16x32_bf16 v[168:171], a[172:175], v[100:103], v[168:171]\n"
"v_mfma_f32_16x16x32_bf16 v[72:75], a[168:171], v[100:103], v[72:75]\n"
"v_mfma_f32_16x16x32_bf16 v[108:111], a[160:163], v[100:103], v[108:111]\n"
"s_waitcnt vmcnt(11)\n"
"v_max3_u32 v136, v136, v95, v112\n"
"v_max3_u32 v136, v136, v113, v114\n"
"v_mfma_f32_16x16x32_bf16 v[68:71], v[144:147], v[76:79], v[68:71]\n"
"buffer_load_dwordx4 a[248:251], v0, s[8:11], s90 offen offset:1024 sc1\n"
"v_mfma_f32_16x16x32_bf16 v[208:211], v[220:223], v[76:79], v[208:211]\n"
"v_max_u32 v136, v136, v115\n"
"v_cmp_eq_u32 vcc, -1, v136\n"
"v_mfma_f32_16x16x32_bf16 v[200:203], a[172:175], v[76:79], v[200:203]\n"
"v_mfma_f32_16x16x32_bf16 v[148:151], a[168:171], v[76:79], v[148:151]\n"
"s_cbranch_vccnz Ls53_%=\n"
"Lb53_%=:\n"
"v_mfma_f32_16x16x32_bf16 v[36:39], a[160:163], v[76:79], v[36:39]\n"
"v_mfma_f32_16x16x32_bf16 v[168:171], a[184:187], v[92:95], v[168:171]\n"
"buffer_load_dwordx4 a[240:243], v0, s[8:11], s90 offen offset:2048 sc1\n"
"v_mfma_f32_16x16x32_bf16 v[72:75], v[172:175], v[92:95], v[72:75]\n"
"v_mfma_f32_16x16x32_bf16 v[108:111], v[32:35], v[92:95], v[108:111]\n"
"v_mfma_f32_16x16x32_bf16 v[68:71], a[180:183], v[112:115], v[68:71]\n"
"v_mfma_f32_16x16x32_bf16 v[208:211], a[188:191], v[112:115], v[208:211]\n"
"buffer_load_dwordx4 a[12:15], v0, s[8:11], s90 offen offset:3072 sc1\n"
"v_mfma_f32_16x16x32_bf16 v[200:203], a[184:187], v[112:115], v[200:203]\n"
"s_add_u32 s90, s52, 0x14000\n"
"ds_write_b128 v2, v[168:171] offset:12288\n"
"v_mfma_f32_16x16x32_bf16 v[148:151], v[172:175], v[112:115], v[148:151]\n"
"ds_write_b128 v2, v[72:75] offset:13312\n"
"v_mfma_f32_16x16x32_bf16 v[36:39], v[32:35], v[112:115], v[36:39]\n"
"buffer_load_dwordx4 a[0:3], v0, s[8:11], s90 offen offset:0 sc1\n"
"v_mfma_f32_16x16x32_bf16 v[156:159], a[180:183], v[92:95], v[156:159]\n"
"v_mfma_f32_16x16x32_bf16 v[104:107], a[188:191], v[92:95], v[104:107]\n"
"ds_write_b128 v2, v[108:111] offset:16384\n"
"buffer_load_dwordx4 a[8:11], v0, s[8:11], s90 offen offset:1024 sc1\n"
"ds_write_b128 v2, v[68:71] offset:10240\n"
"buffer_load_dwordx4 v[48:51], v0, s[8:11], s0 offen offset:0 sc1\n"
"ds_write_b128 v2, v[208:211] offset:11264\n"
"ds_write_b128 v2, v[200:203] offset:14336\n"
"buffer_load_dwordx4 v[164:167], v0, s[8:11], s0 offen offset:1024 sc1\n"
"ds_write_b128 v2, v[148:151] offset:15360\n"
"buffer_load_dwordx4 v[224:227], v0, s[8:11], s0 offen offset:2048 sc1\n"
"ds_write_b128 v2, v[36:39] offset:17408\n"
"buffer_load_dwordx4 v[140:143], v0, s[8:11], s0 offen offset:3072 sc1\n"
"s_add_u32 s0, s63, 0x17000\n"
"buffer_load_dwordx4 v[228:231], v0, s[8:11], s0 offen offset:0 sc1\n"
"s_add_u32 s0, s63, 0x1b000\n"
"s_waitcnt lgkmcnt(0)\n"
"s_barrier\n"
"buffer_load_dwordx4 v[188:191], v0, s[8:11], s0 offen offset:0 sc1\n"
"ds_read_b128 v[56:59], v8 offset:10240\n"
"ds_read_b128 v[204:207], v9 offset:12288\n"
"ds_read_b128 v[192:195], v10 offset:14336\n"
"ds_read_b128 v[180:183], v8 offset:11264\n"
"ds_read_b128 v[212:215], v9 offset:13312\n"
"ds_read_b128 v[80:83], v10 offset:15360\n"
"buffer_load_dwordx4 v[252:255], v0, s[8:11], s0 offen offset:1024 sc1\n"
"buffer_load_dwordx4 v[28:31], v0, s[8:11], s0 offen offset:2048 sc1\n"
"s_waitcnt lgkmcnt(5)\n"
"v_add_f32 v156, v156, v56\n"
"v_add_f32 v157, v157, v57\n"
"v_add_f32 v158, v158, v58\n"
"v_add_f32 v159, v159, v59\n"
"s_waitcnt lgkmcnt(4)\n"
"v_add_f32 v156, v156, v204\n"
"v_add_f32 v157, v157, v205\n"
"v_add_f32 v158, v158, v206\n"
"v_add_f32 v159, v159, v207\n"
"s_waitcnt lgkmcnt(3)\n"
"v_add_f32 v156, v156, v192\n"
"v_add_f32 v157, v157, v193\n"
"v_add_f32 v158, v158, v194\n"
"v_add_f32 v159, v159, v195\n"
"v_cvt_pk_bf16_f32 v64, v156, v157\n"
"v_cvt_pk_bf16_f32 v65, v158, v159\n"
"buffer_load_dwordx4 v[248:251], v0, s[8:11], s0 offen offset:3072 sc1\n"
"ds_read_b128 v[116:119], v8 offset:17408\n"
"ds_read_b128 v[184:187], v9 offset:16384\n"
"ds_read_b128 v[216:219], v10 offset:17408\n"
"s_add_u32 s0, s63, 0x1c000\n"
"s_waitcnt lgkmcnt(5)\n"
"v_add_f32 v104, v104, v180\n"
"v_add_f32 v105, v105, v181\n"
"v_add_f32 v106, v106, v182\n"
"v_add_f32 v107, v107, v183\n"
"s_waitcnt lgkmcnt(4)\n"
"v_add_f32 v104, v104, v212\n"
"v_add_f32 v105, v105, v213\n"
"v_add_f32 v106, v106, v214\n"
"v_add_f32 v107, v107, v215\n"
"buffer_load_dwordx4 v[88:91], v0, s[8:11], s0 offen offset:0 sc1\n"
"s_add_u32 s0, s72, 0x20000\n"
"s_waitcnt lgkmcnt(3)\n"
"v_add_f32 v104, v104, v80\n"
"v_add_f32 v105, v105, v81\n"
"v_add_f32 v106, v106, v82\n"
"v_add_f32 v107, v107, v83\n"
"v_cvt_pk_bf16_f32 v66, v104, v105\n"
"v_cvt_pk_bf16_f32 v67, v106, v107\n"
"s_add_u32 s1, s67, 0x21000\n"
"buffer_load_dwordx4 v[152:155], v7, s[8:11], s0 offen offset:0 sc1\n"
"s_add_u32 s0, s73, 0x20000\n"
"buffer_load_dwordx4 v[240:243], v7, s[8:11], s0 offen offset:0 sc1\n"
"s_add_u32 s0, s64, 0x16000\n"
"s_waitcnt vmcnt(11) lgkmcnt(0)\n"
"v_max3_u32 v124, v48, v49, v50\n"
"buffer_load_dwordx4 v[120:123], v1, s[8:11], s0 offen offset:0 sc1\n"
"s_waitcnt vmcnt(11)\n"
"v_max3_u32 v124, v124, v51, v164\n"
"v_max3_u32 v124, v124, v165, v166\n"
"buffer_store_dwordx4 v[64:67], v0, s[8:11], s1 offen offset:0 sc1\n"
"v_add_f32 v108, v108, v116\n"
"v_add_f32 v109, v109, v117\n"
"v_add_f32 v110, v110, v118\n"
"v_add_f32 v111, v111, v119\n"
"v_add_f32 v108, v108, v184\n"
"v_add_f32 v109, v109, v185\n"
"v_add_f32 v110, v110, v186\n"
"v_add_f32 v111, v111, v187\n"
"s_waitcnt vmcnt(11)\n"
"v_max3_u32 v124, v124, v167, v224\n"
"v_max3_u32 v124, v124, v225, v226\n"
"v_max_u32 v124, v124, v227\n"
"v_cmp_eq_u32 vcc, -1, v124\n"
"v_add_f32 v108, v108, v216\n"
"v_add_f32 v109, v109, v217\n"
"v_add_f32 v110, v110, v218\n"
"v_add_f32 v111, v111, v219\n"
"v_cvt_pk_bf16_f32 v84, v108, v109\n"
"s_cbranch_vccnz Ls26_%=\n"
"Lb26_%=:\n"
"ds_write_b128 v2, v[48:51] offset:0\n"
"buffer_load_dwordx4 a[4:7], v0, s[8:11], s90 offen offset:2048 sc1\n"
"ds_write_b128 v2, v[164:167] offset:1024\n"
"s_waitcnt vmcnt(11)\n"
"v_max3_u32 v160, v140, v141, v142\n"
"buffer_load_dwordx4 v[44:47], v1, s[8:11], s0 offen offset:2048 sc1\n"
"v_cvt_pk_bf16_f32 v85, v110, v111\n"
"s_add_u32 s1, s68, 0x21000\n"
"ds_write_b128 v2, v[224:227] offset:2048\n"
"s_waitcnt vmcnt(11)\n"
"v_max3_u32 v160, v160, v143, v228\n"
"v_max3_u32 v160, v160, v229, v230\n"
"v_max_u32 v160, v160, v231\n"
"v_cmp_eq_u32 vcc, -1, v160\n"
"buffer_load_dwordx4 v[176:179], v1, s[8:11], s0 offen offset:3072 sc1\n"
"s_cbranch_vccnz Ls27_%=\n"
"Lb27_%=:\n"
"ds_write_b128 v2, v[140:143] offset:3072\n"
"s_waitcnt vmcnt(11)\n"
"v_max3_u32 v244, v188, v189, v190\n"
"ds_write_b128 v2, v[228:231] offset:4096\n"
"buffer_store_dwordx2 v[84:85], v13, s[8:11], s1 offen offset:0 sc1\n"
"v_add_f32 v128, v156, v157\n"
"v_mul_f32 v129, v156, v156\n"
"v_add_f32 v130, v108, v109\n"
"v_mul_f32 v131, v108, v108\n"
"v_fmac_f32 v129, v157, v157\n"
"v_fmac_f32 v131, v109, v109\n"
"s_waitcnt vmcnt(11)\n"
"v_max3_u32 v244, v244, v191, v252\n"
"v_max3_u32 v244, v244, v253, v254\n"
"v_max_u32 v244, v244, v255\n"
"v_cmp_eq_u32 vcc, -1, v244\n"
"v_add_f32 v128, v128, v158\n"
"v_fmac_f32 v129, v158, v158\n"
"v_add_f32 v130, v130, v110\n"
"v_fmac_f32 v131, v110, v110\n"
"v_add_f32 v128, v128, v159\n"
"s_cbranch_vccnz Ls28_%=\n"
"Lb28_%=:\n"
"buffer_load_dwordx4 a[28:31], v0, s[8:11], s90 offen offset:3072 sc1\n"
"ds_write_b128 v2, v[188:191] offset:5120\n"
"s_add_u32 s90, s52, 0x18000\n"
"ds_write_b128 v2, v[252:255] offset:6144\n"
"s_waitcnt vmcnt(11) lgkmcnt(5)\n"
"v_max3_u32 v236, v28, v29, v30\n"
"v_fmac_f32 v129, v159, v159\n"
"v_add_f32 v130, v130, v111\n"
"v_fmac_f32 v131, v111, v111\n"
"v_add_f32 v128, v128, v104\n"
"v_fmac_f32 v129, v104, v104\n"
"v_add_f32 v128, v128, v105\n"
"v_fmac_f32 v129, v105, v105\n"
"v_add_f32 v128, v128, v106\n"
"v_fmac_f32 v129, v106, v106\n"
"s_waitcnt vmcnt(10) lgkmcnt(4)\n"
"v_max3_u32 v236, v236, v31, v248\n"
"v_max3_u32 v236, v236, v249, v250\n"
"v_max_u32 v236, v236, v251\n"
"v_cmp_eq_u32 vcc, -1, v236\n"
"v_add_f32 v128, v128, v107\n"
"v_fmac_f32 v129, v107, v107\n"
"v_fmac_f32 v128, s78, v130\n"
"v_fmac_f32 v129, s78, v131\n"
"buffer_load_dwordx4 a[16:19], v0, s[8:11], s90 offen offset:0 sc1\n"
"buffer_load_dwordx4 a[24:27], v0, s[8:11], s90 offen offset:1024 sc1\n"
"s_cbranch_vccnz Ls29_%=\n"
"Lb29_%=:\n"
"ds_write_b128 v2, v[28:31] offset:7168\n"
"s_waitcnt vmcnt(11) lgkmcnt(3)\n"
"v_max3_u32 v196, v88, v89, v90\n"
"ds_write_b128 v2, v[248:251] offset:8192\n"
"buffer_load_dwordx4 a[20:23], v0, s[8:11], s90 offen offset:2048 sc1\n"
"v_permlane32_swap_b32 v128, v129\n"
"v_add_f32 v128, v128, v129\n"
"v_mov_b32 v129, v128\n"
"s_waitcnt vmcnt(11)\n"
"v_max3_u32 v196, v196, v91, v152\n"
"v_max3_u32 v196, v196, v153, v154\n"
"v_max_u32 v196, v196, v155\n"
"v_cmp_eq_u32 vcc, -1, v196\n"
"v_permlane16_swap_b32 v128, v129\n"
"v_add_f32 v128, v128, v129\n"
"s_add_u32 s1, s71, 0x2b000\n"
"buffer_store_dword v128, v14, s[8:11], s1 offen offset:0 sc1\n"
"s_cbranch_vccnz Ls30_%=\n"
"Lb30_%=:\n"
"ds_write_b128 v2, v[88:91] offset:9216\n"
"v_add_f32 v152, v152, v154\n"
"v_add_f32 v153, v153, v155\n"
"s_add_u32 s1, s63, 0x21000\n"
"ds_write_b64 v21, v[152:153] offset:0\n"
"buffer_load_dwordx4 a[44:47], v0, s[8:11], s90 offen offset:3072 sc1\n"
"s_add_u32 s90, s52, 0x1c000\n"
"s_waitcnt vmcnt(12) lgkmcnt(4)\n"
"v_max3_u32 v40, v240, v241, v242\n"
"v_mul_f32 v108, s78, v108\n"
"v_mul_f32 v109, s78, v109\n"
"v_mul_f32 v110, s78, v110\n"
"v_mul_f32 v111, s78, v111\n"
"s_waitcnt vmcnt(11)\n"
"v_max3_u32 v40, v40, v243, v120\n"
"v_max3_u32 v40, v40, v121, v122\n"
"buffer_load_dwordx4 a[32:35], v0, s[8:11], s90 offen offset:0 sc1\n"
"buffer_load_dwordx4 a[40:43], v0, s[8:11], s90 offen offset:1024 sc1\n"
"buffer_load_dwordx4 a[36:39], v0, s[8:11], s90 offen offset:2048 sc1\n"
"s_waitcnt vmcnt(11) lgkmcnt(0)\n"
"v_max3_u32 v40, v40, v123, v44\n"
"v_max3_u32 v40, v40, v45, v46\n"
"buffer_load_dwordx4 a[60:63], v0, s[8:11], s90 offen offset:3072 sc1\n"
"s_add_u32 s90, s52, 0x20000\n"
"s_waitcnt vmcnt(11)\n"
"v_max3_u32 v40, v40, v47, v176\n"
"v_max3_u32 v40, v40, v177, v178\n"
"v_max_u32 v40, v40, v179\n"
"v_cmp_eq_u32 vcc, -1, v40\n"
"buffer_load_dwordx4 a[48:51], v0, s[8:11], s90 offen offset:0 sc1\n"
"s_cbranch_vccnz Ls31_%=\n"
"Lb31_%=:\n"
"v_add_f32 v240, v240, v242\n"
"v_add_f32 v241, v241, v243\n"
"ds_write_b64 v21, v[240:241] offset:2048\n"
"ds_write_b128 v3, v[120:123] offset:0\n"
"buffer_load_dwordx4 a[56:59], v0, s[8:11], s90 offen offset:1024 sc1\n"
"ds_write_b128 v3, v[44:47] offset:2048\n"
"buffer_load_dwordx4 a[52:55], v0, s[8:11], s90 offen offset:2048 sc1\n"
"ds_write_b128 v3, v[176:179] offset:3072\n"
"buffer_load_dwordx4 a[68:71], v0, s[8:11], s90 offen offset:3072 sc1\n"
"s_add_u32 s90, s52, 0x24000\n"
"buffer_load_dwordx4 a[76:79], v0, s[8:11], s90 offen offset:0 sc1\n"
"buffer_load_dwordx4 v[100:103], v0, s[8:11], s1 offen offset:0 sc1\n"
"s_waitcnt lgkmcnt(0)\n"
"s_barrier\n"
"ds_read_b128 v[144:147], v27 offset:0\n"
"ds_read_b128 v[220:223], v27 offset:256\n"
"ds_read_b128 v[136:139], v27 offset:512\n"
"ds_read_b128 v[76:79], v27 offset:768\n"
"ds_read_b128 v[168:171], v4 offset:0\n"
"ds_read_b128 v[172:175], v4 offset:5120\n"
"ds_read_b128 v[72:75], v4 offset:1024\n"
"ds_read_b128 v[32:35], v4 offset:6144\n"
"ds_read_b128 v[112:115], v4 offset:2048\n"
"ds_read_b128 v[92:95], v4 offset:7168\n"
"buffer_load_dwordx4 v[68:71], v0, s[8:11], s1 offen offset:1024 sc1\n"
"buffer_load_dwordx4 v[208:211], v0, s[8:11], s1 offen offset:2048 sc1\n"
"s_waitcnt lgkmcnt(8)\n"
"v_add_f32 v144, v144, v220\n"
"s_waitcnt lgkmcnt(6)\n"
"v_add_f32 v136, v136, v76\n"
"v_add_f32 v145, v145, v221\n"
"v_add_f32 v137, v137, v77\n"
"s_waitcnt lgkmcnt(5)\n"
"v_mfma_f32_16x16x32_bf16 v[200:203], v[96:99], v[168:171], 0\n"
"buffer_load_dwordx4 v[148:151], v0, s[8:11], s1 offen offset:3072 sc1\n"
"s_add_u32 s1, s63, 0x22000\n"
"s_waitcnt lgkmcnt(4)\n"
"v_mfma_f32_16x16x32_bf16 v[36:39], v[96:99], v[172:175], 0\n"
"v_add_f32 v146, v146, v222\n"
"v_add_f32 v138, v138, v78\n"
"v_mfma_f32_16x16x32_bf16 v[56:59], a[196:199], v[168:171], 0\n"
"v_add_f32 v147, v147, v223\n"
"v_add_f32 v139, v139, v79\n"
"v_mfma_f32_16x16x32_bf16 v[204:207], a[196:199], v[172:175], 0\n"
"buffer_load_dwordx4 v[192:195], v0, s[8:11], s1 offen offset:0 sc1\n"
"s_add_u32 s1, s63, 0x26000\n"
"v_mfma_f32_16x16x32_bf16 v[180:183], a[176:179], v[168:171], 0\n"
"v_add_f32 v144, v144, v136\n"
"v_add_f32 v145, v145, v137\n"
"v_mfma_f32_16x16x32_bf16 v[212:215], a[176:179], v[172:175], 0\n"
"v_add_f32 v146, v146, v138\n"
"v_add_f32 v147, v147, v139\n"
"v_mfma_f32_16x16x32_bf16 v[80:83], v[132:135], v[168:171], 0\n"
"ds_read_b128 v[64:67], v4 offset:3072\n"
"v_add_f32 v144, v144, v146\n"
"v_mfma_f32_16x16x32_bf16 v[116:119], v[132:135], v[172:175], 0\n"
"s_waitcnt lgkmcnt(4)\n"
"ds_read_b128 v[184:187], v4 offset:8192\n"
"buffer_load_dwordx4 v[216:219], v0, s[8:11], s1 offen offset:0 sc1\n"
"v_mfma_f32_16x16x32_bf16 v[200:203], a[200:203], v[72:75], v[200:203]\n"
"s_waitcnt lgkmcnt(4)\n"
"v_add_f32 v145, v145, v147\n"
"v_mul_f32 v220, 0xbacccccd, v144\n"
"v_mfma_f32_16x16x32_bf16 v[36:39], a[200:203], v[32:35], v[36:39]\n"
"v_mul_f32 v145, 0x3acccccd, v145\n"
"v_fma_f32 v145, -v220, v220, v145\n"
"v_mfma_f32_16x16x32_bf16 v[56:59], a[212:215], v[72:75], v[56:59]\n"
"v_max_f32 v145, 0, v145\n"
"v_add_f32 v145, 0x3727c5ac, v145\n"
"v_mfma_f32_16x16x32_bf16 v[204:207], a[212:215], v[32:35], v[204:207]\n"
"v_rsq_f32 v221, v145\n"
"v_mfma_f32_16x16x32_bf16 v[180:183], a[192:195], v[72:75], v[180:183]\n"
"buffer_load_dwordx4 v[124:127], v0, s[8:11], s1 offen offset:1024 sc1\n"
"v_mfma_f32_16x16x32_bf16 v[212:215], a[192:195], v[32:35], v[212:215]\n"
"v_mul_f32 v222, v145, v221\n"
"v_mul_f32 v223, 0x3ea52b5c, v221\n"
"v_mfma_f32_16x16x32_bf16 v[80:83], a[204:207], v[72:75], v[80:83]\n"
"ds_write_b128 v27, v[220:223] offset:8192\n"
"ds_read_b128 v[48:51], v4 offset:4096\n"
"v_mfma_f32_16x16x32_bf16 v[116:119], a[204:207], v[32:35], v[116:119]\n"
"s_waitcnt lgkmcnt(4)\n"
"ds_read_b128 v[164:167], v4 offset:9216\n"
"buffer_load_dwordx4 v[224:227], v0, s[8:11], s1 offen offset:2048 sc1\n"
"v_mfma_f32_16x16x32_bf16 v[200:203], a[216:219], v[112:115], v[200:203]\n"
"v_mfma_f32_16x16x32_bf16 v[36:39], a[216:219], v[92:95], v[36:39]\n"
"v_mfma_f32_16x16x32_bf16 v[56:59], a[228:231], v[112:115], v[56:59]\n"
"v_mfma_f32_16x16x32_bf16 v[204:207], a[228:231], v[92:95], v[204:207]\n"
"v_mfma_f32_16x16x32_bf16 v[180:183], a[208:211], v[112:115], v[180:183]\n"
"buffer_load_dwordx4 v[160:163], v0, s[8:11], s1 offen offset:3072 sc1\n"
"s_add_u32 s1, s63, 0x27000\n"
"v_mfma_f32_16x16x32_bf16 v[212:215], a[208:211], v[92:95], v[212:215]\n"
"v_mfma_f32_16x16x32_bf16 v[80:83], a[220:223], v[112:115], v[80:83]\n"
"ds_read_b128 v[140:143], v4 offset:20480\n"
"v_mfma_f32_16x16x32_bf16 v[116:119], a[220:223], v[92:95], v[116:119]\n"
"s_waitcnt lgkmcnt(4)\n"
"ds_read_b128 v[228:231], v4 offset:25600\n"
"v_mfma_f32_16x16x32_bf16 v[200:203], a[232:235], v[64:67], v[200:203]\n"
"v_mfma_f32_16x16x32_bf16 v[36:39], a[232:235], v[184:187], v[36:39]\n"
"buffer_load_dwordx4 v[84:87], v0, s[8:11], s1 offen offset:0 sc1\n"
"s_add_u32 s1, s72, 0x2b000\n"
"v_mfma_f32_16x16x32_bf16 v[56:59], a[244:247], v[64:67], v[56:59]\n"
"v_mfma_f32_16x16x32_bf16 v[204:207], a[244:247], v[184:187], v[204:207]\n"
"v_mfma_f32_16x16x32_bf16 v[180:183], a[224:227], v[64:67], v[180:183]\n"
"v_mfma_f32_16x16x32_bf16 v[212:215], a[224:227], v[184:187], v[212:215]\n"
"buffer_load_dwordx4 v[244:247], v7, s[8:11], s1 offen offset:0 sc1\n"
"s_add_u32 s1, s73, 0x2b000\n"
"v_mfma_f32_16x16x32_bf16 v[80:83], a[236:239], v[64:67], v[80:83]\n"
"ds_read_b128 v[188:191], v4 offset:21504\n"
"v_mfma_f32_16x16x32_bf16 v[116:119], a[236:239], v[184:187], v[116:119]\n"
"s_waitcnt lgkmcnt(3)\n"
"ds_read_b128 v[252:255], v22 offset:0\n"
"ds_read_b128 v[236:239], v22 offset:1024\n"
"ds_read_b128 v[28:31], v4 offset:26624\n"
"v_mfma_f32_16x16x32_bf16 v[200:203], a[248:251], v[48:51], v[200:203]\n"
"v_mfma_f32_16x16x32_bf16 v[36:39], a[248:251], v[164:167], v[36:39]\n"
"buffer_load_dwordx4 v[248:251], v7, s[8:11], s1 offen offset:0 sc1\n"
"s_add_u32 s1, s64, 0x21000\n"
"v_mfma_f32_16x16x32_bf16 v[56:59], a[12:15], v[48:51], v[56:59]\n"
"v_mfma_f32_16x16x32_bf16 v[204:207], a[12:15], v[164:167], v[204:207]\n"
"v_mfma_f32_16x16x32_bf16 v[180:183], a[240:243], v[48:51], v[180:183]\n"
"s_waitcnt vmcnt(11) lgkmcnt(4)\n"
"v_max3_u32 v128, v100, v101, v102\n"
"v_mfma_f32_16x16x32_bf16 v[212:215], a[240:243], v[164:167], v[212:215]\n"
"buffer_load_dwordx4 v[196:199], v1, s[8:11], s1 offen offset:0 sc1\n"
"v_mfma_f32_16x16x32_bf16 v[80:83], a[252:255], v[48:51], v[80:83]\n"
"ds_read_b128 v[88:91], v4 offset:22528\n"
"v_mfma_f32_16x16x32_bf16 v[116:119], a[252:255], v[164:167], v[116:119]\n"
"ds_read_b128 v[152:155], v20 offset:64\n"
"ds_read_b128 v[40:43], v4 offset:27648\n"
"v_mfma_f32_16x16x32_bf16 v[200:203], a[8:11], v[140:143], v[200:203]\n"
"v_mfma_f32_16x16x32_bf16 v[36:39], a[8:11], v[228:231], v[36:39]\n"
"s_waitcnt vmcnt(11)\n"
"v_max3_u32 v128, v128, v103, v68\n"
"v_max3_u32 v128, v128, v69, v70\n"
"v_mfma_f32_16x16x32_bf16 v[56:59], a[28:31], v[140:143], v[56:59]\n"
"v_mfma_f32_16x16x32_bf16 v[204:207], a[28:31], v[228:231], v[204:207]\n"
"v_mfma_f32_16x16x32_bf16 v[180:183], a[4:7], v[140:143], v[180:183]\n"
"v_mfma_f32_16x16x32_bf16 v[212:215], a[4:7], v[228:231], v[212:215]\n"
"buffer_load_dwordx4 a[72:75], v0, s[8:11], s90 offen offset:1024 sc1\n"
"s_waitcnt vmcnt(11) lgkmcnt(4)\n"
"v_max3_u32 v128, v128, v71, v208\n"
"v_max3_u32 v128, v128, v209, v210\n"
"v_max_u32 v128, v128, v211\n"
"v_mfma_f32_16x16x32_bf16 v[80:83], a[0:3], v[140:143], v[80:83]\n"
"ds_read_b128 v[240:243], v4 offset:23552\n"
"v_cmp_eq_u32 vcc, -1, v128\n"
"v_mfma_f32_16x16x32_bf16 v[116:119], a[0:3], v[228:231], v[116:119]\n"
"ds_read_b128 v[120:123], v4 offset:28672\n"
"s_waitcnt lgkmcnt(3)\n"
"v_fmac_f32 v200, v252, v152\n"
"s_cbranch_vccnz Ls54_%=\n"
"Lb54_%=:\n"
"buffer_load_dwordx4 a[64:67], v0, s[8:11], s90 offen offset:2048 sc1\n"
"ds_write_b128 v2, v[100:103] offset:10240\n"
"buffer_load_dwordx4 v[44:47], v1, s[8:11], s1 offen offset:2048 sc1\n"
"ds_write_b128 v2, v[68:71] offset:11264\n"
"v_fmac_f32 v201, v252, v153\n"
"v_fmac_f32 v202, v252, v154\n"
"v_fmac_f32 v203, v252, v155\n"
"s_waitcnt vmcnt(12) lgkmcnt(4)\n"
"v_max3_u32 v176, v148, v149, v150\n"
"v_mfma_f32_16x16x32_bf16 v[200:203], a[24:27], v[188:191], v[200:203]\n"
"s_waitcnt vmcnt(11)\n"
"v_max3_u32 v176, v176, v151, v192\n"
"v_max3_u32 v176, v176, v193, v194\n"
"v_max_u32 v176, v176, v195\n"
"v_cmp_eq_u32 vcc, -1, v176\n"
"v_mfma_f32_16x16x32_bf16 v[36:39], a[24:27], v[28:31], v[36:39]\n"
"buffer_load_dwordx4 v[76:79], v1, s[8:11], s1 offen offset:3072 sc1\n"
"ds_write_b128 v2, v[208:211] offset:12288\n"
"v_mfma_f32_16x16x32_bf16 v[56:59], a[44:47], v[188:191], v[56:59]\n"
"s_cbranch_vccnz Ls55_%=\n"
"Lb55_%=:\n"
"ds_write_b128 v2, v[148:151] offset:13312\n"
"v_mfma_f32_16x16x32_bf16 v[204:207], a[44:47], v[28:31], v[204:207]\n"
"s_waitcnt vmcnt(11)\n"
"v_max3_u32 v136, v216, v217, v218\n"
"v_fmac_f32 v36, v236, v152\n"
"v_mfma_f32_16x16x32_bf16 v[180:183], a[20:23], v[188:191], v[180:183]\n"
"ds_write_b128 v2, v[192:195] offset:14336\n"
"v_mfma_f32_16x16x32_bf16 v[212:215], a[20:23], v[28:31], v[212:215]\n"
"buffer_load_dwordx4 a[84:87], v0, s[8:11], s90 offen offset:3072 sc1\n"
"s_waitcnt vmcnt(11) lgkmcnt(5)\n"
"v_max3_u32 v136, v136, v219, v124\n"
"v_max3_u32 v136, v136, v125, v126\n"
"v_max_u32 v136, v136, v127\n"
"v_mfma_f32_16x16x32_bf16 v[80:83], a[16:19], v[188:191], v[80:83]\n"
"s_add_u32 s90, s52, 0x28000\n"
"ds_read_b128 v[168:171], v4 offset:24576\n"
"v_fmac_f32 v37, v236, v153\n"
"v_cmp_eq_u32 vcc, -1, v136\n"
"v_mfma_f32_16x16x32_bf16 v[116:119], a[16:19], v[28:31], v[116:119]\n"
"ds_read_b128 v[172:175], v4 offset:29696\n"
"v_fmac_f32 v38, v236, v154\n"
"v_mfma_f32_16x16x32_bf16 v[200:203], a[40:43], v[88:91], v[200:203]\n"
"buffer_load_dwordx4 a[92:95], v0, s[8:11], s90 offen offset:0 sc1\n"
"v_fmac_f32 v39, v236, v155\n"
"s_cbranch_vccnz Ls56_%=\n"
"Lb56_%=:\n"
"ds_read_b128 v[144:147], v20 offset:192\n"
"v_mfma_f32_16x16x32_bf16 v[36:39], a[40:43], v[40:43], v[36:39]\n"
"ds_write_b128 v2, v[216:219] offset:15360\n"
"v_mfma_f32_16x16x32_bf16 v[56:59], a[60:63], v[88:91], v[56:59]\n"
"s_waitcnt vmcnt(11) lgkmcnt(7)\n"
"v_max3_u32 v72, v224, v225, v226\n"
"v_mfma_f32_16x16x32_bf16 v[204:207], a[60:63], v[40:43], v[204:207]\n"
"buffer_load_dwordx4 a[88:91], v0, s[8:11], s90 offen offset:1024 sc1\n"
"ds_write_b128 v2, v[124:127] offset:16384\n"
"v_mfma_f32_16x16x32_bf16 v[180:183], a[36:39], v[88:91], v[180:183]\n"
"s_waitcnt vmcnt(11) lgkmcnt(7)\n"
"v_max3_u32 v72, v72, v227, v160\n"
"v_max3_u32 v72, v72, v161, v162\n"
"v_max_u32 v72, v72, v163\n"
"v_cmp_eq_u32 vcc, -1, v72\n"
"v_mfma_f32_16x16x32_bf16 v[212:215], a[36:39], v[40:43], v[212:215]\n"
"v_mfma_f32_16x16x32_bf16 v[80:83], a[32:35], v[88:91], v[80:83]\n"
"s_cbranch_vccnz Ls57_%=\n"
"Lb57_%=:\n"
"ds_read_b128 v[220:223], v5 offset:0\n"
"ds_write_b128 v2, v[224:227] offset:17408\n"
"v_mfma_f32_16x16x32_bf16 v[116:119], a[32:35], v[40:43], v[116:119]\n"
"buffer_load_dwordx4 a[80:83], v0, s[8:11], s90 offen offset:2048 sc1\n"
"ds_read_b128 v[32:35], v5 offset:5120\n"
"s_waitcnt vmcnt(11) lgkmcnt(7)\n"
"v_max3_u32 v112, v84, v85, v86\n"
"v_mfma_f32_16x16x32_bf16 v[200:203], a[56:59], v[240:243], v[200:203]\n"
"buffer_load_dwordx4 a[100:103], v0, s[8:11], s90 offen offset:3072 sc1\n"
"ds_write_b128 v2, v[160:163] offset:18432\n"
"v_mfma_f32_16x16x32_bf16 v[36:39], a[56:59], v[120:123], v[36:39]\n"
"s_add_u32 s90, s52, 0x2c000\n"
"s_waitcnt lgkmcnt(6)\n"
"v_fmac_f32 v56, v252, v144\n"
"v_fmac_f32 v57, v252, v145\n"
"v_fmac_f32 v58, v252, v146\n"
"v_fmac_f32 v59, v252, v147\n"
"s_waitcnt vmcnt(11)\n"
"v_max3_u32 v112, v112, v87, v244\n"
"v_mfma_f32_16x16x32_bf16 v[56:59], a[68:71], v[240:243], v[56:59]\n"
"buffer_load_dwordx4 a[108:111], v0, s[8:11], s90 offen offset:0 sc1\n"
"v_max3_u32 v112, v112, v245, v246\n"
"v_max_u32 v112, v112, v247\n"
"v_cmp_eq_u32 vcc, -1, v112\n"
"v_mfma_f32_16x16x32_bf16 v[204:207], a[68:71], v[120:123], v[204:207]\n"
"v_mfma_f32_16x16x32_bf16 v[180:183], a[52:55], v[240:243], v[180:183]\n"
"s_cbranch_vccnz Ls58_%=\n"
"Lb58_%=:\n"
"ds_write_b128 v2, v[84:87] offset:19456\n"
"v_mfma_f32_16x16x32_bf16 v[212:215], a[52:55], v[120:123], v[212:215]\n"
"buffer_load_dwordx4 a[104:107], v0, s[8:11], s90 offen offset:1024 sc1\n"
"v_add_f32 v244, v244, v246\n"
"v_add_f32 v245, v245, v247\n"
"s_waitcnt vmcnt(12) lgkmcnt(4)\n"
"v_max3_u32 v92, v248, v249, v250\n"
"v_mfma_f32_16x16x32_bf16 v[80:83], a[48:51], v[240:243], v[80:83]\n"
"ds_read_b128 v[64:67], v5 offset:1024\n"
"ds_write_b64 v21, v[244:245] offset:4096\n"
"s_waitcnt vmcnt(11)\n"
"v_max3_u32 v92, v92, v251, v196\n"
"v_mfma_f32_16x16x32_bf16 v[116:119], a[48:51], v[120:123], v[116:119]\n"
"s_waitcnt vmcnt(10) lgkmcnt(5)\n"
"ds_read_b128 v[184:187], v5 offset:6144\n"
"v_fmac_f32 v204, v236, v144\n"
"v_max3_u32 v92, v92, v197, v198\n"
"v_mfma_f32_16x16x32_bf16 v[200:203], a[72:75], v[168:171], v[200:203]\n"
"buffer_load_dwordx4 a[96:99], v0, s[8:11], s90 offen offset:2048 sc1\n"
"v_mfma_f32_16x16x32_bf16 v[36:39], a[72:75], v[172:175], v[36:39]\n"
"v_fmac_f32 v205, v236, v145\n"
"v_fmac_f32 v206, v236, v146\n"
"v_fmac_f32 v207, v236, v147\n"
"ds_read_b128 v[48:51], v20 offset:128\n"
"buffer_load_dwordx4 a[116:119], v0, s[8:11], s90 offen offset:3072 sc1\n"
"s_add_u32 s90, s52, 0x30000\n"
"buffer_load_dwordx4 a[124:127], v0, s[8:11], s90 offen offset:0 sc1\n"
"s_waitcnt vmcnt(11) lgkmcnt(4)\n"
"v_max3_u32 v92, v92, v199, v44\n"
"v_max3_u32 v92, v92, v45, v46\n"
"buffer_load_dwordx4 a[120:123], v0, s[8:11], s90 offen offset:1024 sc1\n"
"s_waitcnt vmcnt(11) lgkmcnt(1)\n"
"v_max3_u32 v92, v92, v47, v76\n"
"s_waitcnt vmcnt(10)\n"
"v_mfma_f32_16x16x32_bf16 v[56:59], a[84:87], v[168:171], v[56:59]\n"
"v_max3_u32 v92, v92, v77, v78\n"
"v_max_u32 v92, v92, v79\n"
"v_cmp_eq_u32 vcc, -1, v92\n"
"v_mfma_f32_16x16x32_bf16 v[204:207], a[84:87], v[172:175], v[204:207]\n"
"v_mfma_f32_16x16x32_bf16 v[180:183], a[64:67], v[168:171], v[180:183]\n"
"s_cbranch_vccnz Ls59_%=\n"
"Lb59_%=:\n"
"v_add_f32 v248, v248, v250\n"
"v_add_f32 v249, v249, v251\n"
"v_mfma_f32_16x16x32_bf16 v[212:215], a[64:67], v[172:175], v[212:215]\n"
"buffer_load_dwordx4 a[112:115], v0, s[8:11], s90 offen offset:2048 sc1\n"
"ds_write_b64 v21, v[248:249] offset:6144\n"
"ds_write_b128 v3, v[196:199] offset:128\n"
"v_mfma_f32_16x16x32_bf16 v[80:83], a[76:79], v[168:171], v[80:83]\n"
"ds_read_b128 v[164:167], v5 offset:2048\n"
"v_mfma_f32_16x16x32_bf16 v[116:119], a[76:79], v[172:175], v[116:119]\n"
"s_waitcnt vmcnt(9) lgkmcnt(3)\n"
"buffer_load_dwordx4 a[132:135], v0, s[8:11], s90 offen offset:3072 sc1\n"
"ds_read_b128 v[140:143], v5 offset:7168\n"
"v_mfma_f32_16x16x32_bf16 v[200:203], a[88:91], v[220:223], v[200:203]\n"
"s_add_u32 s90, s52, 0x34000\n"
"v_fmac_f32 v180, v252, v48\n"
"v_fmac_f32 v181, v252, v49\n"
"v_mfma_f32_16x16x32_bf16 v[36:39], a[88:91], v[32:35], v[36:39]\n"
"ds_write_b128 v3, v[44:47] offset:2176\n"
"v_fmac_f32 v182, v252, v50\n"
"v_fmac_f32 v183, v252, v51\n"
"buffer_load_dwordx4 a[140:143], v0, s[8:11], s90 offen offset:0 sc1\n"
"s_waitcnt vmcnt(9)\n"
"v_mfma_f32_16x16x32_bf16 v[56:59], a[100:103], v[220:223], v[56:59]\n"
"ds_write_b128 v3, v[76:79] offset:3200\n"
"v_mfma_f32_16x16x32_bf16 v[204:207], a[100:103], v[32:35], v[204:207]\n"
"v_mfma_f32_16x16x32_bf16 v[180:183], a[80:83], v[220:223], v[180:183]\n"
"v_mfma_f32_16x16x32_bf16 v[212:215], a[80:83], v[32:35], v[212:215]\n"
"buffer_load_dwordx4 a[136:139], v0, s[8:11], s90 offen offset:1024 sc1\n"
"v_mfma_f32_16x16x32_bf16 v[80:83], a[92:95], v[220:223], v[80:83]\n"
"ds_read_b128 v[228:231], v5 offset:3072\n"
"v_mfma_f32_16x16x32_bf16 v[116:119], a[92:95], v[32:35], v[116:119]\n"
"ds_read_b128 v[128:131], v5 offset:8192\n"
"s_nop 2\n"
"v_fmac_f32 v212, v236, v48\n"
"v_fmac_f32 v213, v236, v49\n"
"v_fmac_f32 v214, v236, v50\n"
"v_fmac_f32 v215, v236, v51\n"
"ds_read_b128 v[100:103], v20 offset:0\n"
"s_waitcnt vmcnt(8) lgkmcnt(7)\n"
"v_mfma_f32_16x16x32_bf16 v[200:203], a[104:107], v[64:67], v[200:203]\n"
"buffer_load_dwordx4 a[128:131], v0, s[8:11], s90 offen offset:2048 sc1\n"
"v_mfma_f32_16x16x32_bf16 v[36:39], a[104:107], v[184:187], v[36:39]\n"
"s_waitcnt lgkmcnt(3)\n"
"s_barrier\n"
"buffer_load_dwordx4 a[152:155], v0, s[8:11], s90 offen offset:3072 sc1\n"
"ds_read_b128 v[68:71], v27 offset:4096\n"
"ds_read_b128 v[208:211], v27 offset:4352\n"
"ds_read_b128 v[176:179], v27 offset:4608\n"
"ds_read_b128 v[148:151], v27 offset:4864\n"
"ds_read_b128 v[192:195], v0 offset:0\n"
"ds_read_b128 v[188:191], v0 offset:1024\n"
"ds_read_b128 v[28:31], v0 offset:2048\n"
"ds_read_b128 v[152:155], v0 offset:3072\n"
"ds_read_b128 v[136:139], v4 offset:10240\n"
"ds_read_b128 v[216:219], v4 offset:15360\n"
"ds_read_b128 v[124:127], v4 offset:11264\n"
"ds_read_b128 v[88:91], v4 offset:16384\n"
"s_waitcnt lgkmcnt(14)\n"
"ds_read_b128 v[72:75], v4 offset:12288\n"
"s_waitcnt lgkmcnt(14)\n"
"ds_read_b128 v[224:227], v4 offset:17408\n"
"s_waitcnt vmcnt(8)\n"
"v_mfma_f32_16x16x32_bf16 v[56:59], a[116:119], v[64:67], v[56:59]\n"
"s_add_u32 s90, s52, 0x38000\n"
"v_mfma_f32_16x16x32_bf16 v[204:207], a[116:119], v[184:187], v[204:207]\n"
"buffer_load_dwordx4 a[144:147], v0, s[8:11], s90 offen offset:0 sc1\n"
"v_mfma_f32_16x16x32_bf16 v[180:183], a[96:99], v[64:67], v[180:183]\n"
"v_mfma_f32_16x16x32_bf16 v[212:215], a[96:99], v[184:187], v[212:215]\n"
"v_mfma_f32_16x16x32_bf16 v[80:83], a[108:111], v[64:67], v[80:83]\n"
"buffer_load_dwordx4 a[164:167], v0, s[8:11], s90 offen offset:1024 sc1\n"
"s_waitcnt lgkmcnt(14)\n"
"ds_read_b128 v[40:43], v5 offset:4096\n"
"v_mfma_f32_16x16x32_bf16 v[116:119], a[108:111], v[184:187], v[116:119]\n"
"s_waitcnt lgkmcnt(14)\n"
"ds_read_b128 v[160:163], v5 offset:9216\n"
"s_waitcnt lgkmcnt(14)\n"
"v_add_f32 v68, v68, v208\n"
"v_fmac_f32 v80, v252, v100\n"
"s_waitcnt lgkmcnt(11)\n"
"v_mfma_f32_16x16x32_bf16 v[112:115], v[96:99], v[192:195], 0\n"
"v_fmac_f32 v81, v252, v101\n"
"v_fmac_f32 v82, v252, v102\n"
"v_mfma_f32_16x16x32_bf16 v[84:87], a[196:199], v[192:195], 0\n"
"buffer_load_dwordx4 a[148:151], v0, s[8:11], s90 offen offset:2048 sc1\n"
"v_mfma_f32_16x16x32_bf16 v[240:243], a[176:179], v[192:195], 0\n"
"s_waitcnt lgkmcnt(10)\n"
"ds_read_b128 v[244:247], v0 offset:4096\n"
"v_fmac_f32 v83, v252, v103\n"
"v_mfma_f32_16x16x32_bf16 v[112:115], a[200:203], v[188:191], v[112:115]\n"
"v_add_f32 v176, v176, v148\n"
"v_add_f32 v69, v69, v209\n"
"v_mfma_f32_16x16x32_bf16 v[84:87], a[212:215], v[188:191], v[84:87]\n"
"v_add_f32 v177, v177, v149\n"
"v_add_f32 v70, v70, v210\n"
"v_mfma_f32_16x16x32_bf16 v[240:243], a[192:195], v[188:191], v[240:243]\n"
"s_waitcnt lgkmcnt(10)\n"
"ds_read_b128 v[120:123], v0 offset:5120\n"
"v_add_f32 v178, v178, v150\n"
"v_mfma_f32_16x16x32_bf16 v[112:115], a[216:219], v[28:31], v[112:115]\n"
"buffer_load_dwordx4 a[156:159], v0, s[8:11], s90 offen offset:3072 sc1\n"
"v_mfma_f32_16x16x32_bf16 v[84:87], a[228:231], v[28:31], v[84:87]\n"
"s_add_u32 s90, s52, 0x3c000\n"
"v_add_f32 v71, v71, v211\n"
"v_add_f32 v179, v179, v151\n"
"v_mfma_f32_16x16x32_bf16 v[240:243], a[208:211], v[28:31], v[240:243]\n"
"s_waitcnt lgkmcnt(10)\n"
"ds_read_b128 v[144:147], v0 offset:6144\n"
"v_add_f32 v68, v68, v176\n"
"v_mfma_f32_16x16x32_bf16 v[112:115], a[232:235], v[152:155], v[112:115]\n"
"v_add_f32 v69, v69, v177\n"
"v_add_f32 v70, v70, v178\n"
"v_mfma_f32_16x16x32_bf16 v[84:87], a[244:247], v[152:155], v[84:87]\n"
"v_add_f32 v71, v71, v179\n"
"v_add_f32 v68, v68, v70\n"
"v_mfma_f32_16x16x32_bf16 v[240:243], a[224:227], v[152:155], v[240:243]\n"
"buffer_load_dwordx4 a[172:175], v0, s[8:11], s90 offen offset:0 sc1\n"
"ds_read_b128 v[92:95], v0 offset:7168\n"
"s_waitcnt lgkmcnt(3)\n"
"v_mfma_f32_16x16x32_bf16 v[112:115], a[248:251], v[244:247], v[112:115]\n"
"v_add_f32 v69, v69, v71\n"
"v_mul_f32 v208, 0xbacccccd, v68\n"
"v_mfma_f32_16x16x32_bf16 v[84:87], a[12:15], v[244:247], v[84:87]\n"
"v_mul_f32 v69, 0x3acccccd, v69\n"
"v_fma_f32 v69, -v208, v208, v69\n"
"v_mfma_f32_16x16x32_bf16 v[240:243], a[240:243], v[244:247], v[240:243]\n"
"s_waitcnt lgkmcnt(2)\n"
"ds_read_b128 v[248:251], v0 offset:8192\n"
"v_max_f32 v69, 0, v69\n"
"v_mfma_f32_16x16x32_bf16 v[112:115], a[8:11], v[120:123], v[112:115]\n"
"buffer_load_dwordx4 a[168:171], v0, s[8:11], s90 offen offset:1024 sc1\n"
"v_mfma_f32_16x16x32_bf16 v[84:87], a[28:31], v[120:123], v[84:87]\n"
"v_add_f32 v69, 0x3727c5ac, v69\n"
"v_mfma_f32_16x16x32_bf16 v[240:243], a[4:7], v[120:123], v[240:243]\n"
"s_waitcnt lgkmcnt(2)\n"
"ds_read_b128 v[196:199], v0 offset:9216\n"
"v_mfma_f32_16x16x32_bf16 v[112:115], a[24:27], v[144:147], v[112:115]\n"
"v_rsq_f32 v209, v69\n"
"v_mfma_f32_16x16x32_bf16 v[84:87], a[44:47], v[144:147], v[84:87]\n"
"buffer_load_dwordx4 a[160:163], v0, s[8:11], s90 offen offset:2048 sc1\n"
"v_mfma_f32_16x16x32_bf16 v[240:243], a[20:23], v[144:147], v[240:243]\n"
"s_waitcnt lgkmcnt(2)\n"
"ds_read_b128 v[168:171], v0 offset:10240\n"
"v_mul_f32 v210, v69, v209\n"
"v_mfma_f32_16x16x32_bf16 v[112:115], a[40:43], v[92:95], v[112:115]\n"
"v_mul_f32 v211, 0x3ea52b5c, v209\n"
"v_mfma_f32_16x16x32_bf16 v[84:87], a[60:63], v[92:95], v[84:87]\n"
"ds_write_b128 v27, v[208:211] offset:8448\n"
"ds_read_b128 v[172:175], v6 offset:0\n"
"v_mfma_f32_16x16x32_bf16 v[240:243], a[36:39], v[92:95], v[240:243]\n"
"s_waitcnt lgkmcnt(4)\n"
"buffer_load_dwordx4 a[184:187], v0, s[8:11], s90 offen offset:3072 sc1\n"
"ds_read_b128 v[44:47], v20 offset:64\n"
"ds_read_b128 v[76:79], v0 offset:11264\n"
"v_mfma_f32_16x16x32_bf16 v[112:115], a[56:59], v[248:251], v[112:115]\n"
"s_add_u32 s90, s52, 0x40000\n"
"v_mfma_f32_16x16x32_bf16 v[84:87], a[68:71], v[248:251], v[84:87]\n"
"v_mfma_f32_16x16x32_bf16 v[240:243], a[52:55], v[248:251], v[240:243]\n"
"s_waitcnt vmcnt(14)\n"
"ds_read_b128 v[220:223], v0 offset:12288\n"
"v_mfma_f32_16x16x32_bf16 v[200:203], a[120:123], v[164:167], v[200:203]\n"
"buffer_load_dwordx4 a[180:183], v0, s[8:11], s90 offen offset:0 sc1\n"
"v_mfma_f32_16x16x32_bf16 v[36:39], a[120:123], v[140:143], v[36:39]\n"
"s_waitcnt vmcnt(13)\n"
"v_mfma_f32_16x16x32_bf16 v[56:59], a[132:135], v[164:167], v[56:59]\n"
"v_mfma_f32_16x16x32_bf16 v[204:207], a[132:135], v[140:143], v[204:207]\n"
"v_mfma_f32_16x16x32_bf16 v[180:183], a[112:115], v[164:167], v[180:183]\n"
"v_mfma_f32_16x16x32_bf16 v[212:215], a[112:115], v[140:143], v[212:215]\n"
"buffer_load_dwordx4 a[188:191], v0, s[8:11], s90 offen offset:1024 sc1\n"
"v_mfma_f32_16x16x32_bf16 v[80:83], a[124:127], v[164:167], v[80:83]\n"
"ds_read_b128 v[32:35], v5 offset:20480\n"
"v_mfma_f32_16x16x32_bf16 v[116:119], a[124:127], v[140:143], v[116:119]\n"
"s_waitcnt vmcnt(12) lgkmcnt(6)\n"
"ds_read_b128 v[48:51], v5 offset:25600\n"
"v_mfma_f32_16x16x32_bf16 v[200:203], a[136:139], v[228:231], v[200:203]\n"
"s_waitcnt lgkmcnt(4)\n"
"v_fmac_f32 v112, v172, v44\n"
"v_fmac_f32 v113, v172, v45\n"
"v_mfma_f32_16x16x32_bf16 v[36:39], a[136:139], v[128:131], v[36:39]\n"
"s_waitcnt vmcnt(10)\n"
"v_fmac_f32 v116, v236, v100\n"
"v_fmac_f32 v117, v236, v101\n"
"v_mfma_f32_16x16x32_bf16 v[56:59], a[152:155], v[228:231], v[56:59]\n"
"v_fmac_f32 v118, v236, v102\n"
"v_fmac_f32 v119, v236, v103\n"
"v_cndmask_b32 v174, v173, 1.0, s[46:47]\n"
"v_cndmask_b32 v254, v253, 1.0, s[46:47]\n"
"v_cndmask_b32 v238, v237, 1.0, s[46:47]\n"
"v_mfma_f32_16x16x32_bf16 v[204:207], a[152:155], v[128:131], v[204:207]\n"
"v_fmac_f32 v114, v172, v46\n"
"v_fmac_f32 v115, v172, v47\n"
"ds_read_b128 v[64:67], v20 offset:192\n"
"v_mfma_f32_16x16x32_bf16 v[180:183], a[128:131], v[228:231], v[180:183]\n"
"s_add_u32 m0, s21, 0x0\n"
"s_nop 0\n"
"buffer_load_dwordx4 v0, s[8:11], s22 offen offset:0 sc1 lds\n"
"v_mfma_f32_16x16x32_bf16 v[112:115], a[72:75], v[196:199], v[112:115]\n"
"v_mfma_f32_16x16x32_bf16 v[84:87], a[84:87], v[196:199], v[84:87]\n"
"v_mfma_f32_16x16x32_bf16 v[240:243], a[64:67], v[196:199], v[240:243]\n"
"ds_read_b128 v[184:187], v0 offset:13312\n"
"v_mfma_f32_16x16x32_bf16 v[112:115], a[88:91], v[168:171], v[112:115]\n"
"buffer_load_dwordx4 v0, s[8:11], s22 offen offset:1024 sc1 lds\n"
"v_mfma_f32_16x16x32_bf16 v[212:215], a[128:131], v[128:131], v[212:215]\n"
"v_mfma_f32_16x16x32_bf16 v[80:83], a[140:143], v[228:231], v[80:83]\n"
"ds_read_b128 v[192:195], v5 offset:21504\n"
"v_mfma_f32_16x16x32_bf16 v[116:119], a[140:143], v[128:131], v[116:119]\n"
"s_waitcnt vmcnt(10) lgkmcnt(3)\n"
"ds_read_b128 v[188:191], v5 offset:26624\n"
"v_mfma_f32_16x16x32_bf16 v[200:203], a[164:167], v[40:43], v[200:203]\n"
"buffer_load_dwordx4 v0, s[8:11], s22 offen offset:2048 sc1 lds\n"
"v_mfma_f32_16x16x32_bf16 v[36:39], a[164:167], v[160:163], v[36:39]\n"
"s_waitcnt vmcnt(9)\n"
"s_waitcnt lgkmcnt(3)\n"
"v_fmac_f32 v84, v172, v64\n"
"v_mfma_f32_16x16x32_bf16 v[56:59], a[156:159], v[40:43], v[56:59]\n"
"v_fmac_f32 v85, v172, v65\n"
"v_fmac_f32 v86, v172, v66\n"
"v_mfma_f32_16x16x32_bf16 v[204:207], a[156:159], v[160:163], v[204:207]\n"
"v_fmac_f32 v87, v172, v67\n"
"ds_read_b128 v[148:151], v20 offset:128\n"
"v_mfma_f32_16x16x32_bf16 v[180:183], a[148:151], v[40:43], v[180:183]\n"
"v_mfma_f32_16x16x32_bf16 v[84:87], a[100:103], v[168:171], v[84:87]\n"
"buffer_load_dwordx4 v0, s[8:11], s22 offen offset:3072 sc1 lds\n"
"v_mfma_f32_16x16x32_bf16 v[240:243], a[80:83], v[168:171], v[240:243]\n"
"s_add_u32 s22, s52, 0x4c000\n"
"ds_read_b128 v[28:31], v0 offset:14336\n"
"v_mfma_f32_16x16x32_bf16 v[112:115], a[104:107], v[76:79], v[112:115]\n"
"v_mfma_f32_16x16x32_bf16 v[84:87], a[116:119], v[76:79], v[84:87]\n"
"v_mfma_f32_16x16x32_bf16 v[212:215], a[148:151], v[160:163], v[212:215]\n"
"s_add_u32 m0, s21, 0x1000\n"
"s_nop 0\n"
"buffer_load_dwordx4 v0, s[8:11], s22 offen offset:0 sc1 lds\n"
"v_mfma_f32_16x16x32_bf16 v[80:83], a[144:147], v[40:43], v[80:83]\n"
"ds_read_b128 v[176:179], v5 offset:22528\n"
"v_mfma_f32_16x16x32_bf16 v[116:119], a[144:147], v[160:163], v[116:119]\n"
"s_waitcnt vmcnt(9) lgkmcnt(4)\n"
"ds_read_b128 v[152:155], v5 offset:27648\n"
"v_mfma_f32_16x16x32_bf16 v[200:203], a[168:171], v[32:35], v[200:203]\n"
"s_waitcnt lgkmcnt(3)\n"
"v_fmac_f32 v240, v172, v148\n"
"v_fmac_f32 v241, v172, v149\n"
"v_mfma_f32_16x16x32_bf16 v[36:39], a[168:171], v[48:51], v[36:39]\n"
"buffer_load_dwordx4 v0, s[8:11], s22 offen offset:1024 sc1 lds\n"
"v_mfma_f32_16x16x32_bf16 v[244:247], v[96:99], v[136:139], 0\n"
"v_fmac_f32 v242, v172, v150\n"
"s_waitcnt vmcnt(8)\n"
"v_mfma_f32_16x16x32_bf16 v[56:59], a[184:187], v[32:35], v[56:59]\n"
"v_fmac_f32 v243, v172, v151\n"
"v_mfma_f32_16x16x32_bf16 v[204:207], a[184:187], v[48:51], v[204:207]\n"
"s_nop 0\n"
"v_mfma_f32_16x16x32_bf16 v[240:243], a[96:99], v[76:79], v[240:243]\n"
"ds_read_b128 v[120:123], v0 offset:15360\n"
"v_mfma_f32_16x16x32_bf16 v[112:115], a[120:123], v[220:223], v[112:115]\n"
"buffer_load_dwordx4 v0, s[8:11], s22 offen offset:2048 sc1 lds\n"
"v_mfma_f32_16x16x32_bf16 v[84:87], a[132:135], v[220:223], v[84:87]\n"
"v_mfma_f32_16x16x32_bf16 v[240:243], a[112:115], v[220:223], v[240:243]\n"
"ds_read_b128 v[144:147], v0 offset:16384\n"
"v_mfma_f32_16x16x32_bf16 v[112:115], a[136:139], v[184:187], v[112:115]\n"
"v_mfma_f32_16x16x32_bf16 v[84:87], a[152:155], v[184:187], v[84:87]\n"
"v_mfma_f32_16x16x32_bf16 v[240:243], a[128:131], v[184:187], v[240:243]\n"
"s_waitcnt lgkmcnt(4)\n"
"ds_read_b128 v[68:71], v0 offset:17408\n"
"v_mfma_f32_16x16x32_bf16 v[112:115], a[164:167], v[28:31], v[112:115]\n"
"buffer_load_dwordx4 v0, s[8:11], s22 offen offset:3072 sc1 lds\n"
"v_mfma_f32_16x16x32_bf16 v[84:87], a[156:159], v[28:31], v[84:87]\n"
"s_add_u32 s22, s55, 0x9000\n"
"v_mfma_f32_16x16x32_bf16 v[240:243], a[148:151], v[28:31], v[240:243]\n"
"s_waitcnt lgkmcnt(2)\n"
"ds_read_b128 v[208:211], v0 offset:18432\n"
"v_mfma_f32_16x16x32_bf16 v[112:115], a[168:171], v[120:123], v[112:115]\n"
"v_mfma_f32_16x16x32_bf16 v[84:87], a[184:187], v[120:123], v[84:87]\n"
"v_mfma_f32_16x16x32_bf16 v[240:243], a[160:163], v[120:123], v[240:243]\n"
"s_waitcnt vmcnt(8) lgkmcnt(2)\n"
"ds_read_b128 v[92:95], v0 offset:19456\n"
"v_mfma_f32_16x16x32_bf16 v[112:115], a[188:191], v[144:147], v[112:115]\n"
"v_mfma_f32_16x16x32_bf16 v[180:183], a[160:163], v[32:35], v[180:183]\n"
"v_mfma_f32_16x16x32_bf16 v[212:215], a[160:163], v[48:51], v[212:215]\n"
"v_mfma_f32_16x16x32_bf16 v[80:83], a[172:175], v[32:35], v[80:83]\n"
"ds_read_b128 v[248:251], v5 offset:23552\n"
"v_mfma_f32_16x16x32_bf16 v[116:119], a[172:175], v[48:51], v[116:119]\n"
"ds_read_b128 v[164:167], v5 offset:28672\n"
"v_mfma_f32_16x16x32_bf16 v[200:203], a[188:191], v[192:195], v[200:203]\n"
"v_mfma_f32_16x16x32_bf16 v[36:39], a[188:191], v[188:191], v[36:39]\n"
"v_mfma_f32_16x16x32_bf16 v[140:143], v[96:99], v[216:219], 0\n"
"v_mfma_f32_16x16x32_bf16 v[100:103], a[196:199], v[136:139], 0\n"
"v_mfma_f32_16x16x32_bf16 v[44:47], a[196:199], v[216:219], 0\n"
"buffer_load_dwordx4 a[196:199], v0, s[8:11], s90 offen offset:2048 sc1\n"
"v_mfma_f32_16x16x32_bf16 v[196:199], a[176:179], v[136:139], 0\n"
"v_mfma_f32_16x16x32_bf16 v[228:231], a[176:179], v[216:219], 0\n"
"buffer_load_dwordx4 a[176:179], v0, s[8:11], s90 offen offset:3072 sc1\n"
"v_mfma_f32_16x16x32_bf16 v[128:131], v[132:135], v[136:139], 0\n"
"s_add_u32 s90, s52, 0x44000\n"
"ds_read_b128 v[64:67], v4 offset:13312\n"
"v_mfma_f32_16x16x32_bf16 v[168:171], v[132:135], v[216:219], 0\n"
"ds_read_b128 v[40:43], v4 offset:18432\n"
"v_mfma_f32_16x16x32_bf16 v[244:247], a[200:203], v[124:127], v[244:247]\n"
"v_mfma_f32_16x16x32_bf16 v[140:143], a[200:203], v[88:91], v[140:143]\n"
"buffer_load_dwordx4 a[200:203], v0, s[8:11], s90 offen offset:0 sc1\n"
"v_mfma_f32_16x16x32_bf16 v[100:103], a[212:215], v[124:127], v[100:103]\n"
"v_mfma_f32_16x16x32_bf16 v[44:47], a[212:215], v[88:91], v[44:47]\n"
"buffer_load_dwordx4 a[212:215], v0, s[8:11], s90 offen offset:1024 sc1\n"
"v_mfma_f32_16x16x32_bf16 v[196:199], a[192:195], v[124:127], v[196:199]\n"
"v_mfma_f32_16x16x32_bf16 v[228:231], a[192:195], v[88:91], v[228:231]\n"
"v_mfma_f32_16x16x32_bf16 v[128:131], a[204:207], v[124:127], v[128:131]\n"
"ds_read_b128 v[160:163], v4 offset:14336\n"
"v_mfma_f32_16x16x32_bf16 v[168:171], a[204:207], v[88:91], v[168:171]\n"
"ds_read_b128 v[148:151], v4 offset:19456\n"
"v_mfma_f32_16x16x32_bf16 v[244:247], a[216:219], v[72:75], v[244:247]\n"
"buffer_load_dwordx4 a[192:195], v0, s[8:11], s90 offen offset:2048 sc1\n"
"v_mfma_f32_16x16x32_bf16 v[140:143], a[216:219], v[224:227], v[140:143]\n"
"v_mfma_f32_16x16x32_bf16 v[100:103], a[228:231], v[72:75], v[100:103]\n"
"v_mfma_f32_16x16x32_bf16 v[44:47], a[228:231], v[224:227], v[44:47]\n"
"v_mfma_f32_16x16x32_bf16 v[196:199], a[208:211], v[72:75], v[196:199]\n"
"v_mfma_f32_16x16x32_bf16 v[228:231], a[208:211], v[224:227], v[228:231]\n"
"buffer_load_dwordx4 a[204:207], v0, s[8:11], s90 offen offset:3072 sc1\n"
"v_mfma_f32_16x16x32_bf16 v[128:131], a[220:223], v[72:75], v[128:131]\n"
"ds_read_b128 v[76:79], v4 offset:30720\n"
"v_add_u32 v221, s21, v0\n"
"v_mfma_f32_16x16x32_bf16 v[168:171], a[220:223], v[224:227], v[168:171]\n"
"s_waitcnt lgkmcnt(3)\n"
"ds_read_b128 v[184:187], v4 offset:35840\n"
"v_mfma_f32_16x16x32_bf16 v[244:247], a[232:235], v[64:67], v[244:247]\n"
"s_waitcnt vmcnt(13)\n"
"ds_read_b128 a[216:219], v221 offset:0\n"
"v_mfma_f32_16x16x32_bf16 v[140:143], a[232:235], v[40:43], v[140:143]\n"
"s_waitcnt vmcnt(12)\n"
"ds_read_b128 a[228:231], v221 offset:1024\n"
"v_mfma_f32_16x16x32_bf16 v[100:103], a[244:247], v[64:67], v[100:103]\n"
"s_waitcnt vmcnt(11)\n"
"ds_read_b128 a[208:211], v221 offset:2048\n"
"v_mfma_f32_16x16x32_bf16 v[44:47], a[244:247], v[40:43], v[44:47]\n"
"s_waitcnt vmcnt(10)\n"
"ds_read_b128 a[220:223], v221 offset:3072\n"
"s_waitcnt vmcnt(9)\n"
"ds_read_b128 a[232:235], v221 offset:4096\n"
"s_waitcnt vmcnt(8) lgkmcnt(8)\n"
"ds_read_b128 a[244:247], v221 offset:5120\n"
"v_mfma_f32_16x16x32_bf16 v[196:199], a[224:227], v[64:67], v[196:199]\n"
"v_mfma_f32_16x16x32_bf16 v[228:231], a[224:227], v[40:43], v[228:231]\n"
"s_waitcnt vmcnt(7) lgkmcnt(8)\n"
"ds_read_b128 a[224:227], v221 offset:6144\n"
"v_mfma_f32_16x16x32_bf16 v[128:131], a[236:239], v[64:67], v[128:131]\n"
"ds_read_b128 v[28:31], v4 offset:31744\n"
"v_mfma_f32_16x16x32_bf16 v[168:171], a[236:239], v[40:43], v[168:171]\n"
"s_waitcnt vmcnt(6)\n"
"ds_read_b128 a[236:239], v221 offset:7168\n"
"ds_read_b128 v[120:123], v22 offset:256\n"
"ds_read_b128 v[32:35], v22 offset:1280\n"
"ds_read_b128 v[48:51], v4 offset:36864\n"
"v_mfma_f32_16x16x32_bf16 v[244:247], a[248:251], v[160:163], v[244:247]\n"
"s_add_u32 s90, s54, 0x0\n"
"v_mfma_f32_16x16x32_bf16 v[140:143], a[248:251], v[148:151], v[140:143]\n"
"buffer_load_dwordx4 a[248:251], v0, s[8:11], s90 offen offset:0 sc1\n"
"v_mfma_f32_16x16x32_bf16 v[100:103], a[12:15], v[160:163], v[100:103]\n"
"v_mfma_f32_16x16x32_bf16 v[44:47], a[12:15], v[148:151], v[44:47]\n"
"buffer_load_dwordx4 a[12:15], v0, s[8:11], s90 offen offset:1024 sc1\n"
"v_mfma_f32_16x16x32_bf16 v[196:199], a[240:243], v[160:163], v[196:199]\n"
"v_mfma_f32_16x16x32_bf16 v[228:231], a[240:243], v[148:151], v[228:231]\n"
"buffer_load_dwordx4 a[240:243], v0, s[8:11], s90 offen offset:2048 sc1\n"
"v_mfma_f32_16x16x32_bf16 v[128:131], a[252:255], v[160:163], v[128:131]\n"
"ds_read_b128 v[96:99], v4 offset:32768\n"
"v_mfma_f32_16x16x32_bf16 v[168:171], a[252:255], v[148:151], v[168:171]\n"
"s_waitcnt lgkmcnt(10)\n"
"ds_read_b128 v[136:139], v20 offset:64\n"
"ds_read_b128 v[132:135], v4 offset:37888\n"
"v_mfma_f32_16x16x32_bf16 v[244:247], a[8:11], v[76:79], v[244:247]\n"
"v_mfma_f32_16x16x32_bf16 v[140:143], a[8:11], v[184:187], v[140:143]\n"
"buffer_load_dwordx4 a[252:255], v0, s[8:11], s90 offen offset:3072 sc1\n"
"v_mfma_f32_16x16x32_bf16 v[100:103], a[28:31], v[76:79], v[100:103]\n"
"s_add_u32 s90, s54, 0x1000\n"
"s_waitcnt vmcnt(8) lgkmcnt(8)\n"
"v_mfma_f32_16x16x32_bf16 v[84:87], a[176:179], v[144:147], v[84:87]\n"
"v_mfma_f32_16x16x32_bf16 v[240:243], a[196:199], v[144:147], v[240:243]\n"
"v_mfma_f32_16x16x32_bf16 v[56:59], a[176:179], v[192:195], v[56:59]\n"
"v_mfma_f32_16x16x32_bf16 v[204:207], a[176:179], v[188:191], v[204:207]\n"
"buffer_load_dwordx4 a[8:11], v0, s[8:11], s90 offen offset:0 sc1\n"
"v_mfma_f32_16x16x32_bf16 v[180:183], a[196:199], v[192:195], v[180:183]\n"
"s_waitcnt lgkmcnt(1)\n"
"v_fmac_f32 v244, v120, v136\n"
"v_fmac_f32 v245, v120, v137\n"
"v_mfma_f32_16x16x32_bf16 v[212:215], a[196:199], v[188:191], v[212:215]\n"
"v_fmac_f32 v246, v120, v138\n"
"v_fmac_f32 v247, v120, v139\n"
"v_mfma_f32_16x16x32_bf16 v[80:83], a[180:183], v[192:195], v[80:83]\n"
"ds_read_b128 v[216:219], v5 offset:24576\n"
"v_mfma_f32_16x16x32_bf16 v[116:119], a[180:183], v[188:191], v[116:119]\n"
"ds_read_b128 v[124:127], v5 offset:29696\n"
"s_add_u32 s2, s57, 0x56000\n"
"v_mfma_f32_16x16x32_bf16 v[44:47], a[28:31], v[184:187], v[44:47]\n"
"buffer_load_dwordx4 a[28:31], v0, s[8:11], s90 offen offset:1024 sc1\n"
"s_waitcnt vmcnt(8)\n"
"v_mfma_f32_16x16x32_bf16 v[112:115], a[212:215], v[68:71], v[112:115]\n"
"v_mfma_f32_16x16x32_bf16 v[200:203], a[212:215], v[176:179], v[200:203]\n"
"v_mfma_f32_16x16x32_bf16 v[36:39], a[212:215], v[152:155], v[36:39]\n"
"v_mfma_f32_16x16x32_bf16 v[196:199], a[4:7], v[76:79], v[196:199]\n"
"v_mfma_f32_16x16x32_bf16 v[228:231], a[4:7], v[184:187], v[228:231]\n"
"buffer_load_dwordx4 a[4:7], v0, s[8:11], s90 offen offset:2048 sc1\n"
"v_mfma_f32_16x16x32_bf16 v[128:131], a[0:3], v[76:79], v[128:131]\n"
"ds_read_b128 v[88:91], v4 offset:33792\n"
"v_mfma_f32_16x16x32_bf16 v[168:171], a[0:3], v[184:187], v[168:171]\n"
"ds_read_b128 v[72:75], v4 offset:38912\n"
"v_mfma_f32_16x16x32_bf16 v[244:247], a[24:27], v[28:31], v[244:247]\n"
"v_mfma_f32_16x16x32_bf16 v[140:143], a[24:27], v[48:51], v[140:143]\n"
"buffer_load_dwordx4 a[0:3], v0, s[8:11], s90 offen offset:3072 sc1\n"
"s_waitcnt vmcnt(8) lgkmcnt(4)\n"
"v_mfma_f32_16x16x32_bf16 v[84:87], a[204:207], v[68:71], v[84:87]\n"
"s_add_u32 s90, s54, 0x2000\n"
"v_mfma_f32_16x16x32_bf16 v[240:243], a[192:195], v[68:71], v[240:243]\n"
"v_mfma_f32_16x16x32_bf16 v[112:115], a[228:231], v[208:211], v[112:115]\n"
"v_mfma_f32_16x16x32_bf16 v[84:87], a[220:223], v[208:211], v[84:87]\n"
"buffer_load_dwordx4 a[24:27], v0, s[8:11], s90 offen offset:0 sc1\n"
"v_mfma_f32_16x16x32_bf16 v[240:243], a[208:211], v[208:211], v[240:243]\n"
"v_fmac_f32 v140, v32, v136\n"
"v_fmac_f32 v141, v32, v137\n"
"v_mfma_f32_16x16x32_bf16 v[112:115], a[244:247], v[92:95], v[112:115]\n"
"v_fmac_f32 v142, v32, v138\n"
"v_fmac_f32 v143, v32, v139\n"
"v_mfma_f32_16x16x32_bf16 v[84:87], a[236:239], v[92:95], v[84:87]\n"
"v_mfma_f32_16x16x32_bf16 v[240:243], a[224:227], v[92:95], v[240:243]\n"
"v_mfma_f32_16x16x32_bf16 v[56:59], a[204:207], v[176:179], v[56:59]\n"
"v_mfma_f32_16x16x32_bf16 v[204:207], a[204:207], v[152:155], v[204:207]\n"
"v_mfma_f32_16x16x32_bf16 v[180:183], a[192:195], v[176:179], v[180:183]\n"
"v_mfma_f32_16x16x32_bf16 v[212:215], a[192:195], v[152:155], v[212:215]\n"
"v_mul_f32 v112, v112, v173\n"
"v_mul_f32 v113, v113, v173\n"
"v_mfma_f32_16x16x32_bf16 v[80:83], a[200:203], v[176:179], v[80:83]\n"
"v_mul_f32 v114, v114, v173\n"
"v_mul_f32 v115, v115, v173\n"
"v_mfma_f32_16x16x32_bf16 v[116:119], a[200:203], v[152:155], v[116:119]\n"
"v_mul_f32 v84, v84, v174\n"
"v_mul_f32 v85, v85, v174\n"
"v_mfma_f32_16x16x32_bf16 v[200:203], a[228:231], v[248:251], v[200:203]\n"
"v_mul_f32 v86, v86, v174\n"
"v_mul_f32 v87, v87, v174\n"
"v_mfma_f32_16x16x32_bf16 v[36:39], a[228:231], v[164:167], v[36:39]\n"
"v_cvt_pk_bf16_f32 v112, v112, v113\n"
"v_cvt_pk_bf16_f32 v113, v114, v115\n"
"v_mfma_f32_16x16x32_bf16 v[56:59], a[220:223], v[248:251], v[56:59]\n"
"v_cvt_pk_bf16_f32 v114, v84, v85\n"
"v_cvt_pk_bf16_f32 v115, v86, v87\n"
"v_mfma_f32_16x16x32_bf16 v[204:207], a[220:223], v[164:167], v[204:207]\n"
"ds_write_b64 v23, v[114:115] offset:512\n"
"ds_read_b128 v[224:227], v5 offset:49152\n"
"ds_read_b128 v[64:67], v5 offset:46080\n"
"ds_read_b128 v[40:43], v5 offset:45056\n"
"ds_read_b128 v[220:223], v5 offset:46080\n"
"v_mfma_f32_16x16x32_bf16 v[180:183], a[208:211], v[248:251], v[180:183]\n"
"v_mfma_f32_16x16x32_bf16 v[212:215], a[208:211], v[164:167], v[212:215]\n"
"v_mfma_f32_16x16x32_bf16 v[80:83], a[216:219], v[248:251], v[80:83]\n"
"v_mfma_f32_16x16x32_bf16 v[116:119], a[216:219], v[164:167], v[116:119]\n"
"s_waitcnt lgkmcnt(5)\n"
"v_mfma_f32_16x16x32_bf16 v[200:203], a[244:247], v[216:219], v[200:203]\n"
"v_mfma_f32_16x16x32_bf16 v[36:39], a[244:247], v[124:127], v[36:39]\n"
"v_mfma_f32_16x16x32_bf16 v[56:59], a[236:239], v[216:219], v[56:59]\n"
"v_mfma_f32_16x16x32_bf16 v[204:207], a[236:239], v[124:127], v[204:207]\n"
"v_mfma_f32_16x16x32_bf16 v[180:183], a[224:227], v[216:219], v[180:183]\n"
"v_mfma_f32_16x16x32_bf16 v[212:215], a[224:227], v[124:127], v[212:215]\n"
"v_mfma_f32_16x16x32_bf16 v[80:83], a[232:235], v[216:219], v[80:83]\n"
"v_mfma_f32_16x16x32_bf16 v[116:119], a[232:235], v[124:127], v[116:119]\n"
"v_mfma_f32_16x16x32_bf16 v[100:103], a[44:47], v[28:31], v[100:103]\n"
"v_mul_f32 v200, v200, v253\n"
"v_mul_f32 v201, v201, v253\n"
"v_mul_f32 v202, v202, v253\n"
"v_mul_f32 v203, v203, v253\n"
"v_mul_f32 v56, v56, v254\n"
"v_mul_f32 v57, v57, v254\n"
"v_mfma_f32_16x16x32_bf16 v[44:47], a[44:47], v[48:51], v[44:47]\n"
"buffer_load_dwordx4 a[44:47], v0, s[8:11], s90 offen offset:1024 sc1\n"
"v_mul_f32 v58, v58, v254\n"
"v_mul_f32 v59, v59, v254\n"
"v_cvt_pk_bf16_f32 v200, v200, v201\n"
"v_cvt_pk_bf16_f32 v201, v202, v203\n"
"v_mfma_f32_16x16x32_bf16 v[196:199], a[20:23], v[28:31], v[196:199]\n"
"v_cvt_pk_bf16_f32 v202, v56, v57\n"
"v_cvt_pk_bf16_f32 v203, v58, v59\n"
"v_mfma_f32_16x16x32_bf16 v[228:231], a[20:23], v[48:51], v[228:231]\n"
"buffer_load_dwordx4 a[20:23], v0, s[8:11], s90 offen offset:2048 sc1\n"
"ds_write_b64 v23, v[202:203] offset:1536\n"
"v_mul_f32 v36, v36, v237\n"
"v_mfma_f32_16x16x32_bf16 v[128:131], a[16:19], v[28:31], v[128:131]\n"
"ds_read_b128 v[160:163], v4 offset:34816\n"
"v_mul_f32 v37, v37, v237\n"
"v_mul_f32 v38, v38, v237\n"
"v_mul_f32 v39, v39, v237\n"
"v_mul_f32 v204, v204, v238\n"
"v_mul_f32 v205, v205, v238\n"
"v_mfma_f32_16x16x32_bf16 v[168:171], a[16:19], v[48:51], v[168:171]\n"
"buffer_load_dwordx4 a[16:19], v0, s[8:11], s90 offen offset:3072 sc1\n"
"ds_read_b128 v[148:151], v20 offset:192\n"
"ds_read_b128 v[144:147], v4 offset:39936\n"
"v_mul_f32 v206, v206, v238\n"
"v_mul_f32 v207, v207, v238\n"
"v_mfma_f32_16x16x32_bf16 v[244:247], a[40:43], v[96:99], v[244:247]\n"
"s_add_u32 s90, s54, 0x3000\n"
"v_cvt_pk_bf16_f32 v36, v36, v37\n"
"v_cvt_pk_bf16_f32 v37, v38, v39\n"
"v_cvt_pk_bf16_f32 v38, v204, v205\n"
"v_cvt_pk_bf16_f32 v39, v206, v207\n"
"v_mfma_f32_16x16x32_bf16 v[140:143], a[40:43], v[132:135], v[140:143]\n"
"buffer_load_dwordx4 a[40:43], v0, s[8:11], s90 offen offset:0 sc1\n"
"ds_write_b64 v23, v[38:39] offset:2560\n"
"v_cvt_pk_bf16_f32 v80, v80, v81\n"
"v_mfma_f32_16x16x32_bf16 v[100:103], a[60:63], v[96:99], v[100:103]\n"
"v_cvt_pk_bf16_f32 v81, v82, v83\n"
"v_mov_b32 v82, 0\n"
"v_mov_b32 v83, 0\n"
"v_mfma_f32_16x16x32_bf16 v[44:47], a[60:63], v[132:135], v[44:47]\n"
"buffer_load_dwordx4 a[60:63], v0, s[8:11], s90 offen offset:1024 sc1\n"
"v_permlane16_swap_b32 v82, v202\n"
"v_permlane16_swap_b32 v83, v203\n"
"v_mfma_f32_16x16x32_bf16 v[196:199], a[36:39], v[96:99], v[196:199]\n"
"s_waitcnt lgkmcnt(2)\n"
"v_fmac_f32 v100, v120, v148\n"
"v_fmac_f32 v101, v120, v149\n"
"v_cvt_pk_bf16_f32 v116, v116, v117\n"
"v_cvt_pk_bf16_f32 v117, v118, v119\n"
"v_mov_b32 v118, 0\n"
"v_mov_b32 v119, 0\n"
"v_mfma_f32_16x16x32_bf16 v[228:231], a[36:39], v[132:135], v[228:231]\n"
"buffer_load_dwordx4 a[36:39], v0, s[8:11], s90 offen offset:2048 sc1\n"
"v_permlane16_swap_b32 v118, v38\n"
"v_permlane16_swap_b32 v119, v39\n"
"v_mfma_f32_16x16x32_bf16 v[224:227], v[112:115], v[80:83], v[224:227]\n"
"v_fmac_f32 v102, v120, v150\n"
"v_fmac_f32 v103, v120, v151\n"
"v_mfma_f32_16x16x32_bf16 v[64:67], v[200:203], v[80:83], v[64:67]\n"
"v_mfma_f32_16x16x32_bf16 v[40:43], v[200:203], v[116:119], v[40:43]\n"
"buffer_load_dwordx4 v[192:195], v0, s[8:11], s90 offen offset:3072 sc1\n"
"v_mfma_f32_16x16x32_bf16 v[220:223], v[36:39], v[116:119], v[220:223]\n"
"s_add_u32 s90, s54, 0x4000\n"
"v_mul_f32 v240, v240, v173\n"
"v_mul_f32 v241, v241, v173\n"
"v_mul_f32 v242, v242, v173\n"
"v_mul_f32 v243, v243, v173\n"
"v_cvt_pk_bf16_f32 v240, v240, v241\n"
"v_cvt_pk_bf16_f32 v241, v242, v243\n"
"v_mfma_f32_16x16x32_bf16 v[128:131], a[32:35], v[96:99], v[128:131]\n"
"ds_read_b128 v[188:191], v5 offset:10240\n"
"ds_write_b64 v23, v[240:241] offset:0\n"
"v_mul_f32 v180, v180, v253\n"
"v_mul_f32 v181, v181, v253\n"
"v_mfma_f32_16x16x32_bf16 v[168:171], a[32:35], v[132:135], v[168:171]\n"
"buffer_load_dwordx4 a[32:35], v0, s[8:11], s90 offen offset:0 sc1\n"
"ds_read_b128 v[76:79], v5 offset:15360\n"
"v_mul_f32 v182, v182, v253\n"
"v_mul_f32 v183, v183, v253\n"
"v_cvt_pk_bf16_f32 v180, v180, v181\n"
"v_mfma_f32_16x16x32_bf16 v[244:247], a[56:59], v[88:91], v[244:247]\n"
"v_cvt_pk_bf16_f32 v181, v182, v183\n"
"v_mfma_f32_16x16x32_bf16 v[140:143], a[56:59], v[72:75], v[140:143]\n"
"ds_write_b64 v23, v[180:181] offset:1024\n"
"v_mul_f32 v212, v212, v237\n"
"v_mul_f32 v213, v213, v237\n"
"v_mul_f32 v214, v214, v237\n"
"v_mfma_f32_16x16x32_bf16 v[100:103], a[68:71], v[88:91], v[100:103]\n"
"buffer_load_dwordx4 a[56:59], v0, s[8:11], s90 offen offset:1024 sc1\n"
"v_mul_f32 v215, v215, v237\n"
"v_cvt_pk_bf16_f32 v212, v212, v213\n"
"v_cvt_pk_bf16_f32 v213, v214, v215\n"
"v_mfma_f32_16x16x32_bf16 v[44:47], a[68:71], v[72:75], v[44:47]\n"
"ds_write_b64 v23, v[212:213] offset:2048\n"
"ds_read_b128 v[184:187], v5 offset:48128\n"
"ds_read_b64_tr_b16 v[68:69], v24 offset:0\n"
"ds_read_b64_tr_b16 v[70:71], v24 offset:1024\n"
"ds_read_b64_tr_b16 v[208:209], v24 offset:512\n"
"ds_read_b64_tr_b16 v[210:211], v24 offset:1536\n"
"ds_read_b64_tr_b16 v[136:137], v24 offset:1024\n"
"ds_read_b64_tr_b16 v[138:139], v24 offset:2048\n"
"ds_read_b64_tr_b16 v[92:93], v24 offset:1536\n"
"s_waitcnt lgkmcnt(13)\n"
"ds_read_b64_tr_b16 v[94:95], v24 offset:2560\n"
"v_mfma_f32_16x16x32_bf16 v[196:199], a[52:55], v[88:91], v[196:199]\n"
"buffer_load_dwordx4 a[68:71], v0, s[8:11], s90 offen offset:2048 sc1\n"
"v_max3_f32 v176, v224, v225, v226\n"
"v_max3_f32 v178, v64, v65, v66\n"
"v_max3_f32 v176, v176, v227, v67\n"
"v_max_f32 v176, v176, v178\n"
"v_mfma_f32_16x16x32_bf16 v[228:231], a[52:55], v[72:75], v[228:231]\n"
"v_fmac_f32 v44, v32, v148\n"
"v_fmac_f32 v45, v32, v149\n"
"v_max3_f32 v177, v40, v41, v42\n"
"v_max3_f32 v179, v220, v221, v222\n"
"v_max3_f32 v177, v177, v43, v223\n"
"v_max_f32 v177, v177, v179\n"
"v_mfma_f32_16x16x32_bf16 v[128:131], a[48:51], v[88:91], v[128:131]\n"
"buffer_load_dwordx4 a[52:55], v0, s[8:11], s90 offen offset:3072 sc1\n"
"ds_read_b128 v[152:155], v5 offset:11264\n"
"v_permlane32_swap_b32 v176, v177\n"
"v_max_f32 v176, v176, v177\n"
"v_mfma_f32_16x16x32_bf16 v[168:171], a[48:51], v[72:75], v[168:171]\n"
"s_add_u32 s90, s54, 0x5000\n"
"v_fmac_f32 v46, v32, v150\n"
"v_fmac_f32 v47, v32, v151\n"
"v_mov_b32 v177, v176\n"
"s_waitcnt lgkmcnt(11)\n"
"ds_read_b128 v[84:87], v20 offset:128\n"
"ds_read_b128 v[248:251], v5 offset:16384\n"
"v_mfma_f32_16x16x32_bf16 v[244:247], a[72:75], v[160:163], v[244:247]\n"
"buffer_load_dwordx4 a[48:51], v0, s[8:11], s90 offen offset:0 sc1\n"
"v_permlane16_swap_b32 v176, v177\n"
"v_max_f32 v176, v176, v177\n"
"v_mov_b32 v177, v176\n"
"v_mfma_f32_16x16x32_bf16 v[140:143], a[72:75], v[144:147], v[140:143]\n"
"v_mfma_f32_16x16x32_bf16 v[100:103], a[84:87], v[160:163], v[100:103]\n"
"v_permlane32_swap_b32 v176, v177\n"
"v_mul_f32 v176, v176, v255\n"
"v_fma_f32 v224, v224, v255, -v176\n"
"v_fma_f32 v225, v225, v255, -v176\n"
"v_fma_f32 v226, v226, v255, -v176\n"
"v_mfma_f32_16x16x32_bf16 v[44:47], a[84:87], v[144:147], v[44:47]\n"
"buffer_load_dwordx4 a[72:75], v0, s[8:11], s90 offen offset:1024 sc1\n"
"v_fma_f32 v227, v227, v255, -v176\n"
"v_fma_f32 v64, v64, v255, -v176\n"
"v_fma_f32 v65, v65, v255, -v176\n"
"v_fma_f32 v66, v66, v255, -v176\n"
"v_mfma_f32_16x16x32_bf16 v[196:199], a[64:67], v[160:163], v[196:199]\n"
"v_fma_f32 v67, v67, v255, -v176\n"
"v_exp_f32 v224, v224\n"
"v_exp_f32 v225, v225\n"
"v_exp_f32 v226, v226\n"
"v_mfma_f32_16x16x32_bf16 v[228:231], a[64:67], v[144:147], v[228:231]\n"
"buffer_load_dwordx4 a[84:87], v0, s[8:11], s90 offen offset:2048 sc1\n"
"v_exp_f32 v227, v227\n"
"v_exp_f32 v64, v64\n"
"v_mfma_f32_16x16x32_bf16 v[128:131], a[76:79], v[160:163], v[128:131]\n"
"ds_read_b128 v[164:167], v5 offset:12288\n"
"v_exp_f32 v65, v65\n"
"v_exp_f32 v66, v66\n"
"v_exp_f32 v67, v67\n"
"v_mfma_f32_16x16x32_bf16 v[168:171], a[76:79], v[144:147], v[168:171]\n"
"buffer_load_dwordx4 a[64:67], v0, s[8:11], s90 offen offset:3072 sc1\n"
"ds_read_b128 v[216:219], v5 offset:17408\n"
"v_cvt_pk_bf16_f32 v224, v224, v225\n"
"v_cvt_pk_bf16_f32 v225, v226, v227\n"
"v_cvt_pk_bf16_f32 v226, v64, v65\n"
"v_mfma_f32_16x16x32_bf16 v[244:247], a[88:91], v[188:191], v[244:247]\n"
"s_add_u32 s90, s54, 0x6000\n"
"v_cvt_pk_bf16_f32 v227, v66, v67\n"
"v_mfma_f32_16x16x32_bf16 v[140:143], a[88:91], v[76:79], v[140:143]\n"
"s_waitcnt lgkmcnt(2)\n"
"v_fmac_f32 v196, v120, v84\n"
"v_fmac_f32 v197, v120, v85\n"
"v_mfma_f32_16x16x32_bf16 v[124:127], v[184:187], v[224:227], 0\n"
"v_fmac_f32 v198, v120, v86\n"
"v_fmac_f32 v199, v120, v87\n"
"v_mfma_f32_16x16x32_bf16 v[56:59], v[68:71], v[224:227], 0\n"
"buffer_load_dwordx4 a[76:79], v0, s[8:11], s90 offen offset:0 sc1\n"
"v_mfma_f32_16x16x32_bf16 v[28:31], v[208:211], v[224:227], 0\n"
"s_add_u32 s90, s52, 0x50000\n"
"v_mul_f32 v177, v177, v239\n"
"v_fma_f32 v40, v40, v239, -v177\n"
"v_fma_f32 v41, v41, v239, -v177\n"
"v_fma_f32 v42, v42, v239, -v177\n"
"v_fma_f32 v43, v43, v239, -v177\n"
"v_fma_f32 v220, v220, v239, -v177\n"
"v_mfma_f32_16x16x32_bf16 v[100:103], a[100:103], v[188:191], v[100:103]\n"
"v_fma_f32 v221, v221, v239, -v177\n"
"v_fma_f32 v222, v222, v239, -v177\n"
"v_fma_f32 v223, v223, v239, -v177\n"
"v_exp_f32 v40, v40\n"
"v_exp_f32 v41, v41\n"
"v_mfma_f32_16x16x32_bf16 v[44:47], a[100:103], v[76:79], v[44:47]\n"
"buffer_load_dwordx4 a[88:91], v0, s[8:11], s90 offen offset:0 sc1\n"
"v_exp_f32 v42, v42\n"
"v_exp_f32 v43, v43\n"
"v_mfma_f32_16x16x32_bf16 v[196:199], a[80:83], v[188:191], v[196:199]\n"
"v_exp_f32 v220, v220\n"
"v_exp_f32 v221, v221\n"
"v_exp_f32 v222, v222\n"
"v_mfma_f32_16x16x32_bf16 v[228:231], a[80:83], v[76:79], v[228:231]\n"
"buffer_load_dwordx4 a[100:103], v0, s[8:11], s90 offen offset:1024 sc1\n"
"v_exp_f32 v223, v223\n"
"v_cvt_pk_bf16_f32 v40, v40, v41\n"
"v_cvt_pk_bf16_f32 v41, v42, v43\n"
"v_mfma_f32_16x16x32_bf16 v[128:131], a[92:95], v[188:191], v[128:131]\n"
"s_add_u32 s6, s53, 0x50800\n"
"ds_read_b128 v[48:51], v5 offset:13312\n"
"v_cvt_pk_bf16_f32 v42, v220, v221\n"
"v_cvt_pk_bf16_f32 v43, v222, v223\n"
"v_mfma_f32_16x16x32_bf16 v[168:171], a[92:95], v[76:79], v[168:171]\n"
"ds_read_b128 v[204:207], v5 offset:18432\n"
"v_fmac_f32 v228, v32, v84\n"
"v_mfma_f32_16x16x32_bf16 v[80:83], v[184:187], v[40:43], 0\n"
"buffer_load_dwordx4 a[80:83], v0, s[8:11], s6 offen offset:0 sc1\n"
"v_mfma_f32_16x16x32_bf16 v[200:203], v[136:139], v[40:43], 0\n"
"v_fmac_f32 v229, v32, v85\n"
"v_fmac_f32 v230, v32, v86\n"
"v_mfma_f32_16x16x32_bf16 v[36:39], v[92:95], v[40:43], 0\n"
"v_fmac_f32 v231, v32, v87\n"
"v_rcp_f32 v124, v124\n"
"ds_read_b128 v[116:119], v20 offset:0\n"
"v_mfma_f32_16x16x32_bf16 v[244:247], a[104:107], v[152:155], v[244:247]\n"
"buffer_load_dwordx4 a[92:95], v0, s[8:11], s6 offen offset:1024 sc1\n"
"v_mul_f32 v56, v56, v124\n"
"v_mul_f32 v57, v57, v124\n"
"v_mul_f32 v58, v58, v124\n"
"v_mul_f32 v59, v59, v124\n"
"v_mfma_f32_16x16x32_bf16 v[140:143], a[104:107], v[248:251], v[140:143]\n"
"s_add_u32 s90, s52, 0x54000\n"
"v_cvt_pk_bf16_f32 v172, v56, v57\n"
"v_cvt_pk_bf16_f32 v173, v58, v59\n"
"v_mul_f32 v28, v28, v124\n"
"v_mul_f32 v29, v29, v124\n"
"v_mul_f32 v30, v30, v124\n"
"v_mul_f32 v31, v31, v124\n"
"v_mfma_f32_16x16x32_bf16 v[100:103], a[116:119], v[152:155], v[100:103]\n"
"buffer_load_dwordx4 a[104:107], v0, s[8:11], s90 offen offset:0 sc1\n"
"v_cvt_pk_bf16_f32 v96, v28, v29\n"
"v_cvt_pk_bf16_f32 v97, v30, v31\n"
"v_rcp_f32 v80, v80\n"
"v_mfma_f32_16x16x32_bf16 v[44:47], a[116:119], v[248:251], v[44:47]\n"
"v_mul_f32 v200, v200, v80\n"
"v_mul_f32 v201, v201, v80\n"
"v_mul_f32 v202, v202, v80\n"
"v_mul_f32 v203, v203, v80\n"
"v_cvt_pk_bf16_f32 v174, v200, v201\n"
"v_cvt_pk_bf16_f32 v175, v202, v203\n"
"v_mfma_f32_16x16x32_bf16 v[196:199], a[96:99], v[152:155], v[196:199]\n"
"v_mul_f32 v36, v36, v80\n"
"v_mul_f32 v37, v37, v80\n"
"v_mul_f32 v38, v38, v80\n"
"v_mul_f32 v39, v39, v80\n"
"v_cvt_pk_bf16_f32 v98, v36, v37\n"
"v_cvt_pk_bf16_f32 v99, v38, v39\n"
"v_mfma_f32_16x16x32_bf16 v[228:231], a[96:99], v[248:251], v[228:231]\n"
"buffer_load_dwordx4 a[116:119], v0, s[8:11], s90 offen offset:1024 sc1\n"
"v_permlane16_swap_b32 v172, v174\n"
"v_permlane16_swap_b32 v173, v175\n"
"v_mfma_f32_16x16x32_bf16 v[128:131], a[108:111], v[152:155], v[128:131]\n"
"s_add_u32 s6, s53, 0x54800\n"
"ds_read_b128 v[240:243], v5 offset:14336\n"
"v_permlane16_swap_b32 v96, v98\n"
"v_permlane16_swap_b32 v97, v99\n"
"s_add_u32 s0, s65, 0x56000\n"
"v_mfma_f32_16x16x32_bf16 v[168:171], a[108:111], v[248:251], v[168:171]\n"
"s_waitcnt vmcnt(25) lgkmcnt(1)\n"
"buffer_load_dwordx4 a[96:99], v0, s[8:11], s6 offen offset:0 sc1\n"
"ds_read_b128 v[132:135], v5 offset:19456\n"
"buffer_store_dwordx4 v[172:175], v11, s[8:11], s0 offen offset:0 sc1\n"
"v_mfma_f32_16x16x32_bf16 v[244:247], a[120:123], v[164:167], v[244:247]\n"
"s_add_u32 s0, s66, 0x56000\n"
"v_fmac_f32 v128, v120, v116\n"
"v_fmac_f32 v129, v120, v117\n"
"v_mfma_f32_16x16x32_bf16 v[140:143], a[120:123], v[216:219], v[140:143]\n"
"v_fmac_f32 v130, v120, v118\n"
"v_fmac_f32 v131, v120, v119\n"
"v_mfma_f32_16x16x32_bf16 v[100:103], a[132:135], v[164:167], v[100:103]\n"
"v_mfma_f32_16x16x32_bf16 v[44:47], a[132:135], v[216:219], v[44:47]\n"
"buffer_store_dwordx2 v[96:97], v12, s[8:11], s0 offen offset:0 sc1\n"
"v_mfma_f32_16x16x32_bf16 v[196:199], a[112:115], v[164:167], v[196:199]\n"
"v_mfma_f32_16x16x32_bf16 v[228:231], a[112:115], v[216:219], v[228:231]\n"
"v_mfma_f32_16x16x32_bf16 v[128:131], a[124:127], v[164:167], v[128:131]\n"
"ds_read_b128 v[180:183], v5 offset:30720\n"
"v_mfma_f32_16x16x32_bf16 v[168:171], a[124:127], v[216:219], v[168:171]\n"
"ds_read_b128 v[212:215], v5 offset:35840\n"
"v_mfma_f32_16x16x32_bf16 v[244:247], a[136:139], v[48:51], v[244:247]\n"
"v_mfma_f32_16x16x32_bf16 v[140:143], a[136:139], v[204:207], v[140:143]\n"
"buffer_load_dwordx4 a[108:111], v0, s[8:11], s6 offen offset:1024 sc1\n"
"v_mfma_f32_16x16x32_bf16 v[100:103], a[152:155], v[48:51], v[100:103]\n"
"s_add_u32 s90, s52, 0x58000\n"
"v_mfma_f32_16x16x32_bf16 v[44:47], a[152:155], v[204:207], v[44:47]\n"
"v_mfma_f32_16x16x32_bf16 v[196:199], a[128:131], v[48:51], v[196:199]\n"
"v_fmac_f32 v168, v32, v116\n"
"v_fmac_f32 v169, v32, v117\n"
"v_mfma_f32_16x16x32_bf16 v[228:231], a[128:131], v[204:207], v[228:231]\n"
"buffer_load_dwordx4 a[120:123], v0, s[8:11], s90 offen offset:0 sc1\n"
"v_mfma_f32_16x16x32_bf16 v[128:131], a[140:143], v[48:51], v[128:131]\n"
"ds_read_b128 v[88:91], v5 offset:31744\n"
"v_fmac_f32 v170, v32, v118\n"
"v_fmac_f32 v171, v32, v119\n"
"v_cndmask_b32 v122, v121, 1.0, s[46:47]\n"
"v_cndmask_b32 v34, v33, 1.0, s[46:47]\n"
"v_mfma_f32_16x16x32_bf16 v[168:171], a[140:143], v[204:207], v[168:171]\n"
"s_waitcnt vmcnt(27) lgkmcnt(3)\n"
"ds_read_b128 v[72:75], v5 offset:36864\n"
"v_mfma_f32_16x16x32_bf16 v[244:247], a[164:167], v[240:243], v[244:247]\n"
"buffer_load_dwordx4 a[132:135], v0, s[8:11], s90 offen offset:1024 sc1\n"
"v_mfma_f32_16x16x32_bf16 v[140:143], a[164:167], v[132:135], v[140:143]\n"
"s_add_u32 s6, s53, 0x58800\n"
"v_mfma_f32_16x16x32_bf16 v[100:103], a[156:159], v[240:243], v[100:103]\n"
"v_mfma_f32_16x16x32_bf16 v[44:47], a[156:159], v[132:135], v[44:47]\n"
"v_mfma_f32_16x16x32_bf16 v[196:199], a[148:151], v[240:243], v[196:199]\n"
"v_mfma_f32_16x16x32_bf16 v[228:231], a[148:151], v[132:135], v[228:231]\n"
"buffer_load_dwordx4 a[112:115], v0, s[8:11], s6 offen offset:0 sc1\n"
"v_mfma_f32_16x16x32_bf16 v[128:131], a[144:147], v[240:243], v[128:131]\n"
"ds_read_b128 v[148:151], v5 offset:32768\n"
"v_mfma_f32_16x16x32_bf16 v[168:171], a[144:147], v[132:135], v[168:171]\n"
"s_waitcnt lgkmcnt(3)\n"
"ds_read_b128 v[252:255], v5 offset:37888\n"
"v_mfma_f32_16x16x32_bf16 v[244:247], a[168:171], v[180:183], v[244:247]\n"
"v_mfma_f32_16x16x32_bf16 v[140:143], a[168:171], v[212:215], v[140:143]\n"
"buffer_load_dwordx4 a[124:127], v0, s[8:11], s6 offen offset:1024 sc1\n"
"v_mfma_f32_16x16x32_bf16 v[100:103], a[184:187], v[180:183], v[100:103]\n"
"s_add_u32 s90, s52, 0x5c000\n"
"v_mfma_f32_16x16x32_bf16 v[44:47], a[184:187], v[212:215], v[44:47]\n"
"v_mfma_f32_16x16x32_bf16 v[196:199], a[160:163], v[180:183], v[196:199]\n"
"v_mfma_f32_16x16x32_bf16 v[228:231], a[160:163], v[212:215], v[228:231]\n"
"buffer_load_dwordx4 a[136:139], v0, s[8:11], s90 offen offset:0 sc1\n"
"v_mfma_f32_16x16x32_bf16 v[128:131], a[172:175], v[180:183], v[128:131]\n"
"ds_read_b128 v[160:163], v5 offset:33792\n"
"v_mfma_f32_16x16x32_bf16 v[168:171], a[172:175], v[212:215], v[168:171]\n"
"s_waitcnt vmcnt(30) lgkmcnt(3)\n"
"ds_read_b128 v[144:147], v5 offset:38912\n"
"v_mfma_f32_16x16x32_bf16 v[244:247], a[188:191], v[88:91], v[244:247]\n"
"v_mfma_f32_16x16x32_bf16 v[140:143], a[188:191], v[72:75], v[140:143]\n"
"v_mfma_f32_16x16x32_bf16 v[100:103], a[176:179], v[88:91], v[100:103]\n"
"buffer_load_dwordx4 a[152:155], v0, s[8:11], s90 offen offset:1024 sc1\n"
"v_mfma_f32_16x16x32_bf16 v[44:47], a[176:179], v[72:75], v[44:47]\n"
"s_add_u32 s6, s53, 0x5c800\n"
"v_mfma_f32_16x16x32_bf16 v[196:199], a[196:199], v[88:91], v[196:199]\n"
"v_mfma_f32_16x16x32_bf16 v[228:231], a[196:199], v[72:75], v[228:231]\n"
"v_mfma_f32_16x16x32_bf16 v[128:131], a[180:183], v[88:91], v[128:131]\n"
"buffer_load_dwordx4 a[128:131], v0, s[8:11], s6 offen offset:0 sc1\n"
"ds_read_b128 v[64:67], v5 offset:34816\n"
"v_mfma_f32_16x16x32_bf16 v[168:171], a[180:183], v[72:75], v[168:171]\n"
"s_waitcnt vmcnt(31) lgkmcnt(3)\n"
"ds_read_b128 v[68:71], v5 offset:39936\n"
"s_add_u32 s4, s57, 0x60000\n"
"v_mfma_f32_16x16x32_bf16 v[244:247], a[212:215], v[148:151], v[244:247]\n"
"v_mfma_f32_16x16x32_bf16 v[140:143], a[212:215], v[252:255], v[140:143]\n"
"v_mfma_f32_16x16x32_bf16 v[100:103], a[204:207], v[148:151], v[100:103]\n"
"buffer_load_dwordx4 a[140:143], v0, s[8:11], s6 offen offset:1024 sc1\n"
"v_mfma_f32_16x16x32_bf16 v[44:47], a[204:207], v[252:255], v[44:47]\n"
"s_add_u32 s90, s52, 0x60000\n"
"v_mfma_f32_16x16x32_bf16 v[196:199], a[192:195], v[148:151], v[196:199]\n"
"v_mfma_f32_16x16x32_bf16 v[228:231], a[192:195], v[252:255], v[228:231]\n"
"v_mfma_f32_16x16x32_bf16 v[128:131], a[200:203], v[148:151], v[128:131]\n"
"buffer_load_dwordx4 a[164:167], v0, s[8:11], s90 offen offset:0 sc1\n"
"v_mfma_f32_16x16x32_bf16 v[168:171], a[200:203], v[252:255], v[168:171]\n"
"s_waitcnt vmcnt(31) lgkmcnt(2)\n"
"v_mfma_f32_16x16x32_bf16 v[244:247], a[228:231], v[160:163], v[244:247]\n"
"v_mfma_f32_16x16x32_bf16 v[140:143], a[228:231], v[144:147], v[140:143]\n"
"v_mfma_f32_16x16x32_bf16 v[100:103], a[220:223], v[160:163], v[100:103]\n"
"buffer_load_dwordx4 a[156:159], v0, s[8:11], s90 offen offset:1024 sc1\n"
"v_mfma_f32_16x16x32_bf16 v[44:47], a[220:223], v[144:147], v[44:47]\n"
"s_add_u32 s6, s53, 0x60800\n"
"v_mfma_f32_16x16x32_bf16 v[196:199], a[208:211], v[160:163], v[196:199]\n"
"v_mfma_f32_16x16x32_bf16 v[228:231], a[208:211], v[144:147], v[228:231]\n"
"v_mfma_f32_16x16x32_bf16 v[128:131], a[216:219], v[160:163], v[128:131]\n"
"v_mfma_f32_16x16x32_bf16 v[168:171], a[216:219], v[144:147], v[168:171]\n"
"s_waitcnt vmcnt(30) lgkmcnt(0)\n"
"buffer_load_dwordx4 a[148:151], v0, s[8:11], s6 offen offset:0 sc1\n"
"v_mfma_f32_16x16x32_bf16 v[244:247], a[244:247], v[64:67], v[244:247]\n"
"v_mfma_f32_16x16x32_bf16 v[140:143], a[244:247], v[68:71], v[140:143]\n"
"v_mfma_f32_16x16x32_bf16 v[100:103], a[236:239], v[64:67], v[100:103]\n"
"v_mfma_f32_16x16x32_bf16 v[44:47], a[236:239], v[68:71], v[44:47]\n"
"buffer_load_dwordx4 a[144:147], v0, s[8:11], s6 offen offset:1024 sc1\n"
"v_mfma_f32_16x16x32_bf16 v[196:199], a[224:227], v[64:67], v[196:199]\n"
"s_add_u32 s90, s52, 0x64000\n"
"v_mfma_f32_16x16x32_bf16 v[228:231], a[224:227], v[68:71], v[228:231]\n"
"v_mfma_f32_16x16x32_bf16 v[128:131], a[232:235], v[64:67], v[128:131]\n"
"v_mul_f32 v244, v244, v121\n"
"v_mul_f32 v245, v245, v121\n"
"v_mfma_f32_16x16x32_bf16 v[168:171], a[232:235], v[68:71], v[168:171]\n"
"buffer_load_dwordx4 a[168:171], v0, s[8:11], s90 offen offset:0 sc1\n"
"s_barrier\n"
"v_mul_f32 v246, v246, v121\n"
"v_mul_f32 v247, v247, v121\n"
"v_mul_f32 v100, v100, v122\n"
"v_mul_f32 v101, v101, v122\n"
"v_mul_f32 v102, v102, v122\n"
"v_mul_f32 v103, v103, v122\n"
"v_cvt_pk_bf16_f32 v244, v244, v245\n"
"v_cvt_pk_bf16_f32 v245, v246, v247\n"
"v_cvt_pk_bf16_f32 v246, v100, v101\n"
"v_cvt_pk_bf16_f32 v247, v102, v103\n"
"buffer_load_dwordx4 a[184:187], v0, s[8:11], s90 offen offset:1024 sc1\n"
"ds_write_b64 v23, v[246:247] offset:4608\n"
"ds_read_b128 v[208:211], v5 offset:47104\n"
"ds_read_b128 v[224:227], v5 offset:46080\n"
"ds_read_b128 v[236:239], v5 offset:45056\n"
"ds_read_b128 v[176:179], v5 offset:46080\n"
"s_add_u32 s6, s53, 0x64800\n"
"buffer_load_dwordx4 a[160:163], v0, s[8:11], s6 offen offset:0 sc1\n"
"v_mul_f32 v140, v140, v33\n"
"v_mul_f32 v141, v141, v33\n"
"v_mul_f32 v142, v142, v33\n"
"v_mul_f32 v143, v143, v33\n"
"v_mul_f32 v44, v44, v34\n"
"v_mul_f32 v45, v45, v34\n"
"v_mul_f32 v46, v46, v34\n"
"v_mul_f32 v47, v47, v34\n"
"v_cvt_pk_bf16_f32 v140, v140, v141\n"
"v_cvt_pk_bf16_f32 v141, v142, v143\n"
"v_cvt_pk_bf16_f32 v142, v44, v45\n"
"v_cvt_pk_bf16_f32 v143, v46, v47\n"
"ds_write_b64 v23, v[142:143] offset:5632\n"
"buffer_load_dwordx4 a[172:175], v0, s[8:11], s6 offen offset:1024 sc1\n"
"buffer_load_dwordx4 v[188:191], v0, s[8:11], s2 offen offset:0 sc1\n"
"s_add_u32 s90, s52, 0x68000\n"
"s_add_u32 s3, s58, 0x56000\n"
"v_cvt_pk_bf16_f32 v128, v128, v129\n"
"v_cvt_pk_bf16_f32 v129, v130, v131\n"
"v_mov_b32 v130, 0\n"
"v_mov_b32 v131, 0\n"
"s_nop 0\n"
"v_permlane16_swap_b32 v130, v246\n"
"v_permlane16_swap_b32 v131, v247\n"
"v_cvt_pk_bf16_f32 v168, v168, v169\n"
"v_cvt_pk_bf16_f32 v169, v170, v171\n"
"v_mov_b32 v170, 0\n"
"v_mov_b32 v171, 0\n"
"buffer_load_dwordx4 v[220:223], v0, s[8:11], s3 offen offset:0 sc1\n"
"v_permlane16_swap_b32 v170, v142\n"
"v_permlane16_swap_b32 v171, v143\n"
"s_waitcnt vmcnt(32) lgkmcnt(4)\n"
"v_mfma_f32_16x16x32_bf16 v[208:211], v[112:115], v[128:131], v[208:211]\n"
"s_waitcnt lgkmcnt(3)\n"
"v_mfma_f32_16x16x32_bf16 v[224:227], v[244:247], v[128:131], v[224:227]\n"
"s_waitcnt lgkmcnt(2)\n"
"v_mfma_f32_16x16x32_bf16 v[236:239], v[244:247], v[168:171], v[236:239]\n"
"s_waitcnt lgkmcnt(1)\n"
"v_mfma_f32_16x16x32_bf16 v[176:179], v[140:143], v[168:171], v[176:179]\n"
"buffer_load_dwordx4 v[76:79], v0, s[8:11], s2 offen offset:1024 sc1\n"
"v_mul_f32 v196, v196, v121\n"
"v_mul_f32 v197, v197, v121\n"
"v_mul_f32 v198, v198, v121\n"
"v_mul_f32 v199, v199, v121\n"
"v_cvt_pk_bf16_f32 v196, v196, v197\n"
"v_cvt_pk_bf16_f32 v197, v198, v199\n"
"ds_write_b64 v23, v[196:197] offset:4096\n"
"v_mul_f32 v228, v228, v33\n"
"v_mul_f32 v229, v229, v33\n"
"v_mul_f32 v230, v230, v33\n"
"v_mul_f32 v231, v231, v33\n"
"v_cvt_pk_bf16_f32 v228, v228, v229\n"
"v_cvt_pk_bf16_f32 v229, v230, v231\n"
"buffer_load_dwordx4 v[184:187], v0, s[8:11], s3 offen offset:1024 sc1\n"
"ds_write_b64 v23, v[228:229] offset:5120\n"
"ds_read_b128 v[136:139], v5 offset:48128\n"
"ds_read_b64_tr_b16 v[92:93], v24 offset:0\n"
"ds_read_b64_tr_b16 v[94:95], v24 offset:4096\n"
"ds_read_b64_tr_b16 v[40:41], v24 offset:512\n"
"ds_read_b64_tr_b16 v[42:43], v24 offset:4608\n"
"ds_read_b64_tr_b16 v[84:85], v24 offset:4096\n"
"ds_read_b64_tr_b16 v[86:87], v24 offset:5120\n"
"ds_read_b64_tr_b16 v[56:57], v24 offset:4608\n"
"buffer_load_dwordx4 v[124:127], v0, s[8:11], s2 offen offset:2048 sc1\n"
"ds_read_b64_tr_b16 v[58:59], v24 offset:5632\n"
"v_max3_f32 v28, v208, v209, v210\n"
"v_max3_f32 v30, v224, v225, v226\n"
"v_max3_f32 v28, v28, v211, v227\n"
"v_max_f32 v28, v28, v30\n"
"v_max3_f32 v29, v236, v237, v238\n"
"v_max3_f32 v31, v176, v177, v178\n"
"v_max3_f32 v29, v29, v239, v179\n"
"v_max_f32 v29, v29, v31\n"
"s_nop 1\n"
"v_permlane32_swap_b32 v28, v29\n"
"v_max_f32 v28, v28, v29\n"
"v_mov_b32 v29, v28\n"
"buffer_load_dwordx4 v[200:203], v0, s[8:11], s3 offen offset:2048 sc1\n"
"s_nop 0\n"
"v_permlane16_swap_b32 v28, v29\n"
"v_max_f32 v28, v28, v29\n"
"v_mov_b32 v29, v28\n"
"s_nop 1\n"
"v_permlane32_swap_b32 v28, v29\n"
"v_mul_f32 v28, v28, v123\n"
"v_fma_f32 v208, v208, v123, -v28\n"
"v_fma_f32 v209, v209, v123, -v28\n"
"v_fma_f32 v210, v210, v123, -v28\n"
"v_fma_f32 v211, v211, v123, -v28\n"
"v_fma_f32 v224, v224, v123, -v28\n"
"v_fma_f32 v225, v225, v123, -v28\n"
"v_fma_f32 v226, v226, v123, -v28\n"
"v_fma_f32 v227, v227, v123, -v28\n"
"v_exp_f32 v208, v208\n"
"v_exp_f32 v209, v209\n"
"v_exp_f32 v210, v210\n"
"v_exp_f32 v211, v211\n"
"v_exp_f32 v224, v224\n"
"v_exp_f32 v225, v225\n"
"v_exp_f32 v226, v226\n"
"v_exp_f32 v227, v227\n"
"v_cvt_pk_bf16_f32 v208, v208, v209\n"
"v_cvt_pk_bf16_f32 v209, v210, v211\n"
"v_cvt_pk_bf16_f32 v210, v224, v225\n"
"v_cvt_pk_bf16_f32 v211, v226, v227\n"
"buffer_load_dwordx4 a[188:191], v0, s[8:11], s90 offen offset:0 sc1\n"
"buffer_load_dwordx4 v[80:83], v0, s[8:11], s2 offen offset:3072 sc1\n"
"s_waitcnt vmcnt(32) lgkmcnt(1)\n"
"v_mfma_f32_16x16x32_bf16 v[36:39], v[136:139], v[208:211], 0\n"
"v_mfma_f32_16x16x32_bf16 v[152:155], v[92:95], v[208:211], 0\n"
"v_mfma_f32_16x16x32_bf16 v[248:251], v[40:43], v[208:211], 0\n"
"buffer_load_dwordx4 v[172:175], v0, s[8:11], s3 offen offset:3072 sc1\n"
"s_add_u32 s2, s57, 0x57000\n"
"v_mul_f32 v29, v29, v35\n"
"v_fma_f32 v236, v236, v35, -v29\n"
"v_fma_f32 v237, v237, v35, -v29\n"
"v_fma_f32 v238, v238, v35, -v29\n"
"v_fma_f32 v239, v239, v35, -v29\n"
"v_fma_f32 v176, v176, v35, -v29\n"
"v_fma_f32 v177, v177, v35, -v29\n"
"v_fma_f32 v178, v178, v35, -v29\n"
"v_fma_f32 v179, v179, v35, -v29\n"
"v_exp_f32 v236, v236\n"
"v_exp_f32 v237, v237\n"
"v_exp_f32 v238, v238\n"
"v_exp_f32 v239, v239\n"
"v_exp_f32 v176, v176\n"
"v_exp_f32 v177, v177\n"
"v_exp_f32 v178, v178\n"
"v_exp_f32 v179, v179\n"
"v_cvt_pk_bf16_f32 v236, v236, v237\n"
"v_cvt_pk_bf16_f32 v237, v238, v239\n"
"v_cvt_pk_bf16_f32 v238, v176, v177\n"
"v_cvt_pk_bf16_f32 v239, v178, v179\n"
"buffer_load_dwordx4 a[176:179], v0, s[8:11], s90 offen offset:1024 sc1\n"
"buffer_load_dwordx4 v[96:99], v0, s[8:11], s2 offen offset:0 sc1\n"
"v_mfma_f32_16x16x32_bf16 v[164:167], v[136:139], v[236:239], 0\n"
"s_add_u32 s6, s53, 0x68800\n"
"s_add_u32 s3, s58, 0x57000\n"
"v_mfma_f32_16x16x32_bf16 v[216:219], v[84:87], v[236:239], 0\n"
"s_waitcnt vmcnt(32) lgkmcnt(0)\n"
"v_mfma_f32_16x16x32_bf16 v[48:51], v[56:59], v[236:239], 0\n"
"v_rcp_f32 v36, v36\n"
"s_nop 0\n"
"v_mul_f32 v152, v152, v36\n"
"v_mul_f32 v153, v153, v36\n"
"v_mul_f32 v154, v154, v36\n"
"v_mul_f32 v155, v155, v36\n"
"v_cvt_pk_bf16_f32 v116, v152, v153\n"
"v_cvt_pk_bf16_f32 v117, v154, v155\n"
"v_mul_f32 v248, v248, v36\n"
"v_mul_f32 v249, v249, v36\n"
"v_mul_f32 v250, v250, v36\n"
"v_mul_f32 v251, v251, v36\n"
"v_cvt_pk_bf16_f32 v204, v248, v249\n"
"v_cvt_pk_bf16_f32 v205, v250, v251\n"
"v_rcp_f32 v164, v164\n"
"buffer_load_dwordx4 a[196:199], v0, s[8:11], s6 offen offset:0 sc1\n"
"buffer_load_dwordx4 v[240:243], v0, s[8:11], s3 offen offset:0 sc1\n"
"s_waitcnt vmcnt(12)\n"
"v_max3_u32 v132, v188, v189, v190\n"
"v_mul_f32 v216, v216, v164\n"
"v_mul_f32 v217, v217, v164\n"
"v_mul_f32 v218, v218, v164\n"
"v_mul_f32 v219, v219, v164\n"
"v_cvt_pk_bf16_f32 v118, v216, v217\n"
"v_cvt_pk_bf16_f32 v119, v218, v219\n"
"v_mul_f32 v48, v48, v164\n"
"v_mul_f32 v49, v49, v164\n"
"v_mul_f32 v50, v50, v164\n"
"v_mul_f32 v51, v51, v164\n"
"v_cvt_pk_bf16_f32 v206, v48, v49\n"
"s_waitcnt vmcnt(11)\n"
"v_max3_u32 v132, v132, v191, v220\n"
"v_max3_u32 v132, v132, v221, v222\n"
"v_max_u32 v132, v132, v223\n"
"v_cmp_eq_u32 vcc, -1, v132\n"
"v_cvt_pk_bf16_f32 v207, v50, v51\n"
"v_permlane16_swap_b32 v116, v118\n"
"v_permlane16_swap_b32 v117, v119\n"
"s_cbranch_vccnz Ls60_%=\n"
"Lb60_%=:\n"
"v_mfma_f32_16x16x32_bf16 v[60:63], a[248:251], v[188:191], v[60:63]\n"
"buffer_load_dwordx4 a[180:183], v0, s[8:11], s6 offen offset:1024 sc1\n"
"v_permlane16_swap_b32 v204, v206\n"
"v_permlane16_swap_b32 v205, v207\n"
"v_mfma_f32_16x16x32_bf16 v[52:55], a[12:15], v[188:191], v[52:55]\n"
"s_add_u32 s90, s52, 0x6c000\n"
"s_add_u32 s1, s65, 0x60000\n"
"buffer_store_dwordx4 v[116:119], v11, s[8:11], s1 offen offset:0 sc1\n"
"v_mfma_f32_16x16x32_bf16 v[180:183], a[240:243], v[188:191], 0\n"
"s_add_u32 s1, s66, 0x60000\n"
"s_waitcnt vmcnt(12)\n"
"v_max3_u32 v212, v76, v77, v78\n"
"v_mfma_f32_16x16x32_bf16 v[88:91], a[252:255], v[188:191], 0\n"
"v_mfma_f32_16x16x32_bf16 v[232:235], a[8:11], v[188:191], v[232:235]\n"
"s_waitcnt vmcnt(11)\n"
"v_max3_u32 v212, v212, v79, v184\n"
"v_max3_u32 v212, v212, v185, v186\n"
"v_mfma_f32_16x16x32_bf16 v[72:75], a[248:251], v[220:223], 0\n"
"buffer_store_dwordx2 v[204:205], v12, s[8:11], s1 offen offset:0 sc1\n"
"v_mfma_f32_16x16x32_bf16 v[148:151], a[12:15], v[220:223], 0\n"
"v_max_u32 v212, v212, v187\n"
"v_cmp_eq_u32 vcc, -1, v212\n"
"v_mfma_f32_16x16x32_bf16 v[252:255], a[240:243], v[220:223], 0\n"
"v_mfma_f32_16x16x32_bf16 v[160:163], a[252:255], v[220:223], 0\n"
"s_cbranch_vccnz Ls61_%=\n"
"Lb61_%=:\n"
"v_mfma_f32_16x16x32_bf16 v[144:147], a[8:11], v[220:223], 0\n"
"s_waitcnt vmcnt(11)\n"
"v_max3_u32 v64, v124, v125, v126\n"
"v_mfma_f32_16x16x32_bf16 v[60:63], a[28:31], v[76:79], v[60:63]\n"
"buffer_load_dwordx4 a[212:215], v0, s[8:11], s90 offen offset:0 sc1\n"
"v_mfma_f32_16x16x32_bf16 v[52:55], a[4:7], v[76:79], v[52:55]\n"
"v_mfma_f32_16x16x32_bf16 v[180:183], a[0:3], v[76:79], v[180:183]\n"
"v_mfma_f32_16x16x32_bf16 v[88:91], a[24:27], v[76:79], v[88:91]\n"
"s_waitcnt vmcnt(11)\n"
"v_max3_u32 v64, v64, v127, v200\n"
"v_max3_u32 v64, v64, v201, v202\n"
"v_mfma_f32_16x16x32_bf16 v[232:235], a[44:47], v[76:79], v[232:235]\n"
"v_max_u32 v64, v64, v203\n"
"v_cmp_eq_u32 vcc, -1, v64\n"
"v_mfma_f32_16x16x32_bf16 v[72:75], a[28:31], v[184:187], v[72:75]\n"
"buffer_load_dwordx4 a[204:207], v0, s[8:11], s90 offen offset:1024 sc1\n"
"v_mfma_f32_16x16x32_bf16 v[148:151], a[4:7], v[184:187], v[148:151]\n"
"s_add_u32 s6, s53, 0x6c800\n"
"v_mfma_f32_16x16x32_bf16 v[252:255], a[0:3], v[184:187], v[252:255]\n"
"s_cbranch_vccnz Ls62_%=\n"
"Lb62_%=:\n"
"v_mfma_f32_16x16x32_bf16 v[160:163], a[24:27], v[184:187], v[160:163]\n"
"v_mfma_f32_16x16x32_bf16 v[144:147], a[44:47], v[184:187], v[144:147]\n"
"buffer_load_dwordx4 a[192:195], v0, s[8:11], s6 offen offset:0 sc1\n"
"v_mfma_f32_16x16x32_bf16 v[60:63], a[20:23], v[124:127], v[60:63]\n"
"v_mfma_f32_16x16x32_bf16 v[52:55], a[16:19], v[124:127], v[52:55]\n"
"v_mfma_f32_16x16x32_bf16 v[180:183], a[40:43], v[124:127], v[180:183]\n"
"s_waitcnt vmcnt(11)\n"
"v_max3_u32 v68, v80, v81, v82\n"
"v_mfma_f32_16x16x32_bf16 v[88:91], a[60:63], v[124:127], v[88:91]\n"
"buffer_load_dwordx4 a[200:203], v0, s[8:11], s6 offen offset:1024 sc1\n"
"v_mfma_f32_16x16x32_bf16 v[232:235], a[36:39], v[124:127], v[232:235]\n"
"s_add_u32 s90, s52, 0x70000\n"
"v_mfma_f32_16x16x32_bf16 v[72:75], a[20:23], v[200:203], v[72:75]\n"
"v_mfma_f32_16x16x32_bf16 v[148:151], a[16:19], v[200:203], v[148:151]\n"
"v_mfma_f32_16x16x32_bf16 v[252:255], a[40:43], v[200:203], v[252:255]\n"
"buffer_load_dwordx4 a[228:231], v0, s[8:11], s90 offen offset:0 sc1\n"
"v_mfma_f32_16x16x32_bf16 v[160:163], a[60:63], v[200:203], v[160:163]\n"
"s_waitcnt vmcnt(12)\n"
"v_max3_u32 v68, v68, v83, v172\n"
"v_max3_u32 v68, v68, v173, v174\n"
"v_mfma_f32_16x16x32_bf16 v[144:147], a[36:39], v[200:203], v[144:147]\n"
"v_max_u32 v68, v68, v175\n"
"v_cmp_eq_u32 vcc, -1, v68\n"
"s_cbranch_vccnz Ls63_%=\n"
"Lb63_%=:\n"
"v_mfma_f32_16x16x32_bf16 v[60:63], v[192:195], v[80:83], v[60:63]\n"
"buffer_load_dwordx4 a[220:223], v0, s[8:11], s90 offen offset:1024 sc1\n"
"v_mfma_f32_16x16x32_bf16 v[52:55], a[32:35], v[80:83], v[52:55]\n"
"s_add_u32 s6, s53, 0x70800\n"
"v_mfma_f32_16x16x32_bf16 v[180:183], a[56:59], v[80:83], v[180:183]\n"
"v_mfma_f32_16x16x32_bf16 v[88:91], a[68:71], v[80:83], v[88:91]\n"
"v_mfma_f32_16x16x32_bf16 v[232:235], a[52:55], v[80:83], v[232:235]\n"
"buffer_load_dwordx4 a[208:211], v0, s[8:11], s6 offen offset:0 sc1\n"
"v_mfma_f32_16x16x32_bf16 v[72:75], v[192:195], v[172:175], v[72:75]\n"
"s_waitcnt vmcnt(12)\n"
"v_max3_u32 v100, v96, v97, v98\n"
"v_mfma_f32_16x16x32_bf16 v[148:151], a[32:35], v[172:175], v[148:151]\n"
"v_mfma_f32_16x16x32_bf16 v[252:255], a[56:59], v[172:175], v[252:255]\n"
"v_mfma_f32_16x16x32_bf16 v[160:163], a[68:71], v[172:175], v[160:163]\n"
"buffer_load_dwordx4 a[216:219], v0, s[8:11], s6 offen offset:1024 sc1\n"
"v_mfma_f32_16x16x32_bf16 v[144:147], a[52:55], v[172:175], v[144:147]\n"
"s_add_u32 s90, s52, 0x74000\n"
"buffer_load_dwordx4 a[244:247], v0, s[8:11], s90 offen offset:0 sc1\n"
"s_waitcnt vmcnt(12)\n"
"v_max3_u32 v100, v100, v99, v240\n"
"v_max3_u32 v100, v100, v241, v242\n"
"v_max_u32 v100, v100, v243\n"
"v_cmp_eq_u32 vcc, -1, v100\n"
"s_cbranch_vccnz Ls64_%=\n"
"Lb64_%=:\n"
"v_mfma_f32_16x16x32_bf16 v[180:183], a[84:87], v[96:99], v[180:183]\n"
"v_mfma_f32_16x16x32_bf16 v[88:91], a[64:67], v[96:99], v[88:91]\n"
"buffer_load_dwordx4 a[236:239], v0, s[8:11], s90 offen offset:1024 sc1\n"
"v_mfma_f32_16x16x32_bf16 v[232:235], a[76:79], v[96:99], v[232:235]\n"
"s_add_u32 s6, s53, 0x74800\n"
"v_mfma_f32_16x16x32_bf16 v[72:75], a[48:51], v[240:243], v[72:75]\n"
"v_mfma_f32_16x16x32_bf16 v[148:151], a[72:75], v[240:243], v[148:151]\n"
"v_mfma_f32_16x16x32_bf16 v[252:255], a[84:87], v[240:243], v[252:255]\n"
"v_mfma_f32_16x16x32_bf16 v[160:163], a[64:67], v[240:243], v[160:163]\n"
"buffer_load_dwordx4 a[224:227], v0, s[8:11], s6 offen offset:0 sc1\n"
"ds_write_b128 v2, v[180:183] offset:2048\n"
"v_mfma_f32_16x16x32_bf16 v[144:147], a[76:79], v[240:243], v[144:147]\n"
"ds_write_b128 v2, v[88:91] offset:3072\n"
"v_mfma_f32_16x16x32_bf16 v[60:63], a[48:51], v[96:99], v[60:63]\n"
"buffer_load_dwordx4 a[232:235], v0, s[8:11], s6 offen offset:1024 sc1\n"
"v_mfma_f32_16x16x32_bf16 v[52:55], a[72:75], v[96:99], v[52:55]\n"
"s_add_u32 s90, s52, 0x78000\n"
"ds_write_b128 v2, v[232:235] offset:6144\n"
"buffer_load_dwordx4 v[44:47], v0, s[8:11], s90 offen offset:0 sc1\n"
"ds_write_b128 v2, v[72:75] offset:0\n"
"buffer_load_dwordx4 v[112:115], v0, s[8:11], s90 offen offset:1024 sc1\n"
"ds_write_b128 v2, v[148:151] offset:1024\n"
"s_add_u32 s6, s53, 0x78800\n"
"ds_write_b128 v2, v[252:255] offset:4096\n"
"buffer_load_dwordx4 v[128:131], v0, s[8:11], s6 offen offset:0 sc1\n"
"ds_write_b128 v2, v[160:163] offset:5120\n"
"buffer_load_dwordx4 v[244:247], v0, s[8:11], s6 offen offset:1024 sc1\n"
"s_add_u32 s90, s52, 0x7c000\n"
"ds_write_b128 v2, v[144:147] offset:7168\n"
"buffer_load_dwordx4 v[140:143], v0, s[8:11], s90 offen offset:0 sc1\n"
"buffer_load_dwordx4 v[168:171], v0, s[8:11], s4 offen offset:0 sc1\n"
"s_add_u32 s5, s58, 0x60000\n"
"buffer_load_dwordx4 v[196:199], v0, s[8:11], s5 offen offset:0 sc1\n"
"s_waitcnt lgkmcnt(0)\n"
"s_barrier\n"
"buffer_load_dwordx4 v[228:231], v0, s[8:11], s4 offen offset:1024 sc1\n"
"ds_read_b128 v[120:123], v8 offset:0\n"
"ds_read_b128 v[224:227], v9 offset:2048\n"
"ds_read_b128 v[92:95], v10 offset:4096\n"
"ds_read_b128 v[40:43], v8 offset:1024\n"
"ds_read_b128 v[208:211], v9 offset:3072\n"
"ds_read_b128 v[32:35], v10 offset:5120\n"
"buffer_load_dwordx4 v[28:31], v0, s[8:11], s5 offen offset:1024 sc1\n"
"s_waitcnt lgkmcnt(5)\n"
"v_add_f32 v60, v60, v120\n"
"v_add_f32 v61, v61, v121\n"
"v_add_f32 v62, v62, v122\n"
"v_add_f32 v63, v63, v123\n"
"ds_read_b128 v[176:179], v8 offset:7168\n"
"buffer_load_dwordx4 v[136:139], v0, s[8:11], s4 offen offset:2048 sc1\n"
"s_waitcnt lgkmcnt(5)\n"
"v_add_f32 v60, v60, v224\n"
"v_add_f32 v61, v61, v225\n"
"v_add_f32 v62, v62, v226\n"
"v_add_f32 v63, v63, v227\n"
"ds_read_b128 v[84:87], v9 offset:6144\n"
"s_waitcnt lgkmcnt(5)\n"
"v_add_f32 v60, v60, v92\n"
"v_add_f32 v61, v61, v93\n"
"v_add_f32 v62, v62, v94\n"
"v_add_f32 v63, v63, v95\n"
"ds_read_b128 v[56:59], v10 offset:7168\n"
"buffer_load_dwordx4 v[236:239], v0, s[8:11], s5 offen offset:2048 sc1\n"
"v_cvt_pk_bf16_f32 v152, v60, v61\n"
"v_cvt_pk_bf16_f32 v153, v62, v63\n"
"s_waitcnt lgkmcnt(5)\n"
"v_add_f32 v52, v52, v40\n"
"v_add_f32 v53, v53, v41\n"
"v_add_f32 v54, v54, v42\n"
"v_add_f32 v55, v55, v43\n"
"s_waitcnt lgkmcnt(4)\n"
"v_add_f32 v52, v52, v208\n"
"v_add_f32 v53, v53, v209\n"
"v_add_f32 v54, v54, v210\n"
"v_add_f32 v55, v55, v211\n"
"s_waitcnt lgkmcnt(3)\n"
"v_add_f32 v52, v52, v32\n"
"v_add_f32 v53, v53, v33\n"
"v_add_f32 v54, v54, v34\n"
"v_add_f32 v55, v55, v35\n"
"v_cvt_pk_bf16_f32 v154, v52, v53\n"
"v_cvt_pk_bf16_f32 v155, v54, v55\n"
"s_add_u32 s0, s67, 0x40000\n"
"buffer_load_dwordx4 v[36:39], v0, s[8:11], s4 offen offset:3072 sc1\n"
"buffer_load_dwordx4 v[248:251], v0, s[8:11], s5 offen offset:3072 sc1\n"
"s_add_u32 s4, s57, 0x61000\n"
"buffer_load_dwordx4 v[216:219], v0, s[8:11], s4 offen offset:0 sc1\n"
"s_add_u32 s5, s58, 0x61000\n"
"buffer_load_dwordx4 v[164:167], v0, s[8:11], s5 offen offset:0 sc1\n"
"buffer_store_dwordx4 v[152:155], v0, s[8:11], s0 offen offset:0 sc1\n"
"s_waitcnt lgkmcnt(0)\n"
"v_add_f32 v232, v232, v176\n"
"v_add_f32 v233, v233, v177\n"
"v_add_f32 v234, v234, v178\n"
"v_add_f32 v235, v235, v179\n"
"v_add_f32 v232, v232, v84\n"
"v_add_f32 v233, v233, v85\n"
"v_add_f32 v234, v234, v86\n"
"v_add_f32 v235, v235, v87\n"
"v_add_f32 v232, v232, v56\n"
"v_add_f32 v233, v233, v57\n"
"v_add_f32 v234, v234, v58\n"
"v_add_f32 v235, v235, v59\n"
"v_cvt_pk_bf16_f32 v48, v232, v233\n"
"v_cvt_pk_bf16_f32 v49, v234, v235\n"
"s_add_u32 s0, s68, 0x40000\n"
"buffer_store_dwordx2 v[48:49], v13, s[8:11], s0 offen offset:0 sc1\n"
"v_add_f32 v132, v60, v61\n"
"v_mul_f32 v133, v60, v60\n"
"v_add_f32 v134, v232, v233\n"
"v_mul_f32 v135, v232, v232\n"
"v_fmac_f32 v133, v61, v61\n"
"v_fmac_f32 v135, v233, v233\n"
"s_waitcnt vmcnt(11)\n"
"v_max3_u32 v116, v168, v169, v170\n"
"v_add_f32 v132, v132, v62\n"
"v_fmac_f32 v133, v62, v62\n"
"v_add_f32 v134, v134, v234\n"
"v_fmac_f32 v135, v234, v234\n"
"v_add_f32 v132, v132, v63\n"
"v_fmac_f32 v133, v63, v63\n"
"v_add_f32 v134, v134, v235\n"
"v_fmac_f32 v135, v235, v235\n"
"v_add_f32 v132, v132, v52\n"
"v_fmac_f32 v133, v52, v52\n"
"v_add_f32 v132, v132, v53\n"
"v_fmac_f32 v133, v53, v53\n"
"v_add_f32 v132, v132, v54\n"
"v_fmac_f32 v133, v54, v54\n"
"v_add_f32 v132, v132, v55\n"
"v_fmac_f32 v133, v55, v55\n"
"v_fmac_f32 v132, s78, v134\n"
"s_waitcnt vmcnt(10)\n"
"v_max3_u32 v116, v116, v171, v196\n"
"v_max3_u32 v116, v116, v197, v198\n"
"v_max_u32 v116, v116, v199\n"
"v_cmp_eq_u32 vcc, -1, v116\n"
"v_fmac_f32 v133, s78, v135\n"
"buffer_load_dwordx4 v[188:191], v0, s[8:11], s90 offen offset:1024 sc1\n"
"s_add_u32 s6, s53, 0x7c800\n"
"s_cbranch_vccnz Ls82_%=\n"
"Lb82_%=:\n"
"v_mfma_f32_16x16x32_bf16 v[156:159], a[248:251], v[168:171], v[156:159]\n"
"buffer_load_dwordx4 v[204:207], v0, s[8:11], s6 offen offset:0 sc1\n"
"v_permlane32_swap_b32 v132, v133\n"
"v_add_f32 v132, v132, v133\n"
"v_mov_b32 v133, v132\n"
"v_mfma_f32_16x16x32_bf16 v[104:107], a[12:15], v[168:171], v[104:107]\n"
"s_waitcnt vmcnt(11)\n"
"v_max3_u32 v212, v228, v229, v230\n"
"v_permlane16_swap_b32 v132, v133\n"
"v_add_f32 v132, v132, v133\n"
"s_add_u32 s0, s71, 0x4a000\n"
"v_mfma_f32_16x16x32_bf16 v[220:223], a[240:243], v[168:171], 0\n"
"buffer_load_dwordx4 v[76:79], v0, s[8:11], s6 offen offset:1024 sc1\n"
"v_mfma_f32_16x16x32_bf16 v[64:67], a[252:255], v[168:171], 0\n"
"s_add_u32 s90, s52, 0x80000\n"
"buffer_store_dword v132, v14, s[8:11], s0 offen offset:0 sc1\n"
"ds_read_b128 v[184:187], v20 offset:768\n"
"s_add_u32 s0, s63, 0x40000\n"
"v_mfma_f32_16x16x32_bf16 v[108:111], a[8:11], v[168:171], v[108:111]\n"
"s_waitcnt vmcnt(12)\n"
"v_max3_u32 v212, v212, v231, v28\n"
"v_max3_u32 v212, v212, v29, v30\n"
"v_mfma_f32_16x16x32_bf16 v[124:127], a[248:251], v[196:199], 0\n"
"v_max_u32 v212, v212, v31\n"
"v_cmp_eq_u32 vcc, -1, v212\n"
"v_mfma_f32_16x16x32_bf16 v[200:203], a[12:15], v[196:199], 0\n"
"v_mfma_f32_16x16x32_bf16 v[68:71], a[240:243], v[196:199], 0\n"
"s_cbranch_vccnz Ls83_%=\n"
"Lb83_%=:\n"
"v_mfma_f32_16x16x32_bf16 v[80:83], a[252:255], v[196:199], 0\n"
"buffer_load_dwordx4 a[248:251], v0, s[8:11], s90 offen offset:0 sc1\n"
"v_mfma_f32_16x16x32_bf16 v[172:175], a[8:11], v[196:199], 0\n"
"s_waitcnt vmcnt(12)\n"
"v_max3_u32 v100, v136, v137, v138\n"
"v_mfma_f32_16x16x32_bf16 v[156:159], a[28:31], v[228:231], v[156:159]\n"
"v_mfma_f32_16x16x32_bf16 v[104:107], a[4:7], v[228:231], v[104:107]\n"
"s_waitcnt lgkmcnt(0)\n"
"v_add_f32_e64 v60, -v60, -v184\n"
"v_add_f32_e64 v61, -v61, -v185\n"
"v_mfma_f32_16x16x32_bf16 v[220:223], a[0:3], v[228:231], v[220:223]\n"
"v_add_f32_e64 v62, -v62, -v186\n"
"v_add_f32_e64 v63, -v63, -v187\n"
"ds_read_b128 v[180:183], v20 offset:832\n"
"v_mfma_f32_16x16x32_bf16 v[64:67], a[24:27], v[228:231], v[64:67]\n"
"buffer_load_dwordx4 a[12:15], v0, s[8:11], s90 offen offset:1024 sc1\n"
"v_mfma_f32_16x16x32_bf16 v[108:111], a[44:47], v[228:231], v[108:111]\n"
"s_add_u32 s6, s53, 0x80800\n"
"s_waitcnt vmcnt(12)\n"
"v_max3_u32 v100, v100, v139, v236\n"
"v_mfma_f32_16x16x32_bf16 v[124:127], a[28:31], v[28:31], v[124:127]\n"
"v_max3_u32 v100, v100, v237, v238\n"
"v_max_u32 v100, v100, v239\n"
"v_mfma_f32_16x16x32_bf16 v[200:203], a[4:7], v[28:31], v[200:203]\n"
"v_cmp_eq_u32 vcc, -1, v100\n"
"v_mfma_f32_16x16x32_bf16 v[68:71], a[0:3], v[28:31], v[68:71]\n"
"buffer_load_dwordx4 a[240:243], v0, s[8:11], s6 offen offset:0 sc1\n"
"v_mfma_f32_16x16x32_bf16 v[80:83], a[24:27], v[28:31], v[80:83]\n"
"s_cbranch_vccnz Ls84_%=\n"
"Lb84_%=:\n"
"v_mfma_f32_16x16x32_bf16 v[172:175], a[44:47], v[28:31], v[172:175]\n"
"s_waitcnt vmcnt(12)\n"
"v_max3_u32 v240, v36, v37, v38\n"
"v_mfma_f32_16x16x32_bf16 v[156:159], a[20:23], v[136:139], v[156:159]\n"
"buffer_load_dwordx4 a[252:255], v0, s[8:11], s6 offen offset:1024 sc1\n"
"v_mfma_f32_16x16x32_bf16 v[104:107], a[16:19], v[136:139], v[104:107]\n"
"s_add_u32 s90, s52, 0x84000\n"
"s_waitcnt vmcnt(12)\n"
"v_max3_u32 v240, v240, v39, v248\n"
"v_mfma_f32_16x16x32_bf16 v[220:223], a[40:43], v[136:139], v[220:223]\n"
"v_max3_u32 v240, v240, v249, v250\n"
"v_max_u32 v240, v240, v251\n"
"v_mfma_f32_16x16x32_bf16 v[64:67], a[60:63], v[136:139], v[64:67]\n"
"v_cmp_eq_u32 vcc, -1, v240\n"
"v_mfma_f32_16x16x32_bf16 v[108:111], a[36:39], v[136:139], v[108:111]\n"
"buffer_load_dwordx4 a[8:11], v0, s[8:11], s90 offen offset:0 sc1\n"
"v_mfma_f32_16x16x32_bf16 v[124:127], a[20:23], v[236:239], v[124:127]\n"
"s_cbranch_vccnz Ls85_%=\n"
"Lb85_%=:\n"
"v_mfma_f32_16x16x32_bf16 v[200:203], a[16:19], v[236:239], v[200:203]\n"
"s_waitcnt vmcnt(12) lgkmcnt(0)\n"
"v_max3_u32 v88, v216, v217, v218\n"
"v_add_f32_e64 v52, -v52, -v180\n"
"v_mfma_f32_16x16x32_bf16 v[68:71], a[40:43], v[236:239], v[68:71]\n"
"v_add_f32_e64 v53, -v53, -v181\n"
"v_add_f32_e64 v54, -v54, -v182\n"
"v_mfma_f32_16x16x32_bf16 v[80:83], a[60:63], v[236:239], v[80:83]\n"
"v_add_f32_e64 v55, -v55, -v183\n"
"ds_read_b128 v[96:99], v20 offset:896\n"
"v_mfma_f32_16x16x32_bf16 v[172:175], a[36:39], v[236:239], v[172:175]\n"
"buffer_load_dwordx4 a[28:31], v0, s[8:11], s90 offen offset:1024 sc1\n"
"v_mfma_f32_16x16x32_bf16 v[156:159], v[192:195], v[36:39], v[156:159]\n"
"s_add_u32 s6, s53, 0x84800\n"
"s_waitcnt vmcnt(12)\n"
"v_max3_u32 v88, v88, v219, v164\n"
"v_mfma_f32_16x16x32_bf16 v[104:107], a[32:35], v[36:39], v[104:107]\n"
"v_max3_u32 v88, v88, v165, v166\n"
"v_max_u32 v88, v88, v167\n"
"v_mfma_f32_16x16x32_bf16 v[220:223], a[56:59], v[36:39], v[220:223]\n"
"v_cmp_eq_u32 vcc, -1, v88\n"
"v_mfma_f32_16x16x32_bf16 v[64:67], a[68:71], v[36:39], v[64:67]\n"
"buffer_load_dwordx4 a[4:7], v0, s[8:11], s6 offen offset:0 sc1\n"
"v_mfma_f32_16x16x32_bf16 v[108:111], a[52:55], v[36:39], v[108:111]\n"
"s_cbranch_vccnz Ls86_%=\n"
"Lb86_%=:\n"
"v_mfma_f32_16x16x32_bf16 v[124:127], v[192:195], v[248:251], v[124:127]\n"
"v_mfma_f32_16x16x32_bf16 v[200:203], a[32:35], v[248:251], v[200:203]\n"
"s_waitcnt lgkmcnt(0)\n"
"v_fma_f32 v232, -v232, s78, -v96\n"
"v_fma_f32 v233, -v233, s78, -v97\n"
"v_mfma_f32_16x16x32_bf16 v[68:71], a[56:59], v[248:251], v[68:71]\n"
"buffer_load_dwordx4 a[0:3], v0, s[8:11], s6 offen offset:1024 sc1\n"
"v_mfma_f32_16x16x32_bf16 v[80:83], a[68:71], v[248:251], v[80:83]\n"
"s_add_u32 s90, s52, 0x88000\n"
"v_fma_f32 v234, -v234, s78, -v98\n"
"v_fma_f32 v235, -v235, s78, -v99\n"
"v_mfma_f32_16x16x32_bf16 v[172:175], a[52:55], v[248:251], v[172:175]\n"
"v_mfma_f32_16x16x32_bf16 v[220:223], a[84:87], v[216:219], v[220:223]\n"
"v_mfma_f32_16x16x32_bf16 v[64:67], a[64:67], v[216:219], v[64:67]\n"
"buffer_load_dwordx4 a[24:27], v0, s[8:11], s90 offen offset:0 sc1\n"
"v_mfma_f32_16x16x32_bf16 v[108:111], a[76:79], v[216:219], v[108:111]\n"
"v_mfma_f32_16x16x32_bf16 v[124:127], a[48:51], v[164:167], v[124:127]\n"
"v_mfma_f32_16x16x32_bf16 v[200:203], a[72:75], v[164:167], v[200:203]\n"
"v_mfma_f32_16x16x32_bf16 v[68:71], a[84:87], v[164:167], v[68:71]\n"
"v_mfma_f32_16x16x32_bf16 v[80:83], a[64:67], v[164:167], v[80:83]\n"
"buffer_load_dwordx4 a[44:47], v0, s[8:11], s90 offen offset:1024 sc1\n"
"ds_write_b128 v2, v[220:223] offset:12288\n"
"v_mfma_f32_16x16x32_bf16 v[172:175], a[76:79], v[164:167], v[172:175]\n"
"s_add_u32 s6, s53, 0x88800\n"
"ds_write_b128 v2, v[64:67] offset:13312\n"
"v_mfma_f32_16x16x32_bf16 v[156:159], a[48:51], v[216:219], v[156:159]\n"
"buffer_load_dwordx4 a[20:23], v0, s[8:11], s6 offen offset:0 sc1\n"
"v_mfma_f32_16x16x32_bf16 v[104:107], a[72:75], v[216:219], v[104:107]\n"
"ds_write_b128 v2, v[108:111] offset:16384\n"
"buffer_load_dwordx4 a[16:19], v0, s[8:11], s6 offen offset:1024 sc1\n"
"s_add_u32 s90, s52, 0x8c000\n"
"ds_write_b128 v2, v[124:127] offset:10240\n"
"buffer_load_dwordx4 v[72:75], v0, s[8:11], s0 offen offset:0 sc1\n"
"ds_write_b128 v2, v[200:203] offset:11264\n"
"buffer_load_dwordx4 v[148:151], v0, s[8:11], s0 offen offset:1024 sc1\n"
"ds_write_b128 v2, v[68:71] offset:14336\n"
"ds_write_b128 v2, v[80:83] offset:15360\n"
"buffer_load_dwordx4 v[252:255], v0, s[8:11], s0 offen offset:2048 sc1\n"
"ds_write_b128 v2, v[172:175] offset:17408\n"
"buffer_load_dwordx4 v[160:163], v0, s[8:11], s0 offen offset:3072 sc1\n"
"s_add_u32 s0, s63, 0x41000\n"
"buffer_load_dwordx4 v[144:147], v0, s[8:11], s0 offen offset:0 sc1\n"
"s_add_u32 s0, s63, 0x45000\n"
"buffer_load_dwordx4 v[120:123], v0, s[8:11], s0 offen offset:0 sc1\n"
"s_waitcnt lgkmcnt(0)\n"
"s_barrier\n"
"ds_read_b128 v[224:227], v8 offset:10240\n"
"ds_read_b128 v[92:95], v9 offset:12288\n"
"ds_read_b128 v[40:43], v10 offset:14336\n"
"ds_read_b128 v[208:211], v8 offset:11264\n"
"ds_read_b128 v[32:35], v9 offset:13312\n"
"buffer_load_dwordx4 v[152:155], v0, s[8:11], s0 offen offset:1024 sc1\n"
"ds_read_b128 v[176:179], v10 offset:15360\n"
"buffer_load_dwordx4 v[84:87], v0, s[8:11], s0 offen offset:2048 sc1\n"
"s_waitcnt lgkmcnt(5)\n"
"v_add_f32 v156, v156, v224\n"
"v_add_f32 v157, v157, v225\n"
"v_add_f32 v158, v158, v226\n"
"v_add_f32 v159, v159, v227\n"
"s_waitcnt lgkmcnt(4)\n"
"v_add_f32 v156, v156, v92\n"
"v_add_f32 v157, v157, v93\n"
"v_add_f32 v158, v158, v94\n"
"v_add_f32 v159, v159, v95\n"
"s_waitcnt lgkmcnt(3)\n"
"v_add_f32 v156, v156, v40\n"
"v_add_f32 v157, v157, v41\n"
"buffer_load_dwordx4 v[56:59], v0, s[8:11], s0 offen offset:3072 sc1\n"
"ds_read_b128 v[48:51], v8 offset:17408\n"
"ds_read_b128 v[116:119], v9 offset:16384\n"
"s_add_u32 s0, s63, 0x46000\n"
"v_add_f32 v158, v158, v42\n"
"v_add_f32 v159, v159, v43\n"
"v_cvt_pk_bf16_f32 v132, v156, v157\n"
"v_cvt_pk_bf16_f32 v133, v158, v159\n"
"s_waitcnt lgkmcnt(4)\n"
"v_add_f32 v104, v104, v208\n"
"v_add_f32 v105, v105, v209\n"
"v_add_f32 v106, v106, v210\n"
"v_add_f32 v107, v107, v211\n"
"s_waitcnt lgkmcnt(3)\n"
"v_add_f32 v104, v104, v32\n"
"v_add_f32 v105, v105, v33\n"
"buffer_load_dwordx4 v[168:171], v0, s[8:11], s0 offen offset:0 sc1\n"
"ds_read_b128 v[212:215], v10 offset:17408\n"
"s_add_u32 s0, s72, 0x4a000\n"
"v_add_f32 v106, v106, v34\n"
"v_add_f32 v107, v107, v35\n"
"s_waitcnt lgkmcnt(3)\n"
"v_add_f32 v104, v104, v176\n"
"v_add_f32 v105, v105, v177\n"
"v_add_f32 v106, v106, v178\n"
"v_add_f32 v107, v107, v179\n"
"v_cvt_pk_bf16_f32 v134, v104, v105\n"
"v_cvt_pk_bf16_f32 v135, v106, v107\n"
"s_add_u32 s1, s67, 0x4b000\n"
"buffer_load_dwordx4 v[196:199], v7, s[8:11], s0 offen offset:0 sc1\n"
"buffer_store_dwordx4 v[132:135], v0, s[8:11], s1 offen offset:0 sc1\n"
"s_waitcnt lgkmcnt(2)\n"
"v_add_f32 v108, v108, v48\n"
"v_add_f32 v109, v109, v49\n"
"v_add_f32 v110, v110, v50\n"
"v_add_f32 v111, v111, v51\n"
"s_waitcnt lgkmcnt(1)\n"
"v_add_f32 v108, v108, v116\n"
"v_add_f32 v109, v109, v117\n"
"s_waitcnt vmcnt(11)\n"
"v_max3_u32 v184, v72, v73, v74\n"
"v_add_f32 v110, v110, v118\n"
"v_add_f32 v111, v111, v119\n"
"s_waitcnt lgkmcnt(0)\n"
"v_add_f32 v108, v108, v212\n"
"v_add_f32 v109, v109, v213\n"
"v_add_f32 v110, v110, v214\n"
"v_add_f32 v111, v111, v215\n"
"v_cvt_pk_bf16_f32 v228, v108, v109\n"
"v_cvt_pk_bf16_f32 v229, v110, v111\n"
"s_add_u32 s1, s68, 0x4b000\n"
"buffer_store_dwordx2 v[228:229], v13, s[8:11], s1 offen offset:0 sc1\n"
"v_add_f32 v100, v156, v157\n"
"v_mul_f32 v101, v156, v156\n"
"s_waitcnt vmcnt(11)\n"
"v_max3_u32 v184, v184, v75, v148\n"
"v_max3_u32 v184, v184, v149, v150\n"
"v_add_f32 v102, v108, v109\n"
"v_mul_f32 v103, v108, v108\n"
"v_fmac_f32 v101, v157, v157\n"
"v_fmac_f32 v103, v109, v109\n"
"v_add_f32 v100, v100, v158\n"
"v_fmac_f32 v101, v158, v158\n"
"v_add_f32 v102, v102, v110\n"
"v_fmac_f32 v103, v110, v110\n"
"v_add_f32 v100, v100, v159\n"
"v_fmac_f32 v101, v159, v159\n"
"v_add_f32 v102, v102, v111\n"
"v_fmac_f32 v103, v111, v111\n"
"v_add_f32 v100, v100, v104\n"
"v_fmac_f32 v101, v104, v104\n"
"v_add_f32 v100, v100, v105\n"
"v_fmac_f32 v101, v105, v105\n"
"s_waitcnt vmcnt(10)\n"
"v_max3_u32 v184, v184, v151, v252\n"
"v_max3_u32 v184, v184, v253, v254\n"
"v_max_u32 v184, v184, v255\n"
"v_cmp_eq_u32 vcc, -1, v184\n"
"v_add_f32 v100, v100, v106\n"
"v_fmac_f32 v101, v106, v106\n"
"v_add_f32 v100, v100, v107\n"
"buffer_load_dwordx4 a[40:43], v0, s[8:11], s90 offen offset:0 sc1\n"
"buffer_load_dwordx4 a[60:63], v0, s[8:11], s90 offen offset:1024 sc1\n"
"s_add_u32 s6, s53, 0x8c800\n"
"s_cbranch_vccnz Ls65_%=\n"
"Lb65_%=:\n"
"ds_write_b128 v2, v[72:75] offset:0\n"
"s_waitcnt vmcnt(11)\n"
"v_max3_u32 v28, v160, v161, v162\n"
"ds_write_b128 v2, v[148:151] offset:1024\n"
"buffer_load_dwordx4 a[36:39], v0, s[8:11], s6 offen offset:0 sc1\n"
"v_fmac_f32 v101, v107, v107\n"
"v_fmac_f32 v100, s78, v102\n"
"v_fmac_f32 v101, s78, v103\n"
"ds_write_b128 v2, v[252:255] offset:2048\n"
"s_waitcnt vmcnt(11)\n"
"v_max3_u32 v28, v28, v163, v144\n"
"v_max3_u32 v28, v28, v145, v146\n"
"v_max_u32 v28, v28, v147\n"
"v_cmp_eq_u32 vcc, -1, v28\n"
"v_permlane32_swap_b32 v100, v101\n"
"v_add_f32 v100, v100, v101\n"
"v_mov_b32 v101, v100\n"
"buffer_load_dwordx4 a[32:35], v0, s[8:11], s6 offen offset:1024 sc1\n"
"s_add_u32 s90, s52, 0x90000\n"
"s_cbranch_vccnz Ls66_%=\n"
"Lb66_%=:\n"
"ds_write_b128 v2, v[160:163] offset:3072\n"
"buffer_load_dwordx4 a[56:59], v0, s[8:11], s90 offen offset:0 sc1\n"
"ds_write_b128 v2, v[144:147] offset:4096\n"
"s_waitcnt vmcnt(12)\n"
"v_max3_u32 v136, v120, v121, v122\n"
"s_waitcnt vmcnt(11)\n"
"v_max3_u32 v136, v136, v123, v152\n"
"v_max3_u32 v136, v136, v153, v154\n"
"v_max_u32 v136, v136, v155\n"
"v_cmp_eq_u32 vcc, -1, v136\n"
"v_permlane16_swap_b32 v100, v101\n"
"v_add_f32 v100, v100, v101\n"
"s_add_u32 s1, s71, 0x55000\n"
"buffer_load_dwordx4 a[68:71], v0, s[8:11], s90 offen offset:1024 sc1\n"
"s_add_u32 s6, s53, 0x90800\n"
"s_cbranch_vccnz Ls67_%=\n"
"Lb67_%=:\n"
"ds_write_b128 v2, v[120:123] offset:5120\n"
"buffer_store_dword v100, v14, s[8:11], s1 offen offset:0 sc1\n"
"ds_write_b128 v2, v[152:155] offset:6144\n"
"ds_read_b128 v[240:243], v20 offset:768\n"
"s_waitcnt vmcnt(12) lgkmcnt(6)\n"
"v_max3_u32 v180, v84, v85, v86\n"
"s_add_u32 s1, s63, 0x4b000\n"
"buffer_load_dwordx4 a[52:55], v0, s[8:11], s6 offen offset:0 sc1\n"
"s_waitcnt vmcnt(12) lgkmcnt(5)\n"
"v_max3_u32 v180, v180, v87, v56\n"
"v_max3_u32 v180, v180, v57, v58\n"
"v_max_u32 v180, v180, v59\n"
"v_cmp_eq_u32 vcc, -1, v180\n"
"s_cbranch_vccnz Ls68_%=\n"
"Lb68_%=:\n"
"ds_write_b128 v2, v[84:87] offset:7168\n"
"buffer_load_dwordx4 a[84:87], v0, s[8:11], s6 offen offset:1024 sc1\n"
"ds_write_b128 v2, v[56:59] offset:8192\n"
"s_add_u32 s90, s52, 0x94000\n"
"s_waitcnt vmcnt(12) lgkmcnt(5)\n"
"v_max3_u32 v236, v168, v169, v170\n"
"buffer_load_dwordx4 a[64:67], v0, s[8:11], s90 offen offset:0 sc1\n"
"s_waitcnt lgkmcnt(2)\n"
"v_add_f32_e64 v156, -v156, -v240\n"
"v_add_f32_e64 v157, -v157, -v241\n"
"s_waitcnt vmcnt(12)\n"
"v_max3_u32 v236, v236, v171, v196\n"
"v_max3_u32 v236, v236, v197, v198\n"
"v_max_u32 v236, v236, v199\n"
"v_cmp_eq_u32 vcc, -1, v236\n"
"v_add_f32_e64 v158, -v158, -v242\n"
"v_add_f32_e64 v159, -v159, -v243\n"
"ds_read_b128 v[36:39], v20 offset:832\n"
"s_cbranch_vccnz Ls69_%=\n"
"Lb69_%=:\n"
"ds_write_b128 v2, v[168:171] offset:9216\n"
"buffer_load_dwordx4 a[76:79], v0, s[8:11], s90 offen offset:1024 sc1\n"
"s_add_u32 s6, s53, 0x94800\n"
"v_add_f32 v196, v196, v198\n"
"v_add_f32 v197, v197, v199\n"
"ds_write_b64 v21, v[196:197] offset:0\n"
"buffer_load_dwordx4 a[48:51], v0, s[8:11], s6 offen offset:0 sc1\n"
"s_waitcnt lgkmcnt(2)\n"
"v_add_f32_e64 v104, -v104, -v36\n"
"v_add_f32_e64 v105, -v105, -v37\n"
"v_add_f32_e64 v106, -v106, -v38\n"
"v_add_f32_e64 v107, -v107, -v39\n"
"ds_read_b128 v[88:91], v20 offset:896\n"
"buffer_load_dwordx4 a[72:75], v0, s[8:11], s6 offen offset:1024 sc1\n"
"s_add_u32 s90, s52, 0x98000\n"
"s_waitcnt lgkmcnt(1)\n"
"s_barrier\n"
"buffer_load_dwordx4 v[192:195], v0, s[8:11], s90 offen offset:0 sc1\n"
"ds_read_b128 v[96:99], v27 offset:0\n"
"ds_read_b128 v[248:251], v27 offset:256\n"
"ds_read_b128 v[220:223], v27 offset:512\n"
"ds_read_b128 v[164:167], v27 offset:768\n"
"ds_read_b128 v[64:67], v4 offset:0\n"
"ds_read_b128 v[216:219], v4 offset:5120\n"
"ds_read_b128 v[124:127], v4 offset:1024\n"
"ds_read_b128 v[200:203], v4 offset:6144\n"
"ds_read_b128 v[68:71], v4 offset:2048\n"
"ds_read_b128 v[80:83], v4 offset:7168\n"
"s_waitcnt lgkmcnt(10)\n"
"v_fma_f32 v108, -v108, s78, -v88\n"
"v_fma_f32 v109, -v109, s78, -v89\n"
"v_fma_f32 v110, -v110, s78, -v90\n"
"v_fma_f32 v111, -v111, s78, -v91\n"
"buffer_load_dwordx4 v[172:175], v0, s[8:11], s90 offen offset:1024 sc1\n"
"s_add_u32 s6, s53, 0x98800\n"
"buffer_load_dwordx4 v[224:227], v0, s[8:11], s6 offen offset:0 sc1\n"
"s_waitcnt lgkmcnt(8)\n"
"v_add_f32 v96, v96, v248\n"
"buffer_load_dwordx4 v[92:95], v0, s[8:11], s1 offen offset:0 sc1\n"
"s_waitcnt lgkmcnt(6)\n"
"v_add_f32 v220, v220, v164\n"
"v_add_f32 v97, v97, v249\n"
"v_add_f32 v221, v221, v165\n"
"s_waitcnt lgkmcnt(5)\n"
"v_mfma_f32_16x16x32_bf16 v[40:43], a[88:91], v[64:67], 0\n"
"v_add_f32 v98, v98, v250\n"
"s_waitcnt lgkmcnt(4)\n"
"v_mfma_f32_16x16x32_bf16 v[208:211], a[88:91], v[216:219], 0\n"
"v_add_f32 v222, v222, v166\n"
"v_add_f32 v99, v99, v251\n"
"v_mfma_f32_16x16x32_bf16 v[32:35], a[100:103], v[64:67], 0\n"
"buffer_load_dwordx4 v[176:179], v0, s[8:11], s1 offen offset:1024 sc1\n"
"v_mfma_f32_16x16x32_bf16 v[132:135], a[100:103], v[216:219], 0\n"
"v_add_f32 v223, v223, v167\n"
"v_add_f32 v96, v96, v220\n"
"v_mfma_f32_16x16x32_bf16 v[48:51], a[80:83], v[64:67], 0\n"
"v_add_f32 v97, v97, v221\n"
"v_add_f32 v98, v98, v222\n"
"v_mfma_f32_16x16x32_bf16 v[116:119], a[80:83], v[216:219], 0\n"
"v_add_f32 v99, v99, v223\n"
"v_add_f32 v96, v96, v98\n"
"v_mfma_f32_16x16x32_bf16 v[212:215], a[92:95], v[64:67], 0\n"
"ds_read_b128 v[228:231], v4 offset:3072\n"
"buffer_load_dwordx4 v[184:187], v0, s[8:11], s1 offen offset:2048 sc1\n"
"v_mfma_f32_16x16x32_bf16 v[72:75], a[92:95], v[216:219], 0\n"
"s_waitcnt lgkmcnt(4)\n"
"ds_read_b128 v[148:151], v4 offset:8192\n"
"v_add_f32 v97, v97, v99\n"
"v_mfma_f32_16x16x32_bf16 v[40:43], a[104:107], v[124:127], v[40:43]\n"
"s_waitcnt lgkmcnt(4)\n"
"v_mul_f32 v248, 0xbacccccd, v96\n"
"v_mul_f32 v97, 0x3acccccd, v97\n"
"v_mfma_f32_16x16x32_bf16 v[208:211], a[104:107], v[200:203], v[208:211]\n"
"v_fma_f32 v97, -v248, v248, v97\n"
"v_max_f32 v97, 0, v97\n"
"v_mfma_f32_16x16x32_bf16 v[32:35], a[116:119], v[124:127], v[32:35]\n"
"v_add_f32 v97, 0x3727c5ac, v97\n"
"v_mfma_f32_16x16x32_bf16 v[132:135], a[116:119], v[200:203], v[132:135]\n"
"buffer_load_dwordx4 v[252:255], v0, s[8:11], s1 offen offset:3072 sc1\n"
"s_add_u32 s1, s63, 0x4c000\n"
"v_mfma_f32_16x16x32_bf16 v[48:51], a[96:99], v[124:127], v[48:51]\n"
"v_rsq_f32 v249, v97\n"
"v_mfma_f32_16x16x32_bf16 v[116:119], a[96:99], v[200:203], v[116:119]\n"
"v_mul_f32 v250, v97, v249\n"
"v_mul_f32 v251, v249, v249\n"
"v_mfma_f32_16x16x32_bf16 v[212:215], a[108:111], v[124:127], v[212:215]\n"
"ds_write_b128 v27, v[248:251] offset:8192\n"
"ds_read_b128 v[28:31], v4 offset:4096\n"
"v_mfma_f32_16x16x32_bf16 v[72:75], a[108:111], v[200:203], v[72:75]\n"
"s_waitcnt lgkmcnt(4)\n"
"ds_read_b128 v[160:163], v4 offset:9216\n"
"buffer_load_dwordx4 v[144:147], v0, s[8:11], s1 offen offset:0 sc1\n"
"s_add_u32 s1, s63, 0x50000\n"
"v_mfma_f32_16x16x32_bf16 v[40:43], a[120:123], v[68:71], v[40:43]\n"
"v_mfma_f32_16x16x32_bf16 v[208:211], a[120:123], v[80:83], v[208:211]\n"
"v_mfma_f32_16x16x32_bf16 v[32:35], a[132:135], v[68:71], v[32:35]\n"
"v_mfma_f32_16x16x32_bf16 v[132:135], a[132:135], v[80:83], v[132:135]\n"
"buffer_load_dwordx4 v[136:139], v0, s[8:11], s1 offen offset:0 sc1\n"
"v_mfma_f32_16x16x32_bf16 v[48:51], a[112:115], v[68:71], v[48:51]\n"
"v_mfma_f32_16x16x32_bf16 v[116:119], a[112:115], v[80:83], v[116:119]\n"
"v_mfma_f32_16x16x32_bf16 v[212:215], a[124:127], v[68:71], v[212:215]\n"
"ds_read_b128 v[120:123], v4 offset:20480\n"
"v_mfma_f32_16x16x32_bf16 v[72:75], a[124:127], v[80:83], v[72:75]\n"
"s_waitcnt lgkmcnt(4)\n"
"ds_read_b128 v[100:103], v4 offset:25600\n"
"buffer_load_dwordx4 v[152:155], v0, s[8:11], s1 offen offset:1024 sc1\n"
"v_mfma_f32_16x16x32_bf16 v[40:43], a[136:139], v[228:231], v[40:43]\n"
"v_mfma_f32_16x16x32_bf16 v[208:211], a[136:139], v[148:151], v[208:211]\n"
"v_mfma_f32_16x16x32_bf16 v[32:35], a[152:155], v[228:231], v[32:35]\n"
"v_mfma_f32_16x16x32_bf16 v[132:135], a[152:155], v[148:151], v[132:135]\n"
"v_mfma_f32_16x16x32_bf16 v[48:51], a[128:131], v[228:231], v[48:51]\n"
"buffer_load_dwordx4 v[180:183], v0, s[8:11], s1 offen offset:2048 sc1\n"
"v_mfma_f32_16x16x32_bf16 v[116:119], a[128:131], v[148:151], v[116:119]\n"
"v_mfma_f32_16x16x32_bf16 v[212:215], a[140:143], v[228:231], v[212:215]\n"
"ds_read_b128 v[84:87], v4 offset:21504\n"
"v_mfma_f32_16x16x32_bf16 v[72:75], a[140:143], v[148:151], v[72:75]\n"
"s_waitcnt lgkmcnt(3)\n"
"ds_read_b128 v[56:59], v22 offset:0\n"
"ds_read_b128 v[240:243], v22 offset:1024\n"
"ds_read_b128 v[236:239], v4 offset:26624\n"
"buffer_load_dwordx4 v[168:171], v0, s[8:11], s1 offen offset:3072 sc1\n"
"s_add_u32 s1, s63, 0x51000\n"
"v_mfma_f32_16x16x32_bf16 v[40:43], a[164:167], v[28:31], v[40:43]\n"
"v_mfma_f32_16x16x32_bf16 v[208:211], a[164:167], v[160:163], v[208:211]\n"
"v_mfma_f32_16x16x32_bf16 v[32:35], a[156:159], v[28:31], v[32:35]\n"
"v_mfma_f32_16x16x32_bf16 v[132:135], a[156:159], v[160:163], v[132:135]\n"
"v_mfma_f32_16x16x32_bf16 v[48:51], a[148:151], v[28:31], v[48:51]\n"
"buffer_load_dwordx4 v[196:199], v0, s[8:11], s1 offen offset:0 sc1\n"
"s_add_u32 s1, s72, 0x55000\n"
"v_mfma_f32_16x16x32_bf16 v[116:119], a[148:151], v[160:163], v[116:119]\n"
"v_mfma_f32_16x16x32_bf16 v[212:215], a[144:147], v[28:31], v[212:215]\n"
"ds_read_b128 v[36:39], v4 offset:22528\n"
"v_mfma_f32_16x16x32_bf16 v[72:75], a[144:147], v[160:163], v[72:75]\n"
"s_waitcnt lgkmcnt(5)\n"
"ds_read_b128 v[88:91], v20 offset:256\n"
"ds_read_b128 v[164:167], v20 offset:512\n"
"ds_read_b128 v[220:223], v4 offset:27648\n"
"buffer_load_dwordx4 v[64:67], v7, s[8:11], s1 offen offset:0 sc1\n"
"v_mfma_f32_16x16x32_bf16 v[40:43], a[168:171], v[120:123], v[40:43]\n"
"v_mfma_f32_16x16x32_bf16 v[208:211], a[168:171], v[100:103], v[208:211]\n"
"v_mfma_f32_16x16x32_bf16 v[32:35], a[184:187], v[120:123], v[32:35]\n"
"v_mfma_f32_16x16x32_bf16 v[132:135], a[184:187], v[100:103], v[132:135]\n"
"v_mfma_f32_16x16x32_bf16 v[48:51], a[160:163], v[120:123], v[48:51]\n"
"v_mfma_f32_16x16x32_bf16 v[116:119], a[160:163], v[100:103], v[116:119]\n"
"v_mfma_f32_16x16x32_bf16 v[212:215], a[172:175], v[120:123], v[212:215]\n"
"ds_read_b128 v[216:219], v4 offset:23552\n"
"v_mfma_f32_16x16x32_bf16 v[72:75], a[172:175], v[100:103], v[72:75]\n"
"ds_read_b128 v[96:99], v4 offset:28672\n"
"s_waitcnt lgkmcnt(4)\n"
"v_fmac_f32 v40, v56, v88\n"
"v_fmac_f32 v41, v56, v89\n"
"v_fmac_f32 v42, v56, v90\n"
"v_fmac_f32 v43, v56, v91\n"
"s_waitcnt lgkmcnt(3)\n"
"v_fmac_f32 v40, v58, v164\n"
"v_fmac_f32 v41, v58, v165\n"
"v_fmac_f32 v42, v58, v166\n"
"v_fmac_f32 v43, v58, v167\n"
"s_waitcnt vmcnt(10)\n"
"v_max3_u32 v124, v92, v93, v94\n"
"v_mfma_f32_16x16x32_bf16 v[40:43], a[188:191], v[84:87], v[40:43]\n"
"s_add_u32 m0, s20, 0x0\n"
"s_nop 0\n"
"buffer_load_dwordx4 v0, s[8:11], s22 offen offset:0 sc1 lds\n"
"s_waitcnt vmcnt(10)\n"
"v_max3_u32 v124, v124, v95, v176\n"
"v_mfma_f32_16x16x32_bf16 v[208:211], a[188:191], v[236:239], v[208:211]\n"
"v_max3_u32 v124, v124, v177, v178\n"
"v_mfma_f32_16x16x32_bf16 v[32:35], a[176:179], v[84:87], v[32:35]\n"
"v_mfma_f32_16x16x32_bf16 v[132:135], a[176:179], v[236:239], v[132:135]\n"
"buffer_load_dwordx4 v0, s[8:11], s22 offen offset:1024 sc1 lds\n"
"v_mfma_f32_16x16x32_bf16 v[48:51], a[196:199], v[84:87], v[48:51]\n"
"v_mfma_f32_16x16x32_bf16 v[116:119], a[196:199], v[236:239], v[116:119]\n"
"s_waitcnt vmcnt(10) lgkmcnt(2)\n"
"v_max3_u32 v124, v124, v179, v184\n"
"v_fmac_f32 v208, v240, v88\n"
"v_max3_u32 v124, v124, v185, v186\n"
"v_max_u32 v124, v124, v187\n"
"v_cmp_eq_u32 vcc, -1, v124\n"
"v_mfma_f32_16x16x32_bf16 v[212:215], a[180:183], v[84:87], v[212:215]\n"
"ds_read_b128 v[248:251], v4 offset:24576\n"
"v_fmac_f32 v209, v240, v89\n"
"v_mfma_f32_16x16x32_bf16 v[72:75], a[180:183], v[236:239], v[72:75]\n"
"s_cbranch_vccnz Ls87_%=\n"
"Lb87_%=:\n"
"ds_read_b128 v[200:203], v4 offset:29696\n"
"ds_write_b128 v2, v[92:95] offset:10240\n"
"v_mfma_f32_16x16x32_bf16 v[40:43], a[212:215], v[36:39], v[40:43]\n"
"buffer_load_dwordx4 v0, s[8:11], s22 offen offset:2048 sc1 lds\n"
"ds_write_b128 v2, v[176:179] offset:11264\n"
"v_fmac_f32 v210, v240, v90\n"
"v_fmac_f32 v211, v240, v91\n"
"v_fmac_f32 v208, v242, v164\n"
"v_fmac_f32 v209, v242, v165\n"
"v_fmac_f32 v210, v242, v166\n"
"v_fmac_f32 v211, v242, v167\n"
"s_waitcnt vmcnt(10) lgkmcnt(4)\n"
"v_max3_u32 v68, v252, v253, v254\n"
"v_mfma_f32_16x16x32_bf16 v[208:211], a[212:215], v[220:223], v[208:211]\n"
"ds_write_b128 v2, v[184:187] offset:12288\n"
"v_mfma_f32_16x16x32_bf16 v[32:35], a[204:207], v[36:39], v[32:35]\n"
"buffer_load_dwordx4 v0, s[8:11], s22 offen offset:3072 sc1 lds\n"
"s_waitcnt vmcnt(10)\n"
"v_max3_u32 v68, v68, v255, v144\n"
"v_max3_u32 v68, v68, v145, v146\n"
"v_max_u32 v68, v68, v147\n"
"v_mfma_f32_16x16x32_bf16 v[132:135], a[204:207], v[220:223], v[132:135]\n"
"s_add_u32 s22, s55, 0xa000\n"
"v_cmp_eq_u32 vcc, -1, v68\n"
"v_mfma_f32_16x16x32_bf16 v[48:51], a[192:195], v[36:39], v[48:51]\n"
"s_add_u32 m0, s20, 0x1000\n"
"s_nop 0\n"
"buffer_load_dwordx4 v0, s[8:11], s22 offen offset:0 sc1 lds\n"
"s_cbranch_vccnz Ls88_%=\n"
"Lb88_%=:\n"
"v_mfma_f32_16x16x32_bf16 v[116:119], a[192:195], v[220:223], v[116:119]\n"
"ds_write_b128 v2, v[252:255] offset:13312\n"
"v_mfma_f32_16x16x32_bf16 v[212:215], a[200:203], v[36:39], v[212:215]\n"
"ds_read_b128 v[80:83], v5 offset:0\n"
"s_waitcnt vmcnt(10) lgkmcnt(5)\n"
"v_max3_u32 v228, v136, v137, v138\n"
"s_waitcnt vmcnt(9)\n"
"v_max3_u32 v228, v228, v139, v152\n"
"v_max3_u32 v228, v228, v153, v154\n"
"v_mfma_f32_16x16x32_bf16 v[72:75], a[200:203], v[220:223], v[72:75]\n"
"ds_read_b128 v[148:151], v20 offset:320\n"
"ds_read_b128 v[28:31], v20 offset:576\n"
"ds_read_b128 v[160:163], v5 offset:5120\n"
"v_max_u32 v228, v228, v155\n"
"v_cmp_eq_u32 vcc, -1, v228\n"
"v_mfma_f32_16x16x32_bf16 v[40:43], a[228:231], v[216:219], v[40:43]\n"
"buffer_load_dwordx4 v0, s[8:11], s22 offen offset:1024 sc1 lds\n"
"ds_write_b128 v2, v[144:147] offset:14336\n"
"v_mfma_f32_16x16x32_bf16 v[208:211], a[228:231], v[96:99], v[208:211]\n"
"s_add_u32 s22, s55, 0xa800\n"
"s_cbranch_vccnz Ls89_%=\n"
"Lb89_%=:\n"
"s_waitcnt vmcnt(9) lgkmcnt(7)\n"
"v_max3_u32 v120, v180, v181, v182\n"
"v_mfma_f32_16x16x32_bf16 v[32:35], a[220:223], v[216:219], v[32:35]\n"
"ds_write_b128 v2, v[136:139] offset:15360\n"
"v_mfma_f32_16x16x32_bf16 v[132:135], a[220:223], v[96:99], v[132:135]\n"
"s_add_u32 m0, s21, 0x0\n"
"s_nop 0\n"
"buffer_load_dwordx4 v0, s[8:11], s22 offen offset:0 sc1 lds\n"
"ds_write_b128 v2, v[152:155] offset:16384\n"
"v_mfma_f32_16x16x32_bf16 v[48:51], a[208:211], v[216:219], v[48:51]\n"
"buffer_load_dwordx4 v0, s[8:11], s22 offen offset:1024 sc1 lds\n"
"s_waitcnt vmcnt(10) lgkmcnt(8)\n"
"v_max3_u32 v120, v120, v183, v168\n"
"v_max3_u32 v120, v120, v169, v170\n"
"v_max_u32 v120, v120, v171\n"
"v_mfma_f32_16x16x32_bf16 v[116:119], a[208:211], v[96:99], v[116:119]\n"
"s_waitcnt lgkmcnt(5)\n"
"v_fmac_f32 v32, v56, v148\n"
"v_fmac_f32 v33, v56, v149\n"
"v_cmp_eq_u32 vcc, -1, v120\n"
"v_mfma_f32_16x16x32_bf16 v[212:215], a[216:219], v[216:219], v[212:215]\n"
"ds_read_b128 v[100:103], v5 offset:1024\n"
"v_fmac_f32 v34, v56, v150\n"
"v_mfma_f32_16x16x32_bf16 v[72:75], a[216:219], v[96:99], v[72:75]\n"
"s_cbranch_vccnz Ls90_%=\n"
"Lb90_%=:\n"
"ds_read_b128 v[84:87], v5 offset:6144\n"
"ds_write_b128 v2, v[180:183] offset:17408\n"
"v_mfma_f32_16x16x32_bf16 v[40:43], a[244:247], v[248:251], v[40:43]\n"
"buffer_load_dwordx4 v0, s[8:11], s22 offen offset:2048 sc1 lds\n"
"ds_write_b128 v2, v[168:171] offset:18432\n"
"v_mfma_f32_16x16x32_bf16 v[208:211], a[244:247], v[200:203], v[208:211]\n"
"v_fmac_f32 v35, v56, v151\n"
"s_waitcnt lgkmcnt(6)\n"
"v_fmac_f32 v32, v58, v28\n"
"v_fmac_f32 v33, v58, v29\n"
"v_fmac_f32 v34, v58, v30\n"
"v_fmac_f32 v35, v58, v31\n"
"s_waitcnt vmcnt(10)\n"
"v_max3_u32 v236, v196, v197, v198\n"
"v_mfma_f32_16x16x32_bf16 v[32:35], a[236:239], v[248:251], v[32:35]\n"
"buffer_load_dwordx4 v0, s[8:11], s22 offen offset:3072 sc1 lds\n"
"s_waitcnt vmcnt(10)\n"
"v_max3_u32 v236, v236, v199, v64\n"
"v_max3_u32 v236, v236, v65, v66\n"
"v_max_u32 v236, v236, v67\n"
"v_mfma_f32_16x16x32_bf16 v[132:135], a[236:239], v[200:203], v[132:135]\n"
"s_add_u32 s22, s55, 0xb800\n"
"v_cmp_eq_u32 vcc, -1, v236\n"
"v_mfma_f32_16x16x32_bf16 v[48:51], a[224:227], v[248:251], v[48:51]\n"
"s_add_u32 m0, s21, 0x1000\n"
"s_nop 0\n"
"buffer_load_dwordx4 v0, s[8:11], s22 offen offset:0 sc1 lds\n"
"s_cbranch_vccnz Ls91_%=\n"
"Lb91_%=:\n"
"v_mfma_f32_16x16x32_bf16 v[116:119], a[224:227], v[200:203], v[116:119]\n"
"ds_write_b128 v2, v[196:199] offset:19456\n"
"v_add_f32 v64, v64, v66\n"
"v_mfma_f32_16x16x32_bf16 v[212:215], a[232:235], v[248:251], v[212:215]\n"
"buffer_load_dwordx4 v[124:127], v0, s[8:11], s6 offen offset:1024 sc1\n"
"ds_read_b128 v[92:95], v5 offset:2048\n"
"v_add_f32 v65, v65, v67\n"
"v_mfma_f32_16x16x32_bf16 v[72:75], a[232:235], v[200:203], v[72:75]\n"
"s_add_u32 s90, s52, 0x9c000\n"
"ds_read_b128 v[176:179], v5 offset:7168\n"
"ds_write_b64 v21, v[64:65] offset:4096\n"
"v_mfma_f32_16x16x32_bf16 v[40:43], v[44:47], v[80:83], v[40:43]\n"
"buffer_load_dwordx4 v[88:91], v0, s[8:11], s90 offen offset:0 sc1\n"
"v_mfma_f32_16x16x32_bf16 v[208:211], v[44:47], v[160:163], v[208:211]\n"
"v_fmac_f32 v132, v240, v148\n"
"v_fmac_f32 v133, v240, v149\n"
"v_mfma_f32_16x16x32_bf16 v[32:35], v[112:115], v[80:83], v[32:35]\n"
"v_fmac_f32 v134, v240, v150\n"
"v_fmac_f32 v135, v240, v151\n"
"v_fmac_f32 v132, v242, v28\n"
"v_fmac_f32 v133, v242, v29\n"
"v_fmac_f32 v134, v242, v30\n"
"v_fmac_f32 v135, v242, v31\n"
"buffer_load_dwordx4 v[164:167], v0, s[8:11], s90 offen offset:1024 sc1\n"
"s_add_u32 s6, s53, 0x9c800\n"
"v_mfma_f32_16x16x32_bf16 v[132:135], v[112:115], v[160:163], v[132:135]\n"
"v_mfma_f32_16x16x32_bf16 v[48:51], v[128:131], v[80:83], v[48:51]\n"
"v_mfma_f32_16x16x32_bf16 v[116:119], v[128:131], v[160:163], v[116:119]\n"
"buffer_load_dwordx4 v[184:187], v0, s[8:11], s6 offen offset:0 sc1\n"
"v_mfma_f32_16x16x32_bf16 v[212:215], v[244:247], v[80:83], v[212:215]\n"
"ds_read_b128 v[68:71], v5 offset:3072\n"
"s_waitcnt lgkmcnt(1)\n"
"s_barrier\n"
"ds_read_b128 v[252:255], v27 offset:4096\n"
"ds_read_b128 v[36:39], v27 offset:4352\n"
"ds_read_b128 v[220:223], v27 offset:4608\n"
"ds_read_b128 v[144:147], v27 offset:4864\n"
"ds_read_b128 v[228:231], v4 offset:10240\n"
"ds_read_b128 v[136:139], v4 offset:15360\n"
"ds_read_b128 v[152:155], v4 offset:11264\n"
"ds_read_b128 v[216:219], v4 offset:16384\n"
"ds_read_b128 v[96:99], v4 offset:12288\n"
"ds_read_b128 v[120:123], v4 offset:17408\n"
"v_mfma_f32_16x16x32_bf16 v[72:75], v[244:247], v[160:163], v[72:75]\n"
"ds_read_b128 v[180:183], v20 offset:384\n"
"ds_read_b128 v[168:171], v20 offset:640\n"
"ds_read_b128 v[236:239], v5 offset:8192\n"
"v_mfma_f32_16x16x32_bf16 v[40:43], v[140:143], v[100:103], v[40:43]\n"
"buffer_load_dwordx4 v0, s[8:11], s22 offen offset:1024 sc1 lds\n"
"v_mfma_f32_16x16x32_bf16 v[208:211], v[140:143], v[84:87], v[208:211]\n"
"v_mfma_f32_16x16x32_bf16 v[32:35], v[188:191], v[100:103], v[32:35]\n"
"v_mfma_f32_16x16x32_bf16 v[132:135], v[188:191], v[84:87], v[132:135]\n"
"v_mfma_f32_16x16x32_bf16 v[48:51], v[204:207], v[100:103], v[48:51]\n"
"buffer_load_dwordx4 v0, s[8:11], s22 offen offset:2048 sc1 lds\n"
"v_mfma_f32_16x16x32_bf16 v[116:119], v[204:207], v[84:87], v[116:119]\n"
"s_waitcnt lgkmcnt(11)\n"
"v_add_f32 v252, v252, v36\n"
"v_mfma_f32_16x16x32_bf16 v[212:215], v[76:79], v[100:103], v[212:215]\n"
"ds_read_b128 v[196:199], v5 offset:4096\n"
"v_mfma_f32_16x16x32_bf16 v[72:75], v[76:79], v[84:87], v[72:75]\n"
"ds_read_b128 v[248:251], v5 offset:9216\n"
"v_mfma_f32_16x16x32_bf16 v[40:43], a[248:251], v[92:95], v[40:43]\n"
"buffer_load_dwordx4 v0, s[8:11], s22 offen offset:3072 sc1 lds\n"
"v_mfma_f32_16x16x32_bf16 v[208:211], a[248:251], v[176:179], v[208:211]\n"
"s_waitcnt lgkmcnt(11)\n"
"v_add_f32 v220, v220, v144\n"
"v_add_f32 v253, v253, v37\n"
"v_mfma_f32_16x16x32_bf16 v[32:35], a[12:15], v[92:95], v[32:35]\n"
"v_add_f32 v221, v221, v145\n"
"v_add_f32 v254, v254, v38\n"
"v_mfma_f32_16x16x32_bf16 v[132:135], a[12:15], v[176:179], v[132:135]\n"
"s_waitcnt lgkmcnt(10)\n"
"v_add_f32 v222, v222, v146\n"
"v_add_f32 v255, v255, v39\n"
"v_mfma_f32_16x16x32_bf16 v[200:203], a[88:91], v[228:231], 0\n"
"s_waitcnt lgkmcnt(9)\n"
"v_add_f32 v223, v223, v147\n"
"v_add_f32 v252, v252, v220\n"
"v_mfma_f32_16x16x32_bf16 v[64:67], a[88:91], v[136:139], 0\n"
"buffer_load_dwordx4 a[88:91], v0, s[8:11], s6 offen offset:1024 sc1\n"
"v_mfma_f32_16x16x32_bf16 v[148:151], a[100:103], v[228:231], 0\n"
"s_add_u32 s90, s55, 0x0\n"
"v_add_f32 v253, v253, v221\n"
"v_add_f32 v254, v254, v222\n"
"v_mfma_f32_16x16x32_bf16 v[28:31], a[100:103], v[136:139], 0\n"
"buffer_load_dwordx4 a[100:103], v0, s[8:11], s90 offen offset:0 sc1\n"
"v_mfma_f32_16x16x32_bf16 v[80:83], a[80:83], v[228:231], 0\n"
"s_waitcnt lgkmcnt(4)\n"
"v_fmac_f32 v48, v56, v180\n"
"v_fmac_f32 v49, v56, v181\n"
"v_mfma_f32_16x16x32_bf16 v[160:163], a[80:83], v[136:139], 0\n"
"v_fmac_f32 v50, v56, v182\n"
"v_fmac_f32 v51, v56, v183\n"
"v_mfma_f32_16x16x32_bf16 v[100:103], a[92:95], v[228:231], 0\n"
"buffer_load_dwordx4 a[80:83], v0, s[8:11], s90 offen offset:1024 sc1\n"
"ds_read_b128 v[84:87], v4 offset:13312\n"
"v_mfma_f32_16x16x32_bf16 v[144:147], a[92:95], v[136:139], 0\n"
"ds_read_b128 v[228:231], v4 offset:18432\n"
"v_add_f32 v255, v255, v223\n"
"v_mfma_f32_16x16x32_bf16 v[200:203], a[104:107], v[152:155], v[200:203]\n"
"s_waitcnt lgkmcnt(5)\n"
"v_fmac_f32 v48, v58, v168\n"
"v_fmac_f32 v49, v58, v169\n"
"v_mfma_f32_16x16x32_bf16 v[64:67], a[104:107], v[216:219], v[64:67]\n"
"v_fmac_f32 v50, v58, v170\n"
"v_fmac_f32 v51, v58, v171\n"
"v_mfma_f32_16x16x32_bf16 v[148:151], a[116:119], v[152:155], v[148:151]\n"
"buffer_load_dwordx4 a[92:95], v0, s[8:11], s90 offen offset:2048 sc1\n"
"v_mfma_f32_16x16x32_bf16 v[48:51], a[240:243], v[92:95], v[48:51]\n"
"v_add_f32 v252, v252, v254\n"
"v_add_f32 v253, v253, v255\n"
"v_mfma_f32_16x16x32_bf16 v[116:119], a[240:243], v[176:179], v[116:119]\n"
"v_mul_f32 v36, 0xbacccccd, v252\n"
"v_mul_f32 v253, 0x3acccccd, v253\n"
"v_mfma_f32_16x16x32_bf16 v[212:215], a[252:255], v[92:95], v[212:215]\n"
"ds_read_b128 v[136:139], v5 offset:20480\n"
"v_fma_f32 v253, -v36, v36, v253\n"
"v_mfma_f32_16x16x32_bf16 v[72:75], a[252:255], v[176:179], v[72:75]\n"
"ds_read_b128 v[220:223], v5 offset:25600\n"
"v_max_f32 v253, 0, v253\n"
"v_mfma_f32_16x16x32_bf16 v[40:43], a[8:11], v[68:71], v[40:43]\n"
"s_waitcnt lgkmcnt(4)\n"
"buffer_load_dwordx4 a[104:107], v0, s[8:11], s90 offen offset:3072 sc1\n"
"v_mfma_f32_16x16x32_bf16 v[208:211], a[8:11], v[236:239], v[208:211]\n"
"s_add_u32 s90, s55, 0x1000\n"
"v_fmac_f32 v116, v240, v180\n"
"v_fmac_f32 v117, v240, v181\n"
"v_mfma_f32_16x16x32_bf16 v[32:35], a[28:31], v[68:71], v[32:35]\n"
"v_fmac_f32 v118, v240, v182\n"
"v_fmac_f32 v119, v240, v183\n"
"v_mfma_f32_16x16x32_bf16 v[132:135], a[28:31], v[236:239], v[132:135]\n"
"v_fmac_f32 v116, v242, v168\n"
"v_fmac_f32 v117, v242, v169\n"
"v_mfma_f32_16x16x32_bf16 v[48:51], a[4:7], v[68:71], v[48:51]\n"
"v_fmac_f32 v118, v242, v170\n"
"v_fmac_f32 v119, v242, v171\n"
"v_mfma_f32_16x16x32_bf16 v[28:31], a[116:119], v[216:219], v[28:31]\n"
"buffer_load_dwordx4 a[116:119], v0, s[8:11], s90 offen offset:0 sc1\n"
"v_mfma_f32_16x16x32_bf16 v[116:119], a[4:7], v[236:239], v[116:119]\n"
"s_add_u32 s90, s56, 0x1400\n"
"v_add_f32 v253, 0x3727c5ac, v253\n"
"v_mfma_f32_16x16x32_bf16 v[212:215], a[0:3], v[68:71], v[212:215]\n"
"ds_read_b128 v[92:95], v5 offset:21504\n"
"v_mfma_f32_16x16x32_bf16 v[72:75], a[0:3], v[236:239], v[72:75]\n"
"ds_read_b128 v[176:179], v20 offset:448\n"
"ds_read_b128 v[180:183], v20 offset:704\n"
"ds_read_b128 v[168:171], v5 offset:26624\n"
"v_mfma_f32_16x16x32_bf16 v[40:43], a[24:27], v[196:199], v[40:43]\n"
"v_rsq_f32 v37, v253\n"
"v_mfma_f32_16x16x32_bf16 v[208:211], a[24:27], v[248:251], v[208:211]\n"
"v_mul_f32 v38, v253, v37\n"
"v_mul_f32 v39, v37, v37\n"
"v_mfma_f32_16x16x32_bf16 v[32:35], a[44:47], v[196:199], v[32:35]\n"
"ds_write_b128 v27, v[36:39] offset:8448\n"
"v_mfma_f32_16x16x32_bf16 v[132:135], a[44:47], v[248:251], v[132:135]\n"
"v_mfma_f32_16x16x32_bf16 v[48:51], a[20:23], v[196:199], v[48:51]\n"
"v_mfma_f32_16x16x32_bf16 v[116:119], a[20:23], v[248:251], v[116:119]\n"
"v_mfma_f32_16x16x32_bf16 v[212:215], a[16:19], v[196:199], v[212:215]\n"
"ds_read_b128 v[68:71], v5 offset:22528\n"
"v_mfma_f32_16x16x32_bf16 v[72:75], a[16:19], v[248:251], v[72:75]\n"
"s_waitcnt lgkmcnt(6)\n"
"ds_read_b128 v[236:239], v5 offset:27648\n"
"v_mfma_f32_16x16x32_bf16 v[40:43], a[40:43], v[136:139], v[40:43]\n"
"v_mfma_f32_16x16x32_bf16 v[208:211], a[40:43], v[220:223], v[208:211]\n"
"v_mfma_f32_16x16x32_bf16 v[32:35], a[60:63], v[136:139], v[32:35]\n"
"v_mfma_f32_16x16x32_bf16 v[132:135], a[60:63], v[220:223], v[132:135]\n"
"s_waitcnt lgkmcnt(5)\n"
"v_fmac_f32 v212, v56, v176\n"
"v_fmac_f32 v213, v56, v177\n"
"v_mfma_f32_16x16x32_bf16 v[48:51], a[36:39], v[136:139], v[48:51]\n"
"v_fmac_f32 v214, v56, v178\n"
"v_fmac_f32 v215, v56, v179\n"
"v_mfma_f32_16x16x32_bf16 v[116:119], a[36:39], v[220:223], v[116:119]\n"
"s_waitcnt lgkmcnt(4)\n"
"v_fmac_f32 v212, v58, v180\n"
"v_fmac_f32 v213, v58, v181\n"
"v_mfma_f32_16x16x32_bf16 v[80:83], a[96:99], v[152:155], v[80:83]\n"
"v_fmac_f32 v214, v58, v182\n"
"v_fmac_f32 v215, v58, v183\n"
"v_mfma_f32_16x16x32_bf16 v[160:163], a[96:99], v[216:219], v[160:163]\n"
"buffer_load_dwordx4 a[96:99], v0, s[8:11], s90 offen offset:0 sc1\n"
"v_mfma_f32_16x16x32_bf16 v[212:215], a[32:35], v[136:139], v[212:215]\n"
"ds_read_b128 v[252:255], v5 offset:23552\n"
"v_mfma_f32_16x16x32_bf16 v[72:75], a[32:35], v[220:223], v[72:75]\n"
"ds_read_b128 v[36:39], v5 offset:28672\n"
"v_mfma_f32_16x16x32_bf16 v[40:43], a[56:59], v[92:95], v[40:43]\n"
"s_waitcnt lgkmcnt(4)\n"
"v_mfma_f32_16x16x32_bf16 v[208:211], a[56:59], v[168:171], v[208:211]\n"
"v_mfma_f32_16x16x32_bf16 v[32:35], a[68:71], v[92:95], v[32:35]\n"
"v_mfma_f32_16x16x32_bf16 v[132:135], a[68:71], v[168:171], v[132:135]\n"
"v_mfma_f32_16x16x32_bf16 v[48:51], a[52:55], v[92:95], v[48:51]\n"
"v_mfma_f32_16x16x32_bf16 v[116:119], a[52:55], v[168:171], v[116:119]\n"
"v_fmac_f32 v72, v240, v176\n"
"v_fmac_f32 v73, v240, v177\n"
"v_mfma_f32_16x16x32_bf16 v[212:215], a[84:87], v[92:95], v[212:215]\n"
"ds_read_b128 v[196:199], v5 offset:24576\n"
"v_fmac_f32 v74, v240, v178\n"
"v_mfma_f32_16x16x32_bf16 v[100:103], a[108:111], v[152:155], v[100:103]\n"
"ds_read_b128 v[248:251], v4 offset:14336\n"
"v_fmac_f32 v75, v240, v179\n"
"v_mfma_f32_16x16x32_bf16 v[144:147], a[108:111], v[216:219], v[144:147]\n"
"buffer_load_dwordx4 a[108:111], v0, s[8:11], s90 offen offset:1024 sc1\n"
"ds_read_b128 v[136:139], v4 offset:19456\n"
"v_mfma_f32_16x16x32_bf16 v[200:203], a[120:123], v[96:99], v[200:203]\n"
"v_fmac_f32 v72, v242, v180\n"
"v_fmac_f32 v73, v242, v181\n"
"v_mfma_f32_16x16x32_bf16 v[64:67], a[120:123], v[120:123], v[64:67]\n"
"buffer_load_dwordx4 a[120:123], v0, s[8:11], s90 offen offset:2048 sc1\n"
"v_mfma_f32_16x16x32_bf16 v[148:151], a[132:135], v[96:99], v[148:151]\n"
"v_fmac_f32 v74, v242, v182\n"
"v_fmac_f32 v75, v242, v183\n"
"v_mfma_f32_16x16x32_bf16 v[28:31], a[132:135], v[120:123], v[28:31]\n"
"buffer_load_dwordx4 a[132:135], v0, s[8:11], s90 offen offset:3072 sc1\n"
"v_mfma_f32_16x16x32_bf16 v[72:75], a[84:87], v[168:171], v[72:75]\n"
"s_waitcnt vmcnt(25) lgkmcnt(3)\n"
"ds_read_b128 v[220:223], v5 offset:29696\n"
"s_add_u32 s90, s56, 0x2400\n"
"v_mfma_f32_16x16x32_bf16 v[40:43], a[64:67], v[68:71], v[40:43]\n"
"s_bitcmp1_b32 s61, 0\n"
"s_cselect_b32 s2, s60, s59\n"
"s_add_u32 s2, s2, 0x6a000\n"
"v_mfma_f32_16x16x32_bf16 v[208:211], a[64:67], v[236:239], v[208:211]\n"
"v_mfma_f32_16x16x32_bf16 v[32:35], a[76:79], v[68:71], v[32:35]\n"
"v_mfma_f32_16x16x32_bf16 v[132:135], a[76:79], v[236:239], v[132:135]\n"
"v_mfma_f32_16x16x32_bf16 v[48:51], a[48:51], v[68:71], v[48:51]\n"
"v_mfma_f32_16x16x32_bf16 v[116:119], a[48:51], v[236:239], v[116:119]\n"
"v_mfma_f32_16x16x32_bf16 v[212:215], a[72:75], v[68:71], v[212:215]\n"
"v_mfma_f32_16x16x32_bf16 v[72:75], a[72:75], v[236:239], v[72:75]\n"
"v_mfma_f32_16x16x32_bf16 v[40:43], v[192:195], v[252:255], v[40:43]\n"
"v_mfma_f32_16x16x32_bf16 v[208:211], v[192:195], v[36:39], v[208:211]\n"
"v_mfma_f32_16x16x32_bf16 v[32:35], v[172:175], v[252:255], v[32:35]\n"
"v_mfma_f32_16x16x32_bf16 v[132:135], v[172:175], v[36:39], v[132:135]\n"
"v_mfma_f32_16x16x32_bf16 v[48:51], v[224:227], v[252:255], v[48:51]\n"
"v_mfma_f32_16x16x32_bf16 v[116:119], v[224:227], v[36:39], v[116:119]\n"
"s_waitcnt vmcnt(16) lgkmcnt(0)\n"
"v_mfma_f32_16x16x32_bf16 v[212:215], v[124:127], v[252:255], v[212:215]\n"
"v_mfma_f32_16x16x32_bf16 v[72:75], v[124:127], v[36:39], v[72:75]\n"
"s_waitcnt vmcnt(15)\n"
"v_mfma_f32_16x16x32_bf16 v[40:43], v[88:91], v[196:199], v[40:43]\n"
"v_mfma_f32_16x16x32_bf16 v[208:211], v[88:91], v[220:223], v[208:211]\n"
"s_waitcnt vmcnt(14)\n"
"v_mfma_f32_16x16x32_bf16 v[32:35], v[164:167], v[196:199], v[32:35]\n"
"v_mfma_f32_16x16x32_bf16 v[132:135], v[164:167], v[220:223], v[132:135]\n"
"s_waitcnt vmcnt(13)\n"
"v_mfma_f32_16x16x32_bf16 v[48:51], v[184:187], v[196:199], v[48:51]\n"
"v_mfma_f32_16x16x32_bf16 v[116:119], v[184:187], v[220:223], v[116:119]\n"
"s_waitcnt vmcnt(9)\n"
"v_mul_f32 v92, v40, v59\n"
"v_mul_f32 v93, v41, v59\n"
"v_mfma_f32_16x16x32_bf16 v[212:215], a[88:91], v[196:199], v[212:215]\n"
"v_mul_f32 v94, v42, v59\n"
"v_mul_f32 v95, v43, v59\n"
"v_mfma_f32_16x16x32_bf16 v[72:75], a[88:91], v[220:223], v[72:75]\n"
"v_mul_f32 v92, -v92, |v40|\n"
"v_mul_f32 v93, -v93, |v41|\n"
"v_mul_f32 v94, -v94, |v42|\n"
"v_mul_f32 v95, -v95, |v43|\n"
"v_cvt_pk_bf16_f32 v40, v92, v93\n"
"v_cvt_pk_bf16_f32 v41, v94, v95\n"
"v_mfma_f32_16x16x32_bf16 v[80:83], a[112:115], v[96:99], v[80:83]\n"
"v_mul_f32 v92, v32, v59\n"
"v_mul_f32 v93, v33, v59\n"
"v_mul_f32 v94, v34, v59\n"
"v_mul_f32 v95, v35, v59\n"
"v_mul_f32 v92, -v92, |v32|\n"
"v_mul_f32 v93, -v93, |v33|\n"
"v_mfma_f32_16x16x32_bf16 v[160:163], a[112:115], v[120:123], v[160:163]\n"
"buffer_load_dwordx4 a[112:115], v0, s[8:11], s90 offen offset:0 sc1\n"
"v_mul_f32 v94, -v94, |v34|\n"
"v_mul_f32 v95, -v95, |v35|\n"
"v_cvt_pk_bf16_f32 v42, v92, v93\n"
"v_cvt_pk_bf16_f32 v43, v94, v95\n"
"v_mfma_f32_16x16x32_bf16 v[100:103], a[124:127], v[96:99], v[100:103]\n"
"s_add_u32 s90, s55, 0x2800\n"
"ds_read_b128 v[152:155], v4 offset:30720\n"
"v_pk_max_u16 v40, v40, s37\n"
"v_pk_max_u16 v41, v41, s37\n"
"v_pk_max_u16 v42, v42, s37\n"
"v_pk_max_u16 v43, v43, s37\n"
"s_add_u32 s0, s69, 0x6a000\n"
"v_mfma_f32_16x16x32_bf16 v[144:147], a[124:127], v[120:123], v[144:147]\n"
"buffer_load_dwordx4 a[124:127], v0, s[8:11], s90 offen offset:0 sc1\n"
"ds_read_b128 v[176:179], v4 offset:35840\n"
"buffer_store_dwordx4 v[40:43], v0, s[8:11], s0 offen offset:0 sc1\n"
"v_mfma_f32_16x16x32_bf16 v[200:203], a[136:139], v[84:87], v[200:203]\n"
"v_mul_f32 v216, v208, v243\n"
"v_mul_f32 v217, v209, v243\n"
"v_mul_f32 v218, v210, v243\n"
"v_mul_f32 v219, v211, v243\n"
"v_mul_f32 v216, -v216, |v208|\n"
"v_mul_f32 v217, -v217, |v209|\n"
"v_mfma_f32_16x16x32_bf16 v[64:67], a[136:139], v[228:231], v[64:67]\n"
"buffer_load_dwordx4 a[136:139], v0, s[8:11], s90 offen offset:1024 sc1\n"
"v_mul_f32 v218, -v218, |v210|\n"
"v_mul_f32 v219, -v219, |v211|\n"
"v_cvt_pk_bf16_f32 v208, v216, v217\n"
"v_cvt_pk_bf16_f32 v209, v218, v219\n"
"v_mfma_f32_16x16x32_bf16 v[148:151], a[152:155], v[84:87], v[148:151]\n"
"v_mul_f32 v216, v132, v243\n"
"v_mul_f32 v217, v133, v243\n"
"v_mul_f32 v218, v134, v243\n"
"v_mul_f32 v219, v135, v243\n"
"v_mul_f32 v216, -v216, |v132|\n"
"v_mul_f32 v217, -v217, |v133|\n"
"v_mfma_f32_16x16x32_bf16 v[28:31], a[152:155], v[228:231], v[28:31]\n"
"buffer_load_dwordx4 a[152:155], v0, s[8:11], s90 offen offset:2048 sc1\n"
"v_mul_f32 v218, -v218, |v134|\n"
"v_mul_f32 v219, -v219, |v135|\n"
"v_cvt_pk_bf16_f32 v210, v216, v217\n"
"v_cvt_pk_bf16_f32 v211, v218, v219\n"
"v_mfma_f32_16x16x32_bf16 v[80:83], a[128:131], v[84:87], v[80:83]\n"
"v_pk_max_u16 v208, v208, s37\n"
"v_pk_max_u16 v209, v209, s37\n"
"v_pk_max_u16 v210, v210, s37\n"
"v_pk_max_u16 v211, v211, s37\n"
"s_add_u32 s0, s69, 0x77800\n"
"v_mfma_f32_16x16x32_bf16 v[160:163], a[128:131], v[228:231], v[160:163]\n"
"buffer_store_dwordx4 v[208:211], v0, s[8:11], s0 offen offset:0 sc1\n"
"v_mul_f32 v180, v48, v59\n"
"v_mfma_f32_16x16x32_bf16 v[100:103], a[140:143], v[84:87], v[100:103]\n"
"ds_read_b128 v[168:171], v4 offset:31744\n"
"v_mul_f32 v181, v49, v59\n"
"v_mul_f32 v182, v50, v59\n"
"v_mul_f32 v183, v51, v59\n"
"v_mul_f32 v180, -v180, |v48|\n"
"v_mul_f32 v181, -v181, |v49|\n"
"v_mfma_f32_16x16x32_bf16 v[144:147], a[140:143], v[228:231], v[144:147]\n"
"ds_read_b128 v[68:71], v22 offset:256\n"
"ds_read_b128 v[236:239], v22 offset:1280\n"
"ds_read_b128 v[252:255], v4 offset:36864\n"
"v_mul_f32 v182, -v182, |v50|\n"
"v_mul_f32 v183, -v183, |v51|\n"
"v_cvt_pk_bf16_f32 v48, v180, v181\n"
"v_mfma_f32_16x16x32_bf16 v[200:203], a[164:167], v[248:251], v[200:203]\n"
"buffer_load_dwordx4 a[128:131], v0, s[8:11], s90 offen offset:3072 sc1\n"
"v_cvt_pk_bf16_f32 v49, v182, v183\n"
"v_mul_f32 v180, v212, v59\n"
"v_mul_f32 v181, v213, v59\n"
"v_mul_f32 v182, v214, v59\n"
"v_mfma_f32_16x16x32_bf16 v[64:67], a[164:167], v[136:139], v[64:67]\n"
"s_add_u32 s90, s55, 0x3800\n"
"v_mul_f32 v183, v215, v59\n"
"v_mul_f32 v180, -v180, |v212|\n"
"v_mul_f32 v181, -v181, |v213|\n"
"v_mul_f32 v182, -v182, |v214|\n"
"v_mul_f32 v183, -v183, |v215|\n"
"v_cvt_pk_bf16_f32 v50, v180, v181\n"
"v_mfma_f32_16x16x32_bf16 v[148:151], a[156:159], v[248:251], v[148:151]\n"
"v_cvt_pk_bf16_f32 v51, v182, v183\n"
"v_pk_max_u16 v48, v48, s37\n"
"v_pk_max_u16 v49, v49, s37\n"
"v_pk_max_u16 v50, v50, s37\n"
"v_pk_max_u16 v51, v51, s37\n"
"s_add_u32 s0, s70, 0x6a000\n"
"v_mfma_f32_16x16x32_bf16 v[28:31], a[156:159], v[136:139], v[28:31]\n"
"buffer_load_dwordx4 a[140:143], v0, s[8:11], s90 offen offset:0 sc1\n"
"buffer_store_dwordx4 v[48:51], v0, s[8:11], s0 offen offset:0 sc1\n"
"v_mfma_f32_16x16x32_bf16 v[80:83], a[148:151], v[248:251], v[80:83]\n"
"v_mul_f32 v36, v116, v243\n"
"v_mul_f32 v37, v117, v243\n"
"v_mul_f32 v38, v118, v243\n"
"v_mul_f32 v39, v119, v243\n"
"v_mul_f32 v36, -v36, |v116|\n"
"v_mul_f32 v37, -v37, |v117|\n"
"v_mfma_f32_16x16x32_bf16 v[160:163], a[148:151], v[136:139], v[160:163]\n"
"v_mul_f32 v38, -v38, |v118|\n"
"v_mul_f32 v39, -v39, |v119|\n"
"v_cvt_pk_bf16_f32 v116, v36, v37\n"
"v_cvt_pk_bf16_f32 v117, v38, v39\n"
"v_mul_f32 v36, v72, v243\n"
"v_mul_f32 v37, v73, v243\n"
"v_mfma_f32_16x16x32_bf16 v[100:103], a[144:147], v[248:251], v[100:103]\n"
"ds_read_b128 v[196:199], v4 offset:32768\n"
"v_mul_f32 v38, v74, v243\n"
"v_mul_f32 v39, v75, v243\n"
"v_mul_f32 v36, -v36, |v72|\n"
"v_mul_f32 v37, -v37, |v73|\n"
"v_mul_f32 v38, -v38, |v74|\n"
"v_mfma_f32_16x16x32_bf16 v[144:147], a[144:147], v[136:139], v[144:147]\n"
"s_waitcnt lgkmcnt(1)\n"
"buffer_load_dwordx4 a[164:167], v0, s[8:11], s90 offen offset:1024 sc1\n"
"ds_read_b128 v[220:223], v20 offset:256\n"
"ds_read_b128 v[32:35], v20 offset:512\n"
"ds_read_b128 v[92:95], v4 offset:37888\n"
"v_mul_f32 v39, -v39, |v75|\n"
"v_mfma_f32_16x16x32_bf16 v[200:203], a[168:171], v[152:155], v[200:203]\n"
"v_cvt_pk_bf16_f32 v118, v36, v37\n"
"v_cvt_pk_bf16_f32 v119, v38, v39\n"
"v_pk_max_u16 v116, v116, s37\n"
"v_pk_max_u16 v117, v117, s37\n"
"v_pk_max_u16 v118, v118, s37\n"
"v_pk_max_u16 v119, v119, s37\n"
"v_mfma_f32_16x16x32_bf16 v[64:67], a[168:171], v[176:179], v[64:67]\n"
"s_add_u32 s0, s70, 0x77800\n"
"buffer_store_dwordx4 v[116:119], v0, s[8:11], s0 offen offset:0 sc1\n"
"v_mfma_f32_16x16x32_bf16 v[148:151], a[184:187], v[152:155], v[148:151]\n"
"s_cmp_eq_u32 s62, 0\n"
"s_cselect_b64 exec, -1, 0\n"
"v_swap_b32 v40, v208\n"
"v_swap_b32 v41, v209\n"
"v_swap_b32 v42, v210\n"
"v_swap_b32 v43, v211\n"
"s_mov_b64 exec, -1\n"
"v_mfma_f32_16x16x32_bf16 v[28:31], a[184:187], v[176:179], v[28:31]\n"
"s_waitcnt lgkmcnt(2)\n"
"v_fmac_f32 v200, v68, v220\n"
"v_fmac_f32 v201, v68, v221\n"
"s_cmp_eq_u32 s62, 0\n"
"s_cselect_b64 exec, -1, 0\n"
"v_swap_b32 v48, v116\n"
"v_swap_b32 v49, v117\n"
"v_swap_b32 v50, v118\n"
"v_swap_b32 v51, v119\n"
"s_mov_b64 exec, -1\n"
"v_mfma_f32_16x16x32_bf16 v[80:83], a[160:163], v[152:155], v[80:83]\n"
"buffer_load_dwordx4 a[156:159], v0, s[8:11], s90 offen offset:2048 sc1\n"
"v_mfma_f32_16x16x32_bf16 v[160:163], a[160:163], v[176:179], v[160:163]\n"
"v_fmac_f32 v202, v68, v222\n"
"v_fmac_f32 v203, v68, v223\n"
"v_mfma_f32_16x16x32_bf16 v[100:103], a[172:175], v[152:155], v[100:103]\n"
"buffer_load_dwordx4 a[148:151], v0, s[8:11], s90 offen offset:3072 sc1\n"
"ds_read_b128 v[96:99], v4 offset:33792\n"
"v_mfma_f32_16x16x32_bf16 v[144:147], a[172:175], v[176:179], v[144:147]\n"
"s_waitcnt vmcnt(21) lgkmcnt(1)\n"
"ds_read_b128 v[120:123], v4 offset:38912\n"
"s_add_u32 s90, s55, 0x4800\n"
"v_mfma_f32_16x16x32_bf16 v[60:63], a[100:103], v[40:43], v[60:63]\n"
"s_waitcnt vmcnt(20)\n"
"v_fmac_f32 v200, v70, v32\n"
"v_fmac_f32 v201, v70, v33\n"
"v_mfma_f32_16x16x32_bf16 v[52:55], a[80:83], v[40:43], v[52:55]\n"
"s_waitcnt vmcnt(19)\n"
"v_fmac_f32 v202, v70, v34\n"
"v_fmac_f32 v203, v70, v35\n"
"v_mfma_f32_16x16x32_bf16 v[132:135], a[92:95], v[40:43], 0\n"
"buffer_load_dwordx4 a[144:147], v0, s[8:11], s90 offen offset:0 sc1\n"
"v_mfma_f32_16x16x32_bf16 v[200:203], a[188:191], v[168:171], v[200:203]\n"
"v_mfma_f32_16x16x32_bf16 v[64:67], a[188:191], v[252:255], v[64:67]\n"
"v_mfma_f32_16x16x32_bf16 v[148:151], a[176:179], v[168:171], v[148:151]\n"
"v_mfma_f32_16x16x32_bf16 v[28:31], a[176:179], v[252:255], v[28:31]\n"
"v_mfma_f32_16x16x32_bf16 v[80:83], a[196:199], v[168:171], v[80:83]\n"
"buffer_load_dwordx4 a[168:171], v0, s[8:11], s90 offen offset:1024 sc1\n"
"v_mfma_f32_16x16x32_bf16 v[160:163], a[196:199], v[252:255], v[160:163]\n"
"v_mfma_f32_16x16x32_bf16 v[100:103], a[180:183], v[168:171], v[100:103]\n"
"ds_read_b128 v[216:219], v4 offset:34816\n"
"v_mfma_f32_16x16x32_bf16 v[144:147], a[180:183], v[252:255], v[144:147]\n"
"ds_read_b128 v[84:87], v4 offset:39936\n"
"v_fmac_f32 v64, v236, v220\n"
"v_mfma_f32_16x16x32_bf16 v[200:203], a[212:215], v[196:199], v[200:203]\n"
"s_waitcnt vmcnt(20)\n"
"buffer_load_dwordx4 a[184:187], v0, s[8:11], s90 offen offset:2048 sc1\n"
"v_mfma_f32_16x16x32_bf16 v[228:231], a[104:107], v[40:43], 0\n"
"s_waitcnt vmcnt(20) lgkmcnt(3)\n"
"v_fmac_f32 v65, v236, v221\n"
"v_fmac_f32 v66, v236, v222\n"
"v_mfma_f32_16x16x32_bf16 v[232:235], a[116:119], v[40:43], v[232:235]\n"
"v_fmac_f32 v67, v236, v223\n"
"v_fmac_f32 v64, v238, v32\n"
"v_mfma_f32_16x16x32_bf16 v[56:59], a[100:103], v[208:211], 0\n"
"v_fmac_f32 v65, v238, v33\n"
"v_fmac_f32 v66, v238, v34\n"
"v_mfma_f32_16x16x32_bf16 v[212:215], a[80:83], v[208:211], 0\n"
"v_fmac_f32 v67, v238, v35\n"
"v_mfma_f32_16x16x32_bf16 v[180:183], a[92:95], v[208:211], 0\n"
"buffer_load_dwordx4 a[160:163], v0, s[8:11], s90 offen offset:3072 sc1\n"
"v_mfma_f32_16x16x32_bf16 v[64:67], a[212:215], v[92:95], v[64:67]\n"
"s_add_u32 s90, s55, 0x5800\n"
"v_mfma_f32_16x16x32_bf16 v[148:151], a[204:207], v[196:199], v[148:151]\n"
"v_mfma_f32_16x16x32_bf16 v[28:31], a[204:207], v[92:95], v[28:31]\n"
"v_mfma_f32_16x16x32_bf16 v[80:83], a[192:195], v[196:199], v[80:83]\n"
"v_mfma_f32_16x16x32_bf16 v[160:163], a[192:195], v[92:95], v[160:163]\n"
"buffer_load_dwordx4 a[172:175], v0, s[8:11], s90 offen offset:0 sc1\n"
"v_mfma_f32_16x16x32_bf16 v[100:103], a[200:203], v[196:199], v[100:103]\n"
"ds_read_b128 v[248:251], v5 offset:10240\n"
"v_mfma_f32_16x16x32_bf16 v[144:147], a[200:203], v[92:95], v[144:147]\n"
"ds_read_b128 v[240:243], v20 offset:320\n"
"ds_read_b128 v[136:139], v20 offset:576\n"
"ds_read_b128 v[72:75], v5 offset:15360\n"
"v_mfma_f32_16x16x32_bf16 v[200:203], a[228:231], v[96:99], v[200:203]\n"
"s_waitcnt lgkmcnt(4)\n"
"v_mfma_f32_16x16x32_bf16 v[64:67], a[228:231], v[120:123], v[64:67]\n"
"buffer_load_dwordx4 a[188:191], v0, s[8:11], s90 offen offset:1024 sc1\n"
"v_mfma_f32_16x16x32_bf16 v[148:151], a[220:223], v[96:99], v[148:151]\n"
"v_mfma_f32_16x16x32_bf16 v[28:31], a[220:223], v[120:123], v[28:31]\n"
"v_mfma_f32_16x16x32_bf16 v[80:83], a[208:211], v[96:99], v[80:83]\n"
"v_mfma_f32_16x16x32_bf16 v[160:163], a[208:211], v[120:123], v[160:163]\n"
"v_mfma_f32_16x16x32_bf16 v[100:103], a[216:219], v[96:99], v[100:103]\n"
"buffer_load_dwordx4 a[176:179], v0, s[8:11], s90 offen offset:2048 sc1\n"
"ds_read_b128 v[36:39], v5 offset:11264\n"
"v_mfma_f32_16x16x32_bf16 v[144:147], a[216:219], v[120:123], v[144:147]\n"
"ds_read_b128 v[152:155], v5 offset:16384\n"
"v_mfma_f32_16x16x32_bf16 v[200:203], a[244:247], v[216:219], v[200:203]\n"
"s_waitcnt vmcnt(23) lgkmcnt(4)\n"
"v_fmac_f32 v148, v68, v240\n"
"v_fmac_f32 v149, v68, v241\n"
"v_mfma_f32_16x16x32_bf16 v[64:67], a[244:247], v[84:87], v[64:67]\n"
"v_fmac_f32 v150, v68, v242\n"
"v_fmac_f32 v151, v68, v243\n"
"v_mfma_f32_16x16x32_bf16 v[176:179], a[104:107], v[208:211], 0\n"
"buffer_load_dwordx4 a[196:199], v0, s[8:11], s90 offen offset:3072 sc1\n"
"v_mfma_f32_16x16x32_bf16 v[168:171], a[116:119], v[208:211], 0\n"
"s_add_u32 s90, s55, 0x6800\n"
"s_waitcnt lgkmcnt(3)\n"
"v_fmac_f32 v148, v70, v136\n"
"v_mfma_f32_16x16x32_bf16 v[60:63], a[96:99], v[48:51], v[60:63]\n"
"s_waitcnt vmcnt(23)\n"
"v_fmac_f32 v149, v70, v137\n"
"v_fmac_f32 v150, v70, v138\n"
"v_mfma_f32_16x16x32_bf16 v[52:55], a[108:111], v[48:51], v[52:55]\n"
"s_waitcnt vmcnt(22)\n"
"v_fmac_f32 v151, v70, v139\n"
"v_mfma_f32_16x16x32_bf16 v[132:135], a[120:123], v[48:51], v[132:135]\n"
"s_nop 0\n"
"v_mfma_f32_16x16x32_bf16 v[148:151], a[236:239], v[216:219], v[148:151]\n"
"buffer_load_dwordx4 a[180:183], v0, s[8:11], s90 offen offset:0 sc1\n"
"v_mfma_f32_16x16x32_bf16 v[28:31], a[236:239], v[84:87], v[28:31]\n"
"v_mfma_f32_16x16x32_bf16 v[80:83], a[224:227], v[216:219], v[80:83]\n"
"v_mfma_f32_16x16x32_bf16 v[160:163], a[224:227], v[84:87], v[160:163]\n"
"v_mfma_f32_16x16x32_bf16 v[100:103], a[232:235], v[216:219], v[100:103]\n"
"buffer_load_dwordx4 a[212:215], v0, s[8:11], s90 offen offset:1024 sc1\n"
"ds_read_b128 v[252:255], v5 offset:12288\n"
"v_mfma_f32_16x16x32_bf16 v[144:147], a[232:235], v[84:87], v[144:147]\n"
"ds_read_b128 v[40:43], v5 offset:17408\n"
"v_mfma_f32_16x16x32_bf16 v[200:203], v[44:47], v[248:251], v[200:203]\n"
"s_waitcnt lgkmcnt(2)\n"
"v_fmac_f32 v28, v236, v240\n"
"v_fmac_f32 v29, v236, v241\n"
"v_mfma_f32_16x16x32_bf16 v[64:67], v[44:47], v[72:75], v[64:67]\n"
"v_fmac_f32 v30, v236, v242\n"
"v_fmac_f32 v31, v236, v243\n"
"v_mfma_f32_16x16x32_bf16 v[148:151], v[112:115], v[248:251], v[148:151]\n"
"s_waitcnt vmcnt(23)\n"
"buffer_load_dwordx4 a[204:207], v0, s[8:11], s90 offen offset:2048 sc1\n"
"v_mfma_f32_16x16x32_bf16 v[228:231], a[132:135], v[48:51], v[228:231]\n"
"s_waitcnt vmcnt(23)\n"
"v_fmac_f32 v28, v238, v136\n"
"v_fmac_f32 v29, v238, v137\n"
"v_mfma_f32_16x16x32_bf16 v[232:235], a[112:115], v[48:51], v[232:235]\n"
"v_fmac_f32 v30, v238, v138\n"
"v_fmac_f32 v31, v238, v139\n"
"v_mfma_f32_16x16x32_bf16 v[56:59], a[96:99], v[116:119], v[56:59]\n"
"s_nop 0\n"
"v_mfma_f32_16x16x32_bf16 v[28:31], v[112:115], v[72:75], v[28:31]\n"
"v_mfma_f32_16x16x32_bf16 v[80:83], v[128:131], v[248:251], v[80:83]\n"
"buffer_load_dwordx4 a[192:195], v0, s[8:11], s90 offen offset:3072 sc1\n"
"v_mfma_f32_16x16x32_bf16 v[160:163], v[128:131], v[72:75], v[160:163]\n"
"s_add_u32 s90, s55, 0x7800\n"
"v_mfma_f32_16x16x32_bf16 v[100:103], v[244:247], v[248:251], v[100:103]\n"
"ds_read_b128 v[220:223], v5 offset:13312\n"
"v_mfma_f32_16x16x32_bf16 v[144:147], v[244:247], v[72:75], v[144:147]\n"
"ds_read_b128 v[32:35], v20 offset:384\n"
"ds_read_b128 v[196:199], v20 offset:640\n"
"ds_read_b128 v[92:95], v5 offset:18432\n"
"v_mfma_f32_16x16x32_bf16 v[200:203], v[140:143], v[36:39], v[200:203]\n"
"buffer_load_dwordx4 v[96:99], v16, s[8:11], s2 offen sc1\n"
"v_mfma_f32_16x16x32_bf16 v[64:67], v[140:143], v[152:155], v[64:67]\n"
"v_mfma_f32_16x16x32_bf16 v[148:151], v[188:191], v[36:39], v[148:151]\n"
"v_mfma_f32_16x16x32_bf16 v[28:31], v[188:191], v[152:155], v[28:31]\n"
"buffer_load_dwordx4 v[120:123], v17, s[8:11], s2 offen sc1\n"
"v_mfma_f32_16x16x32_bf16 v[80:83], v[204:207], v[36:39], v[80:83]\n"
"s_bitcmp1_b32 s61, 1\n"
"s_cselect_b32 s2, s60, s59\n"
"s_add_u32 s2, s2, 0x6a400\n"
"v_mfma_f32_16x16x32_bf16 v[160:163], v[204:207], v[152:155], v[160:163]\n"
"v_mfma_f32_16x16x32_bf16 v[100:103], v[76:79], v[36:39], v[100:103]\n"
"ds_read_b128 v[208:211], v5 offset:14336\n"
"v_mfma_f32_16x16x32_bf16 v[144:147], v[76:79], v[152:155], v[144:147]\n"
"s_waitcnt lgkmcnt(5)\n"
"ds_read_b128 v[216:219], v5 offset:19456\n"
"v_mfma_f32_16x16x32_bf16 v[200:203], a[248:251], v[252:255], v[200:203]\n"
"buffer_load_dwordx4 v[84:87], v16, s[8:11], s2 offen sc1\n"
"v_mfma_f32_16x16x32_bf16 v[64:67], a[248:251], v[40:43], v[64:67]\n"
"s_waitcnt lgkmcnt(4)\n"
"v_fmac_f32 v80, v68, v32\n"
"v_fmac_f32 v81, v68, v33\n"
"v_mfma_f32_16x16x32_bf16 v[148:151], a[12:15], v[252:255], v[148:151]\n"
"v_fmac_f32 v82, v68, v34\n"
"v_fmac_f32 v83, v68, v35\n"
"v_mfma_f32_16x16x32_bf16 v[28:31], a[12:15], v[40:43], v[28:31]\n"
"s_waitcnt lgkmcnt(3)\n"
"v_fmac_f32 v80, v70, v196\n"
"v_fmac_f32 v81, v70, v197\n"
"v_mfma_f32_16x16x32_bf16 v[212:215], a[108:111], v[116:119], v[212:215]\n"
"buffer_load_dwordx4 v[44:47], v17, s[8:11], s2 offen sc1\n"
"v_mfma_f32_16x16x32_bf16 v[180:183], a[120:123], v[116:119], v[180:183]\n"
"s_bitcmp1_b32 s61, 2\n"
"s_cselect_b32 s2, s60, s59\n"
"s_add_u32 s2, s2, 0x6a800\n"
"v_mfma_f32_16x16x32_bf16 v[176:179], a[132:135], v[116:119], v[176:179]\n"
"v_fmac_f32 v82, v70, v198\n"
"v_fmac_f32 v83, v70, v199\n"
"v_mfma_f32_16x16x32_bf16 v[168:171], a[112:115], v[116:119], v[168:171]\n"
"s_nop 0\n"
"v_mfma_f32_16x16x32_bf16 v[80:83], a[240:243], v[252:255], v[80:83]\n"
"v_mfma_f32_16x16x32_bf16 v[160:163], a[240:243], v[40:43], v[160:163]\n"
"buffer_load_dwordx4 v[240:243], v16, s[8:11], s2 offen sc1\n"
"v_mfma_f32_16x16x32_bf16 v[100:103], a[252:255], v[252:255], v[100:103]\n"
"ds_read_b128 v[48:51], v5 offset:30720\n"
"v_mfma_f32_16x16x32_bf16 v[144:147], a[252:255], v[40:43], v[144:147]\n"
"ds_read_b128 v[136:139], v5 offset:35840\n"
"v_mfma_f32_16x16x32_bf16 v[200:203], a[8:11], v[220:223], v[200:203]\n"
"s_waitcnt lgkmcnt(3)\n"
"v_mfma_f32_16x16x32_bf16 v[64:67], a[8:11], v[92:95], v[64:67]\n"
"buffer_load_dwordx4 v[112:115], v17, s[8:11], s2 offen sc1\n"
"v_mfma_f32_16x16x32_bf16 v[148:151], a[28:31], v[220:223], v[148:151]\n"
"s_bitcmp1_b32 s61, 3\n"
"s_cselect_b32 s2, s60, s59\n"
"s_add_u32 s2, s2, 0x6ac00\n"
"v_mfma_f32_16x16x32_bf16 v[28:31], a[28:31], v[92:95], v[28:31]\n"
"v_fmac_f32 v160, v236, v32\n"
"v_fmac_f32 v161, v236, v33\n"
"v_mfma_f32_16x16x32_bf16 v[80:83], a[4:7], v[220:223], v[80:83]\n"
"v_fmac_f32 v162, v236, v34\n"
"v_fmac_f32 v163, v236, v35\n"
"v_fmac_f32 v160, v238, v196\n"
"v_fmac_f32 v161, v238, v197\n"
"v_fmac_f32 v162, v238, v198\n"
"v_fmac_f32 v163, v238, v199\n"
"buffer_load_dwordx4 v[128:131], v16, s[8:11], s2 offen sc1\n"
"s_nop 0\n"
"v_mfma_f32_16x16x32_bf16 v[160:163], a[4:7], v[92:95], v[160:163]\n"
"v_mfma_f32_16x16x32_bf16 v[100:103], a[0:3], v[220:223], v[100:103]\n"
"ds_read_b128 v[248:251], v5 offset:31744\n"
"v_mfma_f32_16x16x32_bf16 v[144:147], a[0:3], v[92:95], v[144:147]\n"
"ds_read_b128 v[244:247], v20 offset:448\n"
"ds_read_b128 v[72:75], v20 offset:704\n"
"ds_read_b128 v[140:143], v5 offset:36864\n"
"v_mfma_f32_16x16x32_bf16 v[200:203], a[24:27], v[208:211], v[200:203]\n"
"s_waitcnt vmcnt(30) lgkmcnt(6)\n"
"buffer_load_dwordx4 v[188:191], v17, s[8:11], s2 offen sc1\n"
"v_mfma_f32_16x16x32_bf16 v[64:67], a[24:27], v[216:219], v[64:67]\n"
"s_bitcmp1_b32 s61, 4\n"
"s_cselect_b32 s2, s60, s59\n"
"s_add_u32 s2, s2, 0x6b000\n"
"v_mfma_f32_16x16x32_bf16 v[148:151], a[44:47], v[208:211], v[148:151]\n"
"v_mfma_f32_16x16x32_bf16 v[28:31], a[44:47], v[216:219], v[28:31]\n"
"v_mfma_f32_16x16x32_bf16 v[80:83], a[20:23], v[208:211], v[80:83]\n"
"v_mfma_f32_16x16x32_bf16 v[160:163], a[20:23], v[216:219], v[160:163]\n"
"buffer_load_dwordx4 v[204:207], v16, s[8:11], s2 offen sc1\n"
"v_mfma_f32_16x16x32_bf16 v[100:103], a[16:19], v[208:211], v[100:103]\n"
"ds_read_b128 v[36:39], v5 offset:32768\n"
"v_mfma_f32_16x16x32_bf16 v[144:147], a[16:19], v[216:219], v[144:147]\n"
"s_waitcnt lgkmcnt(5)\n"
"ds_read_b128 v[76:79], v5 offset:37888\n"
"v_mfma_f32_16x16x32_bf16 v[200:203], a[40:43], v[48:51], v[200:203]\n"
"v_mfma_f32_16x16x32_bf16 v[64:67], a[40:43], v[136:139], v[64:67]\n"
"buffer_load_dwordx4 v[152:155], v17, s[8:11], s2 offen sc1\n"
"v_mfma_f32_16x16x32_bf16 v[148:151], a[60:63], v[48:51], v[148:151]\n"
"s_bitcmp1_b32 s61, 5\n"
"s_cselect_b32 s2, s60, s59\n"
"s_add_u32 s2, s2, 0x6b400\n"
"v_mfma_f32_16x16x32_bf16 v[28:31], a[60:63], v[136:139], v[28:31]\n"
"s_waitcnt vmcnt(30) lgkmcnt(4)\n"
"v_fmac_f32 v100, v68, v244\n"
"v_fmac_f32 v101, v68, v245\n"
"v_mfma_f32_16x16x32_bf16 v[80:83], a[36:39], v[48:51], v[80:83]\n"
"v_fmac_f32 v102, v68, v246\n"
"v_fmac_f32 v103, v68, v247\n"
"v_mfma_f32_16x16x32_bf16 v[160:163], a[36:39], v[136:139], v[160:163]\n"
"s_waitcnt lgkmcnt(3)\n"
"v_fmac_f32 v100, v70, v72\n"
"v_fmac_f32 v101, v70, v73\n"
"v_fmac_f32 v102, v70, v74\n"
"v_fmac_f32 v103, v70, v75\n"
"buffer_load_dwordx4 v[116:119], v16, s[8:11], s2 offen sc1\n"
"s_nop 0\n"
"v_mfma_f32_16x16x32_bf16 v[100:103], a[32:35], v[48:51], v[100:103]\n"
"ds_read_b128 v[252:255], v5 offset:33792\n"
"v_mfma_f32_16x16x32_bf16 v[144:147], a[32:35], v[136:139], v[144:147]\n"
"ds_read_b128 v[40:43], v5 offset:38912\n"
"v_mfma_f32_16x16x32_bf16 v[200:203], a[56:59], v[248:251], v[200:203]\n"
"s_waitcnt lgkmcnt(4)\n"
"buffer_load_dwordx4 v[32:35], v17, s[8:11], s2 offen sc1\n"
"v_mfma_f32_16x16x32_bf16 v[64:67], a[56:59], v[140:143], v[64:67]\n"
"s_bitcmp1_b32 s61, 6\n"
"s_cselect_b32 s2, s60, s59\n"
"s_add_u32 s2, s2, 0x6b800\n"
"v_mfma_f32_16x16x32_bf16 v[148:151], a[68:71], v[248:251], v[148:151]\n"
"v_fmac_f32 v144, v236, v244\n"
"v_fmac_f32 v145, v236, v245\n"
"v_mfma_f32_16x16x32_bf16 v[28:31], a[68:71], v[140:143], v[28:31]\n"
"v_fmac_f32 v146, v236, v246\n"
"v_fmac_f32 v147, v236, v247\n"
"v_mfma_f32_16x16x32_bf16 v[80:83], a[52:55], v[248:251], v[80:83]\n"
"v_fmac_f32 v144, v238, v72\n"
"v_fmac_f32 v145, v238, v73\n"
"v_mfma_f32_16x16x32_bf16 v[160:163], a[52:55], v[140:143], v[160:163]\n"
"v_fmac_f32 v146, v238, v74\n"
"v_fmac_f32 v147, v238, v75\n"
"v_mfma_f32_16x16x32_bf16 v[100:103], a[84:87], v[248:251], v[100:103]\n"
"ds_read_b128 v[196:199], v5 offset:34816\n"
"v_mfma_f32_16x16x32_bf16 v[144:147], a[84:87], v[140:143], v[144:147]\n"
"s_waitcnt vmcnt(29) lgkmcnt(3)\n"
"ds_read_b128 v[220:223], v5 offset:39936\n"
"v_mfma_f32_16x16x32_bf16 v[200:203], a[64:67], v[36:39], v[200:203]\n"
"s_bitcmp1_b32 s61, 0\n"
"s_cselect_b32 s4, s60, s59\n"
"s_add_u32 s4, s4, 0x85000\n"
"v_mfma_f32_16x16x32_bf16 v[64:67], a[64:67], v[76:79], v[64:67]\n"
"buffer_load_dwordx4 a[200:203], v0, s[8:11], s90 offen offset:0 sc1\n"
"v_mfma_f32_16x16x32_bf16 v[148:151], a[76:79], v[36:39], v[148:151]\n"
"s_waitcnt vmcnt(12)\n"
"v_min3_u32 v92, v96, v97, v98\n"
"v_mfma_f32_16x16x32_bf16 v[28:31], a[76:79], v[76:79], v[28:31]\n"
"s_waitcnt vmcnt(11)\n"
"v_min3_u32 v92, v92, v99, v120\n"
"v_min3_u32 v92, v92, v121, v122\n"
"v_mfma_f32_16x16x32_bf16 v[80:83], a[48:51], v[36:39], v[80:83]\n"
"buffer_load_dwordx4 a[228:231], v0, s[8:11], s90 offen offset:1024 sc1\n"
"v_mfma_f32_16x16x32_bf16 v[160:163], a[48:51], v[76:79], v[160:163]\n"
"v_min_u32 v92, v92, v123\n"
"v_cmp_lt_i32 vcc, -1, v92\n"
"v_mfma_f32_16x16x32_bf16 v[100:103], a[72:75], v[36:39], v[100:103]\n"
"v_mfma_f32_16x16x32_bf16 v[144:147], a[72:75], v[76:79], v[144:147]\n"
"s_waitcnt lgkmcnt(2)\n"
"v_mfma_f32_16x16x32_bf16 v[200:203], v[192:195], v[252:255], v[200:203]\n"
"s_cbranch_vccnz Ls70_%=\n"
"Lb70_%=:\n"
"v_mfma_f32_16x16x32_bf16 v[64:67], v[192:195], v[40:43], v[64:67]\n"
"buffer_load_dwordx4 a[220:223], v0, s[8:11], s90 offen offset:2048 sc1\n"
"v_mfma_f32_16x16x32_bf16 v[148:151], v[172:175], v[252:255], v[148:151]\n"
"s_waitcnt vmcnt(12)\n"
"v_min3_u32 v208, v84, v85, v86\n"
"v_mfma_f32_16x16x32_bf16 v[28:31], v[172:175], v[40:43], v[28:31]\n"
"s_waitcnt vmcnt(11)\n"
"v_min3_u32 v208, v208, v87, v44\n"
"v_min3_u32 v208, v208, v45, v46\n"
"v_mfma_f32_16x16x32_bf16 v[80:83], v[224:227], v[252:255], v[80:83]\n"
"v_min_u32 v208, v208, v47\n"
"v_cmp_lt_i32 vcc, -1, v208\n"
"v_mfma_f32_16x16x32_bf16 v[160:163], v[224:227], v[40:43], v[160:163]\n"
"v_mfma_f32_16x16x32_bf16 v[100:103], v[124:127], v[252:255], v[100:103]\n"
"s_cbranch_vccnz Ls71_%=\n"
"Lb71_%=:\n"
"v_mfma_f32_16x16x32_bf16 v[144:147], v[124:127], v[40:43], v[144:147]\n"
"s_waitcnt lgkmcnt(0)\n"
"buffer_load_dwordx4 a[208:211], v0, s[8:11], s90 offen offset:3072 sc1\n"
"v_mfma_f32_16x16x32_bf16 v[200:203], v[88:91], v[196:199], v[200:203]\n"
"s_add_u32 s90, s55, 0x8800\n"
"s_waitcnt vmcnt(11)\n"
"v_min3_u32 v216, v240, v241, v242\n"
"v_mfma_f32_16x16x32_bf16 v[64:67], v[88:91], v[220:223], v[64:67]\n"
"v_mfma_f32_16x16x32_bf16 v[148:151], v[164:167], v[196:199], v[148:151]\n"
"buffer_load_dwordx4 a[216:219], v0, s[8:11], s90 offen offset:0 sc1\n"
"v_mfma_f32_16x16x32_bf16 v[28:31], v[164:167], v[220:223], v[28:31]\n"
"s_waitcnt vmcnt(11)\n"
"v_min3_u32 v216, v216, v243, v112\n"
"v_min3_u32 v216, v216, v113, v114\n"
"v_mfma_f32_16x16x32_bf16 v[80:83], v[184:187], v[196:199], v[80:83]\n"
"v_min_u32 v216, v216, v115\n"
"v_cmp_lt_i32 vcc, -1, v216\n"
"v_mfma_f32_16x16x32_bf16 v[160:163], v[184:187], v[220:223], v[160:163]\n"
"v_mul_f32 v48, v200, v71\n"
"v_mul_f32 v49, v201, v71\n"
"v_mfma_f32_16x16x32_bf16 v[100:103], a[88:91], v[196:199], v[100:103]\n"
"s_cbranch_vccnz Ls72_%=\n"
"Lb72_%=:\n"
"v_mfma_f32_16x16x32_bf16 v[144:147], a[88:91], v[220:223], v[144:147]\n"
"buffer_load_dwordx4 a[244:247], v0, s[8:11], s90 offen offset:1024 sc1\n"
"v_mul_f32 v50, v202, v71\n"
"v_mul_f32 v51, v203, v71\n"
"v_mul_f32 v48, -v48, |v200|\n"
"v_mul_f32 v49, -v49, |v201|\n"
"v_mfma_f32_16x16x32_bf16 v[60:63], a[124:127], v[96:99], v[60:63]\n"
"v_add_u32 v136, s20, v0\n"
"v_mul_f32 v50, -v50, |v202|\n"
"v_mul_f32 v51, -v51, |v203|\n"
"v_cvt_pk_bf16_f32 v200, v48, v49\n"
"v_cvt_pk_bf16_f32 v201, v50, v51\n"
"v_mul_f32 v48, v148, v71\n"
"v_mfma_f32_16x16x32_bf16 v[52:55], a[136:139], v[96:99], v[52:55]\n"
"ds_read_b128 a[236:239], v136 offset:0\n"
"ds_read_b128 a[224:227], v136 offset:1024\n"
"v_mul_f32 v49, v149, v71\n"
"v_mul_f32 v50, v150, v71\n"
"v_mul_f32 v51, v151, v71\n"
"v_mul_f32 v48, -v48, |v148|\n"
"v_mfma_f32_16x16x32_bf16 v[132:135], a[152:155], v[96:99], v[132:135]\n"
"ds_read_b128 a[232:235], v136 offset:2048\n"
"ds_read_b128 a[248:251], v136 offset:3072\n"
"ds_read_b128 a[12:15], v136 offset:4096\n"
"ds_read_b128 a[240:243], v136 offset:5120\n"
"v_mul_f32 v49, -v49, |v149|\n"
"v_mul_f32 v50, -v50, |v150|\n"
"v_mfma_f32_16x16x32_bf16 v[228:231], a[128:131], v[96:99], v[228:231]\n"
"v_add_u32 v137, s21, v0\n"
"v_mul_f32 v51, -v51, |v151|\n"
"v_cvt_pk_bf16_f32 v202, v48, v49\n"
"v_cvt_pk_bf16_f32 v203, v50, v51\n"
"v_pk_max_u16 v200, v200, s37\n"
"v_pk_max_u16 v201, v201, s37\n"
"v_mfma_f32_16x16x32_bf16 v[232:235], a[140:143], v[96:99], v[232:235]\n"
"ds_read_b128 a[252:255], v137 offset:0\n"
"ds_read_b128 a[8:11], v137 offset:1024\n"
"buffer_load_dwordx4 v[244:247], v16, s[8:11], s2 offen sc1\n"
"v_pk_max_u16 v202, v202, s37\n"
"v_pk_max_u16 v203, v203, s37\n"
"v_mfma_f32_16x16x32_bf16 v[56:59], a[124:127], v[120:123], v[56:59]\n"
"s_add_u32 s1, s69, 0x85000\n"
"ds_read_b128 a[28:31], v137 offset:2048\n"
"ds_read_b128 a[4:7], v137 offset:3072\n"
"ds_read_b128 a[0:3], v137 offset:4096\n"
"ds_read_b128 a[24:27], v137 offset:5120\n"
"ds_read_b128 a[44:47], v137 offset:6144\n"
"ds_read_b128 a[20:23], v137 offset:7168\n"
"v_mfma_f32_16x16x32_bf16 v[212:215], a[136:139], v[120:123], v[212:215]\n"
"s_add_u32 s90, s55, 0xc800\n"
"buffer_load_dwordx4 a[16:19], v0, s[8:11], s90 offen offset:0 sc1\n"
"buffer_store_dwordx4 v[200:203], v0, s[8:11], s1 offen offset:0 sc1\n"
"v_mfma_f32_16x16x32_bf16 v[180:183], a[152:155], v[120:123], v[180:183]\n"
"s_waitcnt vmcnt(14)\n"
"v_min3_u32 v72, v128, v129, v130\n"
"v_mul_f32 v248, v64, v239\n"
"v_mul_f32 v249, v65, v239\n"
"v_mul_f32 v250, v66, v239\n"
"v_mul_f32 v251, v67, v239\n"
"v_mul_f32 v248, -v248, |v64|\n"
"v_mfma_f32_16x16x32_bf16 v[176:179], a[128:131], v[120:123], v[176:179]\n"
"s_waitcnt vmcnt(13) lgkmcnt(13)\n"
"v_min3_u32 v72, v72, v131, v188\n"
"v_min3_u32 v72, v72, v189, v190\n"
"v_mul_f32 v249, -v249, |v65|\n"
"v_mul_f32 v250, -v250, |v66|\n"
"v_mul_f32 v251, -v251, |v67|\n"
"v_cvt_pk_bf16_f32 v64, v248, v249\n"
"v_mfma_f32_16x16x32_bf16 v[168:171], a[140:143], v[120:123], v[168:171]\n"
"buffer_load_dwordx4 a[40:43], v0, s[8:11], s90 offen offset:1024 sc1\n"
"v_cvt_pk_bf16_f32 v65, v250, v251\n"
"v_mul_f32 v248, v28, v239\n"
"v_mul_f32 v249, v29, v239\n"
"v_mul_f32 v250, v30, v239\n"
"v_mfma_f32_16x16x32_bf16 v[60:63], a[164:167], v[84:87], v[60:63]\n"
"buffer_load_dwordx4 v[140:143], v17, s[8:11], s2 offen sc1\n"
"v_mul_f32 v251, v31, v239\n"
"v_mul_f32 v248, -v248, |v28|\n"
"v_mul_f32 v249, -v249, |v29|\n"
"v_mul_f32 v250, -v250, |v30|\n"
"v_mfma_f32_16x16x32_bf16 v[52:55], a[156:159], v[84:87], v[52:55]\n"
"s_bitcmp1_b32 s61, 7\n"
"s_cselect_b32 s2, s60, s59\n"
"s_add_u32 s2, s2, 0x6bc00\n"
"v_mul_f32 v251, -v251, |v31|\n"
"v_cvt_pk_bf16_f32 v66, v248, v249\n"
"v_cvt_pk_bf16_f32 v67, v250, v251\n"
"v_pk_max_u16 v64, v64, s37\n"
"v_mfma_f32_16x16x32_bf16 v[132:135], a[148:151], v[84:87], v[132:135]\n"
"v_min_u32 v72, v72, v191\n"
"v_cmp_lt_i32 vcc, -1, v72\n"
"v_pk_max_u16 v65, v65, s37\n"
"v_pk_max_u16 v66, v66, s37\n"
"v_pk_max_u16 v67, v67, s37\n"
"s_add_u32 s1, s69, 0x92800\n"
"v_mfma_f32_16x16x32_bf16 v[228:231], a[144:147], v[84:87], v[228:231]\n"
"s_cbranch_vccnz Ls73_%=\n"
"Lb73_%=:\n"
"buffer_store_dwordx4 v[64:67], v0, s[8:11], s1 offen offset:0 sc1\n"
"v_mfma_f32_16x16x32_bf16 v[232:235], a[168:171], v[84:87], v[232:235]\n"
"s_waitcnt vmcnt(15) lgkmcnt(1)\n"
"v_min3_u32 v36, v204, v205, v206\n"
"v_mul_f32 v76, v80, v71\n"
"v_mul_f32 v77, v81, v71\n"
"v_mul_f32 v78, v82, v71\n"
"v_mul_f32 v79, v83, v71\n"
"v_mul_f32 v76, -v76, |v80|\n"
"v_mfma_f32_16x16x32_bf16 v[56:59], a[164:167], v[44:47], v[56:59]\n"
"buffer_load_dwordx4 v[92:95], v16, s[8:11], s2 offen sc1\n"
"v_mul_f32 v77, -v77, |v81|\n"
"v_mul_f32 v78, -v78, |v82|\n"
"v_mul_f32 v79, -v79, |v83|\n"
"v_cvt_pk_bf16_f32 v80, v76, v77\n"
"v_mfma_f32_16x16x32_bf16 v[212:215], a[156:159], v[44:47], v[212:215]\n"
"s_waitcnt vmcnt(15) lgkmcnt(0)\n"
"v_min3_u32 v36, v36, v207, v152\n"
"v_min3_u32 v36, v36, v153, v154\n"
"v_cvt_pk_bf16_f32 v81, v78, v79\n"
"v_mul_f32 v76, v100, v71\n"
"v_mul_f32 v77, v101, v71\n"
"v_mul_f32 v78, v102, v71\n"
"v_mfma_f32_16x16x32_bf16 v[180:183], a[148:151], v[44:47], v[180:183]\n"
"v_min_u32 v36, v36, v155\n"
"v_cmp_lt_i32 vcc, -1, v36\n"
"v_mul_f32 v79, v103, v71\n"
"v_mul_f32 v76, -v76, |v100|\n"
"v_mul_f32 v77, -v77, |v101|\n"
"v_mul_f32 v78, -v78, |v102|\n"
"v_mfma_f32_16x16x32_bf16 v[176:179], a[144:147], v[44:47], v[176:179]\n"
"s_cbranch_vccnz Ls74_%=\n"
"Lb74_%=:\n"
"v_mul_f32 v79, -v79, |v103|\n"
"v_cvt_pk_bf16_f32 v82, v76, v77\n"
"v_cvt_pk_bf16_f32 v83, v78, v79\n"
"v_pk_max_u16 v80, v80, s37\n"
"v_pk_max_u16 v81, v81, s37\n"
"v_mfma_f32_16x16x32_bf16 v[168:171], a[168:171], v[44:47], v[168:171]\n"
"buffer_load_dwordx4 a[60:63], v0, s[8:11], s90 offen offset:2048 sc1\n"
"v_pk_max_u16 v82, v82, s37\n"
"v_pk_max_u16 v83, v83, s37\n"
"s_add_u32 s1, s70, 0x85000\n"
"v_mfma_f32_16x16x32_bf16 v[60:63], a[184:187], v[240:243], v[60:63]\n"
"buffer_load_dwordx4 a[36:39], v0, s[8:11], s90 offen offset:3072 sc1\n"
"v_mfma_f32_16x16x32_bf16 v[52:55], a[160:163], v[240:243], v[52:55]\n"
"s_add_u32 s90, s55, 0xd800\n"
"s_waitcnt vmcnt(16)\n"
"v_min3_u32 v192, v116, v117, v118\n"
"v_mfma_f32_16x16x32_bf16 v[132:135], a[172:175], v[240:243], v[132:135]\n"
"s_waitcnt vmcnt(15)\n"
"v_min3_u32 v192, v192, v119, v32\n"
"v_min3_u32 v192, v192, v33, v34\n"
"v_mfma_f32_16x16x32_bf16 v[228:231], a[188:191], v[240:243], v[228:231]\n"
"buffer_load_dwordx4 v[172:175], v17, s[8:11], s2 offen sc1\n"
"v_mfma_f32_16x16x32_bf16 v[232:235], a[176:179], v[240:243], v[232:235]\n"
"s_bitcmp1_b32 s61, 8\n"
"s_cselect_b32 s2, s60, s59\n"
"s_add_u32 s2, s2, 0x6c000\n"
"v_mfma_f32_16x16x32_bf16 v[56:59], a[184:187], v[112:115], v[56:59]\n"
"v_min_u32 v192, v192, v35\n"
"v_cmp_lt_i32 vcc, -1, v192\n"
"v_mfma_f32_16x16x32_bf16 v[212:215], a[160:163], v[112:115], v[212:215]\n"
"v_mfma_f32_16x16x32_bf16 v[180:183], a[172:175], v[112:115], v[180:183]\n"
"s_cbranch_vccnz Ls75_%=\n"
"Lb75_%=:\n"
"buffer_store_dwordx4 v[80:83], v0, s[8:11], s1 offen offset:0 sc1\n"
"v_mfma_f32_16x16x32_bf16 v[176:179], a[188:191], v[112:115], v[176:179]\n"
"v_mul_f32 v224, v160, v239\n"
"v_mul_f32 v225, v161, v239\n"
"v_mul_f32 v226, v162, v239\n"
"v_mul_f32 v227, v163, v239\n"
"v_mul_f32 v224, -v224, |v160|\n"
"v_mul_f32 v225, -v225, |v161|\n"
"v_mfma_f32_16x16x32_bf16 v[168:171], a[176:179], v[112:115], v[168:171]\n"
"v_mul_f32 v226, -v226, |v162|\n"
"v_mul_f32 v227, -v227, |v163|\n"
"v_cvt_pk_bf16_f32 v160, v224, v225\n"
"v_cvt_pk_bf16_f32 v161, v226, v227\n"
"v_mul_f32 v224, v144, v239\n"
"v_mul_f32 v225, v145, v239\n"
"v_mfma_f32_16x16x32_bf16 v[60:63], a[196:199], v[128:131], v[60:63]\n"
"buffer_load_dwordx4 v[252:255], v16, s[8:11], s2 offen sc1\n"
"v_mul_f32 v226, v146, v239\n"
"v_mul_f32 v227, v147, v239\n"
"v_mul_f32 v224, -v224, |v144|\n"
"v_mul_f32 v225, -v225, |v145|\n"
"v_mfma_f32_16x16x32_bf16 v[52:55], a[180:183], v[128:131], v[52:55]\n"
"s_waitcnt vmcnt(11)\n"
"v_min3_u32 v208, v244, v245, v246\n"
"v_mul_f32 v226, -v226, |v146|\n"
"v_mul_f32 v227, -v227, |v147|\n"
"v_cvt_pk_bf16_f32 v162, v224, v225\n"
"v_cvt_pk_bf16_f32 v163, v226, v227\n"
"v_pk_max_u16 v160, v160, s37\n"
"v_mfma_f32_16x16x32_bf16 v[132:135], a[212:215], v[128:131], v[132:135]\n"
"buffer_load_dwordx4 v[124:127], v17, s[8:11], s2 offen sc1\n"
"v_pk_max_u16 v161, v161, s37\n"
"v_pk_max_u16 v162, v162, s37\n"
"v_pk_max_u16 v163, v163, s37\n"
"s_add_u32 s1, s70, 0x92800\n"
"v_mfma_f32_16x16x32_bf16 v[228:231], a[204:207], v[128:131], v[228:231]\n"
"s_bitcmp1_b32 s61, 9\n"
"s_cselect_b32 s2, s60, s59\n"
"s_add_u32 s2, s2, 0x6c400\n"
"v_mfma_f32_16x16x32_bf16 v[232:235], a[192:195], v[128:131], v[232:235]\n"
"v_mfma_f32_16x16x32_bf16 v[56:59], a[196:199], v[188:191], v[56:59]\n"
"buffer_load_dwordx4 v[40:43], v16, s[8:11], s2 offen sc1\n"
"v_mfma_f32_16x16x32_bf16 v[212:215], a[180:183], v[188:191], v[212:215]\n"
"v_mfma_f32_16x16x32_bf16 v[180:183], a[212:215], v[188:191], v[180:183]\n"
"v_mfma_f32_16x16x32_bf16 v[176:179], a[204:207], v[188:191], v[176:179]\n"
"v_mfma_f32_16x16x32_bf16 v[168:171], a[192:195], v[188:191], v[168:171]\n"
"v_mfma_f32_16x16x32_bf16 v[60:63], a[200:203], v[204:207], v[60:63]\n"
"buffer_load_dwordx4 v[88:91], v17, s[8:11], s2 offen sc1\n"
"v_mfma_f32_16x16x32_bf16 v[52:55], a[228:231], v[204:207], v[52:55]\n"
"s_bitcmp1_b32 s61, 10\n"
"s_cselect_b32 s2, s60, s59\n"
"s_add_u32 s2, s2, 0x6c800\n"
"v_mfma_f32_16x16x32_bf16 v[132:135], a[220:223], v[204:207], v[132:135]\n"
"v_mfma_f32_16x16x32_bf16 v[228:231], a[208:211], v[204:207], v[228:231]\n"
"v_mfma_f32_16x16x32_bf16 v[232:235], a[216:219], v[204:207], v[232:235]\n"
"v_mfma_f32_16x16x32_bf16 v[56:59], a[200:203], v[152:155], v[56:59]\n"
"buffer_load_dwordx4 v[164:167], v16, s[8:11], s2 offen sc1\n"
"v_mfma_f32_16x16x32_bf16 v[212:215], a[228:231], v[152:155], v[212:215]\n"
"v_mfma_f32_16x16x32_bf16 v[180:183], a[220:223], v[152:155], v[180:183]\n"
"s_waitcnt vmcnt(11)\n"
"v_min3_u32 v208, v208, v247, v140\n"
"v_min3_u32 v208, v208, v141, v142\n"
"v_mfma_f32_16x16x32_bf16 v[176:179], a[208:211], v[152:155], v[176:179]\n"
"v_min_u32 v208, v208, v143\n"
"v_cmp_lt_i32 vcc, -1, v208\n"
"v_mfma_f32_16x16x32_bf16 v[168:171], a[216:219], v[152:155], v[168:171]\n"
"buffer_load_dwordx4 v[184:187], v17, s[8:11], s2 offen sc1\n"
"v_mfma_f32_16x16x32_bf16 v[60:63], a[244:247], v[116:119], v[60:63]\n"
"s_cbranch_vccnz Ls76_%=\n"
"Lb76_%=:\n"
"v_mfma_f32_16x16x32_bf16 v[52:55], a[236:239], v[116:119], v[52:55]\n"
"s_bitcmp1_b32 s61, 11\n"
"s_cselect_b32 s2, s60, s59\n"
"s_add_u32 s2, s2, 0x6cc00\n"
"v_mfma_f32_16x16x32_bf16 v[132:135], a[224:227], v[116:119], v[132:135]\n"
"v_mfma_f32_16x16x32_bf16 v[228:231], a[232:235], v[116:119], v[228:231]\n"
"v_mfma_f32_16x16x32_bf16 v[232:235], a[248:251], v[116:119], v[232:235]\n"
"buffer_load_dwordx4 v[196:199], v16, s[8:11], s2 offen sc1\n"
"v_mfma_f32_16x16x32_bf16 v[56:59], a[244:247], v[32:35], v[56:59]\n"
"v_mfma_f32_16x16x32_bf16 v[212:215], a[236:239], v[32:35], v[212:215]\n"
"v_mfma_f32_16x16x32_bf16 v[180:183], a[224:227], v[32:35], v[180:183]\n"
"s_waitcnt vmcnt(11)\n"
"v_min3_u32 v216, v92, v93, v94\n"
"v_mfma_f32_16x16x32_bf16 v[176:179], a[232:235], v[32:35], v[176:179]\n"
"v_mfma_f32_16x16x32_bf16 v[168:171], a[248:251], v[32:35], v[168:171]\n"
"buffer_load_dwordx4 v[220:223], v17, s[8:11], s2 offen sc1\n"
"v_mfma_f32_16x16x32_bf16 v[60:63], a[12:15], v[244:247], v[60:63]\n"
"v_mfma_f32_16x16x32_bf16 v[52:55], a[240:243], v[244:247], v[52:55]\n"
"v_mfma_f32_16x16x32_bf16 v[132:135], a[252:255], v[244:247], v[132:135]\n"
"v_mfma_f32_16x16x32_bf16 v[228:231], a[8:11], v[244:247], v[228:231]\n"
"buffer_store_dwordx4 v[160:163], v0, s[8:11], s1 offen offset:0 sc1\n"
"s_cmp_eq_u32 s62, 0\n"
"s_cselect_b64 exec, -1, 0\n"
"v_swap_b32 v200, v64\n"
"v_swap_b32 v201, v65\n"
"v_swap_b32 v202, v66\n"
"v_swap_b32 v203, v67\n"
"s_mov_b64 exec, -1\n"
"v_mfma_f32_16x16x32_bf16 v[232:235], a[28:31], v[244:247], v[232:235]\n"
"s_cmp_eq_u32 s62, 0\n"
"s_cselect_b64 exec, -1, 0\n"
"v_swap_b32 v80, v160\n"
"v_swap_b32 v81, v161\n"
"v_swap_b32 v82, v162\n"
"v_swap_b32 v83, v163\n"
"s_mov_b64 exec, -1\n"
"v_mfma_f32_16x16x32_bf16 v[56:59], a[12:15], v[140:143], v[56:59]\n"
"buffer_load_dwordx4 a[32:35], v0, s[8:11], s90 offen offset:0 sc1\n"
"v_mfma_f32_16x16x32_bf16 v[212:215], a[240:243], v[140:143], v[212:215]\n"
"s_waitcnt vmcnt(11)\n"
"v_min3_u32 v216, v216, v95, v172\n"
"v_min3_u32 v216, v216, v173, v174\n"
"v_mfma_f32_16x16x32_bf16 v[180:183], a[252:255], v[140:143], v[180:183]\n"
"v_min_u32 v216, v216, v175\n"
"v_cmp_lt_i32 vcc, -1, v216\n"
"v_mfma_f32_16x16x32_bf16 v[176:179], a[8:11], v[140:143], v[176:179]\n"
"v_mfma_f32_16x16x32_bf16 v[168:171], a[28:31], v[140:143], v[168:171]\n"
"s_cbranch_vccnz Ls77_%=\n"
"Lb77_%=:\n"
"v_mfma_f32_16x16x32_bf16 v[60:63], a[4:7], v[92:95], v[60:63]\n"
"buffer_load_dwordx4 a[56:59], v0, s[8:11], s90 offen offset:1024 sc1\n"
"v_mfma_f32_16x16x32_bf16 v[52:55], a[0:3], v[92:95], v[52:55]\n"
"v_mfma_f32_16x16x32_bf16 v[132:135], a[24:27], v[92:95], v[132:135]\n"
"v_mfma_f32_16x16x32_bf16 v[228:231], a[44:47], v[92:95], v[228:231]\n"
"v_mfma_f32_16x16x32_bf16 v[232:235], a[20:23], v[92:95], v[232:235]\n"
"buffer_load_dwordx4 a[68:71], v0, s[8:11], s90 offen offset:2048 sc1\n"
"v_mfma_f32_16x16x32_bf16 v[56:59], a[4:7], v[172:175], v[56:59]\n"
"s_waitcnt vmcnt(11)\n"
"v_min3_u32 v148, v252, v253, v254\n"
"v_mfma_f32_16x16x32_bf16 v[212:215], a[0:3], v[172:175], v[212:215]\n"
"v_mfma_f32_16x16x32_bf16 v[180:183], a[24:27], v[172:175], v[180:183]\n"
"v_mfma_f32_16x16x32_bf16 v[176:179], a[44:47], v[172:175], v[176:179]\n"
"v_mfma_f32_16x16x32_bf16 v[168:171], a[20:23], v[172:175], v[168:171]\n"
"buffer_load_dwordx4 a[52:55], v0, s[8:11], s90 offen offset:3072 sc1\n"
"v_mfma_f32_16x16x32_bf16 v[156:159], a[100:103], v[200:203], v[156:159]\n"
"s_add_u32 s90, s55, 0xe800\n"
"s_waitcnt vmcnt(11)\n"
"v_min3_u32 v148, v148, v255, v124\n"
"v_mfma_f32_16x16x32_bf16 v[104:107], a[80:83], v[200:203], v[104:107]\n"
"v_min3_u32 v148, v148, v125, v126\n"
"v_min_u32 v148, v148, v127\n"
"v_mfma_f32_16x16x32_bf16 v[48:51], a[92:95], v[200:203], 0\n"
"v_cmp_lt_i32 vcc, -1, v148\n"
"v_mfma_f32_16x16x32_bf16 v[96:99], a[104:107], v[200:203], 0\n"
"buffer_load_dwordx4 a[84:87], v0, s[8:11], s90 offen offset:0 sc1\n"
"v_mfma_f32_16x16x32_bf16 v[108:111], a[116:119], v[200:203], v[108:111]\n"
"s_cbranch_vccnz Ls78_%=\n"
"Lb78_%=:\n"
"v_mfma_f32_16x16x32_bf16 v[60:63], a[16:19], v[252:255], v[60:63]\n"
"s_waitcnt vmcnt(11)\n"
"v_min3_u32 v136, v40, v41, v42\n"
"v_mfma_f32_16x16x32_bf16 v[52:55], a[40:43], v[252:255], v[52:55]\n"
"v_mfma_f32_16x16x32_bf16 v[132:135], a[60:63], v[252:255], v[132:135]\n"
"v_mfma_f32_16x16x32_bf16 v[228:231], a[36:39], v[252:255], v[228:231]\n"
"buffer_load_dwordx4 a[64:67], v0, s[8:11], s90 offen offset:1024 sc1\n"
"v_mfma_f32_16x16x32_bf16 v[120:123], a[100:103], v[64:67], 0\n"
"s_waitcnt vmcnt(11)\n"
"v_min3_u32 v136, v136, v43, v88\n"
"v_min3_u32 v136, v136, v89, v90\n"
"v_mfma_f32_16x16x32_bf16 v[28:31], a[80:83], v[64:67], 0\n"
"v_min_u32 v136, v136, v91\n"
"v_cmp_lt_i32 vcc, -1, v136\n"
"v_mfma_f32_16x16x32_bf16 v[248:251], a[92:95], v[64:67], 0\n"
"v_mfma_f32_16x16x32_bf16 v[72:75], a[104:107], v[64:67], 0\n"
"s_cbranch_vccnz Ls79_%=\n"
"Lb79_%=:\n"
"v_mfma_f32_16x16x32_bf16 v[84:87], a[116:119], v[64:67], 0\n"
"buffer_load_dwordx4 a[76:79], v0, s[8:11], s90 offen offset:2048 sc1\n"
"v_mfma_f32_16x16x32_bf16 v[156:159], a[96:99], v[80:83], v[156:159]\n"
"s_waitcnt vmcnt(11)\n"
"v_min3_u32 v68, v164, v165, v166\n"
"v_mfma_f32_16x16x32_bf16 v[104:107], a[108:111], v[80:83], v[104:107]\n"
"v_mfma_f32_16x16x32_bf16 v[48:51], a[120:123], v[80:83], v[48:51]\n"
"v_mfma_f32_16x16x32_bf16 v[96:99], a[132:135], v[80:83], v[96:99]\n"
"buffer_load_dwordx4 a[48:51], v0, s[8:11], s90 offen offset:3072 sc1\n"
"v_mfma_f32_16x16x32_bf16 v[108:111], a[112:115], v[80:83], v[108:111]\n"
"s_add_u32 s90, s55, 0xf800\n"
"s_waitcnt vmcnt(11)\n"
"v_min3_u32 v68, v68, v167, v184\n"
"v_mfma_f32_16x16x32_bf16 v[120:123], a[96:99], v[160:163], v[120:123]\n"
"v_min3_u32 v68, v68, v185, v186\n"
"v_min_u32 v68, v68, v187\n"
"v_mfma_f32_16x16x32_bf16 v[28:31], a[108:111], v[160:163], v[28:31]\n"
"v_cmp_lt_i32 vcc, -1, v68\n"
"v_mfma_f32_16x16x32_bf16 v[248:251], a[120:123], v[160:163], v[248:251]\n"
"v_mfma_f32_16x16x32_bf16 v[72:75], a[132:135], v[160:163], v[72:75]\n"
"s_cbranch_vccnz Ls80_%=\n"
"Lb80_%=:\n"
"v_mfma_f32_16x16x32_bf16 v[84:87], a[112:115], v[160:163], v[84:87]\n"
"buffer_load_dwordx4 a[72:75], v0, s[8:11], s90 offen offset:0 sc1\n"
"s_barrier\n"
"buffer_load_dwordx4 a[88:91], v0, s[8:11], s90 offen offset:1024 sc1\n"
"s_waitcnt vmcnt(12)\n"
"v_min3_u32 v36, v196, v197, v198\n"
"s_waitcnt vmcnt(11)\n"
"v_min3_u32 v36, v36, v199, v220\n"
"v_min3_u32 v36, v36, v221, v222\n"
"v_min_u32 v36, v36, v223\n"
"v_cmp_lt_i32 vcc, -1, v36\n"
"s_cbranch_vccnz Ls81_%=\n"
"Lb81_%=:\n"
"buffer_load_dwordx4 a[100:103], v0, s[8:11], s90 offen offset:2048 sc1\n"
"s_waitcnt vmcnt(10)\n"
"v_mfma_f32_16x16x32_bf16 v[232:235], a[32:35], v[252:255], v[232:235]\n"
"v_mfma_f32_16x16x32_bf16 v[56:59], a[16:19], v[124:127], v[56:59]\n"
"v_mfma_f32_16x16x32_bf16 v[212:215], a[40:43], v[124:127], v[212:215]\n"
"v_mfma_f32_16x16x32_bf16 v[180:183], a[60:63], v[124:127], v[180:183]\n"
"v_mfma_f32_16x16x32_bf16 v[176:179], a[36:39], v[124:127], v[176:179]\n"
"buffer_load_dwordx4 a[80:83], v0, s[8:11], s90 offen offset:3072 sc1\n"
"v_mfma_f32_16x16x32_bf16 v[168:171], a[32:35], v[124:127], v[168:171]\n"
"s_waitcnt vmcnt(10)\n"
"s_add_u32 s90, s55, 0x10800\n"
"v_mfma_f32_16x16x32_bf16 v[60:63], a[56:59], v[40:43], v[60:63]\n"
"s_waitcnt vmcnt(9)\n"
"v_mfma_f32_16x16x32_bf16 v[52:55], a[68:71], v[40:43], v[52:55]\n"
"buffer_load_dwordx4 a[92:95], v0, s[8:11], s90 offen offset:0 sc1\n"
"s_waitcnt vmcnt(9)\n"
"v_mfma_f32_16x16x32_bf16 v[132:135], a[52:55], v[40:43], v[132:135]\n"
"buffer_load_dwordx4 a[104:107], v0, s[8:11], s90 offen offset:1024 sc1\n"
"s_waitcnt vmcnt(9)\n"
"v_mfma_f32_16x16x32_bf16 v[228:231], a[84:87], v[40:43], v[228:231]\n"
"buffer_load_dwordx4 v[100:103], v16, s[8:11], s4 offen sc1\n"
"s_waitcnt vmcnt(9)\n"
"v_mfma_f32_16x16x32_bf16 v[232:235], a[64:67], v[40:43], v[232:235]\n"
"v_mfma_f32_16x16x32_bf16 v[56:59], a[56:59], v[88:91], v[56:59]\n"
"v_mfma_f32_16x16x32_bf16 v[212:215], a[68:71], v[88:91], v[212:215]\n"
"buffer_load_dwordx4 v[76:79], v17, s[8:11], s4 offen sc1\n"
"v_mfma_f32_16x16x32_bf16 v[180:183], a[52:55], v[88:91], v[180:183]\n"
"s_bitcmp1_b32 s61, 1\n"
"s_cselect_b32 s4, s60, s59\n"
"s_add_u32 s4, s4, 0x85400\n"
"v_mfma_f32_16x16x32_bf16 v[176:179], a[84:87], v[88:91], v[176:179]\n"
"v_mfma_f32_16x16x32_bf16 v[168:171], a[64:67], v[88:91], v[168:171]\n"
"s_waitcnt vmcnt(9)\n"
"v_mfma_f32_16x16x32_bf16 v[60:63], a[76:79], v[164:167], v[60:63]\n"
"buffer_load_dwordx4 v[44:47], v16, s[8:11], s4 offen sc1\n"
"s_waitcnt vmcnt(9)\n"
"v_mfma_f32_16x16x32_bf16 v[52:55], a[48:51], v[164:167], v[52:55]\n"
"buffer_load_dwordx4 v[240:243], v17, s[8:11], s4 offen sc1\n"
"s_bitcmp1_b32 s61, 2\n"
"s_cselect_b32 s4, s60, s59\n"
"s_add_u32 s4, s4, 0x85800\n"
"s_waitcnt vmcnt(9)\n"
"v_mfma_f32_16x16x32_bf16 v[132:135], a[72:75], v[164:167], v[132:135]\n"
"buffer_load_dwordx4 v[192:195], v16, s[8:11], s4 offen sc1\n"
"s_waitcnt vmcnt(9)\n"
"v_mfma_f32_16x16x32_bf16 v[228:231], a[88:91], v[164:167], v[228:231]\n"
"buffer_load_dwordx4 v[112:115], v17, s[8:11], s4 offen sc1\n"
"s_bitcmp1_b32 s61, 3\n"
"s_cselect_b32 s4, s60, s59\n"
"s_add_u32 s4, s4, 0x85c00\n"
"s_waitcnt vmcnt(9)\n"
"v_mfma_f32_16x16x32_bf16 v[232:235], a[100:103], v[164:167], v[232:235]\n"
"v_mfma_f32_16x16x32_bf16 v[56:59], a[76:79], v[184:187], v[56:59]\n"
"v_mfma_f32_16x16x32_bf16 v[212:215], a[48:51], v[184:187], v[212:215]\n"
"buffer_load_dwordx4 v[236:239], v16, s[8:11], s4 offen sc1\n"
"v_mfma_f32_16x16x32_bf16 v[180:183], a[72:75], v[184:187], v[180:183]\n"
"v_mfma_f32_16x16x32_bf16 v[176:179], a[88:91], v[184:187], v[176:179]\n"
"v_mfma_f32_16x16x32_bf16 v[168:171], a[100:103], v[184:187], v[168:171]\n"
"buffer_load_dwordx4 v[144:147], v17, s[8:11], s4 offen sc1\n"
"s_bitcmp1_b32 s61, 4\n"
"s_cselect_b32 s4, s60, s59\n"
"s_add_u32 s4, s4, 0x86000\n"
"buffer_load_dwordx4 v[224:227], v16, s[8:11], s4 offen sc1\n"
"s_waitcnt vmcnt(9)\n"
"v_mfma_f32_16x16x32_bf16 v[132:135], a[104:107], v[196:199], v[132:135]\n"
"buffer_load_dwordx4 v[128:131], v17, s[8:11], s4 offen sc1\n"
"s_bitcmp1_b32 s61, 5\n"
"s_cselect_b32 s4, s60, s59\n"
"s_add_u32 s4, s4, 0x86400\n"
"s_nop 3\n"
"ds_write_b128 v2, v[132:135] offset:2048\n"
"buffer_load_dwordx4 v[188:191], v16, s[8:11], s4 offen sc1\n"
"buffer_load_dwordx4 v[204:207], v17, s[8:11], s4 offen sc1\n"
"s_bitcmp1_b32 s61, 6\n"
"s_cselect_b32 s4, s60, s59\n"
"s_add_u32 s4, s4, 0x86800\n"
"s_waitcnt vmcnt(11)\n"
"v_min3_u32 v152, v100, v101, v102\n"
"buffer_load_dwordx4 a[116:119], v0, s[8:11], s90 offen offset:2048 sc1\n"
"s_waitcnt vmcnt(11)\n"
"v_min3_u32 v152, v152, v103, v76\n"
"v_min3_u32 v152, v152, v77, v78\n"
"v_min_u32 v152, v152, v79\n"
"v_cmp_lt_i32 vcc, -1, v152\n"
"s_cbranch_vccnz Ls92_%=\n"
"Lb92_%=:\n"
"v_mfma_f32_16x16x32_bf16 v[156:159], a[124:127], v[100:103], v[156:159]\n"
"v_mfma_f32_16x16x32_bf16 v[104:107], a[136:139], v[100:103], v[104:107]\n"
"buffer_load_dwordx4 a[96:99], v0, s[8:11], s90 offen offset:3072 sc1\n"
"v_mfma_f32_16x16x32_bf16 v[48:51], a[152:155], v[100:103], v[48:51]\n"
"s_waitcnt vmcnt(11) lgkmcnt(0)\n"
"v_min3_u32 v208, v44, v45, v46\n"
"v_mfma_f32_16x16x32_bf16 v[96:99], a[128:131], v[100:103], v[96:99]\n"
"v_mfma_f32_16x16x32_bf16 v[108:111], a[140:143], v[100:103], v[108:111]\n"
"buffer_load_dwordx4 v[116:119], v16, s[8:11], s4 offen sc1\n"
"v_mfma_f32_16x16x32_bf16 v[120:123], a[124:127], v[76:79], v[120:123]\n"
"v_mfma_f32_16x16x32_bf16 v[28:31], a[136:139], v[76:79], v[28:31]\n"
"s_waitcnt vmcnt(11)\n"
"v_min3_u32 v208, v208, v47, v240\n"
"v_min3_u32 v208, v208, v241, v242\n"
"v_mfma_f32_16x16x32_bf16 v[248:251], a[152:155], v[76:79], v[248:251]\n"
"v_min_u32 v208, v208, v243\n"
"v_cmp_lt_i32 vcc, -1, v208\n"
"v_mfma_f32_16x16x32_bf16 v[72:75], a[128:131], v[76:79], v[72:75]\n"
"v_mfma_f32_16x16x32_bf16 v[84:87], a[140:143], v[76:79], v[84:87]\n"
"buffer_load_dwordx4 v[32:35], v17, s[8:11], s4 offen sc1\n"
"s_cbranch_vccnz Ls93_%=\n"
"Lb93_%=:\n"
"v_mfma_f32_16x16x32_bf16 v[156:159], a[164:167], v[44:47], v[156:159]\n"
"s_bitcmp1_b32 s61, 7\n"
"s_cselect_b32 s4, s60, s59\n"
"s_add_u32 s4, s4, 0x86c00\n"
"v_mfma_f32_16x16x32_bf16 v[104:107], a[156:159], v[44:47], v[104:107]\n"
"s_waitcnt vmcnt(11)\n"
"v_min3_u32 v244, v192, v193, v194\n"
"v_mfma_f32_16x16x32_bf16 v[48:51], a[148:151], v[44:47], v[48:51]\n"
"v_mfma_f32_16x16x32_bf16 v[96:99], a[144:147], v[44:47], v[96:99]\n"
"v_mfma_f32_16x16x32_bf16 v[108:111], a[168:171], v[44:47], v[108:111]\n"
"buffer_load_dwordx4 v[140:143], v16, s[8:11], s4 offen sc1\n"
"v_mfma_f32_16x16x32_bf16 v[120:123], a[164:167], v[240:243], v[120:123]\n"
"s_waitcnt vmcnt(11)\n"
"v_min3_u32 v244, v244, v195, v112\n"
"v_min3_u32 v244, v244, v113, v114\n"
"v_mfma_f32_16x16x32_bf16 v[28:31], a[156:159], v[240:243], v[28:31]\n"
"v_min_u32 v244, v244, v115\n"
"v_cmp_lt_i32 vcc, -1, v244\n"
"v_mfma_f32_16x16x32_bf16 v[248:251], a[148:151], v[240:243], v[248:251]\n"
"v_mfma_f32_16x16x32_bf16 v[72:75], a[144:147], v[240:243], v[72:75]\n"
"s_cbranch_vccnz Ls94_%=\n"
"Lb94_%=:\n"
"v_mfma_f32_16x16x32_bf16 v[84:87], a[168:171], v[240:243], v[84:87]\n"
"buffer_load_dwordx4 v[216:219], v17, s[8:11], s4 offen sc1\n"
"v_mfma_f32_16x16x32_bf16 v[156:159], a[184:187], v[192:195], v[156:159]\n"
"s_bitcmp1_b32 s61, 8\n"
"s_cselect_b32 s4, s60, s59\n"
"s_add_u32 s4, s4, 0x87000\n"
"v_mfma_f32_16x16x32_bf16 v[104:107], a[160:163], v[192:195], v[104:107]\n"
"s_waitcnt vmcnt(11)\n"
"v_min3_u32 v92, v236, v237, v238\n"
"v_mfma_f32_16x16x32_bf16 v[48:51], a[172:175], v[192:195], v[48:51]\n"
"v_mfma_f32_16x16x32_bf16 v[96:99], a[188:191], v[192:195], v[96:99]\n"
"v_mfma_f32_16x16x32_bf16 v[108:111], a[176:179], v[192:195], v[108:111]\n"
"buffer_load_dwordx4 v[172:175], v16, s[8:11], s4 offen sc1\n"
"v_mfma_f32_16x16x32_bf16 v[120:123], a[184:187], v[112:115], v[120:123]\n"
"s_waitcnt vmcnt(11)\n"
"v_min3_u32 v92, v92, v239, v144\n"
"v_min3_u32 v92, v92, v145, v146\n"
"v_mfma_f32_16x16x32_bf16 v[28:31], a[160:163], v[112:115], v[28:31]\n"
"v_min_u32 v92, v92, v147\n"
"v_cmp_lt_i32 vcc, -1, v92\n"
"v_mfma_f32_16x16x32_bf16 v[248:251], a[172:175], v[112:115], v[248:251]\n"
"v_mfma_f32_16x16x32_bf16 v[72:75], a[188:191], v[112:115], v[72:75]\n"
"s_cbranch_vccnz Ls95_%=\n"
"Lb95_%=:\n"
"v_mfma_f32_16x16x32_bf16 v[84:87], a[176:179], v[112:115], v[84:87]\n"
"buffer_load_dwordx4 v[200:203], v17, s[8:11], s4 offen sc1\n"
"v_mfma_f32_16x16x32_bf16 v[156:159], a[196:199], v[236:239], v[156:159]\n"
"s_bitcmp1_b32 s61, 9\n"
"s_cselect_b32 s4, s60, s59\n"
"s_add_u32 s4, s4, 0x87400\n"
"v_mfma_f32_16x16x32_bf16 v[104:107], a[180:183], v[236:239], v[104:107]\n"
"s_waitcnt vmcnt(11)\n"
"v_min3_u32 v148, v224, v225, v226\n"
"v_mfma_f32_16x16x32_bf16 v[48:51], a[212:215], v[236:239], v[48:51]\n"
"v_mfma_f32_16x16x32_bf16 v[96:99], a[204:207], v[236:239], v[96:99]\n"
"s_waitcnt vmcnt(10)\n"
"v_min3_u32 v148, v148, v227, v128\n"
"v_min3_u32 v148, v148, v129, v130\n"
"v_mfma_f32_16x16x32_bf16 v[108:111], a[192:195], v[236:239], v[108:111]\n"
"buffer_load_dwordx4 v[136:139], v16, s[8:11], s4 offen sc1\n"
"v_mfma_f32_16x16x32_bf16 v[120:123], a[196:199], v[144:147], v[120:123]\n"
"v_min_u32 v148, v148, v131\n"
"v_cmp_lt_i32 vcc, -1, v148\n"
"v_mfma_f32_16x16x32_bf16 v[28:31], a[180:183], v[144:147], v[28:31]\n"
"v_mfma_f32_16x16x32_bf16 v[248:251], a[212:215], v[144:147], v[248:251]\n"
"s_cbranch_vccnz Ls96_%=\n"
"Lb96_%=:\n"
"v_mfma_f32_16x16x32_bf16 v[72:75], a[204:207], v[144:147], v[72:75]\n"
"s_waitcnt vmcnt(10)\n"
"v_min3_u32 v64, v188, v189, v190\n"
"v_mfma_f32_16x16x32_bf16 v[84:87], a[192:195], v[144:147], v[84:87]\n"
"buffer_load_dwordx4 v[80:83], v17, s[8:11], s4 offen sc1\n"
"v_mfma_f32_16x16x32_bf16 v[156:159], a[200:203], v[224:227], v[156:159]\n"
"s_bitcmp1_b32 s61, 10\n"
"s_cselect_b32 s4, s60, s59\n"
"s_add_u32 s4, s4, 0x87800\n"
"v_mfma_f32_16x16x32_bf16 v[104:107], a[228:231], v[224:227], v[104:107]\n"
"v_mfma_f32_16x16x32_bf16 v[48:51], a[220:223], v[224:227], v[48:51]\n"
"s_waitcnt vmcnt(9)\n"
"v_mfma_f32_16x16x32_bf16 v[228:231], a[116:119], v[196:199], v[228:231]\n"
"s_waitcnt vmcnt(8)\n"
"v_min3_u32 v64, v64, v191, v204\n"
"v_min3_u32 v64, v64, v205, v206\n"
"v_mfma_f32_16x16x32_bf16 v[232:235], a[96:99], v[196:199], v[232:235]\n"
"v_min_u32 v64, v64, v207\n"
"v_cmp_lt_i32 vcc, -1, v64\n"
"v_mfma_f32_16x16x32_bf16 v[56:59], a[80:83], v[220:223], v[56:59]\n"
"v_mfma_f32_16x16x32_bf16 v[212:215], a[92:95], v[220:223], v[212:215]\n"
"s_cbranch_vccnz Ls97_%=\n"
"Lb97_%=:\n"
"v_mfma_f32_16x16x32_bf16 v[180:183], a[104:107], v[220:223], v[180:183]\n"
"ds_write_b128 v2, v[228:231] offset:3072\n"
"v_mfma_f32_16x16x32_bf16 v[176:179], a[116:119], v[220:223], v[176:179]\n"
"ds_write_b128 v2, v[232:235] offset:6144\n"
"v_mfma_f32_16x16x32_bf16 v[168:171], a[96:99], v[220:223], v[168:171]\n"
"v_mfma_f32_16x16x32_bf16 v[60:63], a[80:83], v[196:199], v[60:63]\n"
"v_mfma_f32_16x16x32_bf16 v[52:55], a[92:95], v[196:199], v[52:55]\n"
"ds_write_b128 v2, v[56:59] offset:0\n"
"v_mfma_f32_16x16x32_bf16 v[96:99], a[208:211], v[224:227], v[96:99]\n"
"v_mfma_f32_16x16x32_bf16 v[108:111], a[216:219], v[224:227], v[108:111]\n"
"buffer_load_dwordx4 v[68:71], v16, s[8:11], s4 offen sc1\n"
"v_mfma_f32_16x16x32_bf16 v[120:123], a[200:203], v[128:131], v[120:123]\n"
"ds_write_b128 v2, v[212:215] offset:1024\n"
"v_mfma_f32_16x16x32_bf16 v[28:31], a[228:231], v[128:131], v[28:31]\n"
"s_waitcnt vmcnt(8)\n"
"v_min3_u32 v160, v116, v117, v118\n"
"v_mfma_f32_16x16x32_bf16 v[248:251], a[220:223], v[128:131], v[248:251]\n"
"ds_write_b128 v2, v[180:183] offset:4096\n"
"v_mfma_f32_16x16x32_bf16 v[72:75], a[208:211], v[128:131], v[72:75]\n"
"s_waitcnt vmcnt(7)\n"
"v_min3_u32 v160, v160, v119, v32\n"
"v_min3_u32 v160, v160, v33, v34\n"
"v_mfma_f32_16x16x32_bf16 v[84:87], a[216:219], v[128:131], v[84:87]\n"
"buffer_load_dwordx4 v[36:39], v17, s[8:11], s4 offen sc1\n"
"v_mfma_f32_16x16x32_bf16 v[156:159], a[244:247], v[188:191], v[156:159]\n"
"s_bitcmp1_b32 s61, 11\n"
"s_cselect_b32 s4, s60, s59\n"
"s_add_u32 s4, s4, 0x87c00\n"
"v_mfma_f32_16x16x32_bf16 v[104:107], a[236:239], v[188:191], v[104:107]\n"
"ds_write_b128 v2, v[176:179] offset:5120\n"
"v_mfma_f32_16x16x32_bf16 v[48:51], a[224:227], v[188:191], v[48:51]\n"
"ds_write_b128 v2, v[168:171] offset:7168\n"
"v_mfma_f32_16x16x32_bf16 v[96:99], a[232:235], v[188:191], v[96:99]\n"
"v_min_u32 v160, v160, v35\n"
"v_cmp_lt_i32 vcc, -1, v160\n"
"v_mfma_f32_16x16x32_bf16 v[108:111], a[248:251], v[188:191], v[108:111]\n"
"buffer_load_dwordx4 v[252:255], v16, s[8:11], s4 offen sc1\n"
"v_mfma_f32_16x16x32_bf16 v[120:123], a[244:247], v[204:207], v[120:123]\n"
"s_cbranch_vccnz Ls98_%=\n"
"Lb98_%=:\n"
"v_mfma_f32_16x16x32_bf16 v[28:31], a[236:239], v[204:207], v[28:31]\n"
"s_waitcnt vmcnt(8) lgkmcnt(4)\n"
"v_min3_u32 v124, v140, v141, v142\n"
"v_mfma_f32_16x16x32_bf16 v[248:251], a[224:227], v[204:207], v[248:251]\n"
"s_waitcnt vmcnt(7)\n"
"v_min3_u32 v124, v124, v143, v216\n"
"v_min3_u32 v124, v124, v217, v218\n"
"v_mfma_f32_16x16x32_bf16 v[72:75], a[232:235], v[204:207], v[72:75]\n"
"v_min_u32 v124, v124, v219\n"
"v_cmp_lt_i32 vcc, -1, v124\n"
"v_mfma_f32_16x16x32_bf16 v[84:87], a[248:251], v[204:207], v[84:87]\n"
"buffer_load_dwordx4 v[40:43], v17, s[8:11], s4 offen sc1\n"
"v_mfma_f32_16x16x32_bf16 v[156:159], a[12:15], v[116:119], v[156:159]\n"
"s_cbranch_vccnz Ls99_%=\n"
"Lb99_%=:\n"
"v_mfma_f32_16x16x32_bf16 v[104:107], a[240:243], v[116:119], v[104:107]\n"
"s_waitcnt vmcnt(7) lgkmcnt(3)\n"
"v_min3_u32 v88, v172, v173, v174\n"
"v_mfma_f32_16x16x32_bf16 v[48:51], a[252:255], v[116:119], v[48:51]\n"
"v_mfma_f32_16x16x32_bf16 v[96:99], a[8:11], v[116:119], v[96:99]\n"
"s_waitcnt lgkmcnt(0)\n"
"s_barrier\n"
"ds_read_b128 v[164:167], v8 offset:0\n"
"ds_read_b128 v[184:187], v9 offset:2048\n"
"ds_read_b128 v[132:135], v10 offset:4096\n"
"ds_read_b128 v[152:155], v8 offset:1024\n"
"ds_read_b128 v[100:103], v9 offset:3072\n"
"ds_read_b128 v[76:79], v10 offset:5120\n"
"v_mfma_f32_16x16x32_bf16 v[108:111], a[28:31], v[116:119], v[108:111]\n"
"s_waitcnt vmcnt(6)\n"
"v_min3_u32 v88, v88, v175, v200\n"
"v_min3_u32 v88, v88, v201, v202\n"
"v_mfma_f32_16x16x32_bf16 v[120:123], a[12:15], v[32:35], v[120:123]\n"
"v_min_u32 v88, v88, v203\n"
"v_cmp_lt_i32 vcc, -1, v88\n"
"v_mfma_f32_16x16x32_bf16 v[28:31], a[240:243], v[32:35], v[28:31]\n"
"v_mfma_f32_16x16x32_bf16 v[248:251], a[252:255], v[32:35], v[248:251]\n"
"s_cbranch_vccnz Ls100_%=\n"
"Lb100_%=:\n"
"v_mfma_f32_16x16x32_bf16 v[72:75], a[8:11], v[32:35], v[72:75]\n"
"s_waitcnt vmcnt(5)\n"
"v_min3_u32 v208, v136, v137, v138\n"
"v_mfma_f32_16x16x32_bf16 v[84:87], a[28:31], v[32:35], v[84:87]\n"
"v_mfma_f32_16x16x32_bf16 v[156:159], a[4:7], v[140:143], v[156:159]\n"
"s_waitcnt lgkmcnt(5)\n"
"v_add_f32 v60, v60, v164\n"
"v_add_f32 v61, v61, v165\n"
"v_add_f32 v62, v62, v166\n"
"v_add_f32 v63, v63, v167\n"
"s_waitcnt lgkmcnt(4)\n"
"v_add_f32 v60, v60, v184\n"
"ds_read_b128 v[44:47], v8 offset:7168\n"
"v_mfma_f32_16x16x32_bf16 v[104:107], a[0:3], v[140:143], v[104:107]\n"
"s_waitcnt vmcnt(4)\n"
"v_min3_u32 v208, v208, v139, v80\n"
"v_min3_u32 v208, v208, v81, v82\n"
"v_add_f32 v61, v61, v185\n"
"v_add_f32 v62, v62, v186\n"
"v_add_f32 v63, v63, v187\n"
"ds_read_b128 v[244:247], v9 offset:6144\n"
"v_mfma_f32_16x16x32_bf16 v[48:51], a[24:27], v[140:143], v[48:51]\n"
"v_min_u32 v208, v208, v83\n"
"v_cmp_lt_i32 vcc, -1, v208\n"
"s_waitcnt lgkmcnt(5)\n"
"v_add_f32 v60, v60, v132\n"
"v_add_f32 v61, v61, v133\n"
"v_add_f32 v62, v62, v134\n"
"v_mfma_f32_16x16x32_bf16 v[96:99], a[44:47], v[140:143], v[96:99]\n"
"s_cbranch_vccnz Ls101_%=\n"
"Lb101_%=:\n"
"v_add_f32 v63, v63, v135\n"
"v_cvt_pk_bf16_f32 v240, -v60, -v61\n"
"v_cvt_pk_bf16_f32 v241, -v62, -v63\n"
"s_waitcnt lgkmcnt(4)\n"
"v_add_f32 v52, v52, v152\n"
"ds_read_b128 v[192:195], v10 offset:7168\n"
"v_mfma_f32_16x16x32_bf16 v[108:111], a[20:23], v[140:143], v[108:111]\n"
"v_add_f32 v53, v53, v153\n"
"v_add_f32 v54, v54, v154\n"
"v_add_f32 v55, v55, v155\n"
"s_waitcnt lgkmcnt(3)\n"
"v_add_f32 v52, v52, v100\n"
"v_add_f32 v53, v53, v101\n"
"v_mfma_f32_16x16x32_bf16 v[120:123], a[4:7], v[216:219], v[120:123]\n"
"v_add_f32 v54, v54, v102\n"
"v_add_f32 v55, v55, v103\n"
"v_add_f32 v52, v52, v76\n"
"v_add_f32 v53, v53, v77\n"
"v_add_f32 v54, v54, v78\n"
"v_add_f32 v55, v55, v79\n"
"v_mfma_f32_16x16x32_bf16 v[28:31], a[0:3], v[216:219], v[28:31]\n"
"s_waitcnt vmcnt(3)\n"
"v_min3_u32 v92, v68, v69, v70\n"
"v_cvt_pk_bf16_f32 v242, -v52, -v53\n"
"v_cvt_pk_bf16_f32 v243, -v54, -v55\n"
"s_add_u32 s0, s76, 0x0\n"
"v_mfma_f32_16x16x32_bf16 v[248:251], a[24:27], v[216:219], v[248:251]\n"
"buffer_store_dwordx4 v[240:243], v25, s[12:15], s0 offen offset:0 sc1\n"
"v_mfma_f32_16x16x32_bf16 v[72:75], a[44:47], v[216:219], v[72:75]\n"
"s_waitcnt lgkmcnt(2)\n"
"v_add_f32 v232, v232, v44\n"
"v_add_f32 v233, v233, v45\n"
"v_add_f32 v234, v234, v46\n"
"v_add_f32 v235, v235, v47\n"
"s_waitcnt lgkmcnt(1)\n"
"v_add_f32 v232, v232, v244\n"
"v_mfma_f32_16x16x32_bf16 v[84:87], a[20:23], v[216:219], v[84:87]\n"
"v_add_f32 v233, v233, v245\n"
"v_add_f32 v234, v234, v246\n"
"v_add_f32 v235, v235, v247\n"
"s_waitcnt lgkmcnt(0)\n"
"v_add_f32 v232, v232, v192\n"
"v_add_f32 v233, v233, v193\n"
"v_mfma_f32_16x16x32_bf16 v[156:159], a[16:19], v[172:175], v[156:159]\n"
"s_waitcnt vmcnt(3)\n"
"v_min3_u32 v92, v92, v71, v36\n"
"v_min3_u32 v92, v92, v37, v38\n"
"v_add_f32 v234, v234, v194\n"
"v_add_f32 v235, v235, v195\n"
"v_cvt_pk_bf16_f32 v112, -v232, -v233\n"
"v_cvt_pk_bf16_f32 v113, -v234, -v235\n"
"v_mfma_f32_16x16x32_bf16 v[104:107], a[40:43], v[172:175], v[104:107]\n"
"s_add_u32 s0, s77, 0x0\n"
"v_min_u32 v92, v92, v39\n"
"v_cmp_lt_i32 vcc, -1, v92\n"
"v_mfma_f32_16x16x32_bf16 v[48:51], a[60:63], v[172:175], v[48:51]\n"
"buffer_store_dwordx2 v[112:113], v26, s[12:15], s0 offen offset:0 sc1\n"
"v_mov_b32 v236, s88\n"
"v_mov_b32 v237, s89\n"
"v_mfma_f32_16x16x32_bf16 v[96:99], a[36:39], v[172:175], v[96:99]\n"
"s_cbranch_vccnz Ls102_%=\n"
"Lb102_%=:\n"
"buffer_store_dwordx2 v[236:237], v15, s[8:11], s36 offen\n"
"v_mfma_f32_16x16x32_bf16 v[108:111], a[32:35], v[172:175], v[108:111]\n"
"s_waitcnt vmcnt(4)\n"
"v_min3_u32 v148, v252, v253, v254\n"
"v_mfma_f32_16x16x32_bf16 v[120:123], a[16:19], v[200:203], v[120:123]\n"
"v_mfma_f32_16x16x32_bf16 v[28:31], a[40:43], v[200:203], v[28:31]\n"
"v_mfma_f32_16x16x32_bf16 v[248:251], a[60:63], v[200:203], v[248:251]\n"
"v_mfma_f32_16x16x32_bf16 v[72:75], a[36:39], v[200:203], v[72:75]\n"
"s_waitcnt vmcnt(3)\n"
"v_min3_u32 v148, v148, v255, v40\n"
"v_min3_u32 v148, v148, v41, v42\n"
"v_mfma_f32_16x16x32_bf16 v[84:87], a[32:35], v[200:203], v[84:87]\n"
"v_min_u32 v148, v148, v43\n"
"v_cmp_lt_i32 vcc, -1, v148\n"
"v_mfma_f32_16x16x32_bf16 v[156:159], a[56:59], v[136:139], v[156:159]\n"
"v_mfma_f32_16x16x32_bf16 v[104:107], a[68:71], v[136:139], v[104:107]\n"
"s_cbranch_vccnz Ls103_%=\n"
"Lb103_%=:\n"
"v_mfma_f32_16x16x32_bf16 v[48:51], a[52:55], v[136:139], v[48:51]\n"
"v_mfma_f32_16x16x32_bf16 v[96:99], a[84:87], v[136:139], v[96:99]\n"
"v_mfma_f32_16x16x32_bf16 v[108:111], a[64:67], v[136:139], v[108:111]\n"
"v_mfma_f32_16x16x32_bf16 v[120:123], a[56:59], v[80:83], v[120:123]\n"
"v_mfma_f32_16x16x32_bf16 v[28:31], a[68:71], v[80:83], v[28:31]\n"
"v_mfma_f32_16x16x32_bf16 v[248:251], a[52:55], v[80:83], v[248:251]\n"
"v_mfma_f32_16x16x32_bf16 v[72:75], a[84:87], v[80:83], v[72:75]\n"
"v_mfma_f32_16x16x32_bf16 v[84:87], a[64:67], v[80:83], v[84:87]\n"
"v_mfma_f32_16x16x32_bf16 v[156:159], a[76:79], v[68:71], v[156:159]\n"
"v_mfma_f32_16x16x32_bf16 v[104:107], a[48:51], v[68:71], v[104:107]\n"
"v_mfma_f32_16x16x32_bf16 v[48:51], a[72:75], v[68:71], v[48:51]\n"
"v_mfma_f32_16x16x32_bf16 v[96:99], a[88:91], v[68:71], v[96:99]\n"
"v_mfma_f32_16x16x32_bf16 v[108:111], a[100:103], v[68:71], v[108:111]\n"
"v_mfma_f32_16x16x32_bf16 v[120:123], a[76:79], v[36:39], v[120:123]\n"
"v_mfma_f32_16x16x32_bf16 v[28:31], a[48:51], v[36:39], v[28:31]\n"
"v_mfma_f32_16x16x32_bf16 v[248:251], a[72:75], v[36:39], v[248:251]\n"
"v_mfma_f32_16x16x32_bf16 v[72:75], a[88:91], v[36:39], v[72:75]\n"
"v_mfma_f32_16x16x32_bf16 v[84:87], a[100:103], v[36:39], v[84:87]\n"
"v_mfma_f32_16x16x32_bf16 v[48:51], a[104:107], v[252:255], v[48:51]\n"
"v_mfma_f32_16x16x32_bf16 v[96:99], a[116:119], v[252:255], v[96:99]\n"
"v_mfma_f32_16x16x32_bf16 v[108:111], a[96:99], v[252:255], v[108:111]\n"
"v_mfma_f32_16x16x32_bf16 v[120:123], a[80:83], v[40:43], v[120:123]\n"
"v_mfma_f32_16x16x32_bf16 v[28:31], a[92:95], v[40:43], v[28:31]\n"
"v_mfma_f32_16x16x32_bf16 v[248:251], a[104:107], v[40:43], v[248:251]\n"
"v_mfma_f32_16x16x32_bf16 v[72:75], a[116:119], v[40:43], v[72:75]\n"
"v_mfma_f32_16x16x32_bf16 v[84:87], a[96:99], v[40:43], v[84:87]\n"
"v_mfma_f32_16x16x32_bf16 v[156:159], a[80:83], v[252:255], v[156:159]\n"
"ds_write_b128 v2, v[48:51] offset:12288\n"
"v_mfma_f32_16x16x32_bf16 v[104:107], a[92:95], v[252:255], v[104:107]\n"
"ds_write_b128 v2, v[96:99] offset:13312\n"
"ds_write_b128 v2, v[108:111] offset:16384\n"
"ds_write_b128 v2, v[120:123] offset:10240\n"
"ds_write_b128 v2, v[28:31] offset:11264\n"
"ds_write_b128 v2, v[248:251] offset:14336\n"
"ds_write_b128 v2, v[72:75] offset:15360\n"
"ds_write_b128 v2, v[84:87] offset:17408\n"
"s_waitcnt lgkmcnt(0)\n"
"s_barrier\n"
"ds_read_b128 v[144:147], v8 offset:10240\n"
"ds_read_b128 v[64:67], v9 offset:12288\n"
"ds_read_b128 v[228:231], v10 offset:14336\n"
"ds_read_b128 v[220:223], v8 offset:11264\n"
"ds_read_b128 v[196:199], v9 offset:13312\n"
"ds_read_b128 v[56:59], v10 offset:15360\n"
"s_waitcnt lgkmcnt(5)\n"
"v_add_f32 v156, v156, v144\n"
"v_add_f32 v157, v157, v145\n"
"v_add_f32 v158, v158, v146\n"
"v_add_f32 v159, v159, v147\n"
"s_waitcnt lgkmcnt(4)\n"
"v_add_f32 v156, v156, v64\n"
"v_add_f32 v157, v157, v65\n"
"v_add_f32 v158, v158, v66\n"
"v_add_f32 v159, v159, v67\n"
"s_waitcnt lgkmcnt(3)\n"
"v_add_f32 v156, v156, v228\n"
"v_add_f32 v157, v157, v229\n"
"v_add_f32 v158, v158, v230\n"
"v_add_f32 v159, v159, v231\n"
"v_cvt_pk_bf16_f32 v224, -v156, -v157\n"
"v_cvt_pk_bf16_f32 v225, -v158, -v159\n"
"s_waitcnt lgkmcnt(2)\n"
"v_add_f32 v104, v104, v220\n"
"v_add_f32 v105, v105, v221\n"
"v_add_f32 v106, v106, v222\n"
"v_add_f32 v107, v107, v223\n"
"s_waitcnt lgkmcnt(1)\n"
"v_add_f32 v104, v104, v196\n"
"v_add_f32 v105, v105, v197\n"
"v_add_f32 v106, v106, v198\n"
"v_add_f32 v107, v107, v199\n"
"s_waitcnt lgkmcnt(0)\n"
"v_add_f32 v104, v104, v56\n"
"v_add_f32 v105, v105, v57\n"
"v_add_f32 v106, v106, v58\n"
"v_add_f32 v107, v107, v59\n"
"v_cvt_pk_bf16_f32 v226, -v104, -v105\n"
"v_cvt_pk_bf16_f32 v227, -v106, -v107\n"
"s_add_u32 s1, s76, 0x500\n"
"buffer_store_dwordx4 v[224:227], v25, s[12:15], s1 offen offset:0 sc1\n"
"ds_read_b128 v[212:215], v8 offset:17408\n"
"ds_read_b128 v[180:183], v9 offset:16384\n"
"ds_read_b128 v[128:131], v10 offset:17408\n"
"s_waitcnt lgkmcnt(2)\n"
"v_add_f32 v108, v108, v212\n"
"v_add_f32 v109, v109, v213\n"
"v_add_f32 v110, v110, v214\n"
"v_add_f32 v111, v111, v215\n"
"s_waitcnt lgkmcnt(1)\n"
"v_add_f32 v108, v108, v180\n"
"v_add_f32 v109, v109, v181\n"
"v_add_f32 v110, v110, v182\n"
"v_add_f32 v111, v111, v183\n"
"s_waitcnt lgkmcnt(0)\n"
"v_add_f32 v108, v108, v128\n"
"v_add_f32 v109, v109, v129\n"
"v_add_f32 v110, v110, v130\n"
"v_add_f32 v111, v111, v131\n"
"v_cvt_pk_bf16_f32 v176, -v108, -v109\n"
"v_cvt_pk_bf16_f32 v177, -v110, -v111\n"
"s_add_u32 s1, s77, 0x500\n"
"buffer_store_dwordx2 v[176:177], v26, s[12:15], s1 offen offset:0 sc1\n"
"s_endpgm\n"
".Lcode_last:\n"
"Ls0_%=:\n"
"s_sleep 2\n"
"buffer_load_dwordx2 v[200:201], v16, s[8:11], s32 offen sc1\n"
"s_waitcnt vmcnt(0)\n"
"v_cmp_ne_u64 vcc, s[88:89], v[200:201]\n"
"s_cbranch_vccz Lb0_%=\n"
"s_sub_u32 s45, s45, 1\n"
"s_cbranch_scc0 Ls0_%=\n"
"s_mov_b32 s45, 0\n"
"s_branch Lb0_%=\n"
"Ls1_%=:\n"
"s_sleep 2\n"
"buffer_load_dwordx2 v[28:29], v17, s[8:11], s32 offen sc1\n"
"s_waitcnt vmcnt(0)\n"
"v_cmp_ne_u64 vcc, s[88:89], v[28:29]\n"
"s_cbranch_vccz Lb1_%=\n"
"s_sub_u32 s45, s45, 1\n"
"s_cbranch_scc0 Ls1_%=\n"
"s_mov_b32 s45, 0\n"
"s_branch Lb1_%=\n"
"Ls2_%=:\n"
"s_sleep 2\n"
"buffer_load_dwordx2 v[32:33], v18, s[8:11], s75 offen sc1\n"
"s_waitcnt vmcnt(0)\n"
"v_cmp_ne_u64 vcc, s[88:89], v[32:33]\n"
"s_cbranch_vccz Lb2_%=\n"
"s_sub_u32 s45, s45, 1\n"
"s_cbranch_scc0 Ls2_%=\n"
"s_mov_b32 s45, 0\n"
"s_branch Lb2_%=\n"
"Ls4_%=:\n"
"s_sleep 2\n"
"s_add_u32 s7, s57, 0x2c000\n"
"buffer_load_dwordx4 v[116:119], v0, s[8:11], s7 offen offset:0 sc1\n"
"buffer_load_dwordx4 v[36:39], v0, s[8:11], s7 offen offset:1024 sc1\n"
"buffer_load_dwordx4 v[208:211], v0, s[8:11], s7 offen offset:2048 sc1\n"
"buffer_load_dwordx4 v[80:83], v0, s[8:11], s7 offen offset:3072 sc1\n"
"s_add_u32 s7, s57, 0x2d000\n"
"buffer_load_dwordx4 v[72:75], v0, s[8:11], s7 offen offset:0 sc1\n"
"s_add_u32 s7, s58, 0x2c000\n"
"buffer_load_dwordx4 v[32:35], v0, s[8:11], s7 offen offset:0 sc1\n"
"buffer_load_dwordx4 v[220:223], v0, s[8:11], s7 offen offset:1024 sc1\n"
"buffer_load_dwordx4 v[236:239], v0, s[8:11], s7 offen offset:2048 sc1\n"
"buffer_load_dwordx4 v[28:31], v0, s[8:11], s7 offen offset:3072 sc1\n"
"s_add_u32 s7, s58, 0x2d000\n"
"buffer_load_dwordx4 v[216:219], v0, s[8:11], s7 offen offset:0 sc1\n"
"s_waitcnt vmcnt(0)\n"
"v_max3_u32 v48, v116, v117, v118\n"
"v_max3_u32 v48, v48, v119, v32\n"
"v_max3_u32 v48, v48, v33, v34\n"
"v_max_u32 v48, v48, v35\n"
"v_cmp_eq_u32 vcc, -1, v48\n"
"s_cbranch_vccz Lb4_%=\n"
"s_sub_u32 s45, s45, 1\n"
"s_cbranch_scc0 Ls4_%=\n"
"s_mov_b32 s45, 0\n"
"s_branch Lb4_%=\n"
"Ls5_%=:\n"
"s_sleep 2\n"
"s_add_u32 s7, s57, 0x2c400\n"
"buffer_load_dwordx4 v[36:39], v0, s[8:11], s7 offen offset:0 sc1\n"
"buffer_load_dwordx4 v[208:211], v0, s[8:11], s7 offen offset:1024 sc1\n"
"buffer_load_dwordx4 v[80:83], v0, s[8:11], s7 offen offset:2048 sc1\n"
"buffer_load_dwordx4 v[72:75], v0, s[8:11], s7 offen offset:3072 sc1\n"
"s_add_u32 s7, s58, 0x2c400\n"
"buffer_load_dwordx4 v[220:223], v0, s[8:11], s7 offen offset:0 sc1\n"
"buffer_load_dwordx4 v[236:239], v0, s[8:11], s7 offen offset:1024 sc1\n"
"buffer_load_dwordx4 v[28:31], v0, s[8:11], s7 offen offset:2048 sc1\n"
"buffer_load_dwordx4 v[216:219], v0, s[8:11], s7 offen offset:3072 sc1\n"
"s_waitcnt vmcnt(0)\n"
"v_max3_u32 v164, v36, v37, v38\n"
"v_max3_u32 v164, v164, v39, v220\n"
"v_max3_u32 v164, v164, v221, v222\n"
"v_max_u32 v164, v164, v223\n"
"v_cmp_eq_u32 vcc, -1, v164\n"
"s_cbranch_vccz Lb5_%=\n"
"s_sub_u32 s45, s45, 1\n"
"s_cbranch_scc0 Ls5_%=\n"
"s_mov_b32 s45, 0\n"
"s_branch Lb5_%=\n"
"Ls6_%=:\n"
"s_sleep 2\n"
"s_add_u32 s7, s57, 0x2c800\n"
"buffer_load_dwordx4 v[208:211], v0, s[8:11], s7 offen offset:0 sc1\n"
"buffer_load_dwordx4 v[80:83], v0, s[8:11], s7 offen offset:1024 sc1\n"
"buffer_load_dwordx4 v[72:75], v0, s[8:11], s7 offen offset:2048 sc1\n"
"s_add_u32 s7, s58, 0x2c800\n"
"buffer_load_dwordx4 v[236:239], v0, s[8:11], s7 offen offset:0 sc1\n"
"buffer_load_dwordx4 v[28:31], v0, s[8:11], s7 offen offset:1024 sc1\n"
"buffer_load_dwordx4 v[216:219], v0, s[8:11], s7 offen offset:2048 sc1\n"
"s_waitcnt vmcnt(0)\n"
"v_max3_u32 v112, v208, v209, v210\n"
"v_max3_u32 v112, v112, v211, v236\n"
"v_max3_u32 v112, v112, v237, v238\n"
"v_max_u32 v112, v112, v239\n"
"v_cmp_eq_u32 vcc, -1, v112\n"
"s_cbranch_vccz Lb6_%=\n"
"s_sub_u32 s45, s45, 1\n"
"s_cbranch_scc0 Ls6_%=\n"
"s_mov_b32 s45, 0\n"
"s_branch Lb6_%=\n"
"Ls7_%=:\n"
"s_sleep 2\n"
"s_add_u32 s7, s57, 0x2cc00\n"
"buffer_load_dwordx4 v[80:83], v0, s[8:11], s7 offen offset:0 sc1\n"
"buffer_load_dwordx4 v[72:75], v0, s[8:11], s7 offen offset:1024 sc1\n"
"s_add_u32 s7, s58, 0x2cc00\n"
"buffer_load_dwordx4 v[28:31], v0, s[8:11], s7 offen offset:0 sc1\n"
"buffer_load_dwordx4 v[216:219], v0, s[8:11], s7 offen offset:1024 sc1\n"
"s_waitcnt vmcnt(0)\n"
"v_max3_u32 v196, v80, v81, v82\n"
"v_max3_u32 v196, v196, v83, v28\n"
"v_max3_u32 v196, v196, v29, v30\n"
"v_max_u32 v196, v196, v31\n"
"v_cmp_eq_u32 vcc, -1, v196\n"
"s_cbranch_vccz Lb7_%=\n"
"s_sub_u32 s45, s45, 1\n"
"s_cbranch_scc0 Ls7_%=\n"
"s_mov_b32 s45, 0\n"
"s_branch Lb7_%=\n"
"Ls8_%=:\n"
"s_sleep 2\n"
"s_add_u32 s7, s57, 0x2d000\n"
"buffer_load_dwordx4 v[72:75], v0, s[8:11], s7 offen offset:0 sc1\n"
"s_add_u32 s7, s58, 0x2d000\n"
"buffer_load_dwordx4 v[216:219], v0, s[8:11], s7 offen offset:0 sc1\n"
"s_waitcnt vmcnt(0)\n"
"v_max3_u32 v184, v72, v73, v74\n"
"v_max3_u32 v184, v184, v75, v216\n"
"v_max3_u32 v184, v184, v217, v218\n"
"v_max_u32 v184, v184, v219\n"
"v_cmp_eq_u32 vcc, -1, v184\n"
"s_cbranch_vccz Lb8_%=\n"
"s_sub_u32 s45, s45, 1\n"
"s_cbranch_scc0 Ls8_%=\n"
"s_mov_b32 s45, 0\n"
"s_branch Lb8_%=\n"
"Ls32_%=:\n"
"s_sleep 2\n"
"s_add_u32 s7, s57, 0x36000\n"
"buffer_load_dwordx4 v[172:175], v0, s[8:11], s7 offen offset:0 sc1\n"
"buffer_load_dwordx4 v[160:163], v0, s[8:11], s7 offen offset:1024 sc1\n"
"buffer_load_dwordx4 v[76:79], v0, s[8:11], s7 offen offset:2048 sc1\n"
"buffer_load_dwordx4 v[240:243], v0, s[8:11], s7 offen offset:3072 sc1\n"
"s_add_u32 s7, s57, 0x37000\n"
"buffer_load_dwordx4 v[148:151], v0, s[8:11], s7 offen offset:0 sc1\n"
"s_add_u32 s7, s58, 0x36000\n"
"buffer_load_dwordx4 v[152:155], v0, s[8:11], s7 offen offset:0 sc1\n"
"buffer_load_dwordx4 v[192:195], v0, s[8:11], s7 offen offset:1024 sc1\n"
"buffer_load_dwordx4 v[40:43], v0, s[8:11], s7 offen offset:2048 sc1\n"
"buffer_load_dwordx4 v[228:231], v0, s[8:11], s7 offen offset:3072 sc1\n"
"s_add_u32 s7, s58, 0x37000\n"
"buffer_load_dwordx4 v[212:215], v0, s[8:11], s7 offen offset:0 sc1\n"
"s_waitcnt vmcnt(0)\n"
"v_max3_u32 v88, v172, v173, v174\n"
"v_max3_u32 v88, v88, v175, v152\n"
"v_max3_u32 v88, v88, v153, v154\n"
"v_max_u32 v88, v88, v155\n"
"v_cmp_eq_u32 vcc, -1, v88\n"
"s_cbranch_vccz Lb32_%=\n"
"s_sub_u32 s45, s45, 1\n"
"s_cbranch_scc0 Ls32_%=\n"
"s_mov_b32 s45, 0\n"
"s_branch Lb32_%=\n"
"Ls33_%=:\n"
"s_sleep 2\n"
"s_add_u32 s7, s57, 0x36400\n"
"buffer_load_dwordx4 v[160:163], v0, s[8:11], s7 offen offset:0 sc1\n"
"buffer_load_dwordx4 v[76:79], v0, s[8:11], s7 offen offset:1024 sc1\n"
"buffer_load_dwordx4 v[240:243], v0, s[8:11], s7 offen offset:2048 sc1\n"
"buffer_load_dwordx4 v[148:151], v0, s[8:11], s7 offen offset:3072 sc1\n"
"s_add_u32 s7, s58, 0x36400\n"
"buffer_load_dwordx4 v[192:195], v0, s[8:11], s7 offen offset:0 sc1\n"
"buffer_load_dwordx4 v[40:43], v0, s[8:11], s7 offen offset:1024 sc1\n"
"buffer_load_dwordx4 v[228:231], v0, s[8:11], s7 offen offset:2048 sc1\n"
"buffer_load_dwordx4 v[212:215], v0, s[8:11], s7 offen offset:3072 sc1\n"
"s_waitcnt vmcnt(0)\n"
"v_max3_u32 v140, v160, v161, v162\n"
"v_max3_u32 v140, v140, v163, v192\n"
"v_max3_u32 v140, v140, v193, v194\n"
"v_max_u32 v140, v140, v195\n"
"v_cmp_eq_u32 vcc, -1, v140\n"
"s_cbranch_vccz Lb33_%=\n"
"s_sub_u32 s45, s45, 1\n"
"s_cbranch_scc0 Ls33_%=\n"
"s_mov_b32 s45, 0\n"
"s_branch Lb33_%=\n"
"Ls34_%=:\n"
"s_sleep 2\n"
"s_add_u32 s7, s57, 0x36800\n"
"buffer_load_dwordx4 v[76:79], v0, s[8:11], s7 offen offset:0 sc1\n"
"buffer_load_dwordx4 v[240:243], v0, s[8:11], s7 offen offset:1024 sc1\n"
"buffer_load_dwordx4 v[148:151], v0, s[8:11], s7 offen offset:2048 sc1\n"
"s_add_u32 s7, s58, 0x36800\n"
"buffer_load_dwordx4 v[40:43], v0, s[8:11], s7 offen offset:0 sc1\n"
"buffer_load_dwordx4 v[228:231], v0, s[8:11], s7 offen offset:1024 sc1\n"
"buffer_load_dwordx4 v[212:215], v0, s[8:11], s7 offen offset:2048 sc1\n"
"s_waitcnt vmcnt(0)\n"
"v_max3_u32 v164, v76, v77, v78\n"
"v_max3_u32 v164, v164, v79, v40\n"
"v_max3_u32 v164, v164, v41, v42\n"
"v_max_u32 v164, v164, v43\n"
"v_cmp_eq_u32 vcc, -1, v164\n"
"s_cbranch_vccz Lb34_%=\n"
"s_sub_u32 s45, s45, 1\n"
"s_cbranch_scc0 Ls34_%=\n"
"s_mov_b32 s45, 0\n"
"s_branch Lb34_%=\n"
"Ls35_%=:\n"
"s_sleep 2\n"
"s_add_u32 s7, s57, 0x36c00\n"
"buffer_load_dwordx4 v[240:243], v0, s[8:11], s7 offen offset:0 sc1\n"
"buffer_load_dwordx4 v[148:151], v0, s[8:11], s7 offen offset:1024 sc1\n"
"s_add_u32 s7, s58, 0x36c00\n"
"buffer_load_dwordx4 v[228:231], v0, s[8:11], s7 offen offset:0 sc1\n"
"buffer_load_dwordx4 v[212:215], v0, s[8:11], s7 offen offset:1024 sc1\n"
"s_waitcnt vmcnt(0)\n"
"v_max3_u32 v208, v240, v241, v242\n"
"v_max3_u32 v208, v208, v243, v228\n"
"v_max3_u32 v208, v208, v229, v230\n"
"v_max_u32 v208, v208, v231\n"
"v_cmp_eq_u32 vcc, -1, v208\n"
"s_cbranch_vccz Lb35_%=\n"
"s_sub_u32 s45, s45, 1\n"
"s_cbranch_scc0 Ls35_%=\n"
"s_mov_b32 s45, 0\n"
"s_branch Lb35_%=\n"
"Ls36_%=:\n"
"s_sleep 2\n"
"s_add_u32 s7, s57, 0x37000\n"
"buffer_load_dwordx4 v[148:151], v0, s[8:11], s7 offen offset:0 sc1\n"
"s_add_u32 s7, s58, 0x37000\n"
"buffer_load_dwordx4 v[212:215], v0, s[8:11], s7 offen offset:0 sc1\n"
"s_waitcnt vmcnt(0)\n"
"v_max3_u32 v196, v148, v149, v150\n"
"v_max3_u32 v196, v196, v151, v212\n"
"v_max3_u32 v196, v196, v213, v214\n"
"v_max_u32 v196, v196, v215\n"
"v_cmp_eq_u32 vcc, -1, v196\n"
"s_cbranch_vccz Lb36_%=\n"
"s_sub_u32 s45, s45, 1\n"
"s_cbranch_scc0 Ls36_%=\n"
"s_mov_b32 s45, 0\n"
"s_branch Lb36_%=\n"
"Ls9_%=:\n"
"s_sleep 2\n"
"s_add_u32 s7, s72, 0xa000\n"
"buffer_load_dwordx4 v[192:195], v7, s[8:11], s7 offen offset:0 sc1\n"
"s_add_u32 s7, s63, 0x0\n"
"buffer_load_dwordx4 v[100:103], v0, s[8:11], s7 offen offset:0 sc1\n"
"buffer_load_dwordx4 v[224:227], v0, s[8:11], s7 offen offset:1024 sc1\n"
"buffer_load_dwordx4 v[152:155], v0, s[8:11], s7 offen offset:2048 sc1\n"
"buffer_load_dwordx4 v[160:163], v0, s[8:11], s7 offen offset:3072 sc1\n"
"s_add_u32 s7, s63, 0x1000\n"
"buffer_load_dwordx4 v[164:167], v0, s[8:11], s7 offen offset:0 sc1\n"
"s_add_u32 s7, s63, 0x5000\n"
"buffer_load_dwordx4 v[144:147], v0, s[8:11], s7 offen offset:0 sc1\n"
"buffer_load_dwordx4 v[176:179], v0, s[8:11], s7 offen offset:1024 sc1\n"
"buffer_load_dwordx4 v[68:71], v0, s[8:11], s7 offen offset:2048 sc1\n"
"buffer_load_dwordx4 v[92:95], v0, s[8:11], s7 offen offset:3072 sc1\n"
"s_add_u32 s7, s63, 0x6000\n"
"buffer_load_dwordx4 v[48:51], v0, s[8:11], s7 offen offset:0 sc1\n"
"s_waitcnt vmcnt(0)\n"
"v_max3_u32 v44, v100, v101, v102\n"
"v_max3_u32 v44, v44, v103, v224\n"
"v_max3_u32 v44, v44, v225, v226\n"
"v_max3_u32 v44, v44, v227, v152\n"
"v_max3_u32 v44, v44, v153, v154\n"
"v_max_u32 v44, v44, v155\n"
"v_cmp_eq_u32 vcc, -1, v44\n"
"s_cbranch_vccz Lb9_%=\n"
"s_sub_u32 s45, s45, 1\n"
"s_cbranch_scc0 Ls9_%=\n"
"s_mov_b32 s45, 0\n"
"s_branch Lb9_%=\n"
"Ls10_%=:\n"
"s_sleep 2\n"
"s_add_u32 s7, s72, 0xa000\n"
"buffer_load_dwordx4 v[192:195], v7, s[8:11], s7 offen offset:0 sc1\n"
"s_add_u32 s7, s63, 0xc00\n"
"buffer_load_dwordx4 v[160:163], v0, s[8:11], s7 offen offset:0 sc1\n"
"buffer_load_dwordx4 v[164:167], v0, s[8:11], s7 offen offset:1024 sc1\n"
"s_add_u32 s7, s63, 0x5000\n"
"buffer_load_dwordx4 v[144:147], v0, s[8:11], s7 offen offset:0 sc1\n"
"buffer_load_dwordx4 v[176:179], v0, s[8:11], s7 offen offset:1024 sc1\n"
"buffer_load_dwordx4 v[68:71], v0, s[8:11], s7 offen offset:2048 sc1\n"
"buffer_load_dwordx4 v[92:95], v0, s[8:11], s7 offen offset:3072 sc1\n"
"s_add_u32 s7, s63, 0x6000\n"
"buffer_load_dwordx4 v[48:51], v0, s[8:11], s7 offen offset:0 sc1\n"
"s_waitcnt vmcnt(0)\n"
"v_max3_u32 v220, v160, v161, v162\n"
"v_max3_u32 v220, v220, v163, v164\n"
"v_max3_u32 v220, v220, v165, v166\n"
"v_max_u32 v220, v220, v167\n"
"v_cmp_eq_u32 vcc, -1, v220\n"
"s_cbranch_vccz Lb10_%=\n"
"s_sub_u32 s45, s45, 1\n"
"s_cbranch_scc0 Ls10_%=\n"
"s_mov_b32 s45, 0\n"
"s_branch Lb10_%=\n"
"Ls11_%=:\n"
"s_sleep 2\n"
"s_add_u32 s7, s72, 0xa000\n"
"buffer_load_dwordx4 v[192:195], v7, s[8:11], s7 offen offset:0 sc1\n"
"s_add_u32 s7, s63, 0x5000\n"
"buffer_load_dwordx4 v[144:147], v0, s[8:11], s7 offen offset:0 sc1\n"
"buffer_load_dwordx4 v[176:179], v0, s[8:11], s7 offen offset:1024 sc1\n"
"buffer_load_dwordx4 v[68:71], v0, s[8:11], s7 offen offset:2048 sc1\n"
"buffer_load_dwordx4 v[92:95], v0, s[8:11], s7 offen offset:3072 sc1\n"
"s_add_u32 s7, s63, 0x6000\n"
"buffer_load_dwordx4 v[48:51], v0, s[8:11], s7 offen offset:0 sc1\n"
"s_waitcnt vmcnt(0)\n"
"v_max3_u32 v36, v144, v145, v146\n"
"v_max3_u32 v36, v36, v147, v176\n"
"v_max3_u32 v36, v36, v177, v178\n"
"v_max_u32 v36, v36, v179\n"
"v_cmp_eq_u32 vcc, -1, v36\n"
"s_cbranch_vccz Lb11_%=\n"
"s_sub_u32 s45, s45, 1\n"
"s_cbranch_scc0 Ls11_%=\n"
"s_mov_b32 s45, 0\n"
"s_branch Lb11_%=\n"
"Ls12_%=:\n"
"s_sleep 2\n"
"s_add_u32 s7, s72, 0xa000\n"
"buffer_load_dwordx4 v[192:195], v7, s[8:11], s7 offen offset:0 sc1\n"
"s_add_u32 s7, s63, 0x5800\n"
"buffer_load_dwordx4 v[68:71], v0, s[8:11], s7 offen offset:0 sc1\n"
"buffer_load_dwordx4 v[92:95], v0, s[8:11], s7 offen offset:1024 sc1\n"
"buffer_load_dwordx4 v[48:51], v0, s[8:11], s7 offen offset:2048 sc1\n"
"s_waitcnt vmcnt(0)\n"
"v_max3_u32 v112, v68, v69, v70\n"
"v_max3_u32 v112, v112, v71, v92\n"
"v_max3_u32 v112, v112, v93, v94\n"
"v_max_u32 v112, v112, v95\n"
"v_cmp_eq_u32 vcc, -1, v112\n"
"s_cbranch_vccz Lb12_%=\n"
"s_sub_u32 s45, s45, 1\n"
"s_cbranch_scc0 Ls12_%=\n"
"s_mov_b32 s45, 0\n"
"s_branch Lb12_%=\n"
"Ls13_%=:\n"
"s_sleep 2\n"
"s_add_u32 s7, s72, 0xa000\n"
"buffer_load_dwordx4 v[192:195], v7, s[8:11], s7 offen offset:0 sc1\n"
"s_add_u32 s7, s63, 0x6000\n"
"buffer_load_dwordx4 v[48:51], v0, s[8:11], s7 offen offset:0 sc1\n"
"s_waitcnt vmcnt(0)\n"
"v_max3_u32 v40, v48, v49, v50\n"
"v_max3_u32 v40, v40, v51, v192\n"
"v_max3_u32 v40, v40, v193, v194\n"
"v_max_u32 v40, v40, v195\n"
"v_cmp_eq_u32 vcc, -1, v40\n"
"s_cbranch_vccz Lb13_%=\n"
"s_sub_u32 s45, s45, 1\n"
"s_cbranch_scc0 Ls13_%=\n"
"s_mov_b32 s45, 0\n"
"s_branch Lb13_%=\n"
"Ls37_%=:\n"
"s_sleep 2\n"
"s_add_u32 s7, s72, 0x15000\n"
"buffer_load_dwordx4 v[192:195], v7, s[8:11], s7 offen offset:0 sc1\n"
"s_add_u32 s7, s63, 0xb000\n"
"buffer_load_dwordx4 v[56:59], v0, s[8:11], s7 offen offset:0 sc1\n"
"buffer_load_dwordx4 v[216:219], v0, s[8:11], s7 offen offset:1024 sc1\n"
"buffer_load_dwordx4 v[172:175], v0, s[8:11], s7 offen offset:2048 sc1\n"
"buffer_load_dwordx4 v[224:227], v0, s[8:11], s7 offen offset:3072 sc1\n"
"s_add_u32 s7, s63, 0xc000\n"
"buffer_load_dwordx4 v[152:155], v0, s[8:11], s7 offen offset:0 sc1\n"
"s_add_u32 s7, s63, 0x10000\n"
"buffer_load_dwordx4 v[160:163], v0, s[8:11], s7 offen offset:0 sc1\n"
"buffer_load_dwordx4 v[164:167], v0, s[8:11], s7 offen offset:1024 sc1\n"
"buffer_load_dwordx4 v[176:179], v0, s[8:11], s7 offen offset:2048 sc1\n"
"buffer_load_dwordx4 v[32:35], v0, s[8:11], s7 offen offset:3072 sc1\n"
"s_add_u32 s7, s63, 0x11000\n"
"buffer_load_dwordx4 v[40:43], v0, s[8:11], s7 offen offset:0 sc1\n"
"s_waitcnt vmcnt(0)\n"
"v_max3_u32 v128, v56, v57, v58\n"
"v_max3_u32 v128, v128, v59, v216\n"
"v_max3_u32 v128, v128, v217, v218\n"
"v_max3_u32 v128, v128, v219, v172\n"
"v_max3_u32 v128, v128, v173, v174\n"
"v_max_u32 v128, v128, v175\n"
"v_cmp_eq_u32 vcc, -1, v128\n"
"s_cbranch_vccz Lb37_%=\n"
"s_sub_u32 s45, s45, 1\n"
"s_cbranch_scc0 Ls37_%=\n"
"s_mov_b32 s45, 0\n"
"s_branch Lb37_%=\n"
"Ls38_%=:\n"
"s_sleep 2\n"
"s_add_u32 s7, s72, 0x15000\n"
"buffer_load_dwordx4 v[192:195], v7, s[8:11], s7 offen offset:0 sc1\n"
"s_add_u32 s7, s63, 0xbc00\n"
"buffer_load_dwordx4 v[224:227], v0, s[8:11], s7 offen offset:0 sc1\n"
"buffer_load_dwordx4 v[152:155], v0, s[8:11], s7 offen offset:1024 sc1\n"
"s_add_u32 s7, s63, 0x10000\n"
"buffer_load_dwordx4 v[160:163], v0, s[8:11], s7 offen offset:0 sc1\n"
"buffer_load_dwordx4 v[164:167], v0, s[8:11], s7 offen offset:1024 sc1\n"
"buffer_load_dwordx4 v[176:179], v0, s[8:11], s7 offen offset:2048 sc1\n"
"buffer_load_dwordx4 v[32:35], v0, s[8:11], s7 offen offset:3072 sc1\n"
"s_add_u32 s7, s63, 0x11000\n"
"buffer_load_dwordx4 v[40:43], v0, s[8:11], s7 offen offset:0 sc1\n"
"s_waitcnt vmcnt(0)\n"
"v_max3_u32 v200, v224, v225, v226\n"
"v_max3_u32 v200, v200, v227, v152\n"
"v_max3_u32 v200, v200, v153, v154\n"
"v_max_u32 v200, v200, v155\n"
"v_cmp_eq_u32 vcc, -1, v200\n"
"s_cbranch_vccz Lb38_%=\n"
"s_sub_u32 s45, s45, 1\n"
"s_cbranch_scc0 Ls38_%=\n"
"s_mov_b32 s45, 0\n"
"s_branch Lb38_%=\n"
"Ls39_%=:\n"
"s_sleep 2\n"
"s_add_u32 s7, s72, 0x15000\n"
"buffer_load_dwordx4 v[192:195], v7, s[8:11], s7 offen offset:0 sc1\n"
"s_add_u32 s7, s63, 0x10000\n"
"buffer_load_dwordx4 v[160:163], v0, s[8:11], s7 offen offset:0 sc1\n"
"buffer_load_dwordx4 v[164:167], v0, s[8:11], s7 offen offset:1024 sc1\n"
"buffer_load_dwordx4 v[176:179], v0, s[8:11], s7 offen offset:2048 sc1\n"
"buffer_load_dwordx4 v[32:35], v0, s[8:11], s7 offen offset:3072 sc1\n"
"s_add_u32 s7, s63, 0x11000\n"
"buffer_load_dwordx4 v[40:43], v0, s[8:11], s7 offen offset:0 sc1\n"
"s_waitcnt vmcnt(0)\n"
"v_max3_u32 v212, v160, v161, v162\n"
"v_max3_u32 v212, v212, v163, v164\n"
"v_max3_u32 v212, v212, v165, v166\n"
"v_max_u32 v212, v212, v167\n"
"v_cmp_eq_u32 vcc, -1, v212\n"
"s_cbranch_vccz Lb39_%=\n"
"s_sub_u32 s45, s45, 1\n"
"s_cbranch_scc0 Ls39_%=\n"
"s_mov_b32 s45, 0\n"
"s_branch Lb39_%=\n"
"Ls40_%=:\n"
"s_sleep 2\n"
"s_add_u32 s7, s72, 0x15000\n"
"buffer_load_dwordx4 v[192:195], v7, s[8:11], s7 offen offset:0 sc1\n"
"s_add_u32 s7, s63, 0x10800\n"
"buffer_load_dwordx4 v[176:179], v0, s[8:11], s7 offen offset:0 sc1\n"
"buffer_load_dwordx4 v[32:35], v0, s[8:11], s7 offen offset:1024 sc1\n"
"buffer_load_dwordx4 v[40:43], v0, s[8:11], s7 offen offset:2048 sc1\n"
"s_waitcnt vmcnt(0)\n"
"v_max3_u32 v84, v176, v177, v178\n"
"v_max3_u32 v84, v84, v179, v32\n"
"v_max3_u32 v84, v84, v33, v34\n"
"v_max_u32 v84, v84, v35\n"
"v_cmp_eq_u32 vcc, -1, v84\n"
"s_cbranch_vccz Lb40_%=\n"
"s_sub_u32 s45, s45, 1\n"
"s_cbranch_scc0 Ls40_%=\n"
"s_mov_b32 s45, 0\n"
"s_branch Lb40_%=\n"
"Ls41_%=:\n"
"s_sleep 2\n"
"s_add_u32 s7, s72, 0x15000\n"
"buffer_load_dwordx4 v[192:195], v7, s[8:11], s7 offen offset:0 sc1\n"
"s_add_u32 s7, s63, 0x11000\n"
"buffer_load_dwordx4 v[40:43], v0, s[8:11], s7 offen offset:0 sc1\n"
"s_waitcnt vmcnt(0)\n"
"v_max3_u32 v136, v40, v41, v42\n"
"v_max3_u32 v136, v136, v43, v192\n"
"v_max3_u32 v136, v136, v193, v194\n"
"v_max_u32 v136, v136, v195\n"
"v_cmp_eq_u32 vcc, -1, v136\n"
"s_cbranch_vccz Lb41_%=\n"
"s_sub_u32 s45, s45, 1\n"
"s_cbranch_scc0 Ls41_%=\n"
"s_mov_b32 s45, 0\n"
"s_branch Lb41_%=\n"
"Ls3_%=:\n"
"s_sleep 2\n"
"buffer_load_dwordx2 v[144:145], v18, s[8:11], s39 offen sc1\n"
"s_waitcnt vmcnt(0)\n"
"v_cmp_ne_u64 vcc, s[88:89], v[144:145]\n"
"s_cbranch_vccz Lb3_%=\n"
"s_sub_u32 s45, s45, 1\n"
"s_cbranch_scc0 Ls3_%=\n"
"s_mov_b32 s45, 0\n"
"s_branch Lb3_%=\n"
"Ls14_%=:\n"
"s_sleep 2\n"
"s_bitcmp1_b32 s61, 0\n"
"s_cselect_b32 s7, s60, s59\n"
"s_add_u32 s7, s7, 0x6a000\n"
"buffer_load_dwordx4 v[124:127], v16, s[8:11], s7 offen sc1\n"
"buffer_load_dwordx4 v[112:115], v17, s[8:11], s7 offen sc1\n"
"s_bitcmp1_b32 s61, 1\n"
"s_cselect_b32 s7, s60, s59\n"
"s_add_u32 s7, s7, 0x6a400\n"
"buffer_load_dwordx4 v[96:99], v16, s[8:11], s7 offen sc1\n"
"buffer_load_dwordx4 v[72:75], v17, s[8:11], s7 offen sc1\n"
"s_bitcmp1_b32 s61, 2\n"
"s_cselect_b32 s7, s60, s59\n"
"s_add_u32 s7, s7, 0x6a800\n"
"buffer_load_dwordx4 v[216:219], v16, s[8:11], s7 offen sc1\n"
"buffer_load_dwordx4 v[240:243], v17, s[8:11], s7 offen sc1\n"
"s_bitcmp1_b32 s61, 3\n"
"s_cselect_b32 s7, s60, s59\n"
"s_add_u32 s7, s7, 0x6ac00\n"
"buffer_load_dwordx4 v[80:83], v16, s[8:11], s7 offen sc1\n"
"buffer_load_dwordx4 v[228:231], v17, s[8:11], s7 offen sc1\n"
"s_bitcmp1_b32 s61, 4\n"
"s_cselect_b32 s7, s60, s59\n"
"s_add_u32 s7, s7, 0x6b000\n"
"buffer_load_dwordx4 v[200:203], v16, s[8:11], s7 offen sc1\n"
"buffer_load_dwordx4 v[148:151], v17, s[8:11], s7 offen sc1\n"
"s_bitcmp1_b32 s61, 5\n"
"s_cselect_b32 s7, s60, s59\n"
"s_add_u32 s7, s7, 0x6b400\n"
"buffer_load_dwordx4 v[36:39], v16, s[8:11], s7 offen sc1\n"
"buffer_load_dwordx4 v[204:207], v17, s[8:11], s7 offen sc1\n"
"s_waitcnt vmcnt(0)\n"
"v_max3_u32 v120, v124, v125, v126\n"
"v_max3_u32 v120, v120, v127, v112\n"
"v_max3_u32 v120, v120, v113, v114\n"
"v_max_u32 v120, v120, v115\n"
"v_cmp_eq_u32 vcc, -1, v120\n"
"s_cbranch_vccz Lb14_%=\n"
"s_sub_u32 s45, s45, 1\n"
"s_cbranch_scc0 Ls14_%=\n"
"s_mov_b32 s45, 0\n"
"s_branch Lb14_%=\n"
"Ls15_%=:\n"
"s_sleep 2\n"
"s_bitcmp1_b32 s61, 1\n"
"s_cselect_b32 s7, s60, s59\n"
"s_add_u32 s7, s7, 0x6a400\n"
"buffer_load_dwordx4 v[96:99], v16, s[8:11], s7 offen sc1\n"
"buffer_load_dwordx4 v[72:75], v17, s[8:11], s7 offen sc1\n"
"s_bitcmp1_b32 s61, 2\n"
"s_cselect_b32 s7, s60, s59\n"
"s_add_u32 s7, s7, 0x6a800\n"
"buffer_load_dwordx4 v[216:219], v16, s[8:11], s7 offen sc1\n"
"buffer_load_dwordx4 v[240:243], v17, s[8:11], s7 offen sc1\n"
"s_bitcmp1_b32 s61, 3\n"
"s_cselect_b32 s7, s60, s59\n"
"s_add_u32 s7, s7, 0x6ac00\n"
"buffer_load_dwordx4 v[80:83], v16, s[8:11], s7 offen sc1\n"
"buffer_load_dwordx4 v[228:231], v17, s[8:11], s7 offen sc1\n"
"s_bitcmp1_b32 s61, 4\n"
"s_cselect_b32 s7, s60, s59\n"
"s_add_u32 s7, s7, 0x6b000\n"
"buffer_load_dwordx4 v[200:203], v16, s[8:11], s7 offen sc1\n"
"buffer_load_dwordx4 v[148:151], v17, s[8:11], s7 offen sc1\n"
"s_bitcmp1_b32 s61, 5\n"
"s_cselect_b32 s7, s60, s59\n"
"s_add_u32 s7, s7, 0x6b400\n"
"buffer_load_dwordx4 v[36:39], v16, s[8:11], s7 offen sc1\n"
"buffer_load_dwordx4 v[204:207], v17, s[8:11], s7 offen sc1\n"
"s_bitcmp1_b32 s61, 6\n"
"s_cselect_b32 s7, s60, s59\n"
"s_add_u32 s7, s7, 0x6b800\n"
"buffer_load_dwordx4 v[28:31], v16, s[8:11], s7 offen sc1\n"
"s_waitcnt vmcnt(0)\n"
"v_max3_u32 v64, v96, v97, v98\n"
"v_max3_u32 v64, v64, v99, v72\n"
"v_max3_u32 v64, v64, v73, v74\n"
"v_max_u32 v64, v64, v75\n"
"v_cmp_eq_u32 vcc, -1, v64\n"
"s_cbranch_vccz Lb15_%=\n"
"s_sub_u32 s45, s45, 1\n"
"s_cbranch_scc0 Ls15_%=\n"
"s_mov_b32 s45, 0\n"
"s_branch Lb15_%=\n"
"Ls16_%=:\n"
"s_sleep 2\n"
"s_bitcmp1_b32 s61, 2\n"
"s_cselect_b32 s7, s60, s59\n"
"s_add_u32 s7, s7, 0x6a800\n"
"buffer_load_dwordx4 v[216:219], v16, s[8:11], s7 offen sc1\n"
"buffer_load_dwordx4 v[240:243], v17, s[8:11], s7 offen sc1\n"
"s_bitcmp1_b32 s61, 3\n"
"s_cselect_b32 s7, s60, s59\n"
"s_add_u32 s7, s7, 0x6ac00\n"
"buffer_load_dwordx4 v[80:83], v16, s[8:11], s7 offen sc1\n"
"buffer_load_dwordx4 v[228:231], v17, s[8:11], s7 offen sc1\n"
"s_bitcmp1_b32 s61, 4\n"
"s_cselect_b32 s7, s60, s59\n"
"s_add_u32 s7, s7, 0x6b000\n"
"buffer_load_dwordx4 v[200:203], v16, s[8:11], s7 offen sc1\n"
"buffer_load_dwordx4 v[148:151], v17, s[8:11], s7 offen sc1\n"
"s_bitcmp1_b32 s61, 5\n"
"s_cselect_b32 s7, s60, s59\n"
"s_add_u32 s7, s7, 0x6b400\n"
"buffer_load_dwordx4 v[36:39], v16, s[8:11], s7 offen sc1\n"
"buffer_load_dwordx4 v[204:207], v17, s[8:11], s7 offen sc1\n"
"s_bitcmp1_b32 s61, 6\n"
"s_cselect_b32 s7, s60, s59\n"
"s_add_u32 s7, s7, 0x6b800\n"
"buffer_load_dwordx4 v[28:31], v16, s[8:11], s7 offen sc1\n"
"buffer_load_dwordx4 v[248:251], v17, s[8:11], s7 offen sc1\n"
"s_bitcmp1_b32 s61, 7\n"
"s_cselect_b32 s7, s60, s59\n"
"s_add_u32 s7, s7, 0x6bc00\n"
"buffer_load_dwordx4 v[116:119], v16, s[8:11], s7 offen sc1\n"
"s_waitcnt vmcnt(0)\n"
"v_max3_u32 v168, v216, v217, v218\n"
"v_max3_u32 v168, v168, v219, v240\n"
"v_max3_u32 v168, v168, v241, v242\n"
"v_max_u32 v168, v168, v243\n"
"v_cmp_eq_u32 vcc, -1, v168\n"
"s_cbranch_vccz Lb16_%=\n"
"s_sub_u32 s45, s45, 1\n"
"s_cbranch_scc0 Ls16_%=\n"
"s_mov_b32 s45, 0\n"
"s_branch Lb16_%=\n"
"Ls17_%=:\n"
"s_sleep 2\n"
"s_bitcmp1_b32 s61, 3\n"
"s_cselect_b32 s7, s60, s59\n"
"s_add_u32 s7, s7, 0x6ac00\n"
"buffer_load_dwordx4 v[80:83], v16, s[8:11], s7 offen sc1\n"
"buffer_load_dwordx4 v[228:231], v17, s[8:11], s7 offen sc1\n"
"s_bitcmp1_b32 s61, 4\n"
"s_cselect_b32 s7, s60, s59\n"
"s_add_u32 s7, s7, 0x6b000\n"
"buffer_load_dwordx4 v[200:203], v16, s[8:11], s7 offen sc1\n"
"buffer_load_dwordx4 v[148:151], v17, s[8:11], s7 offen sc1\n"
"s_bitcmp1_b32 s61, 5\n"
"s_cselect_b32 s7, s60, s59\n"
"s_add_u32 s7, s7, 0x6b400\n"
"buffer_load_dwordx4 v[36:39], v16, s[8:11], s7 offen sc1\n"
"buffer_load_dwordx4 v[204:207], v17, s[8:11], s7 offen sc1\n"
"s_bitcmp1_b32 s61, 6\n"
"s_cselect_b32 s7, s60, s59\n"
"s_add_u32 s7, s7, 0x6b800\n"
"buffer_load_dwordx4 v[28:31], v16, s[8:11], s7 offen sc1\n"
"buffer_load_dwordx4 v[248:251], v17, s[8:11], s7 offen sc1\n"
"s_bitcmp1_b32 s61, 7\n"
"s_cselect_b32 s7, s60, s59\n"
"s_add_u32 s7, s7, 0x6bc00\n"
"buffer_load_dwordx4 v[116:119], v16, s[8:11], s7 offen sc1\n"
"buffer_load_dwordx4 v[76:79], v17, s[8:11], s7 offen sc1\n"
"s_waitcnt vmcnt(0)\n"
"v_max3_u32 v68, v80, v81, v82\n"
"v_max3_u32 v68, v68, v83, v228\n"
"v_max3_u32 v68, v68, v229, v230\n"
"v_max_u32 v68, v68, v231\n"
"v_cmp_eq_u32 vcc, -1, v68\n"
"s_cbranch_vccz Lb17_%=\n"
"s_sub_u32 s45, s45, 1\n"
"s_cbranch_scc0 Ls17_%=\n"
"s_mov_b32 s45, 0\n"
"s_branch Lb17_%=\n"
"Ls18_%=:\n"
"s_sleep 2\n"
"s_bitcmp1_b32 s61, 4\n"
"s_cselect_b32 s7, s60, s59\n"
"s_add_u32 s7, s7, 0x6b000\n"
"buffer_load_dwordx4 v[200:203], v16, s[8:11], s7 offen sc1\n"
"buffer_load_dwordx4 v[148:151], v17, s[8:11], s7 offen sc1\n"
"s_bitcmp1_b32 s61, 5\n"
"s_cselect_b32 s7, s60, s59\n"
"s_add_u32 s7, s7, 0x6b400\n"
"buffer_load_dwordx4 v[36:39], v16, s[8:11], s7 offen sc1\n"
"buffer_load_dwordx4 v[204:207], v17, s[8:11], s7 offen sc1\n"
"s_bitcmp1_b32 s61, 6\n"
"s_cselect_b32 s7, s60, s59\n"
"s_add_u32 s7, s7, 0x6b800\n"
"buffer_load_dwordx4 v[28:31], v16, s[8:11], s7 offen sc1\n"
"buffer_load_dwordx4 v[248:251], v17, s[8:11], s7 offen sc1\n"
"s_bitcmp1_b32 s61, 7\n"
"s_cselect_b32 s7, s60, s59\n"
"s_add_u32 s7, s7, 0x6bc00\n"
"buffer_load_dwordx4 v[116:119], v16, s[8:11], s7 offen sc1\n"
"buffer_load_dwordx4 v[76:79], v17, s[8:11], s7 offen sc1\n"
"s_bitcmp1_b32 s61, 8\n"
"s_cselect_b32 s7, s60, s59\n"
"s_add_u32 s7, s7, 0x6c000\n"
"buffer_load_dwordx4 v[184:187], v16, s[8:11], s7 offen sc1\n"
"s_waitcnt vmcnt(0)\n"
"v_max3_u32 v48, v200, v201, v202\n"
"v_max3_u32 v48, v48, v203, v148\n"
"v_max3_u32 v48, v48, v149, v150\n"
"v_max_u32 v48, v48, v151\n"
"v_cmp_eq_u32 vcc, -1, v48\n"
"s_cbranch_vccz Lb18_%=\n"
"s_sub_u32 s45, s45, 1\n"
"s_cbranch_scc0 Ls18_%=\n"
"s_mov_b32 s45, 0\n"
"s_branch Lb18_%=\n"
"Ls19_%=:\n"
"s_sleep 2\n"
"s_bitcmp1_b32 s61, 5\n"
"s_cselect_b32 s7, s60, s59\n"
"s_add_u32 s7, s7, 0x6b400\n"
"buffer_load_dwordx4 v[36:39], v16, s[8:11], s7 offen sc1\n"
"buffer_load_dwordx4 v[204:207], v17, s[8:11], s7 offen sc1\n"
"s_bitcmp1_b32 s61, 6\n"
"s_cselect_b32 s7, s60, s59\n"
"s_add_u32 s7, s7, 0x6b800\n"
"buffer_load_dwordx4 v[28:31], v16, s[8:11], s7 offen sc1\n"
"buffer_load_dwordx4 v[248:251], v17, s[8:11], s7 offen sc1\n"
"s_bitcmp1_b32 s61, 7\n"
"s_cselect_b32 s7, s60, s59\n"
"s_add_u32 s7, s7, 0x6bc00\n"
"buffer_load_dwordx4 v[116:119], v16, s[8:11], s7 offen sc1\n"
"buffer_load_dwordx4 v[76:79], v17, s[8:11], s7 offen sc1\n"
"s_bitcmp1_b32 s61, 8\n"
"s_cselect_b32 s7, s60, s59\n"
"s_add_u32 s7, s7, 0x6c000\n"
"buffer_load_dwordx4 v[184:187], v16, s[8:11], s7 offen sc1\n"
"buffer_load_dwordx4 v[92:95], v17, s[8:11], s7 offen sc1\n"
"s_bitcmp1_b32 s61, 9\n"
"s_cselect_b32 s7, s60, s59\n"
"s_add_u32 s7, s7, 0x6c400\n"
"buffer_load_dwordx4 v[160:163], v16, s[8:11], s7 offen sc1\n"
"s_waitcnt vmcnt(0)\n"
"v_max3_u32 v224, v36, v37, v38\n"
"v_max3_u32 v224, v224, v39, v204\n"
"v_max3_u32 v224, v224, v205, v206\n"
"v_max_u32 v224, v224, v207\n"
"v_cmp_eq_u32 vcc, -1, v224\n"
"s_cbranch_vccz Lb19_%=\n"
"s_sub_u32 s45, s45, 1\n"
"s_cbranch_scc0 Ls19_%=\n"
"s_mov_b32 s45, 0\n"
"s_branch Lb19_%=\n"
"Ls20_%=:\n"
"s_sleep 2\n"
"s_bitcmp1_b32 s61, 6\n"
"s_cselect_b32 s7, s60, s59\n"
"s_add_u32 s7, s7, 0x6b800\n"
"buffer_load_dwordx4 v[28:31], v16, s[8:11], s7 offen sc1\n"
"buffer_load_dwordx4 v[248:251], v17, s[8:11], s7 offen sc1\n"
"s_bitcmp1_b32 s61, 7\n"
"s_cselect_b32 s7, s60, s59\n"
"s_add_u32 s7, s7, 0x6bc00\n"
"buffer_load_dwordx4 v[116:119], v16, s[8:11], s7 offen sc1\n"
"buffer_load_dwordx4 v[76:79], v17, s[8:11], s7 offen sc1\n"
"s_bitcmp1_b32 s61, 8\n"
"s_cselect_b32 s7, s60, s59\n"
"s_add_u32 s7, s7, 0x6c000\n"
"buffer_load_dwordx4 v[184:187], v16, s[8:11], s7 offen sc1\n"
"buffer_load_dwordx4 v[92:95], v17, s[8:11], s7 offen sc1\n"
"s_bitcmp1_b32 s61, 9\n"
"s_cselect_b32 s7, s60, s59\n"
"s_add_u32 s7, s7, 0x6c400\n"
"buffer_load_dwordx4 v[160:163], v16, s[8:11], s7 offen sc1\n"
"buffer_load_dwordx4 v[136:139], v17, s[8:11], s7 offen sc1\n"
"s_bitcmp1_b32 s61, 10\n"
"s_cselect_b32 s7, s60, s59\n"
"s_add_u32 s7, s7, 0x6c800\n"
"buffer_load_dwordx4 v[88:91], v16, s[8:11], s7 offen sc1\n"
"buffer_load_dwordx4 v[120:123], v17, s[8:11], s7 offen sc1\n"
"s_waitcnt vmcnt(0)\n"
"v_max3_u32 v188, v28, v29, v30\n"
"v_max3_u32 v188, v188, v31, v248\n"
"v_max3_u32 v188, v188, v249, v250\n"
"v_max_u32 v188, v188, v251\n"
"v_cmp_eq_u32 vcc, -1, v188\n"
"s_cbranch_vccz Lb20_%=\n"
"s_sub_u32 s45, s45, 1\n"
"s_cbranch_scc0 Ls20_%=\n"
"s_mov_b32 s45, 0\n"
"s_branch Lb20_%=\n"
"Ls21_%=:\n"
"s_sleep 2\n"
"s_bitcmp1_b32 s61, 7\n"
"s_cselect_b32 s7, s60, s59\n"
"s_add_u32 s7, s7, 0x6bc00\n"
"buffer_load_dwordx4 v[116:119], v16, s[8:11], s7 offen sc1\n"
"buffer_load_dwordx4 v[76:79], v17, s[8:11], s7 offen sc1\n"
"s_bitcmp1_b32 s61, 8\n"
"s_cselect_b32 s7, s60, s59\n"
"s_add_u32 s7, s7, 0x6c000\n"
"buffer_load_dwordx4 v[184:187], v16, s[8:11], s7 offen sc1\n"
"buffer_load_dwordx4 v[92:95], v17, s[8:11], s7 offen sc1\n"
"s_bitcmp1_b32 s61, 9\n"
"s_cselect_b32 s7, s60, s59\n"
"s_add_u32 s7, s7, 0x6c400\n"
"buffer_load_dwordx4 v[160:163], v16, s[8:11], s7 offen sc1\n"
"buffer_load_dwordx4 v[136:139], v17, s[8:11], s7 offen sc1\n"
"s_bitcmp1_b32 s61, 10\n"
"s_cselect_b32 s7, s60, s59\n"
"s_add_u32 s7, s7, 0x6c800\n"
"buffer_load_dwordx4 v[88:91], v16, s[8:11], s7 offen sc1\n"
"buffer_load_dwordx4 v[120:123], v17, s[8:11], s7 offen sc1\n"
"s_bitcmp1_b32 s61, 11\n"
"s_cselect_b32 s7, s60, s59\n"
"s_add_u32 s7, s7, 0x6cc00\n"
"buffer_load_dwordx4 v[164:167], v16, s[8:11], s7 offen sc1\n"
"buffer_load_dwordx4 v[124:127], v17, s[8:11], s7 offen sc1\n"
"s_waitcnt vmcnt(0)\n"
"v_max3_u32 v180, v116, v117, v118\n"
"v_max3_u32 v180, v180, v119, v76\n"
"v_max3_u32 v180, v180, v77, v78\n"
"v_max_u32 v180, v180, v79\n"
"v_cmp_eq_u32 vcc, -1, v180\n"
"s_cbranch_vccz Lb21_%=\n"
"s_sub_u32 s45, s45, 1\n"
"s_cbranch_scc0 Ls21_%=\n"
"s_mov_b32 s45, 0\n"
"s_branch Lb21_%=\n"
"Ls22_%=:\n"
"s_sleep 2\n"
"s_bitcmp1_b32 s61, 8\n"
"s_cselect_b32 s7, s60, s59\n"
"s_add_u32 s7, s7, 0x6c000\n"
"buffer_load_dwordx4 v[184:187], v16, s[8:11], s7 offen sc1\n"
"buffer_load_dwordx4 v[92:95], v17, s[8:11], s7 offen sc1\n"
"s_bitcmp1_b32 s61, 9\n"
"s_cselect_b32 s7, s60, s59\n"
"s_add_u32 s7, s7, 0x6c400\n"
"buffer_load_dwordx4 v[160:163], v16, s[8:11], s7 offen sc1\n"
"buffer_load_dwordx4 v[136:139], v17, s[8:11], s7 offen sc1\n"
"s_bitcmp1_b32 s61, 10\n"
"s_cselect_b32 s7, s60, s59\n"
"s_add_u32 s7, s7, 0x6c800\n"
"buffer_load_dwordx4 v[88:91], v16, s[8:11], s7 offen sc1\n"
"buffer_load_dwordx4 v[120:123], v17, s[8:11], s7 offen sc1\n"
"s_bitcmp1_b32 s61, 11\n"
"s_cselect_b32 s7, s60, s59\n"
"s_add_u32 s7, s7, 0x6cc00\n"
"buffer_load_dwordx4 v[164:167], v16, s[8:11], s7 offen sc1\n"
"buffer_load_dwordx4 v[124:127], v17, s[8:11], s7 offen sc1\n"
"s_waitcnt vmcnt(0)\n"
"v_max3_u32 v64, v184, v185, v186\n"
"v_max3_u32 v64, v64, v187, v92\n"
"v_max3_u32 v64, v64, v93, v94\n"
"v_max_u32 v64, v64, v95\n"
"v_cmp_eq_u32 vcc, -1, v64\n"
"s_cbranch_vccz Lb22_%=\n"
"s_sub_u32 s45, s45, 1\n"
"s_cbranch_scc0 Ls22_%=\n"
"s_mov_b32 s45, 0\n"
"s_branch Lb22_%=\n"
"Ls23_%=:\n"
"s_sleep 2\n"
"s_bitcmp1_b32 s61, 9\n"
"s_cselect_b32 s7, s60, s59\n"
"s_add_u32 s7, s7, 0x6c400\n"
"buffer_load_dwordx4 v[160:163], v16, s[8:11], s7 offen sc1\n"
"buffer_load_dwordx4 v[136:139], v17, s[8:11], s7 offen sc1\n"
"s_bitcmp1_b32 s61, 10\n"
"s_cselect_b32 s7, s60, s59\n"
"s_add_u32 s7, s7, 0x6c800\n"
"buffer_load_dwordx4 v[88:91], v16, s[8:11], s7 offen sc1\n"
"buffer_load_dwordx4 v[120:123], v17, s[8:11], s7 offen sc1\n"
"s_bitcmp1_b32 s61, 11\n"
"s_cselect_b32 s7, s60, s59\n"
"s_add_u32 s7, s7, 0x6cc00\n"
"buffer_load_dwordx4 v[164:167], v16, s[8:11], s7 offen sc1\n"
"buffer_load_dwordx4 v[124:127], v17, s[8:11], s7 offen sc1\n"
"s_waitcnt vmcnt(0)\n"
"v_max3_u32 v112, v160, v161, v162\n"
"v_max3_u32 v112, v112, v163, v136\n"
"v_max3_u32 v112, v112, v137, v138\n"
"v_max_u32 v112, v112, v139\n"
"v_cmp_eq_u32 vcc, -1, v112\n"
"s_cbranch_vccz Lb23_%=\n"
"s_sub_u32 s45, s45, 1\n"
"s_cbranch_scc0 Ls23_%=\n"
"s_mov_b32 s45, 0\n"
"s_branch Lb23_%=\n"
"Ls24_%=:\n"
"s_sleep 2\n"
"s_bitcmp1_b32 s61, 10\n"
"s_cselect_b32 s7, s60, s59\n"
"s_add_u32 s7, s7, 0x6c800\n"
"buffer_load_dwordx4 v[88:91], v16, s[8:11], s7 offen sc1\n"
"buffer_load_dwordx4 v[120:123], v17, s[8:11], s7 offen sc1\n"
"s_bitcmp1_b32 s61, 11\n"
"s_cselect_b32 s7, s60, s59\n"
"s_add_u32 s7, s7, 0x6cc00\n"
"buffer_load_dwordx4 v[164:167], v16, s[8:11], s7 offen sc1\n"
"buffer_load_dwordx4 v[124:127], v17, s[8:11], s7 offen sc1\n"
"s_waitcnt vmcnt(0)\n"
"v_max3_u32 v40, v88, v89, v90\n"
"v_max3_u32 v40, v40, v91, v120\n"
"v_max3_u32 v40, v40, v121, v122\n"
"v_max_u32 v40, v40, v123\n"
"v_cmp_eq_u32 vcc, -1, v40\n"
"s_cbranch_vccz Lb24_%=\n"
"s_sub_u32 s45, s45, 1\n"
"s_cbranch_scc0 Ls24_%=\n"
"s_mov_b32 s45, 0\n"
"s_branch Lb24_%=\n"
"Ls25_%=:\n"
"s_sleep 2\n"
"s_bitcmp1_b32 s61, 11\n"
"s_cselect_b32 s7, s60, s59\n"
"s_add_u32 s7, s7, 0x6cc00\n"
"buffer_load_dwordx4 v[164:167], v16, s[8:11], s7 offen sc1\n"
"buffer_load_dwordx4 v[124:127], v17, s[8:11], s7 offen sc1\n"
"s_waitcnt vmcnt(0)\n"
"v_max3_u32 v80, v164, v165, v166\n"
"v_max3_u32 v80, v80, v167, v124\n"
"v_max3_u32 v80, v80, v125, v126\n"
"v_max_u32 v80, v80, v127\n"
"v_cmp_eq_u32 vcc, -1, v80\n"
"s_cbranch_vccz Lb25_%=\n"
"s_sub_u32 s45, s45, 1\n"
"s_cbranch_scc0 Ls25_%=\n"
"s_mov_b32 s45, 0\n"
"s_branch Lb25_%=\n"
"Ls42_%=:\n"
"s_sleep 2\n"
"s_bitcmp1_b32 s61, 0\n"
"s_cselect_b32 s7, s60, s59\n"
"s_add_u32 s7, s7, 0x85000\n"
"buffer_load_dwordx4 v[216:219], v16, s[8:11], s7 offen sc1\n"
"buffer_load_dwordx4 v[100:103], v17, s[8:11], s7 offen sc1\n"
"s_bitcmp1_b32 s61, 1\n"
"s_cselect_b32 s7, s60, s59\n"
"s_add_u32 s7, s7, 0x85400\n"
"buffer_load_dwordx4 v[240:243], v16, s[8:11], s7 offen sc1\n"
"buffer_load_dwordx4 v[48:51], v17, s[8:11], s7 offen sc1\n"
"s_bitcmp1_b32 s61, 2\n"
"s_cselect_b32 s7, s60, s59\n"
"s_add_u32 s7, s7, 0x85800\n"
"buffer_load_dwordx4 v[224:227], v16, s[8:11], s7 offen sc1\n"
"buffer_load_dwordx4 v[228:231], v17, s[8:11], s7 offen sc1\n"
"s_bitcmp1_b32 s61, 3\n"
"s_cselect_b32 s7, s60, s59\n"
"s_add_u32 s7, s7, 0x85c00\n"
"buffer_load_dwordx4 v[188:191], v16, s[8:11], s7 offen sc1\n"
"buffer_load_dwordx4 v[204:207], v17, s[8:11], s7 offen sc1\n"
"s_bitcmp1_b32 s61, 4\n"
"s_cselect_b32 s7, s60, s59\n"
"s_add_u32 s7, s7, 0x86000\n"
"buffer_load_dwordx4 v[180:183], v16, s[8:11], s7 offen sc1\n"
"buffer_load_dwordx4 v[28:31], v17, s[8:11], s7 offen sc1\n"
"s_bitcmp1_b32 s61, 5\n"
"s_cselect_b32 s7, s60, s59\n"
"s_add_u32 s7, s7, 0x86400\n"
"buffer_load_dwordx4 v[248:251], v16, s[8:11], s7 offen sc1\n"
"buffer_load_dwordx4 v[116:119], v17, s[8:11], s7 offen sc1\n"
"s_waitcnt vmcnt(0)\n"
"v_max3_u32 v64, v216, v217, v218\n"
"v_max3_u32 v64, v64, v219, v100\n"
"v_max3_u32 v64, v64, v101, v102\n"
"v_max_u32 v64, v64, v103\n"
"v_cmp_eq_u32 vcc, -1, v64\n"
"s_cbranch_vccz Lb42_%=\n"
"s_sub_u32 s45, s45, 1\n"
"s_cbranch_scc0 Ls42_%=\n"
"s_mov_b32 s45, 0\n"
"s_branch Lb42_%=\n"
"Ls43_%=:\n"
"s_sleep 2\n"
"s_bitcmp1_b32 s61, 1\n"
"s_cselect_b32 s7, s60, s59\n"
"s_add_u32 s7, s7, 0x85400\n"
"buffer_load_dwordx4 v[240:243], v16, s[8:11], s7 offen sc1\n"
"buffer_load_dwordx4 v[48:51], v17, s[8:11], s7 offen sc1\n"
"s_bitcmp1_b32 s61, 2\n"
"s_cselect_b32 s7, s60, s59\n"
"s_add_u32 s7, s7, 0x85800\n"
"buffer_load_dwordx4 v[224:227], v16, s[8:11], s7 offen sc1\n"
"buffer_load_dwordx4 v[228:231], v17, s[8:11], s7 offen sc1\n"
"s_bitcmp1_b32 s61, 3\n"
"s_cselect_b32 s7, s60, s59\n"
"s_add_u32 s7, s7, 0x85c00\n"
"buffer_load_dwordx4 v[188:191], v16, s[8:11], s7 offen sc1\n"
"buffer_load_dwordx4 v[204:207], v17, s[8:11], s7 offen sc1\n"
"s_bitcmp1_b32 s61, 4\n"
"s_cselect_b32 s7, s60, s59\n"
"s_add_u32 s7, s7, 0x86000\n"
"buffer_load_dwordx4 v[180:183], v16, s[8:11], s7 offen sc1\n"
"buffer_load_dwordx4 v[28:31], v17, s[8:11], s7 offen sc1\n"
"s_bitcmp1_b32 s61, 5\n"
"s_cselect_b32 s7, s60, s59\n"
"s_add_u32 s7, s7, 0x86400\n"
"buffer_load_dwordx4 v[248:251], v16, s[8:11], s7 offen sc1\n"
"buffer_load_dwordx4 v[116:119], v17, s[8:11], s7 offen sc1\n"
"s_bitcmp1_b32 s61, 6\n"
"s_cselect_b32 s7, s60, s59\n"
"s_add_u32 s7, s7, 0x86800\n"
"buffer_load_dwordx4 v[184:187], v16, s[8:11], s7 offen sc1\n"
"buffer_load_dwordx4 v[88:91], v17, s[8:11], s7 offen sc1\n"
"s_waitcnt vmcnt(0)\n"
"v_max3_u32 v76, v240, v241, v242\n"
"v_max3_u32 v76, v76, v243, v48\n"
"v_max3_u32 v76, v76, v49, v50\n"
"v_max_u32 v76, v76, v51\n"
"v_cmp_eq_u32 vcc, -1, v76\n"
"s_cbranch_vccz Lb43_%=\n"
"s_sub_u32 s45, s45, 1\n"
"s_cbranch_scc0 Ls43_%=\n"
"s_mov_b32 s45, 0\n"
"s_branch Lb43_%=\n"
"Ls44_%=:\n"
"s_sleep 2\n"
"s_bitcmp1_b32 s61, 2\n"
"s_cselect_b32 s7, s60, s59\n"
"s_add_u32 s7, s7, 0x85800\n"
"buffer_load_dwordx4 v[224:227], v16, s[8:11], s7 offen sc1\n"
"buffer_load_dwordx4 v[228:231], v17, s[8:11], s7 offen sc1\n"
"s_bitcmp1_b32 s61, 3\n"
"s_cselect_b32 s7, s60, s59\n"
"s_add_u32 s7, s7, 0x85c00\n"
"buffer_load_dwordx4 v[188:191], v16, s[8:11], s7 offen sc1\n"
"buffer_load_dwordx4 v[204:207], v17, s[8:11], s7 offen sc1\n"
"s_bitcmp1_b32 s61, 4\n"
"s_cselect_b32 s7, s60, s59\n"
"s_add_u32 s7, s7, 0x86000\n"
"buffer_load_dwordx4 v[180:183], v16, s[8:11], s7 offen sc1\n"
"buffer_load_dwordx4 v[28:31], v17, s[8:11], s7 offen sc1\n"
"s_bitcmp1_b32 s61, 5\n"
"s_cselect_b32 s7, s60, s59\n"
"s_add_u32 s7, s7, 0x86400\n"
"buffer_load_dwordx4 v[248:251], v16, s[8:11], s7 offen sc1\n"
"buffer_load_dwordx4 v[116:119], v17, s[8:11], s7 offen sc1\n"
"s_bitcmp1_b32 s61, 6\n"
"s_cselect_b32 s7, s60, s59\n"
"s_add_u32 s7, s7, 0x86800\n"
"buffer_load_dwordx4 v[184:187], v16, s[8:11], s7 offen sc1\n"
"buffer_load_dwordx4 v[88:91], v17, s[8:11], s7 offen sc1\n"
"s_waitcnt vmcnt(0)\n"
"v_max3_u32 v120, v224, v225, v226\n"
"v_max3_u32 v120, v120, v227, v228\n"
"v_max3_u32 v120, v120, v229, v230\n"
"v_max_u32 v120, v120, v231\n"
"v_cmp_eq_u32 vcc, -1, v120\n"
"s_cbranch_vccz Lb44_%=\n"
"s_sub_u32 s45, s45, 1\n"
"s_cbranch_scc0 Ls44_%=\n"
"s_mov_b32 s45, 0\n"
"s_branch Lb44_%=\n"
"Ls45_%=:\n"
"s_sleep 2\n"
"s_bitcmp1_b32 s61, 3\n"
"s_cselect_b32 s7, s60, s59\n"
"s_add_u32 s7, s7, 0x85c00\n"
"buffer_load_dwordx4 v[188:191], v16, s[8:11], s7 offen sc1\n"
"buffer_load_dwordx4 v[204:207], v17, s[8:11], s7 offen sc1\n"
"s_bitcmp1_b32 s61, 4\n"
"s_cselect_b32 s7, s60, s59\n"
"s_add_u32 s7, s7, 0x86000\n"
"buffer_load_dwordx4 v[180:183], v16, s[8:11], s7 offen sc1\n"
"buffer_load_dwordx4 v[28:31], v17, s[8:11], s7 offen sc1\n"
"s_bitcmp1_b32 s61, 5\n"
"s_cselect_b32 s7, s60, s59\n"
"s_add_u32 s7, s7, 0x86400\n"
"buffer_load_dwordx4 v[248:251], v16, s[8:11], s7 offen sc1\n"
"buffer_load_dwordx4 v[116:119], v17, s[8:11], s7 offen sc1\n"
"s_bitcmp1_b32 s61, 6\n"
"s_cselect_b32 s7, s60, s59\n"
"s_add_u32 s7, s7, 0x86800\n"
"buffer_load_dwordx4 v[184:187], v16, s[8:11], s7 offen sc1\n"
"buffer_load_dwordx4 v[88:91], v17, s[8:11], s7 offen sc1\n"
"s_bitcmp1_b32 s61, 7\n"
"s_cselect_b32 s7, s60, s59\n"
"s_add_u32 s7, s7, 0x86c00\n"
"buffer_load_dwordx4 v[124:127], v16, s[8:11], s7 offen sc1\n"
"buffer_load_dwordx4 v[84:87], v17, s[8:11], s7 offen sc1\n"
"s_waitcnt vmcnt(0)\n"
"v_max3_u32 v164, v188, v189, v190\n"
"v_max3_u32 v164, v164, v191, v204\n"
"v_max3_u32 v164, v164, v205, v206\n"
"v_max_u32 v164, v164, v207\n"
"v_cmp_eq_u32 vcc, -1, v164\n"
"s_cbranch_vccz Lb45_%=\n"
"s_sub_u32 s45, s45, 1\n"
"s_cbranch_scc0 Ls45_%=\n"
"s_mov_b32 s45, 0\n"
"s_branch Lb45_%=\n"
"Ls46_%=:\n"
"s_sleep 2\n"
"s_bitcmp1_b32 s61, 4\n"
"s_cselect_b32 s7, s60, s59\n"
"s_add_u32 s7, s7, 0x86000\n"
"buffer_load_dwordx4 v[180:183], v16, s[8:11], s7 offen sc1\n"
"buffer_load_dwordx4 v[28:31], v17, s[8:11], s7 offen sc1\n"
"s_bitcmp1_b32 s61, 5\n"
"s_cselect_b32 s7, s60, s59\n"
"s_add_u32 s7, s7, 0x86400\n"
"buffer_load_dwordx4 v[248:251], v16, s[8:11], s7 offen sc1\n"
"buffer_load_dwordx4 v[116:119], v17, s[8:11], s7 offen sc1\n"
"s_bitcmp1_b32 s61, 6\n"
"s_cselect_b32 s7, s60, s59\n"
"s_add_u32 s7, s7, 0x86800\n"
"buffer_load_dwordx4 v[184:187], v16, s[8:11], s7 offen sc1\n"
"buffer_load_dwordx4 v[88:91], v17, s[8:11], s7 offen sc1\n"
"s_bitcmp1_b32 s61, 7\n"
"s_cselect_b32 s7, s60, s59\n"
"s_add_u32 s7, s7, 0x86c00\n"
"buffer_load_dwordx4 v[124:127], v16, s[8:11], s7 offen sc1\n"
"buffer_load_dwordx4 v[84:87], v17, s[8:11], s7 offen sc1\n"
"s_bitcmp1_b32 s61, 8\n"
"s_cselect_b32 s7, s60, s59\n"
"s_add_u32 s7, s7, 0x87000\n"
"buffer_load_dwordx4 v[44:47], v16, s[8:11], s7 offen sc1\n"
"s_waitcnt vmcnt(0)\n"
"v_max3_u32 v140, v180, v181, v182\n"
"v_max3_u32 v140, v140, v183, v28\n"
"v_max3_u32 v140, v140, v29, v30\n"
"v_max_u32 v140, v140, v31\n"
"v_cmp_eq_u32 vcc, -1, v140\n"
"s_cbranch_vccz Lb46_%=\n"
"s_sub_u32 s45, s45, 1\n"
"s_cbranch_scc0 Ls46_%=\n"
"s_mov_b32 s45, 0\n"
"s_branch Lb46_%=\n"
"Ls47_%=:\n"
"s_sleep 2\n"
"s_bitcmp1_b32 s61, 5\n"
"s_cselect_b32 s7, s60, s59\n"
"s_add_u32 s7, s7, 0x86400\n"
"buffer_load_dwordx4 v[248:251], v16, s[8:11], s7 offen sc1\n"
"buffer_load_dwordx4 v[116:119], v17, s[8:11], s7 offen sc1\n"
"s_bitcmp1_b32 s61, 6\n"
"s_cselect_b32 s7, s60, s59\n"
"s_add_u32 s7, s7, 0x86800\n"
"buffer_load_dwordx4 v[184:187], v16, s[8:11], s7 offen sc1\n"
"buffer_load_dwordx4 v[88:91], v17, s[8:11], s7 offen sc1\n"
"s_bitcmp1_b32 s61, 7\n"
"s_cselect_b32 s7, s60, s59\n"
"s_add_u32 s7, s7, 0x86c00\n"
"buffer_load_dwordx4 v[124:127], v16, s[8:11], s7 offen sc1\n"
"buffer_load_dwordx4 v[84:87], v17, s[8:11], s7 offen sc1\n"
"s_bitcmp1_b32 s61, 8\n"
"s_cselect_b32 s7, s60, s59\n"
"s_add_u32 s7, s7, 0x87000\n"
"buffer_load_dwordx4 v[44:47], v16, s[8:11], s7 offen sc1\n"
"buffer_load_dwordx4 v[176:179], v17, s[8:11], s7 offen sc1\n"
"s_bitcmp1_b32 s61, 9\n"
"s_cselect_b32 s7, s60, s59\n"
"s_add_u32 s7, s7, 0x87400\n"
"buffer_load_dwordx4 v[244:247], v16, s[8:11], s7 offen sc1\n"
"s_waitcnt vmcnt(0)\n"
"v_max3_u32 v56, v248, v249, v250\n"
"v_max3_u32 v56, v56, v251, v116\n"
"v_max3_u32 v56, v56, v117, v118\n"
"v_max_u32 v56, v56, v119\n"
"v_cmp_eq_u32 vcc, -1, v56\n"
"s_cbranch_vccz Lb47_%=\n"
"s_sub_u32 s45, s45, 1\n"
"s_cbranch_scc0 Ls47_%=\n"
"s_mov_b32 s45, 0\n"
"s_branch Lb47_%=\n"
"Ls48_%=:\n"
"s_sleep 2\n"
"s_bitcmp1_b32 s61, 6\n"
"s_cselect_b32 s7, s60, s59\n"
"s_add_u32 s7, s7, 0x86800\n"
"buffer_load_dwordx4 v[184:187], v16, s[8:11], s7 offen sc1\n"
"buffer_load_dwordx4 v[88:91], v17, s[8:11], s7 offen sc1\n"
"s_bitcmp1_b32 s61, 7\n"
"s_cselect_b32 s7, s60, s59\n"
"s_add_u32 s7, s7, 0x86c00\n"
"buffer_load_dwordx4 v[124:127], v16, s[8:11], s7 offen sc1\n"
"buffer_load_dwordx4 v[84:87], v17, s[8:11], s7 offen sc1\n"
"s_bitcmp1_b32 s61, 8\n"
"s_cselect_b32 s7, s60, s59\n"
"s_add_u32 s7, s7, 0x87000\n"
"buffer_load_dwordx4 v[44:47], v16, s[8:11], s7 offen sc1\n"
"buffer_load_dwordx4 v[176:179], v17, s[8:11], s7 offen sc1\n"
"s_bitcmp1_b32 s61, 9\n"
"s_cselect_b32 s7, s60, s59\n"
"s_add_u32 s7, s7, 0x87400\n"
"buffer_load_dwordx4 v[244:247], v16, s[8:11], s7 offen sc1\n"
"buffer_load_dwordx4 v[196:199], v17, s[8:11], s7 offen sc1\n"
"s_bitcmp1_b32 s61, 10\n"
"s_cselect_b32 s7, s60, s59\n"
"s_add_u32 s7, s7, 0x87800\n"
"buffer_load_dwordx4 v[100:103], v16, s[8:11], s7 offen sc1\n"
"buffer_load_dwordx4 v[76:79], v17, s[8:11], s7 offen sc1\n"
"s_waitcnt vmcnt(0)\n"
"v_max3_u32 v64, v184, v185, v186\n"
"v_max3_u32 v64, v64, v187, v88\n"
"v_max3_u32 v64, v64, v89, v90\n"
"v_max_u32 v64, v64, v91\n"
"v_cmp_eq_u32 vcc, -1, v64\n"
"s_cbranch_vccz Lb48_%=\n"
"s_sub_u32 s45, s45, 1\n"
"s_cbranch_scc0 Ls48_%=\n"
"s_mov_b32 s45, 0\n"
"s_branch Lb48_%=\n"
"Ls49_%=:\n"
"s_sleep 2\n"
"s_bitcmp1_b32 s61, 7\n"
"s_cselect_b32 s7, s60, s59\n"
"s_add_u32 s7, s7, 0x86c00\n"
"buffer_load_dwordx4 v[124:127], v16, s[8:11], s7 offen sc1\n"
"buffer_load_dwordx4 v[84:87], v17, s[8:11], s7 offen sc1\n"
"s_bitcmp1_b32 s61, 8\n"
"s_cselect_b32 s7, s60, s59\n"
"s_add_u32 s7, s7, 0x87000\n"
"buffer_load_dwordx4 v[44:47], v16, s[8:11], s7 offen sc1\n"
"buffer_load_dwordx4 v[176:179], v17, s[8:11], s7 offen sc1\n"
"s_bitcmp1_b32 s61, 9\n"
"s_cselect_b32 s7, s60, s59\n"
"s_add_u32 s7, s7, 0x87400\n"
"buffer_load_dwordx4 v[244:247], v16, s[8:11], s7 offen sc1\n"
"buffer_load_dwordx4 v[196:199], v17, s[8:11], s7 offen sc1\n"
"s_bitcmp1_b32 s61, 10\n"
"s_cselect_b32 s7, s60, s59\n"
"s_add_u32 s7, s7, 0x87800\n"
"buffer_load_dwordx4 v[100:103], v16, s[8:11], s7 offen sc1\n"
"buffer_load_dwordx4 v[76:79], v17, s[8:11], s7 offen sc1\n"
"s_bitcmp1_b32 s61, 11\n"
"s_cselect_b32 s7, s60, s59\n"
"s_add_u32 s7, s7, 0x87c00\n"
"buffer_load_dwordx4 v[92:95], v16, s[8:11], s7 offen sc1\n"
"buffer_load_dwordx4 v[112:115], v17, s[8:11], s7 offen sc1\n"
"s_waitcnt vmcnt(0)\n"
"v_max3_u32 v152, v124, v125, v126\n"
"v_max3_u32 v152, v152, v127, v84\n"
"v_max3_u32 v152, v152, v85, v86\n"
"v_max_u32 v152, v152, v87\n"
"v_cmp_eq_u32 vcc, -1, v152\n"
"s_cbranch_vccz Lb49_%=\n"
"s_sub_u32 s45, s45, 1\n"
"s_cbranch_scc0 Ls49_%=\n"
"s_mov_b32 s45, 0\n"
"s_branch Lb49_%=\n"
"Ls50_%=:\n"
"s_sleep 2\n"
"s_bitcmp1_b32 s61, 8\n"
"s_cselect_b32 s7, s60, s59\n"
"s_add_u32 s7, s7, 0x87000\n"
"buffer_load_dwordx4 v[44:47], v16, s[8:11], s7 offen sc1\n"
"buffer_load_dwordx4 v[176:179], v17, s[8:11], s7 offen sc1\n"
"s_bitcmp1_b32 s61, 9\n"
"s_cselect_b32 s7, s60, s59\n"
"s_add_u32 s7, s7, 0x87400\n"
"buffer_load_dwordx4 v[244:247], v16, s[8:11], s7 offen sc1\n"
"buffer_load_dwordx4 v[196:199], v17, s[8:11], s7 offen sc1\n"
"s_bitcmp1_b32 s61, 10\n"
"s_cselect_b32 s7, s60, s59\n"
"s_add_u32 s7, s7, 0x87800\n"
"buffer_load_dwordx4 v[100:103], v16, s[8:11], s7 offen sc1\n"
"buffer_load_dwordx4 v[76:79], v17, s[8:11], s7 offen sc1\n"
"s_bitcmp1_b32 s61, 11\n"
"s_cselect_b32 s7, s60, s59\n"
"s_add_u32 s7, s7, 0x87c00\n"
"buffer_load_dwordx4 v[92:95], v16, s[8:11], s7 offen sc1\n"
"buffer_load_dwordx4 v[112:115], v17, s[8:11], s7 offen sc1\n"
"s_waitcnt vmcnt(0)\n"
"v_max3_u32 v120, v44, v45, v46\n"
"v_max3_u32 v120, v120, v47, v176\n"
"v_max3_u32 v120, v120, v177, v178\n"
"v_max_u32 v120, v120, v179\n"
"v_cmp_eq_u32 vcc, -1, v120\n"
"s_cbranch_vccz Lb50_%=\n"
"s_sub_u32 s45, s45, 1\n"
"s_cbranch_scc0 Ls50_%=\n"
"s_mov_b32 s45, 0\n"
"s_branch Lb50_%=\n"
"Ls51_%=:\n"
"s_sleep 2\n"
"s_bitcmp1_b32 s61, 9\n"
"s_cselect_b32 s7, s60, s59\n"
"s_add_u32 s7, s7, 0x87400\n"
"buffer_load_dwordx4 v[244:247], v16, s[8:11], s7 offen sc1\n"
"buffer_load_dwordx4 v[196:199], v17, s[8:11], s7 offen sc1\n"
"s_bitcmp1_b32 s61, 10\n"
"s_cselect_b32 s7, s60, s59\n"
"s_add_u32 s7, s7, 0x87800\n"
"buffer_load_dwordx4 v[100:103], v16, s[8:11], s7 offen sc1\n"
"buffer_load_dwordx4 v[76:79], v17, s[8:11], s7 offen sc1\n"
"s_bitcmp1_b32 s61, 11\n"
"s_cselect_b32 s7, s60, s59\n"
"s_add_u32 s7, s7, 0x87c00\n"
"buffer_load_dwordx4 v[92:95], v16, s[8:11], s7 offen sc1\n"
"buffer_load_dwordx4 v[112:115], v17, s[8:11], s7 offen sc1\n"
"s_waitcnt vmcnt(0)\n"
"v_max3_u32 v160, v244, v245, v246\n"
"v_max3_u32 v160, v160, v247, v196\n"
"v_max3_u32 v160, v160, v197, v198\n"
"v_max_u32 v160, v160, v199\n"
"v_cmp_eq_u32 vcc, -1, v160\n"
"s_cbranch_vccz Lb51_%=\n"
"s_sub_u32 s45, s45, 1\n"
"s_cbranch_scc0 Ls51_%=\n"
"s_mov_b32 s45, 0\n"
"s_branch Lb51_%=\n"
"Ls52_%=:\n"
"s_sleep 2\n"
"s_bitcmp1_b32 s61, 10\n"
"s_cselect_b32 s7, s60, s59\n"
"s_add_u32 s7, s7, 0x87800\n"
"buffer_load_dwordx4 v[100:103], v16, s[8:11], s7 offen sc1\n"
"buffer_load_dwordx4 v[76:79], v17, s[8:11], s7 offen sc1\n"
"s_bitcmp1_b32 s61, 11\n"
"s_cselect_b32 s7, s60, s59\n"
"s_add_u32 s7, s7, 0x87c00\n"
"buffer_load_dwordx4 v[92:95], v16, s[8:11], s7 offen sc1\n"
"buffer_load_dwordx4 v[112:115], v17, s[8:11], s7 offen sc1\n"
"s_waitcnt vmcnt(0)\n"
"v_max3_u32 v40, v100, v101, v102\n"
"v_max3_u32 v40, v40, v103, v76\n"
"v_max3_u32 v40, v40, v77, v78\n"
"v_max_u32 v40, v40, v79\n"
"v_cmp_eq_u32 vcc, -1, v40\n"
"s_cbranch_vccz Lb52_%=\n"
"s_sub_u32 s45, s45, 1\n"
"s_cbranch_scc0 Ls52_%=\n"
"s_mov_b32 s45, 0\n"
"s_branch Lb52_%=\n"
"Ls53_%=:\n"
"s_sleep 2\n"
"s_bitcmp1_b32 s61, 11\n"
"s_cselect_b32 s7, s60, s59\n"
"s_add_u32 s7, s7, 0x87c00\n"
"buffer_load_dwordx4 v[92:95], v16, s[8:11], s7 offen sc1\n"
"buffer_load_dwordx4 v[112:115], v17, s[8:11], s7 offen sc1\n"
"s_waitcnt vmcnt(0)\n"
"v_max3_u32 v136, v92, v93, v94\n"
"v_max3_u32 v136, v136, v95, v112\n"
"v_max3_u32 v136, v136, v113, v114\n"
"v_max_u32 v136, v136, v115\n"
"v_cmp_eq_u32 vcc, -1, v136\n"
"s_cbranch_vccz Lb53_%=\n"
"s_sub_u32 s45, s45, 1\n"
"s_cbranch_scc0 Ls53_%=\n"
"s_mov_b32 s45, 0\n"
"s_branch Lb53_%=\n"
"Ls26_%=:\n"
"s_sleep 2\n"
"s_add_u32 s7, s72, 0x20000\n"
"buffer_load_dwordx4 v[152:155], v7, s[8:11], s7 offen offset:0 sc1\n"
"s_add_u32 s7, s73, 0x20000\n"
"buffer_load_dwordx4 v[240:243], v7, s[8:11], s7 offen offset:0 sc1\n"
"s_add_u32 s7, s64, 0x16000\n"
"buffer_load_dwordx4 v[120:123], v1, s[8:11], s7 offen offset:0 sc1\n"
"s_add_u32 s7, s63, 0x16000\n"
"buffer_load_dwordx4 v[48:51], v0, s[8:11], s7 offen offset:0 sc1\n"
"buffer_load_dwordx4 v[164:167], v0, s[8:11], s7 offen offset:1024 sc1\n"
"buffer_load_dwordx4 v[224:227], v0, s[8:11], s7 offen offset:2048 sc1\n"
"buffer_load_dwordx4 v[140:143], v0, s[8:11], s7 offen offset:3072 sc1\n"
"s_add_u32 s7, s63, 0x17000\n"
"buffer_load_dwordx4 v[228:231], v0, s[8:11], s7 offen offset:0 sc1\n"
"s_add_u32 s7, s63, 0x1b000\n"
"buffer_load_dwordx4 v[188:191], v0, s[8:11], s7 offen offset:0 sc1\n"
"buffer_load_dwordx4 v[252:255], v0, s[8:11], s7 offen offset:1024 sc1\n"
"buffer_load_dwordx4 v[28:31], v0, s[8:11], s7 offen offset:2048 sc1\n"
"buffer_load_dwordx4 v[248:251], v0, s[8:11], s7 offen offset:3072 sc1\n"
"s_add_u32 s7, s63, 0x1c000\n"
"buffer_load_dwordx4 v[88:91], v0, s[8:11], s7 offen offset:0 sc1\n"
"s_waitcnt vmcnt(0)\n"
"v_max3_u32 v124, v48, v49, v50\n"
"v_max3_u32 v124, v124, v51, v164\n"
"v_max3_u32 v124, v124, v165, v166\n"
"v_max3_u32 v124, v124, v167, v224\n"
"v_max3_u32 v124, v124, v225, v226\n"
"v_max_u32 v124, v124, v227\n"
"v_cmp_eq_u32 vcc, -1, v124\n"
"s_cbranch_vccz Lb26_%=\n"
"s_sub_u32 s45, s45, 1\n"
"s_cbranch_scc0 Ls26_%=\n"
"s_mov_b32 s45, 0\n"
"s_branch Lb26_%=\n"
"Ls27_%=:\n"
"s_sleep 2\n"
"s_add_u32 s7, s72, 0x20000\n"
"buffer_load_dwordx4 v[152:155], v7, s[8:11], s7 offen offset:0 sc1\n"
"s_add_u32 s7, s73, 0x20000\n"
"buffer_load_dwordx4 v[240:243], v7, s[8:11], s7 offen offset:0 sc1\n"
"s_add_u32 s7, s64, 0x16000\n"
"buffer_load_dwordx4 v[120:123], v1, s[8:11], s7 offen offset:0 sc1\n"
"buffer_load_dwordx4 v[44:47], v1, s[8:11], s7 offen offset:2048 sc1\n"
"buffer_load_dwordx4 v[176:179], v1, s[8:11], s7 offen offset:3072 sc1\n"
"s_add_u32 s7, s63, 0x16c00\n"
"buffer_load_dwordx4 v[140:143], v0, s[8:11], s7 offen offset:0 sc1\n"
"buffer_load_dwordx4 v[228:231], v0, s[8:11], s7 offen offset:1024 sc1\n"
"s_add_u32 s7, s63, 0x1b000\n"
"buffer_load_dwordx4 v[188:191], v0, s[8:11], s7 offen offset:0 sc1\n"
"buffer_load_dwordx4 v[252:255], v0, s[8:11], s7 offen offset:1024 sc1\n"
"buffer_load_dwordx4 v[28:31], v0, s[8:11], s7 offen offset:2048 sc1\n"
"buffer_load_dwordx4 v[248:251], v0, s[8:11], s7 offen offset:3072 sc1\n"
"s_add_u32 s7, s63, 0x1c000\n"
"buffer_load_dwordx4 v[88:91], v0, s[8:11], s7 offen offset:0 sc1\n"
"s_waitcnt vmcnt(0)\n"
"v_max3_u32 v160, v140, v141, v142\n"
"v_max3_u32 v160, v160, v143, v228\n"
"v_max3_u32 v160, v160, v229, v230\n"
"v_max_u32 v160, v160, v231\n"
"v_cmp_eq_u32 vcc, -1, v160\n"
"s_cbranch_vccz Lb27_%=\n"
"s_sub_u32 s45, s45, 1\n"
"s_cbranch_scc0 Ls27_%=\n"
"s_mov_b32 s45, 0\n"
"s_branch Lb27_%=\n"
"Ls28_%=:\n"
"s_sleep 2\n"
"s_add_u32 s7, s72, 0x20000\n"
"buffer_load_dwordx4 v[152:155], v7, s[8:11], s7 offen offset:0 sc1\n"
"s_add_u32 s7, s73, 0x20000\n"
"buffer_load_dwordx4 v[240:243], v7, s[8:11], s7 offen offset:0 sc1\n"
"s_add_u32 s7, s64, 0x16000\n"
"buffer_load_dwordx4 v[120:123], v1, s[8:11], s7 offen offset:0 sc1\n"
"buffer_load_dwordx4 v[44:47], v1, s[8:11], s7 offen offset:2048 sc1\n"
"buffer_load_dwordx4 v[176:179], v1, s[8:11], s7 offen offset:3072 sc1\n"
"s_add_u32 s7, s63, 0x1b000\n"
"buffer_load_dwordx4 v[188:191], v0, s[8:11], s7 offen offset:0 sc1\n"
"buffer_load_dwordx4 v[252:255], v0, s[8:11], s7 offen offset:1024 sc1\n"
"buffer_load_dwordx4 v[28:31], v0, s[8:11], s7 offen offset:2048 sc1\n"
"buffer_load_dwordx4 v[248:251], v0, s[8:11], s7 offen offset:3072 sc1\n"
"s_add_u32 s7, s63, 0x1c000\n"
"buffer_load_dwordx4 v[88:91], v0, s[8:11], s7 offen offset:0 sc1\n"
"s_waitcnt vmcnt(0)\n"
"v_max3_u32 v244, v188, v189, v190\n"
"v_max3_u32 v244, v244, v191, v252\n"
"v_max3_u32 v244, v244, v253, v254\n"
"v_max_u32 v244, v244, v255\n"
"v_cmp_eq_u32 vcc, -1, v244\n"
"s_cbranch_vccz Lb28_%=\n"
"s_sub_u32 s45, s45, 1\n"
"s_cbranch_scc0 Ls28_%=\n"
"s_mov_b32 s45, 0\n"
"s_branch Lb28_%=\n"
"Ls29_%=:\n"
"s_sleep 2\n"
"s_add_u32 s7, s72, 0x20000\n"
"buffer_load_dwordx4 v[152:155], v7, s[8:11], s7 offen offset:0 sc1\n"
"s_add_u32 s7, s73, 0x20000\n"
"buffer_load_dwordx4 v[240:243], v7, s[8:11], s7 offen offset:0 sc1\n"
"s_add_u32 s7, s64, 0x16000\n"
"buffer_load_dwordx4 v[120:123], v1, s[8:11], s7 offen offset:0 sc1\n"
"buffer_load_dwordx4 v[44:47], v1, s[8:11], s7 offen offset:2048 sc1\n"
"buffer_load_dwordx4 v[176:179], v1, s[8:11], s7 offen offset:3072 sc1\n"
"s_add_u32 s7, s63, 0x1b800\n"
"buffer_load_dwordx4 v[28:31], v0, s[8:11], s7 offen offset:0 sc1\n"
"buffer_load_dwordx4 v[248:251], v0, s[8:11], s7 offen offset:1024 sc1\n"
"buffer_load_dwordx4 v[88:91], v0, s[8:11], s7 offen offset:2048 sc1\n"
"s_waitcnt vmcnt(0)\n"
"v_max3_u32 v236, v28, v29, v30\n"
"v_max3_u32 v236, v236, v31, v248\n"
"v_max3_u32 v236, v236, v249, v250\n"
"v_max_u32 v236, v236, v251\n"
"v_cmp_eq_u32 vcc, -1, v236\n"
"s_cbranch_vccz Lb29_%=\n"
"s_sub_u32 s45, s45, 1\n"
"s_cbranch_scc0 Ls29_%=\n"
"s_mov_b32 s45, 0\n"
"s_branch Lb29_%=\n"
"Ls30_%=:\n"
"s_sleep 2\n"
"s_add_u32 s7, s72, 0x20000\n"
"buffer_load_dwordx4 v[152:155], v7, s[8:11], s7 offen offset:0 sc1\n"
"s_add_u32 s7, s73, 0x20000\n"
"buffer_load_dwordx4 v[240:243], v7, s[8:11], s7 offen offset:0 sc1\n"
"s_add_u32 s7, s64, 0x16000\n"
"buffer_load_dwordx4 v[120:123], v1, s[8:11], s7 offen offset:0 sc1\n"
"buffer_load_dwordx4 v[44:47], v1, s[8:11], s7 offen offset:2048 sc1\n"
"buffer_load_dwordx4 v[176:179], v1, s[8:11], s7 offen offset:3072 sc1\n"
"s_add_u32 s7, s63, 0x1c000\n"
"buffer_load_dwordx4 v[88:91], v0, s[8:11], s7 offen offset:0 sc1\n"
"s_waitcnt vmcnt(0)\n"
"v_max3_u32 v196, v88, v89, v90\n"
"v_max3_u32 v196, v196, v91, v152\n"
"v_max3_u32 v196, v196, v153, v154\n"
"v_max_u32 v196, v196, v155\n"
"v_cmp_eq_u32 vcc, -1, v196\n"
"s_cbranch_vccz Lb30_%=\n"
"s_sub_u32 s45, s45, 1\n"
"s_cbranch_scc0 Ls30_%=\n"
"s_mov_b32 s45, 0\n"
"s_branch Lb30_%=\n"
"Ls31_%=:\n"
"s_sleep 2\n"
"s_add_u32 s7, s73, 0x20000\n"
"buffer_load_dwordx4 v[240:243], v7, s[8:11], s7 offen offset:0 sc1\n"
"s_add_u32 s7, s64, 0x16000\n"
"buffer_load_dwordx4 v[120:123], v1, s[8:11], s7 offen offset:0 sc1\n"
"buffer_load_dwordx4 v[44:47], v1, s[8:11], s7 offen offset:2048 sc1\n"
"buffer_load_dwordx4 v[176:179], v1, s[8:11], s7 offen offset:3072 sc1\n"
"s_waitcnt vmcnt(0)\n"
"v_max3_u32 v40, v240, v241, v242\n"
"v_max3_u32 v40, v40, v243, v120\n"
"v_max3_u32 v40, v40, v121, v122\n"
"v_max3_u32 v40, v40, v123, v44\n"
"v_max3_u32 v40, v40, v45, v46\n"
"v_max3_u32 v40, v40, v47, v176\n"
"v_max3_u32 v40, v40, v177, v178\n"
"v_max_u32 v40, v40, v179\n"
"v_cmp_eq_u32 vcc, -1, v40\n"
"s_cbranch_vccz Lb31_%=\n"
"s_sub_u32 s45, s45, 1\n"
"s_cbranch_scc0 Ls31_%=\n"
"s_mov_b32 s45, 0\n"
"s_branch Lb31_%=\n"
"Ls54_%=:\n"
"s_sleep 2\n"
"s_add_u32 s7, s72, 0x2b000\n"
"buffer_load_dwordx4 v[244:247], v7, s[8:11], s7 offen offset:0 sc1\n"
"s_add_u32 s7, s73, 0x2b000\n"
"buffer_load_dwordx4 v[248:251], v7, s[8:11], s7 offen offset:0 sc1\n"
"s_add_u32 s7, s64, 0x21000\n"
"buffer_load_dwordx4 v[196:199], v1, s[8:11], s7 offen offset:0 sc1\n"
"s_add_u32 s7, s63, 0x21000\n"
"buffer_load_dwordx4 v[100:103], v0, s[8:11], s7 offen offset:0 sc1\n"
"buffer_load_dwordx4 v[68:71], v0, s[8:11], s7 offen offset:1024 sc1\n"
"buffer_load_dwordx4 v[208:211], v0, s[8:11], s7 offen offset:2048 sc1\n"
"buffer_load_dwordx4 v[148:151], v0, s[8:11], s7 offen offset:3072 sc1\n"
"s_add_u32 s7, s63, 0x22000\n"
"buffer_load_dwordx4 v[192:195], v0, s[8:11], s7 offen offset:0 sc1\n"
"s_add_u32 s7, s63, 0x26000\n"
"buffer_load_dwordx4 v[216:219], v0, s[8:11], s7 offen offset:0 sc1\n"
"buffer_load_dwordx4 v[124:127], v0, s[8:11], s7 offen offset:1024 sc1\n"
"buffer_load_dwordx4 v[224:227], v0, s[8:11], s7 offen offset:2048 sc1\n"
"buffer_load_dwordx4 v[160:163], v0, s[8:11], s7 offen offset:3072 sc1\n"
"s_add_u32 s7, s63, 0x27000\n"
"buffer_load_dwordx4 v[84:87], v0, s[8:11], s7 offen offset:0 sc1\n"
"s_waitcnt vmcnt(0)\n"
"v_max3_u32 v128, v100, v101, v102\n"
"v_max3_u32 v128, v128, v103, v68\n"
"v_max3_u32 v128, v128, v69, v70\n"
"v_max3_u32 v128, v128, v71, v208\n"
"v_max3_u32 v128, v128, v209, v210\n"
"v_max_u32 v128, v128, v211\n"
"v_cmp_eq_u32 vcc, -1, v128\n"
"s_cbranch_vccz Lb54_%=\n"
"s_sub_u32 s45, s45, 1\n"
"s_cbranch_scc0 Ls54_%=\n"
"s_mov_b32 s45, 0\n"
"s_branch Lb54_%=\n"
"Ls55_%=:\n"
"s_sleep 2\n"
"s_add_u32 s7, s72, 0x2b000\n"
"buffer_load_dwordx4 v[244:247], v7, s[8:11], s7 offen offset:0 sc1\n"
"s_add_u32 s7, s73, 0x2b000\n"
"buffer_load_dwordx4 v[248:251], v7, s[8:11], s7 offen offset:0 sc1\n"
"s_add_u32 s7, s64, 0x21000\n"
"buffer_load_dwordx4 v[196:199], v1, s[8:11], s7 offen offset:0 sc1\n"
"buffer_load_dwordx4 v[44:47], v1, s[8:11], s7 offen offset:2048 sc1\n"
"buffer_load_dwordx4 v[76:79], v1, s[8:11], s7 offen offset:3072 sc1\n"
"s_add_u32 s7, s63, 0x21c00\n"
"buffer_load_dwordx4 v[148:151], v0, s[8:11], s7 offen offset:0 sc1\n"
"buffer_load_dwordx4 v[192:195], v0, s[8:11], s7 offen offset:1024 sc1\n"
"s_add_u32 s7, s63, 0x26000\n"
"buffer_load_dwordx4 v[216:219], v0, s[8:11], s7 offen offset:0 sc1\n"
"buffer_load_dwordx4 v[124:127], v0, s[8:11], s7 offen offset:1024 sc1\n"
"buffer_load_dwordx4 v[224:227], v0, s[8:11], s7 offen offset:2048 sc1\n"
"buffer_load_dwordx4 v[160:163], v0, s[8:11], s7 offen offset:3072 sc1\n"
"s_add_u32 s7, s63, 0x27000\n"
"buffer_load_dwordx4 v[84:87], v0, s[8:11], s7 offen offset:0 sc1\n"
"s_waitcnt vmcnt(0)\n"
"v_max3_u32 v176, v148, v149, v150\n"
"v_max3_u32 v176, v176, v151, v192\n"
"v_max3_u32 v176, v176, v193, v194\n"
"v_max_u32 v176, v176, v195\n"
"v_cmp_eq_u32 vcc, -1, v176\n"
"s_cbranch_vccz Lb55_%=\n"
"s_sub_u32 s45, s45, 1\n"
"s_cbranch_scc0 Ls55_%=\n"
"s_mov_b32 s45, 0\n"
"s_branch Lb55_%=\n"
"Ls56_%=:\n"
"s_sleep 2\n"
"s_add_u32 s7, s72, 0x2b000\n"
"buffer_load_dwordx4 v[244:247], v7, s[8:11], s7 offen offset:0 sc1\n"
"s_add_u32 s7, s73, 0x2b000\n"
"buffer_load_dwordx4 v[248:251], v7, s[8:11], s7 offen offset:0 sc1\n"
"s_add_u32 s7, s64, 0x21000\n"
"buffer_load_dwordx4 v[196:199], v1, s[8:11], s7 offen offset:0 sc1\n"
"buffer_load_dwordx4 v[44:47], v1, s[8:11], s7 offen offset:2048 sc1\n"
"buffer_load_dwordx4 v[76:79], v1, s[8:11], s7 offen offset:3072 sc1\n"
"s_add_u32 s7, s63, 0x26000\n"
"buffer_load_dwordx4 v[216:219], v0, s[8:11], s7 offen offset:0 sc1\n"
"buffer_load_dwordx4 v[124:127], v0, s[8:11], s7 offen offset:1024 sc1\n"
"buffer_load_dwordx4 v[224:227], v0, s[8:11], s7 offen offset:2048 sc1\n"
"buffer_load_dwordx4 v[160:163], v0, s[8:11], s7 offen offset:3072 sc1\n"
"s_add_u32 s7, s63, 0x27000\n"
"buffer_load_dwordx4 v[84:87], v0, s[8:11], s7 offen offset:0 sc1\n"
"s_waitcnt vmcnt(0)\n"
"v_max3_u32 v136, v216, v217, v218\n"
"v_max3_u32 v136, v136, v219, v124\n"
"v_max3_u32 v136, v136, v125, v126\n"
"v_max_u32 v136, v136, v127\n"
"v_cmp_eq_u32 vcc, -1, v136\n"
"s_cbranch_vccz Lb56_%=\n"
"s_sub_u32 s45, s45, 1\n"
"s_cbranch_scc0 Ls56_%=\n"
"s_mov_b32 s45, 0\n"
"s_branch Lb56_%=\n"
"Ls57_%=:\n"
"s_sleep 2\n"
"s_add_u32 s7, s72, 0x2b000\n"
"buffer_load_dwordx4 v[244:247], v7, s[8:11], s7 offen offset:0 sc1\n"
"s_add_u32 s7, s73, 0x2b000\n"
"buffer_load_dwordx4 v[248:251], v7, s[8:11], s7 offen offset:0 sc1\n"
"s_add_u32 s7, s64, 0x21000\n"
"buffer_load_dwordx4 v[196:199], v1, s[8:11], s7 offen offset:0 sc1\n"
"buffer_load_dwordx4 v[44:47], v1, s[8:11], s7 offen offset:2048 sc1\n"
"buffer_load_dwordx4 v[76:79], v1, s[8:11], s7 offen offset:3072 sc1\n"
"s_add_u32 s7, s63, 0x26800\n"
"buffer_load_dwordx4 v[224:227], v0, s[8:11], s7 offen offset:0 sc1\n"
"buffer_load_dwordx4 v[160:163], v0, s[8:11], s7 offen offset:1024 sc1\n"
"buffer_load_dwordx4 v[84:87], v0, s[8:11], s7 offen offset:2048 sc1\n"
"s_waitcnt vmcnt(0)\n"
"v_max3_u32 v72, v224, v225, v226\n"
"v_max3_u32 v72, v72, v227, v160\n"
"v_max3_u32 v72, v72, v161, v162\n"
"v_max_u32 v72, v72, v163\n"
"v_cmp_eq_u32 vcc, -1, v72\n"
"s_cbranch_vccz Lb57_%=\n"
"s_sub_u32 s45, s45, 1\n"
"s_cbranch_scc0 Ls57_%=\n"
"s_mov_b32 s45, 0\n"
"s_branch Lb57_%=\n"
"Ls58_%=:\n"
"s_sleep 2\n"
"s_add_u32 s7, s72, 0x2b000\n"
"buffer_load_dwordx4 v[244:247], v7, s[8:11], s7 offen offset:0 sc1\n"
"s_add_u32 s7, s73, 0x2b000\n"
"buffer_load_dwordx4 v[248:251], v7, s[8:11], s7 offen offset:0 sc1\n"
"s_add_u32 s7, s64, 0x21000\n"
"buffer_load_dwordx4 v[196:199], v1, s[8:11], s7 offen offset:0 sc1\n"
"buffer_load_dwordx4 v[44:47], v1, s[8:11], s7 offen offset:2048 sc1\n"
"buffer_load_dwordx4 v[76:79], v1, s[8:11], s7 offen offset:3072 sc1\n"
"s_add_u32 s7, s63, 0x27000\n"
"buffer_load_dwordx4 v[84:87], v0, s[8:11], s7 offen offset:0 sc1\n"
"s_waitcnt vmcnt(0)\n"
"v_max3_u32 v112, v84, v85, v86\n"
"v_max3_u32 v112, v112, v87, v244\n"
"v_max3_u32 v112, v112, v245, v246\n"
"v_max_u32 v112, v112, v247\n"
"v_cmp_eq_u32 vcc, -1, v112\n"
"s_cbranch_vccz Lb58_%=\n"
"s_sub_u32 s45, s45, 1\n"
"s_cbranch_scc0 Ls58_%=\n"
"s_mov_b32 s45, 0\n"
"s_branch Lb58_%=\n"
"Ls59_%=:\n"
"s_sleep 2\n"
"s_add_u32 s7, s73, 0x2b000\n"
"buffer_load_dwordx4 v[248:251], v7, s[8:11], s7 offen offset:0 sc1\n"
"s_add_u32 s7, s64, 0x21000\n"
"buffer_load_dwordx4 v[196:199], v1, s[8:11], s7 offen offset:0 sc1\n"
"buffer_load_dwordx4 v[44:47], v1, s[8:11], s7 offen offset:2048 sc1\n"
"buffer_load_dwordx4 v[76:79], v1, s[8:11], s7 offen offset:3072 sc1\n"
"s_waitcnt vmcnt(0)\n"
"v_max3_u32 v92, v248, v249, v250\n"
"v_max3_u32 v92, v92, v251, v196\n"
"v_max3_u32 v92, v92, v197, v198\n"
"v_max3_u32 v92, v92, v199, v44\n"
"v_max3_u32 v92, v92, v45, v46\n"
"v_max3_u32 v92, v92, v47, v76\n"
"v_max3_u32 v92, v92, v77, v78\n"
"v_max_u32 v92, v92, v79\n"
"v_cmp_eq_u32 vcc, -1, v92\n"
"s_cbranch_vccz Lb59_%=\n"
"s_sub_u32 s45, s45, 1\n"
"s_cbranch_scc0 Ls59_%=\n"
"s_mov_b32 s45, 0\n"
"s_branch Lb59_%=\n"
"Ls60_%=:\n"
"s_sleep 2\n"
"s_add_u32 s7, s57, 0x56000\n"
"buffer_load_dwordx4 v[188:191], v0, s[8:11], s7 offen offset:0 sc1\n"
"buffer_load_dwordx4 v[76:79], v0, s[8:11], s7 offen offset:1024 sc1\n"
"buffer_load_dwordx4 v[124:127], v0, s[8:11], s7 offen offset:2048 sc1\n"
"buffer_load_dwordx4 v[80:83], v0, s[8:11], s7 offen offset:3072 sc1\n"
"s_add_u32 s7, s57, 0x57000\n"
"buffer_load_dwordx4 v[96:99], v0, s[8:11], s7 offen offset:0 sc1\n"
"s_add_u32 s7, s58, 0x56000\n"
"buffer_load_dwordx4 v[220:223], v0, s[8:11], s7 offen offset:0 sc1\n"
"buffer_load_dwordx4 v[184:187], v0, s[8:11], s7 offen offset:1024 sc1\n"
"buffer_load_dwordx4 v[200:203], v0, s[8:11], s7 offen offset:2048 sc1\n"
"buffer_load_dwordx4 v[172:175], v0, s[8:11], s7 offen offset:3072 sc1\n"
"s_add_u32 s7, s58, 0x57000\n"
"buffer_load_dwordx4 v[240:243], v0, s[8:11], s7 offen offset:0 sc1\n"
"s_waitcnt vmcnt(0)\n"
"v_max3_u32 v132, v188, v189, v190\n"
"v_max3_u32 v132, v132, v191, v220\n"
"v_max3_u32 v132, v132, v221, v222\n"
"v_max_u32 v132, v132, v223\n"
"v_cmp_eq_u32 vcc, -1, v132\n"
"s_cbranch_vccz Lb60_%=\n"
"s_sub_u32 s45, s45, 1\n"
"s_cbranch_scc0 Ls60_%=\n"
"s_mov_b32 s45, 0\n"
"s_branch Lb60_%=\n"
"Ls61_%=:\n"
"s_sleep 2\n"
"s_add_u32 s7, s57, 0x56400\n"
"buffer_load_dwordx4 v[76:79], v0, s[8:11], s7 offen offset:0 sc1\n"
"buffer_load_dwordx4 v[124:127], v0, s[8:11], s7 offen offset:1024 sc1\n"
"buffer_load_dwordx4 v[80:83], v0, s[8:11], s7 offen offset:2048 sc1\n"
"buffer_load_dwordx4 v[96:99], v0, s[8:11], s7 offen offset:3072 sc1\n"
"s_add_u32 s7, s58, 0x56400\n"
"buffer_load_dwordx4 v[184:187], v0, s[8:11], s7 offen offset:0 sc1\n"
"buffer_load_dwordx4 v[200:203], v0, s[8:11], s7 offen offset:1024 sc1\n"
"buffer_load_dwordx4 v[172:175], v0, s[8:11], s7 offen offset:2048 sc1\n"
"buffer_load_dwordx4 v[240:243], v0, s[8:11], s7 offen offset:3072 sc1\n"
"s_waitcnt vmcnt(0)\n"
"v_max3_u32 v212, v76, v77, v78\n"
"v_max3_u32 v212, v212, v79, v184\n"
"v_max3_u32 v212, v212, v185, v186\n"
"v_max_u32 v212, v212, v187\n"
"v_cmp_eq_u32 vcc, -1, v212\n"
"s_cbranch_vccz Lb61_%=\n"
"s_sub_u32 s45, s45, 1\n"
"s_cbranch_scc0 Ls61_%=\n"
"s_mov_b32 s45, 0\n"
"s_branch Lb61_%=\n"
"Ls62_%=:\n"
"s_sleep 2\n"
"s_add_u32 s7, s57, 0x56800\n"
"buffer_load_dwordx4 v[124:127], v0, s[8:11], s7 offen offset:0 sc1\n"
"buffer_load_dwordx4 v[80:83], v0, s[8:11], s7 offen offset:1024 sc1\n"
"buffer_load_dwordx4 v[96:99], v0, s[8:11], s7 offen offset:2048 sc1\n"
"s_add_u32 s7, s58, 0x56800\n"
"buffer_load_dwordx4 v[200:203], v0, s[8:11], s7 offen offset:0 sc1\n"
"buffer_load_dwordx4 v[172:175], v0, s[8:11], s7 offen offset:1024 sc1\n"
"buffer_load_dwordx4 v[240:243], v0, s[8:11], s7 offen offset:2048 sc1\n"
"s_waitcnt vmcnt(0)\n"
"v_max3_u32 v64, v124, v125, v126\n"
"v_max3_u32 v64, v64, v127, v200\n"
"v_max3_u32 v64, v64, v201, v202\n"
"v_max_u32 v64, v64, v203\n"
"v_cmp_eq_u32 vcc, -1, v64\n"
"s_cbranch_vccz Lb62_%=\n"
"s_sub_u32 s45, s45, 1\n"
"s_cbranch_scc0 Ls62_%=\n"
"s_mov_b32 s45, 0\n"
"s_branch Lb62_%=\n"
"Ls63_%=:\n"
"s_sleep 2\n"
"s_add_u32 s7, s57, 0x56c00\n"
"buffer_load_dwordx4 v[80:83], v0, s[8:11], s7 offen offset:0 sc1\n"
"buffer_load_dwordx4 v[96:99], v0, s[8:11], s7 offen offset:1024 sc1\n"
"s_add_u32 s7, s58, 0x56c00\n"
"buffer_load_dwordx4 v[172:175], v0, s[8:11], s7 offen offset:0 sc1\n"
"buffer_load_dwordx4 v[240:243], v0, s[8:11], s7 offen offset:1024 sc1\n"
"s_waitcnt vmcnt(0)\n"
"v_max3_u32 v68, v80, v81, v82\n"
"v_max3_u32 v68, v68, v83, v172\n"
"v_max3_u32 v68, v68, v173, v174\n"
"v_max_u32 v68, v68, v175\n"
"v_cmp_eq_u32 vcc, -1, v68\n"
"s_cbranch_vccz Lb63_%=\n"
"s_sub_u32 s45, s45, 1\n"
"s_cbranch_scc0 Ls63_%=\n"
"s_mov_b32 s45, 0\n"
"s_branch Lb63_%=\n"
"Ls64_%=:\n"
"s_sleep 2\n"
"s_add_u32 s7, s57, 0x57000\n"
"buffer_load_dwordx4 v[96:99], v0, s[8:11], s7 offen offset:0 sc1\n"
"s_add_u32 s7, s58, 0x57000\n"
"buffer_load_dwordx4 v[240:243], v0, s[8:11], s7 offen offset:0 sc1\n"
"s_waitcnt vmcnt(0)\n"
"v_max3_u32 v100, v96, v97, v98\n"
"v_max3_u32 v100, v100, v99, v240\n"
"v_max3_u32 v100, v100, v241, v242\n"
"v_max_u32 v100, v100, v243\n"
"v_cmp_eq_u32 vcc, -1, v100\n"
"s_cbranch_vccz Lb64_%=\n"
"s_sub_u32 s45, s45, 1\n"
"s_cbranch_scc0 Ls64_%=\n"
"s_mov_b32 s45, 0\n"
"s_branch Lb64_%=\n"
"Ls82_%=:\n"
"s_sleep 2\n"
"s_add_u32 s7, s57, 0x60000\n"
"buffer_load_dwordx4 v[168:171], v0, s[8:11], s7 offen offset:0 sc1\n"
"buffer_load_dwordx4 v[228:231], v0, s[8:11], s7 offen offset:1024 sc1\n"
"buffer_load_dwordx4 v[136:139], v0, s[8:11], s7 offen offset:2048 sc1\n"
"buffer_load_dwordx4 v[36:39], v0, s[8:11], s7 offen offset:3072 sc1\n"
"s_add_u32 s7, s57, 0x61000\n"
"buffer_load_dwordx4 v[216:219], v0, s[8:11], s7 offen offset:0 sc1\n"
"s_add_u32 s7, s58, 0x60000\n"
"buffer_load_dwordx4 v[196:199], v0, s[8:11], s7 offen offset:0 sc1\n"
"buffer_load_dwordx4 v[28:31], v0, s[8:11], s7 offen offset:1024 sc1\n"
"buffer_load_dwordx4 v[236:239], v0, s[8:11], s7 offen offset:2048 sc1\n"
"buffer_load_dwordx4 v[248:251], v0, s[8:11], s7 offen offset:3072 sc1\n"
"s_add_u32 s7, s58, 0x61000\n"
"buffer_load_dwordx4 v[164:167], v0, s[8:11], s7 offen offset:0 sc1\n"
"s_waitcnt vmcnt(0)\n"
"v_max3_u32 v116, v168, v169, v170\n"
"v_max3_u32 v116, v116, v171, v196\n"
"v_max3_u32 v116, v116, v197, v198\n"
"v_max_u32 v116, v116, v199\n"
"v_cmp_eq_u32 vcc, -1, v116\n"
"s_cbranch_vccz Lb82_%=\n"
"s_sub_u32 s45, s45, 1\n"
"s_cbranch_scc0 Ls82_%=\n"
"s_mov_b32 s45, 0\n"
"s_branch Lb82_%=\n"
"Ls83_%=:\n"
"s_sleep 2\n"
"s_add_u32 s7, s57, 0x60400\n"
"buffer_load_dwordx4 v[228:231], v0, s[8:11], s7 offen offset:0 sc1\n"
"buffer_load_dwordx4 v[136:139], v0, s[8:11], s7 offen offset:1024 sc1\n"
"buffer_load_dwordx4 v[36:39], v0, s[8:11], s7 offen offset:2048 sc1\n"
"buffer_load_dwordx4 v[216:219], v0, s[8:11], s7 offen offset:3072 sc1\n"
"s_add_u32 s7, s58, 0x60400\n"
"buffer_load_dwordx4 v[28:31], v0, s[8:11], s7 offen offset:0 sc1\n"
"buffer_load_dwordx4 v[236:239], v0, s[8:11], s7 offen offset:1024 sc1\n"
"buffer_load_dwordx4 v[248:251], v0, s[8:11], s7 offen offset:2048 sc1\n"
"buffer_load_dwordx4 v[164:167], v0, s[8:11], s7 offen offset:3072 sc1\n"
"s_waitcnt vmcnt(0)\n"
"v_max3_u32 v212, v228, v229, v230\n"
"v_max3_u32 v212, v212, v231, v28\n"
"v_max3_u32 v212, v212, v29, v30\n"
"v_max_u32 v212, v212, v31\n"
"v_cmp_eq_u32 vcc, -1, v212\n"
"s_cbranch_vccz Lb83_%=\n"
"s_sub_u32 s45, s45, 1\n"
"s_cbranch_scc0 Ls83_%=\n"
"s_mov_b32 s45, 0\n"
"s_branch Lb83_%=\n"
"Ls84_%=:\n"
"s_sleep 2\n"
"s_add_u32 s7, s57, 0x60800\n"
"buffer_load_dwordx4 v[136:139], v0, s[8:11], s7 offen offset:0 sc1\n"
"buffer_load_dwordx4 v[36:39], v0, s[8:11], s7 offen offset:1024 sc1\n"
"buffer_load_dwordx4 v[216:219], v0, s[8:11], s7 offen offset:2048 sc1\n"
"s_add_u32 s7, s58, 0x60800\n"
"buffer_load_dwordx4 v[236:239], v0, s[8:11], s7 offen offset:0 sc1\n"
"buffer_load_dwordx4 v[248:251], v0, s[8:11], s7 offen offset:1024 sc1\n"
"buffer_load_dwordx4 v[164:167], v0, s[8:11], s7 offen offset:2048 sc1\n"
"s_waitcnt vmcnt(0)\n"
"v_max3_u32 v100, v136, v137, v138\n"
"v_max3_u32 v100, v100, v139, v236\n"
"v_max3_u32 v100, v100, v237, v238\n"
"v_max_u32 v100, v100, v239\n"
"v_cmp_eq_u32 vcc, -1, v100\n"
"s_cbranch_vccz Lb84_%=\n"
"s_sub_u32 s45, s45, 1\n"
"s_cbranch_scc0 Ls84_%=\n"
"s_mov_b32 s45, 0\n"
"s_branch Lb84_%=\n"
"Ls85_%=:\n"
"s_sleep 2\n"
"s_add_u32 s7, s57, 0x60c00\n"
"buffer_load_dwordx4 v[36:39], v0, s[8:11], s7 offen offset:0 sc1\n"
"buffer_load_dwordx4 v[216:219], v0, s[8:11], s7 offen offset:1024 sc1\n"
"s_add_u32 s7, s58, 0x60c00\n"
"buffer_load_dwordx4 v[248:251], v0, s[8:11], s7 offen offset:0 sc1\n"
"buffer_load_dwordx4 v[164:167], v0, s[8:11], s7 offen offset:1024 sc1\n"
"s_waitcnt vmcnt(0)\n"
"v_max3_u32 v240, v36, v37, v38\n"
"v_max3_u32 v240, v240, v39, v248\n"
"v_max3_u32 v240, v240, v249, v250\n"
"v_max_u32 v240, v240, v251\n"
"v_cmp_eq_u32 vcc, -1, v240\n"
"s_cbranch_vccz Lb85_%=\n"
"s_sub_u32 s45, s45, 1\n"
"s_cbranch_scc0 Ls85_%=\n"
"s_mov_b32 s45, 0\n"
"s_branch Lb85_%=\n"
"Ls86_%=:\n"
"s_sleep 2\n"
"s_add_u32 s7, s57, 0x61000\n"
"buffer_load_dwordx4 v[216:219], v0, s[8:11], s7 offen offset:0 sc1\n"
"s_add_u32 s7, s58, 0x61000\n"
"buffer_load_dwordx4 v[164:167], v0, s[8:11], s7 offen offset:0 sc1\n"
"s_waitcnt vmcnt(0)\n"
"v_max3_u32 v88, v216, v217, v218\n"
"v_max3_u32 v88, v88, v219, v164\n"
"v_max3_u32 v88, v88, v165, v166\n"
"v_max_u32 v88, v88, v167\n"
"v_cmp_eq_u32 vcc, -1, v88\n"
"s_cbranch_vccz Lb86_%=\n"
"s_sub_u32 s45, s45, 1\n"
"s_cbranch_scc0 Ls86_%=\n"
"s_mov_b32 s45, 0\n"
"s_branch Lb86_%=\n"
"Ls65_%=:\n"
"s_sleep 2\n"
"s_add_u32 s7, s72, 0x4a000\n"
"buffer_load_dwordx4 v[196:199], v7, s[8:11], s7 offen offset:0 sc1\n"
"s_add_u32 s7, s63, 0x40000\n"
"buffer_load_dwordx4 v[72:75], v0, s[8:11], s7 offen offset:0 sc1\n"
"buffer_load_dwordx4 v[148:151], v0, s[8:11], s7 offen offset:1024 sc1\n"
"buffer_load_dwordx4 v[252:255], v0, s[8:11], s7 offen offset:2048 sc1\n"
"buffer_load_dwordx4 v[160:163], v0, s[8:11], s7 offen offset:3072 sc1\n"
"s_add_u32 s7, s63, 0x41000\n"
"buffer_load_dwordx4 v[144:147], v0, s[8:11], s7 offen offset:0 sc1\n"
"s_add_u32 s7, s63, 0x45000\n"
"buffer_load_dwordx4 v[120:123], v0, s[8:11], s7 offen offset:0 sc1\n"
"buffer_load_dwordx4 v[152:155], v0, s[8:11], s7 offen offset:1024 sc1\n"
"buffer_load_dwordx4 v[84:87], v0, s[8:11], s7 offen offset:2048 sc1\n"
"buffer_load_dwordx4 v[56:59], v0, s[8:11], s7 offen offset:3072 sc1\n"
"s_add_u32 s7, s63, 0x46000\n"
"buffer_load_dwordx4 v[168:171], v0, s[8:11], s7 offen offset:0 sc1\n"
"s_waitcnt vmcnt(0)\n"
"v_max3_u32 v184, v72, v73, v74\n"
"v_max3_u32 v184, v184, v75, v148\n"
"v_max3_u32 v184, v184, v149, v150\n"
"v_max3_u32 v184, v184, v151, v252\n"
"v_max3_u32 v184, v184, v253, v254\n"
"v_max_u32 v184, v184, v255\n"
"v_cmp_eq_u32 vcc, -1, v184\n"
"s_cbranch_vccz Lb65_%=\n"
"s_sub_u32 s45, s45, 1\n"
"s_cbranch_scc0 Ls65_%=\n"
"s_mov_b32 s45, 0\n"
"s_branch Lb65_%=\n"
"Ls66_%=:\n"
"s_sleep 2\n"
"s_add_u32 s7, s72, 0x4a000\n"
"buffer_load_dwordx4 v[196:199], v7, s[8:11], s7 offen offset:0 sc1\n"
"s_add_u32 s7, s63, 0x40c00\n"
"buffer_load_dwordx4 v[160:163], v0, s[8:11], s7 offen offset:0 sc1\n"
"buffer_load_dwordx4 v[144:147], v0, s[8:11], s7 offen offset:1024 sc1\n"
"s_add_u32 s7, s63, 0x45000\n"
"buffer_load_dwordx4 v[120:123], v0, s[8:11], s7 offen offset:0 sc1\n"
"buffer_load_dwordx4 v[152:155], v0, s[8:11], s7 offen offset:1024 sc1\n"
"buffer_load_dwordx4 v[84:87], v0, s[8:11], s7 offen offset:2048 sc1\n"
"buffer_load_dwordx4 v[56:59], v0, s[8:11], s7 offen offset:3072 sc1\n"
"s_add_u32 s7, s63, 0x46000\n"
"buffer_load_dwordx4 v[168:171], v0, s[8:11], s7 offen offset:0 sc1\n"
"s_waitcnt vmcnt(0)\n"
"v_max3_u32 v28, v160, v161, v162\n"
"v_max3_u32 v28, v28, v163, v144\n"
"v_max3_u32 v28, v28, v145, v146\n"
"v_max_u32 v28, v28, v147\n"
"v_cmp_eq_u32 vcc, -1, v28\n"
"s_cbranch_vccz Lb66_%=\n"
"s_sub_u32 s45, s45, 1\n"
"s_cbranch_scc0 Ls66_%=\n"
"s_mov_b32 s45, 0\n"
"s_branch Lb66_%=\n"
"Ls67_%=:\n"
"s_sleep 2\n"
"s_add_u32 s7, s72, 0x4a000\n"
"buffer_load_dwordx4 v[196:199], v7, s[8:11], s7 offen offset:0 sc1\n"
"s_add_u32 s7, s63, 0x45000\n"
"buffer_load_dwordx4 v[120:123], v0, s[8:11], s7 offen offset:0 sc1\n"
"buffer_load_dwordx4 v[152:155], v0, s[8:11], s7 offen offset:1024 sc1\n"
"buffer_load_dwordx4 v[84:87], v0, s[8:11], s7 offen offset:2048 sc1\n"
"buffer_load_dwordx4 v[56:59], v0, s[8:11], s7 offen offset:3072 sc1\n"
"s_add_u32 s7, s63, 0x46000\n"
"buffer_load_dwordx4 v[168:171], v0, s[8:11], s7 offen offset:0 sc1\n"
"s_waitcnt vmcnt(0)\n"
"v_max3_u32 v136, v120, v121, v122\n"
"v_max3_u32 v136, v136, v123, v152\n"
"v_max3_u32 v136, v136, v153, v154\n"
"v_max_u32 v136, v136, v155\n"
"v_cmp_eq_u32 vcc, -1, v136\n"
"s_cbranch_vccz Lb67_%=\n"
"s_sub_u32 s45, s45, 1\n"
"s_cbranch_scc0 Ls67_%=\n"
"s_mov_b32 s45, 0\n"
"s_branch Lb67_%=\n"
"Ls68_%=:\n"
"s_sleep 2\n"
"s_add_u32 s7, s72, 0x4a000\n"
"buffer_load_dwordx4 v[196:199], v7, s[8:11], s7 offen offset:0 sc1\n"
"s_add_u32 s7, s63, 0x45800\n"
"buffer_load_dwordx4 v[84:87], v0, s[8:11], s7 offen offset:0 sc1\n"
"buffer_load_dwordx4 v[56:59], v0, s[8:11], s7 offen offset:1024 sc1\n"
"buffer_load_dwordx4 v[168:171], v0, s[8:11], s7 offen offset:2048 sc1\n"
"s_waitcnt vmcnt(0)\n"
"v_max3_u32 v180, v84, v85, v86\n"
"v_max3_u32 v180, v180, v87, v56\n"
"v_max3_u32 v180, v180, v57, v58\n"
"v_max_u32 v180, v180, v59\n"
"v_cmp_eq_u32 vcc, -1, v180\n"
"s_cbranch_vccz Lb68_%=\n"
"s_sub_u32 s45, s45, 1\n"
"s_cbranch_scc0 Ls68_%=\n"
"s_mov_b32 s45, 0\n"
"s_branch Lb68_%=\n"
"Ls69_%=:\n"
"s_sleep 2\n"
"s_add_u32 s7, s72, 0x4a000\n"
"buffer_load_dwordx4 v[196:199], v7, s[8:11], s7 offen offset:0 sc1\n"
"s_add_u32 s7, s63, 0x46000\n"
"buffer_load_dwordx4 v[168:171], v0, s[8:11], s7 offen offset:0 sc1\n"
"s_waitcnt vmcnt(0)\n"
"v_max3_u32 v236, v168, v169, v170\n"
"v_max3_u32 v236, v236, v171, v196\n"
"v_max3_u32 v236, v236, v197, v198\n"
"v_max_u32 v236, v236, v199\n"
"v_cmp_eq_u32 vcc, -1, v236\n"
"s_cbranch_vccz Lb69_%=\n"
"s_sub_u32 s45, s45, 1\n"
"s_cbranch_scc0 Ls69_%=\n"
"s_mov_b32 s45, 0\n"
"s_branch Lb69_%=\n"
"Ls87_%=:\n"
"s_sleep 2\n"
"s_add_u32 s7, s72, 0x55000\n"
"buffer_load_dwordx4 v[64:67], v7, s[8:11], s7 offen offset:0 sc1\n"
"s_add_u32 s7, s63, 0x4b000\n"
"buffer_load_dwordx4 v[92:95], v0, s[8:11], s7 offen offset:0 sc1\n"
"buffer_load_dwordx4 v[176:179], v0, s[8:11], s7 offen offset:1024 sc1\n"
"buffer_load_dwordx4 v[184:187], v0, s[8:11], s7 offen offset:2048 sc1\n"
"buffer_load_dwordx4 v[252:255], v0, s[8:11], s7 offen offset:3072 sc1\n"
"s_add_u32 s7, s63, 0x4c000\n"
"buffer_load_dwordx4 v[144:147], v0, s[8:11], s7 offen offset:0 sc1\n"
"s_add_u32 s7, s63, 0x50000\n"
"buffer_load_dwordx4 v[136:139], v0, s[8:11], s7 offen offset:0 sc1\n"
"buffer_load_dwordx4 v[152:155], v0, s[8:11], s7 offen offset:1024 sc1\n"
"buffer_load_dwordx4 v[180:183], v0, s[8:11], s7 offen offset:2048 sc1\n"
"buffer_load_dwordx4 v[168:171], v0, s[8:11], s7 offen offset:3072 sc1\n"
"s_add_u32 s7, s63, 0x51000\n"
"buffer_load_dwordx4 v[196:199], v0, s[8:11], s7 offen offset:0 sc1\n"
"s_waitcnt vmcnt(0)\n"
"v_max3_u32 v124, v92, v93, v94\n"
"v_max3_u32 v124, v124, v95, v176\n"
"v_max3_u32 v124, v124, v177, v178\n"
"v_max3_u32 v124, v124, v179, v184\n"
"v_max3_u32 v124, v124, v185, v186\n"
"v_max_u32 v124, v124, v187\n"
"v_cmp_eq_u32 vcc, -1, v124\n"
"s_cbranch_vccz Lb87_%=\n"
"s_sub_u32 s45, s45, 1\n"
"s_cbranch_scc0 Ls87_%=\n"
"s_mov_b32 s45, 0\n"
"s_branch Lb87_%=\n"
"Ls88_%=:\n"
"s_sleep 2\n"
"s_add_u32 s7, s72, 0x55000\n"
"buffer_load_dwordx4 v[64:67], v7, s[8:11], s7 offen offset:0 sc1\n"
"s_add_u32 s7, s63, 0x4bc00\n"
"buffer_load_dwordx4 v[252:255], v0, s[8:11], s7 offen offset:0 sc1\n"
"buffer_load_dwordx4 v[144:147], v0, s[8:11], s7 offen offset:1024 sc1\n"
"s_add_u32 s7, s63, 0x50000\n"
"buffer_load_dwordx4 v[136:139], v0, s[8:11], s7 offen offset:0 sc1\n"
"buffer_load_dwordx4 v[152:155], v0, s[8:11], s7 offen offset:1024 sc1\n"
"buffer_load_dwordx4 v[180:183], v0, s[8:11], s7 offen offset:2048 sc1\n"
"buffer_load_dwordx4 v[168:171], v0, s[8:11], s7 offen offset:3072 sc1\n"
"s_add_u32 s7, s63, 0x51000\n"
"buffer_load_dwordx4 v[196:199], v0, s[8:11], s7 offen offset:0 sc1\n"
"s_waitcnt vmcnt(0)\n"
"v_max3_u32 v68, v252, v253, v254\n"
"v_max3_u32 v68, v68, v255, v144\n"
"v_max3_u32 v68, v68, v145, v146\n"
"v_max_u32 v68, v68, v147\n"
"v_cmp_eq_u32 vcc, -1, v68\n"
"s_cbranch_vccz Lb88_%=\n"
"s_sub_u32 s45, s45, 1\n"
"s_cbranch_scc0 Ls88_%=\n"
"s_mov_b32 s45, 0\n"
"s_branch Lb88_%=\n"
"Ls89_%=:\n"
"s_sleep 2\n"
"s_add_u32 s7, s72, 0x55000\n"
"buffer_load_dwordx4 v[64:67], v7, s[8:11], s7 offen offset:0 sc1\n"
"s_add_u32 s7, s63, 0x50000\n"
"buffer_load_dwordx4 v[136:139], v0, s[8:11], s7 offen offset:0 sc1\n"
"buffer_load_dwordx4 v[152:155], v0, s[8:11], s7 offen offset:1024 sc1\n"
"buffer_load_dwordx4 v[180:183], v0, s[8:11], s7 offen offset:2048 sc1\n"
"buffer_load_dwordx4 v[168:171], v0, s[8:11], s7 offen offset:3072 sc1\n"
"s_add_u32 s7, s63, 0x51000\n"
"buffer_load_dwordx4 v[196:199], v0, s[8:11], s7 offen offset:0 sc1\n"
"s_waitcnt vmcnt(0)\n"
"v_max3_u32 v228, v136, v137, v138\n"
"v_max3_u32 v228, v228, v139, v152\n"
"v_max3_u32 v228, v228, v153, v154\n"
"v_max_u32 v228, v228, v155\n"
"v_cmp_eq_u32 vcc, -1, v228\n"
"s_cbranch_vccz Lb89_%=\n"
"s_sub_u32 s45, s45, 1\n"
"s_cbranch_scc0 Ls89_%=\n"
"s_mov_b32 s45, 0\n"
"s_branch Lb89_%=\n"
"Ls90_%=:\n"
"s_sleep 2\n"
"s_add_u32 s7, s72, 0x55000\n"
"buffer_load_dwordx4 v[64:67], v7, s[8:11], s7 offen offset:0 sc1\n"
"s_add_u32 s7, s63, 0x50800\n"
"buffer_load_dwordx4 v[180:183], v0, s[8:11], s7 offen offset:0 sc1\n"
"buffer_load_dwordx4 v[168:171], v0, s[8:11], s7 offen offset:1024 sc1\n"
"buffer_load_dwordx4 v[196:199], v0, s[8:11], s7 offen offset:2048 sc1\n"
"s_waitcnt vmcnt(0)\n"
"v_max3_u32 v120, v180, v181, v182\n"
"v_max3_u32 v120, v120, v183, v168\n"
"v_max3_u32 v120, v120, v169, v170\n"
"v_max_u32 v120, v120, v171\n"
"v_cmp_eq_u32 vcc, -1, v120\n"
"s_cbranch_vccz Lb90_%=\n"
"s_sub_u32 s45, s45, 1\n"
"s_cbranch_scc0 Ls90_%=\n"
"s_mov_b32 s45, 0\n"
"s_branch Lb90_%=\n"
"Ls91_%=:\n"
"s_sleep 2\n"
"s_add_u32 s7, s72, 0x55000\n"
"buffer_load_dwordx4 v[64:67], v7, s[8:11], s7 offen offset:0 sc1\n"
"s_add_u32 s7, s63, 0x51000\n"
"buffer_load_dwordx4 v[196:199], v0, s[8:11], s7 offen offset:0 sc1\n"
"s_waitcnt vmcnt(0)\n"
"v_max3_u32 v236, v196, v197, v198\n"
"v_max3_u32 v236, v236, v199, v64\n"
"v_max3_u32 v236, v236, v65, v66\n"
"v_max_u32 v236, v236, v67\n"
"v_cmp_eq_u32 vcc, -1, v236\n"
"s_cbranch_vccz Lb91_%=\n"
"s_sub_u32 s45, s45, 1\n"
"s_cbranch_scc0 Ls91_%=\n"
"s_mov_b32 s45, 0\n"
"s_branch Lb91_%=\n"
"Ls70_%=:\n"
"s_sleep 2\n"
"s_bitcmp1_b32 s61, 0\n"
"s_cselect_b32 s7, s60, s59\n"
"s_add_u32 s7, s7, 0x6a000\n"
"buffer_load_dwordx4 v[96:99], v16, s[8:11], s7 offen sc1\n"
"buffer_load_dwordx4 v[120:123], v17, s[8:11], s7 offen sc1\n"
"s_bitcmp1_b32 s61, 1\n"
"s_cselect_b32 s7, s60, s59\n"
"s_add_u32 s7, s7, 0x6a400\n"
"buffer_load_dwordx4 v[84:87], v16, s[8:11], s7 offen sc1\n"
"buffer_load_dwordx4 v[44:47], v17, s[8:11], s7 offen sc1\n"
"s_bitcmp1_b32 s61, 2\n"
"s_cselect_b32 s7, s60, s59\n"
"s_add_u32 s7, s7, 0x6a800\n"
"buffer_load_dwordx4 v[240:243], v16, s[8:11], s7 offen sc1\n"
"buffer_load_dwordx4 v[112:115], v17, s[8:11], s7 offen sc1\n"
"s_bitcmp1_b32 s61, 3\n"
"s_cselect_b32 s7, s60, s59\n"
"s_add_u32 s7, s7, 0x6ac00\n"
"buffer_load_dwordx4 v[128:131], v16, s[8:11], s7 offen sc1\n"
"buffer_load_dwordx4 v[188:191], v17, s[8:11], s7 offen sc1\n"
"s_bitcmp1_b32 s61, 4\n"
"s_cselect_b32 s7, s60, s59\n"
"s_add_u32 s7, s7, 0x6b000\n"
"buffer_load_dwordx4 v[204:207], v16, s[8:11], s7 offen sc1\n"
"buffer_load_dwordx4 v[152:155], v17, s[8:11], s7 offen sc1\n"
"s_bitcmp1_b32 s61, 5\n"
"s_cselect_b32 s7, s60, s59\n"
"s_add_u32 s7, s7, 0x6b400\n"
"buffer_load_dwordx4 v[116:119], v16, s[8:11], s7 offen sc1\n"
"buffer_load_dwordx4 v[32:35], v17, s[8:11], s7 offen sc1\n"
"s_waitcnt vmcnt(0)\n"
"v_min3_u32 v92, v96, v97, v98\n"
"v_min3_u32 v92, v92, v99, v120\n"
"v_min3_u32 v92, v92, v121, v122\n"
"v_min_u32 v92, v92, v123\n"
"v_cmp_lt_i32 vcc, -1, v92\n"
"s_cbranch_vccz Lb70_%=\n"
"s_sub_u32 s45, s45, 1\n"
"s_cbranch_scc0 Ls70_%=\n"
"s_mov_b32 s45, 0\n"
"s_branch Lb70_%=\n"
"Ls71_%=:\n"
"s_sleep 2\n"
"s_bitcmp1_b32 s61, 1\n"
"s_cselect_b32 s7, s60, s59\n"
"s_add_u32 s7, s7, 0x6a400\n"
"buffer_load_dwordx4 v[84:87], v16, s[8:11], s7 offen sc1\n"
"buffer_load_dwordx4 v[44:47], v17, s[8:11], s7 offen sc1\n"
"s_bitcmp1_b32 s61, 2\n"
"s_cselect_b32 s7, s60, s59\n"
"s_add_u32 s7, s7, 0x6a800\n"
"buffer_load_dwordx4 v[240:243], v16, s[8:11], s7 offen sc1\n"
"buffer_load_dwordx4 v[112:115], v17, s[8:11], s7 offen sc1\n"
"s_bitcmp1_b32 s61, 3\n"
"s_cselect_b32 s7, s60, s59\n"
"s_add_u32 s7, s7, 0x6ac00\n"
"buffer_load_dwordx4 v[128:131], v16, s[8:11], s7 offen sc1\n"
"buffer_load_dwordx4 v[188:191], v17, s[8:11], s7 offen sc1\n"
"s_bitcmp1_b32 s61, 4\n"
"s_cselect_b32 s7, s60, s59\n"
"s_add_u32 s7, s7, 0x6b000\n"
"buffer_load_dwordx4 v[204:207], v16, s[8:11], s7 offen sc1\n"
"buffer_load_dwordx4 v[152:155], v17, s[8:11], s7 offen sc1\n"
"s_bitcmp1_b32 s61, 5\n"
"s_cselect_b32 s7, s60, s59\n"
"s_add_u32 s7, s7, 0x6b400\n"
"buffer_load_dwordx4 v[116:119], v16, s[8:11], s7 offen sc1\n"
"buffer_load_dwordx4 v[32:35], v17, s[8:11], s7 offen sc1\n"
"s_waitcnt vmcnt(0)\n"
"v_min3_u32 v208, v84, v85, v86\n"
"v_min3_u32 v208, v208, v87, v44\n"
"v_min3_u32 v208, v208, v45, v46\n"
"v_min_u32 v208, v208, v47\n"
"v_cmp_lt_i32 vcc, -1, v208\n"
"s_cbranch_vccz Lb71_%=\n"
"s_sub_u32 s45, s45, 1\n"
"s_cbranch_scc0 Ls71_%=\n"
"s_mov_b32 s45, 0\n"
"s_branch Lb71_%=\n"
"Ls72_%=:\n"
"s_sleep 2\n"
"s_bitcmp1_b32 s61, 2\n"
"s_cselect_b32 s7, s60, s59\n"
"s_add_u32 s7, s7, 0x6a800\n"
"buffer_load_dwordx4 v[240:243], v16, s[8:11], s7 offen sc1\n"
"buffer_load_dwordx4 v[112:115], v17, s[8:11], s7 offen sc1\n"
"s_bitcmp1_b32 s61, 3\n"
"s_cselect_b32 s7, s60, s59\n"
"s_add_u32 s7, s7, 0x6ac00\n"
"buffer_load_dwordx4 v[128:131], v16, s[8:11], s7 offen sc1\n"
"buffer_load_dwordx4 v[188:191], v17, s[8:11], s7 offen sc1\n"
"s_bitcmp1_b32 s61, 4\n"
"s_cselect_b32 s7, s60, s59\n"
"s_add_u32 s7, s7, 0x6b000\n"
"buffer_load_dwordx4 v[204:207], v16, s[8:11], s7 offen sc1\n"
"buffer_load_dwordx4 v[152:155], v17, s[8:11], s7 offen sc1\n"
"s_bitcmp1_b32 s61, 5\n"
"s_cselect_b32 s7, s60, s59\n"
"s_add_u32 s7, s7, 0x6b400\n"
"buffer_load_dwordx4 v[116:119], v16, s[8:11], s7 offen sc1\n"
"buffer_load_dwordx4 v[32:35], v17, s[8:11], s7 offen sc1\n"
"s_waitcnt vmcnt(0)\n"
"v_min3_u32 v216, v240, v241, v242\n"
"v_min3_u32 v216, v216, v243, v112\n"
"v_min3_u32 v216, v216, v113, v114\n"
"v_min_u32 v216, v216, v115\n"
"v_cmp_lt_i32 vcc, -1, v216\n"
"s_cbranch_vccz Lb72_%=\n"
"s_sub_u32 s45, s45, 1\n"
"s_cbranch_scc0 Ls72_%=\n"
"s_mov_b32 s45, 0\n"
"s_branch Lb72_%=\n"
"Ls73_%=:\n"
"s_sleep 2\n"
"s_bitcmp1_b32 s61, 3\n"
"s_cselect_b32 s7, s60, s59\n"
"s_add_u32 s7, s7, 0x6ac00\n"
"buffer_load_dwordx4 v[128:131], v16, s[8:11], s7 offen sc1\n"
"buffer_load_dwordx4 v[188:191], v17, s[8:11], s7 offen sc1\n"
"s_bitcmp1_b32 s61, 4\n"
"s_cselect_b32 s7, s60, s59\n"
"s_add_u32 s7, s7, 0x6b000\n"
"buffer_load_dwordx4 v[204:207], v16, s[8:11], s7 offen sc1\n"
"buffer_load_dwordx4 v[152:155], v17, s[8:11], s7 offen sc1\n"
"s_bitcmp1_b32 s61, 5\n"
"s_cselect_b32 s7, s60, s59\n"
"s_add_u32 s7, s7, 0x6b400\n"
"buffer_load_dwordx4 v[116:119], v16, s[8:11], s7 offen sc1\n"
"buffer_load_dwordx4 v[32:35], v17, s[8:11], s7 offen sc1\n"
"s_bitcmp1_b32 s61, 6\n"
"s_cselect_b32 s7, s60, s59\n"
"s_add_u32 s7, s7, 0x6b800\n"
"buffer_load_dwordx4 v[244:247], v16, s[8:11], s7 offen sc1\n"
"buffer_load_dwordx4 v[140:143], v17, s[8:11], s7 offen sc1\n"
"s_waitcnt vmcnt(0)\n"
"v_min3_u32 v72, v128, v129, v130\n"
"v_min3_u32 v72, v72, v131, v188\n"
"v_min3_u32 v72, v72, v189, v190\n"
"v_min_u32 v72, v72, v191\n"
"v_cmp_lt_i32 vcc, -1, v72\n"
"s_cbranch_vccz Lb73_%=\n"
"s_sub_u32 s45, s45, 1\n"
"s_cbranch_scc0 Ls73_%=\n"
"s_mov_b32 s45, 0\n"
"s_branch Lb73_%=\n"
"Ls74_%=:\n"
"s_sleep 2\n"
"s_bitcmp1_b32 s61, 4\n"
"s_cselect_b32 s7, s60, s59\n"
"s_add_u32 s7, s7, 0x6b000\n"
"buffer_load_dwordx4 v[204:207], v16, s[8:11], s7 offen sc1\n"
"buffer_load_dwordx4 v[152:155], v17, s[8:11], s7 offen sc1\n"
"s_bitcmp1_b32 s61, 5\n"
"s_cselect_b32 s7, s60, s59\n"
"s_add_u32 s7, s7, 0x6b400\n"
"buffer_load_dwordx4 v[116:119], v16, s[8:11], s7 offen sc1\n"
"buffer_load_dwordx4 v[32:35], v17, s[8:11], s7 offen sc1\n"
"s_bitcmp1_b32 s61, 6\n"
"s_cselect_b32 s7, s60, s59\n"
"s_add_u32 s7, s7, 0x6b800\n"
"buffer_load_dwordx4 v[244:247], v16, s[8:11], s7 offen sc1\n"
"buffer_load_dwordx4 v[140:143], v17, s[8:11], s7 offen sc1\n"
"s_bitcmp1_b32 s61, 7\n"
"s_cselect_b32 s7, s60, s59\n"
"s_add_u32 s7, s7, 0x6bc00\n"
"buffer_load_dwordx4 v[92:95], v16, s[8:11], s7 offen sc1\n"
"s_waitcnt vmcnt(0)\n"
"v_min3_u32 v36, v204, v205, v206\n"
"v_min3_u32 v36, v36, v207, v152\n"
"v_min3_u32 v36, v36, v153, v154\n"
"v_min_u32 v36, v36, v155\n"
"v_cmp_lt_i32 vcc, -1, v36\n"
"s_cbranch_vccz Lb74_%=\n"
"s_sub_u32 s45, s45, 1\n"
"s_cbranch_scc0 Ls74_%=\n"
"s_mov_b32 s45, 0\n"
"s_branch Lb74_%=\n"
"Ls75_%=:\n"
"s_sleep 2\n"
"s_bitcmp1_b32 s61, 5\n"
"s_cselect_b32 s7, s60, s59\n"
"s_add_u32 s7, s7, 0x6b400\n"
"buffer_load_dwordx4 v[116:119], v16, s[8:11], s7 offen sc1\n"
"buffer_load_dwordx4 v[32:35], v17, s[8:11], s7 offen sc1\n"
"s_bitcmp1_b32 s61, 6\n"
"s_cselect_b32 s7, s60, s59\n"
"s_add_u32 s7, s7, 0x6b800\n"
"buffer_load_dwordx4 v[244:247], v16, s[8:11], s7 offen sc1\n"
"buffer_load_dwordx4 v[140:143], v17, s[8:11], s7 offen sc1\n"
"s_bitcmp1_b32 s61, 7\n"
"s_cselect_b32 s7, s60, s59\n"
"s_add_u32 s7, s7, 0x6bc00\n"
"buffer_load_dwordx4 v[92:95], v16, s[8:11], s7 offen sc1\n"
"buffer_load_dwordx4 v[172:175], v17, s[8:11], s7 offen sc1\n"
"s_waitcnt vmcnt(0)\n"
"v_min3_u32 v192, v116, v117, v118\n"
"v_min3_u32 v192, v192, v119, v32\n"
"v_min3_u32 v192, v192, v33, v34\n"
"v_min_u32 v192, v192, v35\n"
"v_cmp_lt_i32 vcc, -1, v192\n"
"s_cbranch_vccz Lb75_%=\n"
"s_sub_u32 s45, s45, 1\n"
"s_cbranch_scc0 Ls75_%=\n"
"s_mov_b32 s45, 0\n"
"s_branch Lb75_%=\n"
"Ls76_%=:\n"
"s_sleep 2\n"
"s_bitcmp1_b32 s61, 6\n"
"s_cselect_b32 s7, s60, s59\n"
"s_add_u32 s7, s7, 0x6b800\n"
"buffer_load_dwordx4 v[244:247], v16, s[8:11], s7 offen sc1\n"
"buffer_load_dwordx4 v[140:143], v17, s[8:11], s7 offen sc1\n"
"s_bitcmp1_b32 s61, 7\n"
"s_cselect_b32 s7, s60, s59\n"
"s_add_u32 s7, s7, 0x6bc00\n"
"buffer_load_dwordx4 v[92:95], v16, s[8:11], s7 offen sc1\n"
"buffer_load_dwordx4 v[172:175], v17, s[8:11], s7 offen sc1\n"
"s_bitcmp1_b32 s61, 8\n"
"s_cselect_b32 s7, s60, s59\n"
"s_add_u32 s7, s7, 0x6c000\n"
"buffer_load_dwordx4 v[252:255], v16, s[8:11], s7 offen sc1\n"
"buffer_load_dwordx4 v[124:127], v17, s[8:11], s7 offen sc1\n"
"s_bitcmp1_b32 s61, 9\n"
"s_cselect_b32 s7, s60, s59\n"
"s_add_u32 s7, s7, 0x6c400\n"
"buffer_load_dwordx4 v[40:43], v16, s[8:11], s7 offen sc1\n"
"buffer_load_dwordx4 v[88:91], v17, s[8:11], s7 offen sc1\n"
"s_bitcmp1_b32 s61, 10\n"
"s_cselect_b32 s7, s60, s59\n"
"s_add_u32 s7, s7, 0x6c800\n"
"buffer_load_dwordx4 v[164:167], v16, s[8:11], s7 offen sc1\n"
"buffer_load_dwordx4 v[184:187], v17, s[8:11], s7 offen sc1\n"
"s_waitcnt vmcnt(0)\n"
"v_min3_u32 v208, v244, v245, v246\n"
"v_min3_u32 v208, v208, v247, v140\n"
"v_min3_u32 v208, v208, v141, v142\n"
"v_min_u32 v208, v208, v143\n"
"v_cmp_lt_i32 vcc, -1, v208\n"
"s_cbranch_vccz Lb76_%=\n"
"s_sub_u32 s45, s45, 1\n"
"s_cbranch_scc0 Ls76_%=\n"
"s_mov_b32 s45, 0\n"
"s_branch Lb76_%=\n"
"Ls77_%=:\n"
"s_sleep 2\n"
"s_bitcmp1_b32 s61, 7\n"
"s_cselect_b32 s7, s60, s59\n"
"s_add_u32 s7, s7, 0x6bc00\n"
"buffer_load_dwordx4 v[92:95], v16, s[8:11], s7 offen sc1\n"
"buffer_load_dwordx4 v[172:175], v17, s[8:11], s7 offen sc1\n"
"s_bitcmp1_b32 s61, 8\n"
"s_cselect_b32 s7, s60, s59\n"
"s_add_u32 s7, s7, 0x6c000\n"
"buffer_load_dwordx4 v[252:255], v16, s[8:11], s7 offen sc1\n"
"buffer_load_dwordx4 v[124:127], v17, s[8:11], s7 offen sc1\n"
"s_bitcmp1_b32 s61, 9\n"
"s_cselect_b32 s7, s60, s59\n"
"s_add_u32 s7, s7, 0x6c400\n"
"buffer_load_dwordx4 v[40:43], v16, s[8:11], s7 offen sc1\n"
"buffer_load_dwordx4 v[88:91], v17, s[8:11], s7 offen sc1\n"
"s_bitcmp1_b32 s61, 10\n"
"s_cselect_b32 s7, s60, s59\n"
"s_add_u32 s7, s7, 0x6c800\n"
"buffer_load_dwordx4 v[164:167], v16, s[8:11], s7 offen sc1\n"
"buffer_load_dwordx4 v[184:187], v17, s[8:11], s7 offen sc1\n"
"s_bitcmp1_b32 s61, 11\n"
"s_cselect_b32 s7, s60, s59\n"
"s_add_u32 s7, s7, 0x6cc00\n"
"buffer_load_dwordx4 v[196:199], v16, s[8:11], s7 offen sc1\n"
"buffer_load_dwordx4 v[220:223], v17, s[8:11], s7 offen sc1\n"
"s_waitcnt vmcnt(0)\n"
"v_min3_u32 v216, v92, v93, v94\n"
"v_min3_u32 v216, v216, v95, v172\n"
"v_min3_u32 v216, v216, v173, v174\n"
"v_min_u32 v216, v216, v175\n"
"v_cmp_lt_i32 vcc, -1, v216\n"
"s_cbranch_vccz Lb77_%=\n"
"s_sub_u32 s45, s45, 1\n"
"s_cbranch_scc0 Ls77_%=\n"
"s_mov_b32 s45, 0\n"
"s_branch Lb77_%=\n"
"Ls78_%=:\n"
"s_sleep 2\n"
"s_bitcmp1_b32 s61, 8\n"
"s_cselect_b32 s7, s60, s59\n"
"s_add_u32 s7, s7, 0x6c000\n"
"buffer_load_dwordx4 v[252:255], v16, s[8:11], s7 offen sc1\n"
"buffer_load_dwordx4 v[124:127], v17, s[8:11], s7 offen sc1\n"
"s_bitcmp1_b32 s61, 9\n"
"s_cselect_b32 s7, s60, s59\n"
"s_add_u32 s7, s7, 0x6c400\n"
"buffer_load_dwordx4 v[40:43], v16, s[8:11], s7 offen sc1\n"
"buffer_load_dwordx4 v[88:91], v17, s[8:11], s7 offen sc1\n"
"s_bitcmp1_b32 s61, 10\n"
"s_cselect_b32 s7, s60, s59\n"
"s_add_u32 s7, s7, 0x6c800\n"
"buffer_load_dwordx4 v[164:167], v16, s[8:11], s7 offen sc1\n"
"buffer_load_dwordx4 v[184:187], v17, s[8:11], s7 offen sc1\n"
"s_bitcmp1_b32 s61, 11\n"
"s_cselect_b32 s7, s60, s59\n"
"s_add_u32 s7, s7, 0x6cc00\n"
"buffer_load_dwordx4 v[196:199], v16, s[8:11], s7 offen sc1\n"
"buffer_load_dwordx4 v[220:223], v17, s[8:11], s7 offen sc1\n"
"s_waitcnt vmcnt(0)\n"
"v_min3_u32 v148, v252, v253, v254\n"
"v_min3_u32 v148, v148, v255, v124\n"
"v_min3_u32 v148, v148, v125, v126\n"
"v_min_u32 v148, v148, v127\n"
"v_cmp_lt_i32 vcc, -1, v148\n"
"s_cbranch_vccz Lb78_%=\n"
"s_sub_u32 s45, s45, 1\n"
"s_cbranch_scc0 Ls78_%=\n"
"s_mov_b32 s45, 0\n"
"s_branch Lb78_%=\n"
"Ls79_%=:\n"
"s_sleep 2\n"
"s_bitcmp1_b32 s61, 9\n"
"s_cselect_b32 s7, s60, s59\n"
"s_add_u32 s7, s7, 0x6c400\n"
"buffer_load_dwordx4 v[40:43], v16, s[8:11], s7 offen sc1\n"
"buffer_load_dwordx4 v[88:91], v17, s[8:11], s7 offen sc1\n"
"s_bitcmp1_b32 s61, 10\n"
"s_cselect_b32 s7, s60, s59\n"
"s_add_u32 s7, s7, 0x6c800\n"
"buffer_load_dwordx4 v[164:167], v16, s[8:11], s7 offen sc1\n"
"buffer_load_dwordx4 v[184:187], v17, s[8:11], s7 offen sc1\n"
"s_bitcmp1_b32 s61, 11\n"
"s_cselect_b32 s7, s60, s59\n"
"s_add_u32 s7, s7, 0x6cc00\n"
"buffer_load_dwordx4 v[196:199], v16, s[8:11], s7 offen sc1\n"
"buffer_load_dwordx4 v[220:223], v17, s[8:11], s7 offen sc1\n"
"s_waitcnt vmcnt(0)\n"
"v_min3_u32 v136, v40, v41, v42\n"
"v_min3_u32 v136, v136, v43, v88\n"
"v_min3_u32 v136, v136, v89, v90\n"
"v_min_u32 v136, v136, v91\n"
"v_cmp_lt_i32 vcc, -1, v136\n"
"s_cbranch_vccz Lb79_%=\n"
"s_sub_u32 s45, s45, 1\n"
"s_cbranch_scc0 Ls79_%=\n"
"s_mov_b32 s45, 0\n"
"s_branch Lb79_%=\n"
"Ls80_%=:\n"
"s_sleep 2\n"
"s_bitcmp1_b32 s61, 10\n"
"s_cselect_b32 s7, s60, s59\n"
"s_add_u32 s7, s7, 0x6c800\n"
"buffer_load_dwordx4 v[164:167], v16, s[8:11], s7 offen sc1\n"
"buffer_load_dwordx4 v[184:187], v17, s[8:11], s7 offen sc1\n"
"s_bitcmp1_b32 s61, 11\n"
"s_cselect_b32 s7, s60, s59\n"
"s_add_u32 s7, s7, 0x6cc00\n"
"buffer_load_dwordx4 v[196:199], v16, s[8:11], s7 offen sc1\n"
"buffer_load_dwordx4 v[220:223], v17, s[8:11], s7 offen sc1\n"
"s_waitcnt vmcnt(0)\n"
"v_min3_u32 v68, v164, v165, v166\n"
"v_min3_u32 v68, v68, v167, v184\n"
"v_min3_u32 v68, v68, v185, v186\n"
"v_min_u32 v68, v68, v187\n"
"v_cmp_lt_i32 vcc, -1, v68\n"
"s_cbranch_vccz Lb80_%=\n"
"s_sub_u32 s45, s45, 1\n"
"s_cbranch_scc0 Ls80_%=\n"
"s_mov_b32 s45, 0\n"
"s_branch Lb80_%=\n"
"Ls81_%=:\n"
"s_sleep 2\n"
"s_bitcmp1_b32 s61, 11\n"
"s_cselect_b32 s7, s60, s59\n"
"s_add_u32 s7, s7, 0x6cc00\n"
"buffer_load_dwordx4 v[196:199], v16, s[8:11], s7 offen sc1\n"
"buffer_load_dwordx4 v[220:223], v17, s[8:11], s7 offen sc1\n"
"s_waitcnt vmcnt(0)\n"
"v_min3_u32 v36, v196, v197, v198\n"
"v_min3_u32 v36, v36, v199, v220\n"
"v_min3_u32 v36, v36, v221, v222\n"
"v_min_u32 v36, v36, v223\n"
"v_cmp_lt_i32 vcc, -1, v36\n"
"s_cbranch_vccz Lb81_%=\n"
"s_sub_u32 s45, s45, 1\n"
"s_cbranch_scc0 Ls81_%=\n"
"s_mov_b32 s45, 0\n"
"s_branch Lb81_%=\n"
"Ls92_%=:\n"
"s_sleep 2\n"
"s_bitcmp1_b32 s61, 0\n"
"s_cselect_b32 s7, s60, s59\n"
"s_add_u32 s7, s7, 0x85000\n"
"buffer_load_dwordx4 v[100:103], v16, s[8:11], s7 offen sc1\n"
"buffer_load_dwordx4 v[76:79], v17, s[8:11], s7 offen sc1\n"
"s_bitcmp1_b32 s61, 1\n"
"s_cselect_b32 s7, s60, s59\n"
"s_add_u32 s7, s7, 0x85400\n"
"buffer_load_dwordx4 v[44:47], v16, s[8:11], s7 offen sc1\n"
"buffer_load_dwordx4 v[240:243], v17, s[8:11], s7 offen sc1\n"
"s_bitcmp1_b32 s61, 2\n"
"s_cselect_b32 s7, s60, s59\n"
"s_add_u32 s7, s7, 0x85800\n"
"buffer_load_dwordx4 v[192:195], v16, s[8:11], s7 offen sc1\n"
"buffer_load_dwordx4 v[112:115], v17, s[8:11], s7 offen sc1\n"
"s_bitcmp1_b32 s61, 3\n"
"s_cselect_b32 s7, s60, s59\n"
"s_add_u32 s7, s7, 0x85c00\n"
"buffer_load_dwordx4 v[236:239], v16, s[8:11], s7 offen sc1\n"
"buffer_load_dwordx4 v[144:147], v17, s[8:11], s7 offen sc1\n"
"s_bitcmp1_b32 s61, 4\n"
"s_cselect_b32 s7, s60, s59\n"
"s_add_u32 s7, s7, 0x86000\n"
"buffer_load_dwordx4 v[224:227], v16, s[8:11], s7 offen sc1\n"
"buffer_load_dwordx4 v[128:131], v17, s[8:11], s7 offen sc1\n"
"s_bitcmp1_b32 s61, 5\n"
"s_cselect_b32 s7, s60, s59\n"
"s_add_u32 s7, s7, 0x86400\n"
"buffer_load_dwordx4 v[188:191], v16, s[8:11], s7 offen sc1\n"
"buffer_load_dwordx4 v[204:207], v17, s[8:11], s7 offen sc1\n"
"s_waitcnt vmcnt(0)\n"
"v_min3_u32 v152, v100, v101, v102\n"
"v_min3_u32 v152, v152, v103, v76\n"
"v_min3_u32 v152, v152, v77, v78\n"
"v_min_u32 v152, v152, v79\n"
"v_cmp_lt_i32 vcc, -1, v152\n"
"s_cbranch_vccz Lb92_%=\n"
"s_sub_u32 s45, s45, 1\n"
"s_cbranch_scc0 Ls92_%=\n"
"s_mov_b32 s45, 0\n"
"s_branch Lb92_%=\n"
"Ls93_%=:\n"
"s_sleep 2\n"
"s_bitcmp1_b32 s61, 1\n"
"s_cselect_b32 s7, s60, s59\n"
"s_add_u32 s7, s7, 0x85400\n"
"buffer_load_dwordx4 v[44:47], v16, s[8:11], s7 offen sc1\n"
"buffer_load_dwordx4 v[240:243], v17, s[8:11], s7 offen sc1\n"
"s_bitcmp1_b32 s61, 2\n"
"s_cselect_b32 s7, s60, s59\n"
"s_add_u32 s7, s7, 0x85800\n"
"buffer_load_dwordx4 v[192:195], v16, s[8:11], s7 offen sc1\n"
"buffer_load_dwordx4 v[112:115], v17, s[8:11], s7 offen sc1\n"
"s_bitcmp1_b32 s61, 3\n"
"s_cselect_b32 s7, s60, s59\n"
"s_add_u32 s7, s7, 0x85c00\n"
"buffer_load_dwordx4 v[236:239], v16, s[8:11], s7 offen sc1\n"
"buffer_load_dwordx4 v[144:147], v17, s[8:11], s7 offen sc1\n"
"s_bitcmp1_b32 s61, 4\n"
"s_cselect_b32 s7, s60, s59\n"
"s_add_u32 s7, s7, 0x86000\n"
"buffer_load_dwordx4 v[224:227], v16, s[8:11], s7 offen sc1\n"
"buffer_load_dwordx4 v[128:131], v17, s[8:11], s7 offen sc1\n"
"s_bitcmp1_b32 s61, 5\n"
"s_cselect_b32 s7, s60, s59\n"
"s_add_u32 s7, s7, 0x86400\n"
"buffer_load_dwordx4 v[188:191], v16, s[8:11], s7 offen sc1\n"
"buffer_load_dwordx4 v[204:207], v17, s[8:11], s7 offen sc1\n"
"s_bitcmp1_b32 s61, 6\n"
"s_cselect_b32 s7, s60, s59\n"
"s_add_u32 s7, s7, 0x86800\n"
"buffer_load_dwordx4 v[116:119], v16, s[8:11], s7 offen sc1\n"
"buffer_load_dwordx4 v[32:35], v17, s[8:11], s7 offen sc1\n"
"s_waitcnt vmcnt(0)\n"
"v_min3_u32 v208, v44, v45, v46\n"
"v_min3_u32 v208, v208, v47, v240\n"
"v_min3_u32 v208, v208, v241, v242\n"
"v_min_u32 v208, v208, v243\n"
"v_cmp_lt_i32 vcc, -1, v208\n"
"s_cbranch_vccz Lb93_%=\n"
"s_sub_u32 s45, s45, 1\n"
"s_cbranch_scc0 Ls93_%=\n"
"s_mov_b32 s45, 0\n"
"s_branch Lb93_%=\n"
"Ls94_%=:\n"
"s_sleep 2\n"
"s_bitcmp1_b32 s61, 2\n"
"s_cselect_b32 s7, s60, s59\n"
"s_add_u32 s7, s7, 0x85800\n"
"buffer_load_dwordx4 v[192:195], v16, s[8:11], s7 offen sc1\n"
"buffer_load_dwordx4 v[112:115], v17, s[8:11], s7 offen sc1\n"
"s_bitcmp1_b32 s61, 3\n"
"s_cselect_b32 s7, s60, s59\n"
"s_add_u32 s7, s7, 0x85c00\n"
"buffer_load_dwordx4 v[236:239], v16, s[8:11], s7 offen sc1\n"
"buffer_load_dwordx4 v[144:147], v17, s[8:11], s7 offen sc1\n"
"s_bitcmp1_b32 s61, 4\n"
"s_cselect_b32 s7, s60, s59\n"
"s_add_u32 s7, s7, 0x86000\n"
"buffer_load_dwordx4 v[224:227], v16, s[8:11], s7 offen sc1\n"
"buffer_load_dwordx4 v[128:131], v17, s[8:11], s7 offen sc1\n"
"s_bitcmp1_b32 s61, 5\n"
"s_cselect_b32 s7, s60, s59\n"
"s_add_u32 s7, s7, 0x86400\n"
"buffer_load_dwordx4 v[188:191], v16, s[8:11], s7 offen sc1\n"
"buffer_load_dwordx4 v[204:207], v17, s[8:11], s7 offen sc1\n"
"s_bitcmp1_b32 s61, 6\n"
"s_cselect_b32 s7, s60, s59\n"
"s_add_u32 s7, s7, 0x86800\n"
"buffer_load_dwordx4 v[116:119], v16, s[8:11], s7 offen sc1\n"
"buffer_load_dwordx4 v[32:35], v17, s[8:11], s7 offen sc1\n"
"s_bitcmp1_b32 s61, 7\n"
"s_cselect_b32 s7, s60, s59\n"
"s_add_u32 s7, s7, 0x86c00\n"
"buffer_load_dwordx4 v[140:143], v16, s[8:11], s7 offen sc1\n"
"s_waitcnt vmcnt(0)\n"
"v_min3_u32 v244, v192, v193, v194\n"
"v_min3_u32 v244, v244, v195, v112\n"
"v_min3_u32 v244, v244, v113, v114\n"
"v_min_u32 v244, v244, v115\n"
"v_cmp_lt_i32 vcc, -1, v244\n"
"s_cbranch_vccz Lb94_%=\n"
"s_sub_u32 s45, s45, 1\n"
"s_cbranch_scc0 Ls94_%=\n"
"s_mov_b32 s45, 0\n"
"s_branch Lb94_%=\n"
"Ls95_%=:\n"
"s_sleep 2\n"
"s_bitcmp1_b32 s61, 3\n"
"s_cselect_b32 s7, s60, s59\n"
"s_add_u32 s7, s7, 0x85c00\n"
"buffer_load_dwordx4 v[236:239], v16, s[8:11], s7 offen sc1\n"
"buffer_load_dwordx4 v[144:147], v17, s[8:11], s7 offen sc1\n"
"s_bitcmp1_b32 s61, 4\n"
"s_cselect_b32 s7, s60, s59\n"
"s_add_u32 s7, s7, 0x86000\n"
"buffer_load_dwordx4 v[224:227], v16, s[8:11], s7 offen sc1\n"
"buffer_load_dwordx4 v[128:131], v17, s[8:11], s7 offen sc1\n"
"s_bitcmp1_b32 s61, 5\n"
"s_cselect_b32 s7, s60, s59\n"
"s_add_u32 s7, s7, 0x86400\n"
"buffer_load_dwordx4 v[188:191], v16, s[8:11], s7 offen sc1\n"
"buffer_load_dwordx4 v[204:207], v17, s[8:11], s7 offen sc1\n"
"s_bitcmp1_b32 s61, 6\n"
"s_cselect_b32 s7, s60, s59\n"
"s_add_u32 s7, s7, 0x86800\n"
"buffer_load_dwordx4 v[116:119], v16, s[8:11], s7 offen sc1\n"
"buffer_load_dwordx4 v[32:35], v17, s[8:11], s7 offen sc1\n"
"s_bitcmp1_b32 s61, 7\n"
"s_cselect_b32 s7, s60, s59\n"
"s_add_u32 s7, s7, 0x86c00\n"
"buffer_load_dwordx4 v[140:143], v16, s[8:11], s7 offen sc1\n"
"buffer_load_dwordx4 v[216:219], v17, s[8:11], s7 offen sc1\n"
"s_bitcmp1_b32 s61, 8\n"
"s_cselect_b32 s7, s60, s59\n"
"s_add_u32 s7, s7, 0x87000\n"
"buffer_load_dwordx4 v[172:175], v16, s[8:11], s7 offen sc1\n"
"s_waitcnt vmcnt(0)\n"
"v_min3_u32 v92, v236, v237, v238\n"
"v_min3_u32 v92, v92, v239, v144\n"
"v_min3_u32 v92, v92, v145, v146\n"
"v_min_u32 v92, v92, v147\n"
"v_cmp_lt_i32 vcc, -1, v92\n"
"s_cbranch_vccz Lb95_%=\n"
"s_sub_u32 s45, s45, 1\n"
"s_cbranch_scc0 Ls95_%=\n"
"s_mov_b32 s45, 0\n"
"s_branch Lb95_%=\n"
"Ls96_%=:\n"
"s_sleep 2\n"
"s_bitcmp1_b32 s61, 4\n"
"s_cselect_b32 s7, s60, s59\n"
"s_add_u32 s7, s7, 0x86000\n"
"buffer_load_dwordx4 v[224:227], v16, s[8:11], s7 offen sc1\n"
"buffer_load_dwordx4 v[128:131], v17, s[8:11], s7 offen sc1\n"
"s_bitcmp1_b32 s61, 5\n"
"s_cselect_b32 s7, s60, s59\n"
"s_add_u32 s7, s7, 0x86400\n"
"buffer_load_dwordx4 v[188:191], v16, s[8:11], s7 offen sc1\n"
"buffer_load_dwordx4 v[204:207], v17, s[8:11], s7 offen sc1\n"
"s_bitcmp1_b32 s61, 6\n"
"s_cselect_b32 s7, s60, s59\n"
"s_add_u32 s7, s7, 0x86800\n"
"buffer_load_dwordx4 v[116:119], v16, s[8:11], s7 offen sc1\n"
"buffer_load_dwordx4 v[32:35], v17, s[8:11], s7 offen sc1\n"
"s_bitcmp1_b32 s61, 7\n"
"s_cselect_b32 s7, s60, s59\n"
"s_add_u32 s7, s7, 0x86c00\n"
"buffer_load_dwordx4 v[140:143], v16, s[8:11], s7 offen sc1\n"
"buffer_load_dwordx4 v[216:219], v17, s[8:11], s7 offen sc1\n"
"s_bitcmp1_b32 s61, 8\n"
"s_cselect_b32 s7, s60, s59\n"
"s_add_u32 s7, s7, 0x87000\n"
"buffer_load_dwordx4 v[172:175], v16, s[8:11], s7 offen sc1\n"
"buffer_load_dwordx4 v[200:203], v17, s[8:11], s7 offen sc1\n"
"s_bitcmp1_b32 s61, 9\n"
"s_cselect_b32 s7, s60, s59\n"
"s_add_u32 s7, s7, 0x87400\n"
"buffer_load_dwordx4 v[136:139], v16, s[8:11], s7 offen sc1\n"
"s_waitcnt vmcnt(0)\n"
"v_min3_u32 v148, v224, v225, v226\n"
"v_min3_u32 v148, v148, v227, v128\n"
"v_min3_u32 v148, v148, v129, v130\n"
"v_min_u32 v148, v148, v131\n"
"v_cmp_lt_i32 vcc, -1, v148\n"
"s_cbranch_vccz Lb96_%=\n"
"s_sub_u32 s45, s45, 1\n"
"s_cbranch_scc0 Ls96_%=\n"
"s_mov_b32 s45, 0\n"
"s_branch Lb96_%=\n"
"Ls97_%=:\n"
"s_sleep 2\n"
"s_bitcmp1_b32 s61, 5\n"
"s_cselect_b32 s7, s60, s59\n"
"s_add_u32 s7, s7, 0x86400\n"
"buffer_load_dwordx4 v[188:191], v16, s[8:11], s7 offen sc1\n"
"buffer_load_dwordx4 v[204:207], v17, s[8:11], s7 offen sc1\n"
"s_bitcmp1_b32 s61, 6\n"
"s_cselect_b32 s7, s60, s59\n"
"s_add_u32 s7, s7, 0x86800\n"
"buffer_load_dwordx4 v[116:119], v16, s[8:11], s7 offen sc1\n"
"buffer_load_dwordx4 v[32:35], v17, s[8:11], s7 offen sc1\n"
"s_bitcmp1_b32 s61, 7\n"
"s_cselect_b32 s7, s60, s59\n"
"s_add_u32 s7, s7, 0x86c00\n"
"buffer_load_dwordx4 v[140:143], v16, s[8:11], s7 offen sc1\n"
"buffer_load_dwordx4 v[216:219], v17, s[8:11], s7 offen sc1\n"
"s_bitcmp1_b32 s61, 8\n"
"s_cselect_b32 s7, s60, s59\n"
"s_add_u32 s7, s7, 0x87000\n"
"buffer_load_dwordx4 v[172:175], v16, s[8:11], s7 offen sc1\n"
"buffer_load_dwordx4 v[200:203], v17, s[8:11], s7 offen sc1\n"
"s_bitcmp1_b32 s61, 9\n"
"s_cselect_b32 s7, s60, s59\n"
"s_add_u32 s7, s7, 0x87400\n"
"buffer_load_dwordx4 v[136:139], v16, s[8:11], s7 offen sc1\n"
"buffer_load_dwordx4 v[80:83], v17, s[8:11], s7 offen sc1\n"
"s_waitcnt vmcnt(0)\n"
"v_min3_u32 v64, v188, v189, v190\n"
"v_min3_u32 v64, v64, v191, v204\n"
"v_min3_u32 v64, v64, v205, v206\n"
"v_min_u32 v64, v64, v207\n"
"v_cmp_lt_i32 vcc, -1, v64\n"
"s_cbranch_vccz Lb97_%=\n"
"s_sub_u32 s45, s45, 1\n"
"s_cbranch_scc0 Ls97_%=\n"
"s_mov_b32 s45, 0\n"
"s_branch Lb97_%=\n"
"Ls98_%=:\n"
"s_sleep 2\n"
"s_bitcmp1_b32 s61, 6\n"
"s_cselect_b32 s7, s60, s59\n"
"s_add_u32 s7, s7, 0x86800\n"
"buffer_load_dwordx4 v[116:119], v16, s[8:11], s7 offen sc1\n"
"buffer_load_dwordx4 v[32:35], v17, s[8:11], s7 offen sc1\n"
"s_bitcmp1_b32 s61, 7\n"
"s_cselect_b32 s7, s60, s59\n"
"s_add_u32 s7, s7, 0x86c00\n"
"buffer_load_dwordx4 v[140:143], v16, s[8:11], s7 offen sc1\n"
"buffer_load_dwordx4 v[216:219], v17, s[8:11], s7 offen sc1\n"
"s_bitcmp1_b32 s61, 8\n"
"s_cselect_b32 s7, s60, s59\n"
"s_add_u32 s7, s7, 0x87000\n"
"buffer_load_dwordx4 v[172:175], v16, s[8:11], s7 offen sc1\n"
"buffer_load_dwordx4 v[200:203], v17, s[8:11], s7 offen sc1\n"
"s_bitcmp1_b32 s61, 9\n"
"s_cselect_b32 s7, s60, s59\n"
"s_add_u32 s7, s7, 0x87400\n"
"buffer_load_dwordx4 v[136:139], v16, s[8:11], s7 offen sc1\n"
"buffer_load_dwordx4 v[80:83], v17, s[8:11], s7 offen sc1\n"
"s_bitcmp1_b32 s61, 10\n"
"s_cselect_b32 s7, s60, s59\n"
"s_add_u32 s7, s7, 0x87800\n"
"buffer_load_dwordx4 v[68:71], v16, s[8:11], s7 offen sc1\n"
"buffer_load_dwordx4 v[36:39], v17, s[8:11], s7 offen sc1\n"
"s_bitcmp1_b32 s61, 11\n"
"s_cselect_b32 s7, s60, s59\n"
"s_add_u32 s7, s7, 0x87c00\n"
"buffer_load_dwordx4 v[252:255], v16, s[8:11], s7 offen sc1\n"
"s_waitcnt vmcnt(0)\n"
"v_min3_u32 v160, v116, v117, v118\n"
"v_min3_u32 v160, v160, v119, v32\n"
"v_min3_u32 v160, v160, v33, v34\n"
"v_min_u32 v160, v160, v35\n"
"v_cmp_lt_i32 vcc, -1, v160\n"
"s_cbranch_vccz Lb98_%=\n"
"s_sub_u32 s45, s45, 1\n"
"s_cbranch_scc0 Ls98_%=\n"
"s_mov_b32 s45, 0\n"
"s_branch Lb98_%=\n"
"Ls99_%=:\n"
"s_sleep 2\n"
"s_bitcmp1_b32 s61, 7\n"
"s_cselect_b32 s7, s60, s59\n"
"s_add_u32 s7, s7, 0x86c00\n"
"buffer_load_dwordx4 v[140:143], v16, s[8:11], s7 offen sc1\n"
"buffer_load_dwordx4 v[216:219], v17, s[8:11], s7 offen sc1\n"
"s_bitcmp1_b32 s61, 8\n"
"s_cselect_b32 s7, s60, s59\n"
"s_add_u32 s7, s7, 0x87000\n"
"buffer_load_dwordx4 v[172:175], v16, s[8:11], s7 offen sc1\n"
"buffer_load_dwordx4 v[200:203], v17, s[8:11], s7 offen sc1\n"
"s_bitcmp1_b32 s61, 9\n"
"s_cselect_b32 s7, s60, s59\n"
"s_add_u32 s7, s7, 0x87400\n"
"buffer_load_dwordx4 v[136:139], v16, s[8:11], s7 offen sc1\n"
"buffer_load_dwordx4 v[80:83], v17, s[8:11], s7 offen sc1\n"
"s_bitcmp1_b32 s61, 10\n"
"s_cselect_b32 s7, s60, s59\n"
"s_add_u32 s7, s7, 0x87800\n"
"buffer_load_dwordx4 v[68:71], v16, s[8:11], s7 offen sc1\n"
"buffer_load_dwordx4 v[36:39], v17, s[8:11], s7 offen sc1\n"
"s_bitcmp1_b32 s61, 11\n"
"s_cselect_b32 s7, s60, s59\n"
"s_add_u32 s7, s7, 0x87c00\n"
"buffer_load_dwordx4 v[252:255], v16, s[8:11], s7 offen sc1\n"
"buffer_load_dwordx4 v[40:43], v17, s[8:11], s7 offen sc1\n"
"s_waitcnt vmcnt(0)\n"
"v_min3_u32 v124, v140, v141, v142\n"
"v_min3_u32 v124, v124, v143, v216\n"
"v_min3_u32 v124, v124, v217, v218\n"
"v_min_u32 v124, v124, v219\n"
"v_cmp_lt_i32 vcc, -1, v124\n"
"s_cbranch_vccz Lb99_%=\n"
"s_sub_u32 s45, s45, 1\n"
"s_cbranch_scc0 Ls99_%=\n"
"s_mov_b32 s45, 0\n"
"s_branch Lb99_%=\n"
"Ls100_%=:\n"
"s_sleep 2\n"
"s_bitcmp1_b32 s61, 8\n"
"s_cselect_b32 s7, s60, s59\n"
"s_add_u32 s7, s7, 0x87000\n"
"buffer_load_dwordx4 v[172:175], v16, s[8:11], s7 offen sc1\n"
"buffer_load_dwordx4 v[200:203], v17, s[8:11], s7 offen sc1\n"
"s_bitcmp1_b32 s61, 9\n"
"s_cselect_b32 s7, s60, s59\n"
"s_add_u32 s7, s7, 0x87400\n"
"buffer_load_dwordx4 v[136:139], v16, s[8:11], s7 offen sc1\n"
"buffer_load_dwordx4 v[80:83], v17, s[8:11], s7 offen sc1\n"
"s_bitcmp1_b32 s61, 10\n"
"s_cselect_b32 s7, s60, s59\n"
"s_add_u32 s7, s7, 0x87800\n"
"buffer_load_dwordx4 v[68:71], v16, s[8:11], s7 offen sc1\n"
"buffer_load_dwordx4 v[36:39], v17, s[8:11], s7 offen sc1\n"
"s_bitcmp1_b32 s61, 11\n"
"s_cselect_b32 s7, s60, s59\n"
"s_add_u32 s7, s7, 0x87c00\n"
"buffer_load_dwordx4 v[252:255], v16, s[8:11], s7 offen sc1\n"
"buffer_load_dwordx4 v[40:43], v17, s[8:11], s7 offen sc1\n"
"s_waitcnt vmcnt(0)\n"
"v_min3_u32 v88, v172, v173, v174\n"
"v_min3_u32 v88, v88, v175, v200\n"
"v_min3_u32 v88, v88, v201, v202\n"
"v_min_u32 v88, v88, v203\n"
"v_cmp_lt_i32 vcc, -1, v88\n"
"s_cbranch_vccz Lb100_%=\n"
"s_sub_u32 s45, s45, 1\n"
"s_cbranch_scc0 Ls100_%=\n"
"s_mov_b32 s45, 0\n"
"s_branch Lb100_%=\n"
"Ls101_%=:\n"
"s_sleep 2\n"
"s_bitcmp1_b32 s61, 9\n"
"s_cselect_b32 s7, s60, s59\n"
"s_add_u32 s7, s7, 0x87400\n"
"buffer_load_dwordx4 v[136:139], v16, s[8:11], s7 offen sc1\n"
"buffer_load_dwordx4 v[80:83], v17, s[8:11], s7 offen sc1\n"
"s_bitcmp1_b32 s61, 10\n"
"s_cselect_b32 s7, s60, s59\n"
"s_add_u32 s7, s7, 0x87800\n"
"buffer_load_dwordx4 v[68:71], v16, s[8:11], s7 offen sc1\n"
"buffer_load_dwordx4 v[36:39], v17, s[8:11], s7 offen sc1\n"
"s_bitcmp1_b32 s61, 11\n"
"s_cselect_b32 s7, s60, s59\n"
"s_add_u32 s7, s7, 0x87c00\n"
"buffer_load_dwordx4 v[252:255], v16, s[8:11], s7 offen sc1\n"
"buffer_load_dwordx4 v[40:43], v17, s[8:11], s7 offen sc1\n"
"s_waitcnt vmcnt(0)\n"
"v_min3_u32 v208, v136, v137, v138\n"
"v_min3_u32 v208, v208, v139, v80\n"
"v_min3_u32 v208, v208, v81, v82\n"
"v_min_u32 v208, v208, v83\n"
"v_cmp_lt_i32 vcc, -1, v208\n"
"s_cbranch_vccz Lb101_%=\n"
"s_sub_u32 s45, s45, 1\n"
"s_cbranch_scc0 Ls101_%=\n"
"s_mov_b32 s45, 0\n"
"s_branch Lb101_%=\n"
"Ls102_%=:\n"
"s_sleep 2\n"
"s_bitcmp1_b32 s61, 10\n"
"s_cselect_b32 s7, s60, s59\n"
"s_add_u32 s7, s7, 0x87800\n"
"buffer_load_dwordx4 v[68:71], v16, s[8:11], s7 offen sc1\n"
"buffer_load_dwordx4 v[36:39], v17, s[8:11], s7 offen sc1\n"
"s_bitcmp1_b32 s61, 11\n"
"s_cselect_b32 s7, s60, s59\n"
"s_add_u32 s7, s7, 0x87c00\n"
"buffer_load_dwordx4 v[252:255], v16, s[8:11], s7 offen sc1\n"
"buffer_load_dwordx4 v[40:43], v17, s[8:11], s7 offen sc1\n"
"s_waitcnt vmcnt(0)\n"
"v_min3_u32 v92, v68, v69, v70\n"
"v_min3_u32 v92, v92, v71, v36\n"
"v_min3_u32 v92, v92, v37, v38\n"
"v_min_u32 v92, v92, v39\n"
"v_cmp_lt_i32 vcc, -1, v92\n"
"s_cbranch_vccz Lb102_%=\n"
"s_sub_u32 s45, s45, 1\n"
"s_cbranch_scc0 Ls102_%=\n"
"s_mov_b32 s45, 0\n"
"s_branch Lb102_%=\n"
"Ls103_%=:\n"
"s_sleep 2\n"
"s_bitcmp1_b32 s61, 11\n"
"s_cselect_b32 s7, s60, s59\n"
"s_add_u32 s7, s7, 0x87c00\n"
"buffer_load_dwordx4 v[252:255], v16, s[8:11], s7 offen sc1\n"
"buffer_load_dwordx4 v[40:43], v17, s[8:11], s7 offen sc1\n"
"s_waitcnt vmcnt(0)\n"
"v_min3_u32 v148, v252, v253, v254\n"
"v_min3_u32 v148, v148, v255, v40\n"
"v_min3_u32 v148, v148, v41, v42\n"
"v_min_u32 v148, v148, v43\n"
"v_cmp_lt_i32 vcc, -1, v148\n"
"s_cbranch_vccz Lb103_%=\n"
"s_sub_u32 s45, s45, 1\n"
"s_cbranch_scc0 Ls103_%=\n"
"s_mov_b32 s45, 0\n"
"s_branch Lb103_%=\n"
  :: "{v0}"(threadIdx.x), "{s[0:1]}"(__builtin_amdgcn_kernarg_segment_ptr()), "{s2}"(blockIdx.x), "{s[92:93]}"(&tab)
  : "memory", "vcc", "scc", "v255", "a255", "s99");
}

extern "C" void kernel_launch(void* const* d_in, const int* in_sizes, int n_in, void* d_out, int out_size, void* d_ws,
                              size_t ws_size, hipStream_t stream) {
  const u32 F = 0x20000, X = 2048 * 1280;
  k_main<<<256, 256, 0, stream>>>(d_in[0], X, F, d_ws, WS_LIM, F, d_in[5], d_in[7], d_in[1], 640 * 3840, F,
                                  d_in[0], X, F, d_ws, WS_LIM, F, d_in[5], d_in[7], d_in[6], 1728 * 1280, F,
                                  d_in[4], 640 * 3456, F, d_in[2], 640 * 1280, F, d_in[5], 3456, F, d_in[7], 1280, F, d_out, X, F);
}
```
